```python
import torch

def _softmax(x, axis=-1):
    return torch.softmax(x, dim=axis)
def _gelu(x):
    return torch.nn.functional.gelu(x, approximate="tanh")
def _silu(x):
    return torch.nn.functional.silu(x)
def _sigmoid(x):
    return torch.sigmoid(x)
def _layernorm(x, axis=-1, eps=1e-5):
    m = x.mean(dim=axis, keepdim=True)
    v = x.var(dim=axis, keepdim=True, unbiased=False)
    return (x - m) / torch.sqrt(v + eps)
def _scatter_add(tgt, idx, src, axis=0):
    return tgt.clone().index_add_(axis, idx, src)
def _segment_sum(src, seg, n):
    out = torch.zeros((n,) + src.shape[1:], dtype=src.dtype)
    return out.index_add_(0, seg, src)
def _pad(x, pw):
    flat = tuple(v for pair in reversed(pw) for v in pair)
    return torch.nn.functional.pad(x, flat)

kernel_name = 'interleave_layers__softmax_full_mha__moe_arch_mod__prompt'

def setup_inputs():

    _w = (lambda a: (lambda t: t.to(torch.bfloat16) if t.is_floating_point() else t)(torch.as_tensor(a)))
    def _A_setup():
        B, S, D = 1, 2048, 512
        return {
            "x": _w((lambda *s: torch.randn(*s, dtype=torch.bfloat16) * 0.05)(B, S, D)),
            "wqkv": _w((lambda *s: torch.randn(*s, dtype=torch.bfloat16) * 0.05)(D, 3 * D)),
            "wo": _w((lambda *s: torch.randn(*s, dtype=torch.bfloat16) * 0.05)(D, D)),
        }
    _A_in = _A_setup()

    def _B_setup():
        B, S, D, F, C = 1, 2048, 512, 1792, 1
        return {
            "x": _w((lambda *s: torch.randn(*s, dtype=torch.bfloat16) * 0.05)(B, S, D)),
            "w_r": _w((lambda *s: torch.randn(*s, dtype=torch.bfloat16) * 0.05)(D)),
            "w_gate": _w((lambda *s: torch.randn(*s, dtype=torch.bfloat16) * 0.05)(D, F)),
            "w_up": _w((lambda *s: torch.randn(*s, dtype=torch.bfloat16) * 0.05)(D, F)),
            "w_down": _w((lambda *s: torch.randn(*s, dtype=torch.bfloat16) * 0.05)(F, D)),
        }
    _B_in = _B_setup()

    return {
        "A_x": _A_in["x"],
        "A_wqkv": _A_in["wqkv"],
        "A_wo": _A_in["wo"],
        "B_x": _B_in["x"],
        "B_w_r": _B_in["w_r"],
        "B_w_gate": _B_in["w_gate"],
        "B_w_up": _B_in["w_up"],
        "B_w_down": _B_in["w_down"],
    }

def reference(A_x, A_wqkv, A_wo, B_x, B_w_r, B_w_gate, B_w_up, B_w_down):
    def _A_ref(x, wqkv, wo):
        B, S, D = x.shape
        H = 32; Dh = D // H
        qkv = (lambda x, *d: x.permute(*d))((x @ wqkv).reshape(B, S, 3, H, Dh), 2, 0, 3, 1, 4)
        q, k, v = qkv[0], qkv[1], qkv[2]
        scale = Dh ** -0.5
        scores = torch.einsum("bhqd,bhkd->bhqk", q, k) * scale
        p = _softmax(scores, axis=-1)
        o = torch.einsum("bhqk,bhkd->bhqd", p, v)
        return (lambda x, *d: x.permute(*d))(o, 0, 2, 1, 3).reshape(B, S, D) @ wo

    def _B_ref(x, w_r, w_gate, w_up, w_down):
        B, S, D = x.shape
        C = 1
        r = x @ w_r
        idx = torch.argsort(-r, axis=-1)[:, :C]
        idx_e = (lambda x, n, axis: torch.repeat_interleave(x, n, dim=axis))(idx.reshape(B, C, 1), D, axis=2)
        x_sel = (lambda x, i, axis: torch.take_along_dim(x, i, dim=axis))(x, idx_e, axis=1)
        g = _silu(x_sel @ w_gate)
        u = x_sel @ w_up
        y = (g * u) @ w_down
        r_sel = _sigmoid((lambda x, i, axis: torch.take_along_dim(x, i, dim=axis))(r, idx, axis=1))
        y = y * r_sel.reshape(B, C, 1)
        b_off = (torch.arange(B) * S).reshape(B, 1)
        flat_idx = (idx + b_off).reshape(B * C)
        zero = (x * 0.0).reshape(B * S, D)
        upd = _scatter_add(zero, flat_idx, y.reshape(B * C, D), axis=0)
        return x + upd.reshape(B, S, D)

    x = A_x
    x = x + _A_ref(_layernorm(x, axis=-1), A_wqkv, A_wo)
    x = x + _B_ref(_layernorm(x, axis=-1), B_w_r, B_w_gate, B_w_up, B_w_down)
    x = x + _A_ref(_layernorm(x, axis=-1), A_wqkv, A_wo)
    x = x + _B_ref(_layernorm(x, axis=-1), B_w_r, B_w_gate, B_w_up, B_w_down)
    return x
```

```cpp
#include <hip/hip_runtime.h>
#include <stdint.h>

typedef unsigned u32;
typedef unsigned long long u64;
typedef __attribute__((ext_vector_type(2))) u32 u32x2;
typedef __attribute__((ext_vector_type(4))) u32 u32x4;
typedef __attribute__((ext_vector_type(2))) float f32x2;
typedef __attribute__((ext_vector_type(4))) float f32x4;
typedef __attribute__((ext_vector_type(16))) float f32x16;
typedef __attribute__((ext_vector_type(8))) __bf16 bf16x8;
typedef __attribute__((ext_vector_type(2))) __bf16 bf16x2;
typedef __attribute__((ext_vector_type(4))) short s16x4;
typedef __attribute__((ext_vector_type(8))) short s16x8;
#define LAS __attribute__((address_space(3)))
#define DI __device__ __forceinline__
#define MFMA32(a, b, c) __builtin_amdgcn_mfma_f32_32x32x16_bf16((a), (b), (c), 0, 0, 0)
#define MFMA16(a, b, c) __builtin_amdgcn_mfma_f32_16x16x32_bf16((a), (b), (c), 0, 0, 0)
#define SBAR() __builtin_amdgcn_sched_barrier(0)
#define BAR() asm volatile("s_waitcnt lgkmcnt(0)\n\ts_barrier" ::: "memory")
#define DRAIN() asm volatile("s_waitcnt vmcnt(0)" ::: "memory")
#define MEMFENCE() asm volatile("" ::: "memory")

#define STOP_AFTER 99

constexpr u32 OFF_EPOCH = 0, OFF_ABORT = 256;
constexpr u32 OFF_FQ0 = 0x10000, OFF_FA0 = 0x18000, OFF_FO = 0x20000, OFF_FQ1 = 0x28000, OFF_FA1 = 0x30000;
constexpr u32 OFF_TS0 = 0x40000, OFF_TS1 = 0x48000;
constexpr u32 OFF_GC0 = 0x50000, OFF_GC1 = 0x54000;
constexpr u32 OFF_GA0 = 0x58000, OFF_GA1 = 0x5A000;
constexpr u32 OFF_GY = 0x5C000;
constexpr u32 OFF_GS0 = 0x80000, OFF_GS1 = 0x100000;
constexpr u32 OFF_GX0 = 0x180000, OFF_GX1 = 0x1A0000;
constexpr u32 OFF_Q0 = 0x200000, OFF_K0 = 0x400000, OFF_V0 = 0x600000, OFF_O0 = 0x800000, OFF_X1 = 0xA00000;
constexpr u32 OFF_Q1 = 0xC00000, OFF_K1 = 0xE00000, OFF_V1 = 0x1000000, OFF_O1 = 0x1200000;
constexpr u32 WS_BYTES = 0x1400000;

constexpr u32 LDS_BYTES = 163840;
constexpr u32 L_W = 0;
constexpr u32 L_XR = 98304;
constexpr u32 L_TOK = 161792;
constexpr u32 L_K = 0, L_V = 65536, VSTR = 1152;
constexpr u32 L_WO = 81920;
constexpr u32 L_XB = 65536;
constexpr u32 L_SB = 161792;
constexpr u32 L_P = 163328;
constexpr u32 P_CS = L_P, P_HCNT = L_P + 384, P_SEL = L_P + 400;
constexpr u32 FW0 = 98304, FS0 = 155648;
constexpr u32 FW1 = 0, FS1 = 57344;
constexpr u32 SPIN_CAP = 40000;

struct Params {
    const uint16_t* x0; const uint16_t* wqkv; const uint16_t* wo; const uint16_t* wr;
    const uint16_t* wg; const uint16_t* wu; const uint16_t* wd; uint16_t* out; char* ws;
};

typedef __amdgpu_buffer_rsrc_t rsrc_t;

DI u32 pk(float a, float b) { f32x2 v = {a, b}; return __builtin_bit_cast(u32, __builtin_convertvector(v, bf16x2)); }
DI float bflo(u32 v) { return __uint_as_float(v << 16); }
DI float bfhi(u32 v) { return __uint_as_float(v & 0xffff0000u); }
DI float rbf(float a) { return bflo(pk(a, 0.f)); }
DI u32x4 ldw(rsrc_t r, u32 off) { return __builtin_amdgcn_raw_buffer_load_b128(r, off, 0, 16); }
DI void stw(rsrc_t r, u32 off, u32x4 v) { __builtin_amdgcn_raw_buffer_store_b128(v, r, off, 0, 16); }
template <class T> DI T lds_ld(u32 a) { return *(const LAS T*)(uintptr_t)a; }
template <class T> DI void lds_st(u32 a, T v) { *(LAS T*)(uintptr_t)a = v; }
DI s16x4 lds_tr(u32 a) { return __builtin_amdgcn_ds_read_tr16_b64_v4i16((LAS s16x4*)(uintptr_t)a); }
DI bf16x8 frag_tr(u32 a0, u32 a1) { s16x4 lo = lds_tr(a0), hi = lds_tr(a1); return __builtin_bit_cast(bf16x8, __builtin_shufflevector(lo, hi, 0, 1, 2, 3, 4, 5, 6, 7)); }
DI float swap32_add(float v) { auto r = __builtin_amdgcn_permlane32_swap(__float_as_uint(v), __float_as_uint(v), false, false); return __uint_as_float(r[0]) + __uint_as_float(r[1]); }
DI float wave_sum(float v) {
#pragma unroll
    for (int m = 1; m < 32; m <<= 1) v += __shfl_xor(v, m);
    return swap32_add(v);
}
DI void glds(const void* sbase, u32 voff, u32 ldsdst) {
    u32 sv;
    asm volatile("s_mov_b32 %0, m0\n\ts_mov_b32 m0, %3\n\ts_nop 0\n\tglobal_load_lds_dwordx4 %1, %2\n\ts_mov_b32 m0, %0"
                 : "=&s"(sv) : "v"(voff), "s"(sbase), "s"(ldsdst) : "memory");
}
struct Ctx {
    rsrc_t ws; u32 tag; int lane, w, xg, gi;
};
DI bool spin_fail(const Ctx& c, u32& n) {
    __builtin_amdgcn_s_sleep(1);
    ++n;
    if (n > SPIN_CAP) { if (c.lane == 0) __builtin_amdgcn_raw_buffer_store_b32(c.tag, c.ws, OFF_ABORT, 0, 16); return true; }
    if ((n & 127u) == 0u) { u32 a = __builtin_amdgcn_raw_buffer_load_b32(c.ws, OFF_ABORT, 0, 16); if (__builtin_amdgcn_readfirstlane(a) == c.tag) return true; }
    return false;
}
DI void wait_flags(const Ctx& c, u32 off) {
    u32 n = 0;
    for (;;) {
        u32x2 v = __builtin_amdgcn_raw_buffer_load_b64(c.ws, off, 0, 16);
        MEMFENCE();
        if (__all(v[0] == c.tag && v[1] == ~c.tag)) break;
        if (spin_fail(c, n)) break;
    }
    MEMFENCE();
}
DI void set_flag(const Ctx& c, u32 off) { u32x2 v = {c.tag, ~c.tag}; __builtin_amdgcn_raw_buffer_store_b64(v, c.ws, off, 0, 16); }
DI bool unit_ok(u32x4 v, u32 tag) { return v[1] == tag && v[3] == tag; }
DI u32x4 mk_unit(u32 a, u32 b, u32 tag) { return (u32x4){a, tag, b, tag}; }
template <int N> DI void wait_units(const Ctx& c, const u32 (&off)[N], u32x4 (&v)[N]) {
    u32 n = 0;
    for (;;) {
        bool ok = true;
#pragma unroll
        for (int k = 0; k < N; ++k) v[k] = ldw(c.ws, off[k]);
        MEMFENCE();
#pragma unroll
        for (int k = 0; k < N; ++k) ok = ok && unit_ok(v[k], c.tag);
        if (__all(ok)) break;
        if (spin_fail(c, n)) break;
    }
    MEMFENCE();
}
DI u32 lds_poll_ld(u32 a) { return __hip_atomic_load((LAS u32*)(uintptr_t)a, __ATOMIC_RELAXED, __HIP_MEMORY_SCOPE_WORKGROUP); }
DI void helper_sync(const Ctx& c, u32& target) {
    target += 4;
    asm volatile("s_waitcnt lgkmcnt(0)" ::: "memory");
    if (c.lane == 0) __hip_atomic_fetch_add((LAS u32*)(uintptr_t)P_HCNT, 1u, __ATOMIC_RELAXED, __HIP_MEMORY_SCOPE_WORKGROUP);
    u32 n = 0;
    while (lds_poll_ld(P_HCNT) < target) { if (++n > (1u << 24)) break; }
    MEMFENCE();
}
DI void wait_sel(int L) {
    u32 n = 0;
    while (lds_poll_ld(P_SEL + L * 32 + 16) == 0u) { __builtin_amdgcn_s_sleep(1); if (++n > (1u << 22)) break; }
    MEMFENCE();
}

template <int L>
DI void phaseQ(const Ctx& c, const Params& p, u32& hsync);

template <int L>
DI void ffn_chain(const Ctx& c, const Params& p, u32& hsync) {
    const int lane = c.lane, hw = c.w - 4;
    constexpr u32 FW = L ? FW1 : FW0, FS = L ? FS1 : FS0;
    constexpr u32 GC = L ? OFF_GC1 : OFF_GC0, GX = L ? OFF_GX1 : OFF_GX0, GA = L ? OFF_GA1 : OFF_GA0;
    if (hw == 0) {
        u32 off[2] = {GC + c.xg * 2048u + lane * 32u, GC + c.xg * 2048u + lane * 32u + 16u};
        u32x4 v[2];
        wait_units<2>(c, off, v);
        float r = bfhi(v[0][0]); int tok = lane * 32 + (int)(v[0][0] & 31u);
        float mu = __uint_as_float(v[0][2]), rs = __uint_as_float(v[1][0]);
#pragma unroll
        for (int m = 1; m < 64; m <<= 1) {
            float r2 = __shfl_xor(r, m), mu2 = __shfl_xor(mu, m), rs2 = __shfl_xor(rs, m); int t2 = __shfl_xor(tok, m);
            bool take = (r2 > r) || (r2 == r && t2 < tok);
            r = take ? r2 : r; tok = take ? t2 : tok; mu = take ? mu2 : mu; rs = take ? rs2 : rs;
        }
        if (lane == 0) {
            lds_st<u32x4>(P_SEL + L * 32, (u32x4){(u32)tok, __float_as_uint(r), __float_as_uint(mu), __float_as_uint(rs)});
            asm volatile("s_waitcnt lgkmcnt(0)" ::: "memory");
            __hip_atomic_store((LAS u32*)(uintptr_t)(P_SEL + L * 32 + 16), 1u, __ATOMIC_RELAXED, __HIP_MEMORY_SCOPE_WORKGROUP);
        }
    }
    const bool is_gu = c.gi < 14, is_dn = c.gi >= 14 && c.gi < 18;
    if (!is_gu && !is_dn) return;
    wait_sel(L);
    const u32x4 si = lds_ld<u32x4>(P_SEL + L * 32);
    const int sel = __builtin_amdgcn_readfirstlane((int)si[0]) & 2047;
    const float rsel = __uint_as_float(si[1]), mu = __uint_as_float(si[2]), rs = __uint_as_float(si[3]);
    const u32 gxrow = GX + (u32)(sel >> 5) * 2048u;
    const int g16 = lane >> 4, i16 = lane & 15;
    const u32 troff = (u32)((8 * g16 + (i16 >> 2)) * 32 + (i16 & 3) * 8);
    if (is_gu) {
        const int gidx = c.xg * 14 + c.gi;
        u32 off[1] = {gxrow + (u32)(hw * 32 + (lane & 31)) * 16u};
        u32x4 v[1];
        wait_units<1>(c, off, v);
        if (lane < 32) {
            u32x2 h;
            h[0] = pk((bflo(v[0][0]) - mu) * rs, (bfhi(v[0][0]) - mu) * rs);
            h[1] = pk((bflo(v[0][2]) - mu) * rs, (bfhi(v[0][2]) - mu) * rs);
            lds_st<u32x2>(FS + (u32)(hw * 32 + lane) * 8u, h);
        }
        helper_sync(c, hsync);
        f32x4 ag = {}, au = {};
#pragma unroll
        for (int s = 0; s < 4; ++s) {
            const int st = hw * 4 + s;
            bf16x8 a = lds_ld<bf16x8>(FS + (u32)(st * 64 + g16 * 16));
            u32 bo = FW + (u32)st * 1024u + troff;
            ag = MFMA16(a, frag_tr(bo, bo + 128), ag);
            au = MFMA16(a, frag_tr(bo + 16384, bo + 16384 + 128), au);
        }
        if (lane < 16) { lds_st<float>(FS + 4608 + (u32)(hw * 32 + lane) * 4u, ag[0]); lds_st<float>(FS + 4608 + (u32)(hw * 32 + 16 + lane) * 4u, au[0]); }
        helper_sync(c, hsync);
        if (hw == 0) {
            float g = 0.f, u = 0.f;
#pragma unroll
            for (int k = 0; k < 4; ++k) { g += lds_ld<float>(FS + 4608 + (u32)(k * 32 + i16) * 4u); u += lds_ld<float>(FS + 4608 + (u32)(k * 32 + 16 + i16) * 4u); }
            g = rbf(g); u = rbf(u);
            float sg = rbf(g / (1.0f + __expf(-g)));
            float a = rbf(sg * u);
            float a1 = __shfl(a, (lane & 3) * 4 + 1), a2 = __shfl(a, (lane & 3) * 4 + 2), a3 = __shfl(a, (lane & 3) * 4 + 3), a0 = __shfl(a, (lane & 3) * 4);
            if (lane < 4) stw(c.ws, GA + (u32)(gidx * 4 + lane) * 16u, mk_unit(pk(a0, a1), pk(a2, a3), c.tag));
        }
    } else {
        const int didx = c.xg * 4 + (c.gi - 14);
        {
            const int u0 = hw * 112 + lane, u1 = hw * 112 + 64 + (lane < 48 ? lane : 47);
            u32 off[3] = {GA + (u32)u0 * 16u, GA + (u32)u1 * 16u, gxrow + (u32)(didx * 4 + (lane & 3)) * 16u};
            u32x4 v[3];
            wait_units<3>(c, off, v);
            lds_st<u32x2>(FS + 1024 + (u32)u0 * 8u, (u32x2){v[0][0], v[0][2]});
            if (lane < 48) lds_st<u32x2>(FS + 1024 + (u32)u1 * 8u, (u32x2){v[1][0], v[1][2]});
            if (hw == 0 && lane < 4) lds_st<u32x2>(FS + (u32)lane * 8u, (u32x2){v[2][0], v[2][2]});
        }
        helper_sync(c, hsync);
        f32x4 ay = {};
#pragma unroll
        for (int s = 0; s < 14; ++s) {
            const int st = hw * 14 + s;
            bf16x8 a = lds_ld<bf16x8>(FS + 1024 + (u32)(st * 64 + g16 * 16));
            u32 bo = FW + (u32)st * 1024u + troff;
            ay = MFMA16(a, frag_tr(bo, bo + 128), ay);
        }
        if (lane < 16) lds_st<float>(FS + 4608 + (u32)(hw * 16 + lane) * 4u, ay[0]);
        helper_sync(c, hsync);
        if (hw == 0) {
            float y = 0.f;
#pragma unroll
            for (int k = 0; k < 4; ++k) y += lds_ld<float>(FS + 4608 + (u32)(k * 16 + i16) * 4u);
            const float x1 = __uint_as_float((u32)lds_ld<uint16_t>(FS + (u32)i16 * 2u) << 16);
            const float sig = rbf(1.0f / (1.0f + __expf(-rsel)));
            y = rbf(rbf(y) * sig);
            const float h = rbf((x1 - mu) * rs);
            const float x2 = rbf(x1 + rbf(h + y));
            float a0 = __shfl(x2, (lane & 3) * 4), a1 = __shfl(x2, (lane & 3) * 4 + 1), a2 = __shfl(x2, (lane & 3) * 4 + 2), a3 = __shfl(x2, (lane & 3) * 4 + 3);
            if (L == 0) { if (lane < 4) stw(c.ws, OFF_GY + (u32)(didx * 4 + lane) * 16u, mk_unit(pk(a0, a1), pk(a2, a3), c.tag)); }
            else if (lane < 4) *(u32x2*)(p.out + (size_t)sel * 512 + didx * 16 + lane * 4) = (u32x2){pk(a0, a1), pk(a2, a3)};
        }
    }
}

template <int L>
DI void ffn_weights_dma(const Ctx& c, const Params& p) {
    constexpr u32 FW = L ? FW1 : FW0;
    const int hw = c.w - 4, lane = c.lane;
    if (c.gi < 14) {
        const int gidx = c.xg * 14 + c.gi;
        const u32 vo = (u32)((lane >> 1) * 3584 + gidx * 32 + (lane & 1) * 16);
#pragma unroll
        for (int j = 0; j < 4; ++j) {
            const int blk = hw * 4 + j;
            glds(p.wg, vo + (u32)blk * (32u * 3584u), FW + (u32)blk * 1024u);
            glds(p.wu, vo + (u32)blk * (32u * 3584u), FW + 16384u + (u32)blk * 1024u);
        }
    } else if (c.gi < 18) {
        const int didx = c.xg * 4 + (c.gi - 14);
        const u32 vo = (u32)((lane >> 1) * 1024 + didx * 32 + (lane & 1) * 16);
#pragma unroll
        for (int j = 0; j < 14; ++j) {
            const int blk = hw * 14 + j;
            glds(p.wd, vo + (u32)blk * (32u * 1024u), FW + (u32)blk * 1024u);
        }
    }
}
DI void wqkv_dma(const Ctx& c, const Params& p, int hp, int t0, int t1, int wi, int nw) {
    const u32 vo = (u32)((c.lane >> 2) * 3072 + hp * 64 + (c.lane & 3) * 16);
    for (int t = t0; t < t1; ++t)
        for (int g = wi; g < 32; g += nw)
            glds(p.wqkv, vo + (u32)t * 1024u + (u32)g * (16u * 3072u), L_W + (u32)t * 32768u + (u32)g * 1024u);
}

template <int L>
DI void phaseQ(const Ctx& c, const Params& p, u32& hsync) {
    const int lane = c.lane, w = c.w;
    const int rb = 4 * (c.xg & 3) + (c.gi & 3), hp = 8 * (c.xg >> 2) + (c.gi >> 2);
    const int r = lane & 31, h = lane >> 5;
    const int i16 = lane & 15, blk = (lane >> 4) & 1;
    const u32 trA = L_W + (u32)(((L == 0 ? 8 : 4) * h + (i16 >> 2)) * 64 + blk * 32 + (i16 & 3) * 8);
    constexpr u32 TR2 = L == 0 ? 256 : 512;
    constexpr u32 OQ = L ? OFF_Q1 : OFF_Q0, OK_ = L ? OFF_K1 : OFF_K0, OV = L ? OFF_V1 : OFF_V0, TS = L ? OFF_TS1 : OFF_TS0;

    if (w >= 4) {
        const int hw = w - 4;
        if (L == 0) {
            u32x4 r0, r1;
            const u32 tok = (u32)(rb * 128 + hp * 8 + hw * 2);
            const u32 vo = tok * 1024u + (u32)lane * 16u;
            asm volatile("global_load_dwordx4 %0, %1, %2" : "=v"(r0) : "v"(vo), "s"(p.x0));
            asm volatile("global_load_dwordx4 %0, %1, %2 offset:1024" : "=v"(r1) : "v"(vo), "s"(p.x0));
            const u32 wv = (u32)((lane >> 2) * 3072 + hp * 64 + (lane & 3) * 16 + hw * 16 * 3072);
#pragma unroll
            for (int kc = 0; kc < 8; ++kc)
#pragma unroll
                for (int t = 0; t < 3; ++t)
                    glds(p.wqkv, wv + (u32)t * 1024u + (u32)kc * (64u * 3072u), L_W + (u32)t * 32768u + (u32)(kc * 64 + hw * 16) * 64u);
            asm volatile("s_waitcnt vmcnt(24)" : "+v"(r0), "+v"(r1));
            float s0 = 0, q0 = 0, s1 = 0, q1 = 0;
#pragma unroll
            for (int j = 0; j < 4; ++j) {
                float a = bflo(r0[j]), b_ = bfhi(r0[j]); s0 += a + b_; q0 += a * a + b_ * b_;
                float d = bflo(r1[j]), e = bfhi(r1[j]); s1 += d + e; q1 += d * d + e * e;
            }
            s0 = wave_sum(s0); q0 = wave_sum(q0); s1 = wave_sum(s1); q1 = wave_sum(q1);
            const float s = lane ? s1 : s0, q = lane ? q1 : q0;
            const float mu = s * (1.0f / 512.0f), var = fmaxf(q * (1.0f / 512.0f) - mu * mu, 0.f);
            const float A = 1.0f / sqrtf(var + 1e-5f);
            if (lane < 2) stw(c.ws, TS + (tok + (u32)lane) * 16u, mk_unit(__float_as_uint(A), __float_as_uint(A * mu), c.tag));
            f32x16 cs = {};
            const bf16x8 ones = __builtin_bit_cast(bf16x8, (u32x4){0x3f803f80u, 0x3f803f80u, 0x3f803f80u, 0x3f803f80u});
#define QWAIT(n) asm volatile("s_waitcnt vmcnt(" #n ")\n\ts_barrier" ::: "memory")
#define CS_CHUNK(kc) if (hw < 3) { _Pragma("unroll") for (int s_ = 0; s_ < 4; ++s_) { u32 a_ = trA + (u32)hw * 32768u + (u32)((kc) * 4 + s_) * 1024u; cs = MFMA32(frag_tr(a_, a_ + TR2), ones, cs); } }
            QWAIT(22); CS_CHUNK(0); QWAIT(19); CS_CHUNK(1); QWAIT(16); CS_CHUNK(2); QWAIT(13); CS_CHUNK(3);
            QWAIT(10); CS_CHUNK(4); QWAIT(7); CS_CHUNK(5); QWAIT(4); CS_CHUNK(6); QWAIT(1); CS_CHUNK(7);
#undef QWAIT
#undef CS_CHUNK
            if (hw < 3 && r == 0) {
#pragma unroll
                for (int g = 0; g < 4; ++g) lds_st<f32x4>(P_CS + (u32)(hw * 32 + 8 * g + 4 * h) * 4u, (f32x4){cs[4 * g], cs[4 * g + 1], cs[4 * g + 2], cs[4 * g + 3]});
            }
            BAR();
        } else {
            ffn_chain<0>(c, p, hsync);
        }
        DRAIN(); BAR();
    } else {
        const int T = rb * 4 + w;
        f32x16 aq = {}, ak = {}, av = {};
        float A, B;
        if (L == 0) {
            const u32 xr = L_XR + (u32)w * 12288u;
            u32 xv[4];
#pragma unroll
            for (int q = 0; q < 4; ++q) { const int row = 8 * q + (lane >> 3); xv[q] = (u32)((T * 32 + row) * 1024 + (((lane & 7) ^ ((row >> 1) & 7)) * 16)); }
#define XDMA(ch) do { _Pragma("unroll") for (int q = 0; q < 4; ++q) glds(p.x0, xv[q] + (u32)(ch) * 128u, xr + (u32)((ch) % 3) * 4096u + (u32)q * 1024u); } while (0)
            XDMA(0); XDMA(1); XDMA(2);
            u32 xa[4];
#pragma unroll
            for (int s = 0; s < 4; ++s) xa[s] = xr + (u32)(r * 128 + (((2 * s + h) ^ ((r >> 1) & 7)) * 16));
#define QSTEP(ch, n) do { asm volatile("s_waitcnt vmcnt(" #n ")\n\ts_barrier" ::: "memory"); \
                bf16x8 xf[4]; _Pragma("unroll") for (int s = 0; s < 4; ++s) xf[s] = lds_ld<bf16x8>(xa[s] + (u32)((ch) % 3) * 4096u); \
                asm volatile("s_waitcnt lgkmcnt(0)" : "+v"(xf[0]), "+v"(xf[1]), "+v"(xf[2]), "+v"(xf[3]) :: "memory"); \
                if ((ch) + 3 < 8) XDMA((ch) + 3); \
                _Pragma("unroll") for (int s = 0; s < 4; ++s) { const u32 a_ = trA + (u32)((ch) * 4 + s) * 1024u; \
                    aq = MFMA32(frag_tr(a_, a_ + TR2), xf[s], aq); ak = MFMA32(frag_tr(a_ + 32768, a_ + 32768 + TR2), xf[s], ak); \
                    av = MFMA32(xf[s], frag_tr(a_ + 65536, a_ + 65536 + TR2), av); } } while (0)
            QSTEP(0, 8); QSTEP(1, 8); QSTEP(2, 8); QSTEP(3, 8); QSTEP(4, 8); QSTEP(5, 8); QSTEP(6, 4); QSTEP(7, 0);
#undef QSTEP
#undef XDMA
            u32 off[1] = {TS + (u32)(T * 32 + r) * 16u}; u32x4 v[1];
            wait_units<1>(c, off, v);
            A = __uint_as_float(v[0][0]); B = __uint_as_float(v[0][2]);
            BAR();
        } else {
            wait_flags(c, OFF_FO + (u32)(rb * 16 + (lane & 15)) * 128u);
            u32x4 xf[32];
            const u32 xo = OFF_X1 + (u32)T * 32768u + (u32)lane * 16u;
#pragma unroll
            for (int s = 0; s < 32; ++s) xf[s] = ldw(c.ws, xo + (u32)s * 1024u);
            {
                u32 off[1] = {TS + (u32)(T * 32 + r) * 16u}; u32x4 v[1];
                wait_units<1>(c, off, v);
                A = __uint_as_float(v[0][0]); B = __uint_as_float(v[0][2]);
            }
            wait_sel(0);
            const int sel = __builtin_amdgcn_readfirstlane((int)lds_ld<u32>(P_SEL)) & 2047;
            if ((sel >> 5) == T) {
                u32 off[2] = {OFF_GY + (u32)lane * 32u, OFF_GY + (u32)lane * 32u + 16u}; u32x4 v[2];
                wait_units<2>(c, off, v);
                const u32 xl = FS0 + 5120u;
                float s = 0, q = 0;
#pragma unroll
                for (int k = 0; k < 2; ++k) {
                    const int u = lane * 2 + k, q4 = u & 3;
                    lds_st<u32x2>(xl + (u32)((u >> 2) * 32 + (q4 & 1) * 16 + (q4 >> 1) * 8), (u32x2){v[k][0], v[k][2]});
                    float a = bflo(v[k][0]), b_ = bfhi(v[k][0]), d = bflo(v[k][2]), e = bfhi(v[k][2]);
                    s += (a + b_) + (d + e); q += a * a + b_ * b_ + d * d + e * e;
                }
                s = wave_sum(s); q = wave_sum(q);
                const float mu = s * (1.0f / 512.0f), var = fmaxf(q * (1.0f / 512.0f) - mu * mu, 0.f);
                const float A2 = 1.0f / sqrtf(var + 1e-5f);
                if (r == (sel & 31)) {
                    A = A2; B = A2 * mu;
#pragma unroll
                    for (int st = 0; st < 32; ++st) xf[st] = lds_ld<u32x4>(xl + (u32)(st * 32 + h * 16));
                }
            }
#pragma unroll
            for (int st = 0; st < 32; ++st) {
                const u32 a_ = trA + (u32)st * 1024u;
                const bf16x8 x = __builtin_bit_cast(bf16x8, xf[st]);
                aq = MFMA32(frag_tr(a_, a_ + TR2), x, aq);
                ak = MFMA32(frag_tr(a_ + 32768, a_ + 32768 + TR2), x, ak);
                av = MFMA32(x, frag_tr(a_ + 65536, a_ + 65536 + TR2), av);
            }
        }
        const u32 tokl = L_TOK + (u32)w * 256u;
        if (h == 0) lds_st<f32x2>(tokl + (u32)r * 8u, (f32x2){A, B});
        constexpr float QS = 0.25f * 1.4426950408889634f;
#pragma unroll
        for (int t = 0; t < 2; ++t) {
            const f32x16& acc = t ? ak : aq;
            const float sA = t ? A : A * QS, sB = t ? B : B * QS;
            float o[16];
#pragma unroll
            for (int g = 0; g < 4; ++g) {
                const f32x4 cs = lds_ld<f32x4>(P_CS + (u32)(t * 32 + 8 * g + 4 * h) * 4u);
#pragma unroll
                for (int e = 0; e < 4; ++e) o[4 * g + e] = sA * acc[4 * g + e] - sB * cs[e];
            }
#pragma unroll
            for (int a = 0; a < 2; ++a) {
                u32x4 v = {pk(o[8 * a], o[8 * a + 1]), pk(o[8 * a + 2], o[8 * a + 3]), pk(o[8 * a + 4], o[8 * a + 5]), pk(o[8 * a + 6], o[8 * a + 7])};
                stw(c.ws, (t ? OK_ : OQ) + (u32)(((hp * 2 + a) * 64 + T) * 64 + lane) * 16u, v);
            }
        }
        {
            asm volatile("s_waitcnt lgkmcnt(0)" ::: "memory");
            const float cs = lds_ld<float>(P_CS + (u32)(64 + r) * 4u);
            float o[16];
#pragma unroll
            for (int g = 0; g < 4; ++g) {
                const f32x4 ab0 = lds_ld<f32x4>(tokl + (u32)(8 * g + 4 * h) * 8u), ab1 = lds_ld<f32x4>(tokl + (u32)(8 * g + 4 * h + 2) * 8u);
                o[4 * g] = ab0[0] * av[4 * g] - ab0[1] * cs; o[4 * g + 1] = ab0[2] * av[4 * g + 1] - ab0[3] * cs;
                o[4 * g + 2] = ab1[0] * av[4 * g + 2] - ab1[1] * cs; o[4 * g + 3] = ab1[2] * av[4 * g + 3] - ab1[3] * cs;
            }
            const int head = hp * 2 + (r >> 4), d = r & 15;
#pragma unroll
            for (int s = 0; s < 2; ++s) {
                u32x4 v = {pk(o[8 * s], o[8 * s + 1]), pk(o[8 * s + 2], o[8 * s + 3]), pk(o[8 * s + 4], o[8 * s + 5]), pk(o[8 * s + 6], o[8 * s + 7])};
                stw(c.ws, OV + (u32)((head * 64 + T) * 1024 + s * 512 + h * 256 + d * 16), v);
            }
        }
        DRAIN(); BAR();
        if (w == 0 && lane == 0) set_flag(c, (L ? OFF_FQ1 : OFF_FQ0) + (u32)(hp * 16 + rb) * 128u);
    }
}

template <bool USE_C, bool STAGE>
DI void attn_loop(const Ctx& c, u32 kp, u32 vp0, u32 vp1, bf16x8 qf, const f32x16& cinit, f32x16& O, u32 koff, u32 voff, u32x4 rk, u32x4 rv) {
    const f32x16 zero = {};
#define LDF(base, off) lds_ld<bf16x8>((base) + (off))
#define CI (USE_C ? cinit : zero)
    bf16x8 kA = LDF(kp, 0), kB = LDF(kp, 1024), v0 = LDF(vp0, 0), v1 = LDF(vp1, 0);
    f32x16 SA = MFMA32(kA, qf, CI), SB;
    const u32 kw = L_K + (u32)c.w * 1024u + (u32)c.lane * 16u, vw = L_V + (u32)c.w * VSTR + (u32)c.lane * 16u;
#define STEP(SC, SN, kN, kC, tt) do { \
        if (STAGE && (tt) == 5 && ch < 7) {   \
            lds_st<u32x4>(kw + (u32)(ch + 1) * 8192u, rk); lds_st<u32x4>(vw + (u32)(ch + 1) * (8u * VSTR), rv); \
            if (ch < 6) { rk = ldw(c.ws, koff + (u32)(ch + 2) * 8192u); rv = ldw(c.ws, voff + (u32)(ch + 2) * 8192u); } } \
        if (STAGE && (tt) == 6 && ch < 7) BAR(); \
        SBAR(); \
        SN = MFMA32(kN, qf, CI); kC = LDF(kp, ((tt) + 2) * 1024); SBAR(); \
        u32x4 p0, p1; \
        { float e0 = EXP2(SC[0]), e1 = EXP2(SC[1]), e2 = EXP2(SC[2]), e3 = EXP2(SC[3]); p0[0] = pk(e0, e1); \
          float e4 = EXP2(SC[4]), e5 = EXP2(SC[5]); p0[1] = pk(e2, e3); float e6 = EXP2(SC[6]), e7 = EXP2(SC[7]); p0[2] = pk(e4, e5); \
          float f0 = EXP2(SC[8]); p0[3] = pk(e6, e7); float f1 = EXP2(SC[9]); SBAR(); \
          O = MFMA32(v0, __builtin_bit_cast(bf16x8, p0), O); v0 = LDF(vp0, ((tt) + 1) * VSTR); SBAR(); \
          float f2 = EXP2(SC[10]), f3 = EXP2(SC[11]); p1[0] = pk(f0, f1); float f4 = EXP2(SC[12]), f5 = EXP2(SC[13]); p1[1] = pk(f2, f3); \
          float f6 = EXP2(SC[14]), f7 = EXP2(SC[15]); p1[2] = pk(f4, f5); SBAR(); p1[3] = pk(f6, f7); SBAR(); } \
        O = MFMA32(v1, __builtin_bit_cast(bf16x8, p1), O); v1 = LDF(vp1, ((tt) + 1) * VSTR); SBAR(); \
    } while (0)
#define EXP2(x) __builtin_amdgcn_exp2f(x)
    for (int ch = 0; ch < 8; ++ch) {
        asm volatile("" : "+v"(kp), "+v"(vp0), "+v"(vp1));
        STEP(SA, SB, kB, kA, 0); STEP(SB, SA, kA, kB, 1); STEP(SA, SB, kB, kA, 2); STEP(SB, SA, kA, kB, 3);
        STEP(SA, SB, kB, kA, 4); STEP(SB, SA, kA, kB, 5); STEP(SA, SB, kB, kA, 6); STEP(SB, SA, kA, kB, 7);
        kp += 8192; vp0 += 8 * VSTR; vp1 += 8 * VSTR;
    }
#undef STEP
#undef EXP2
#undef LDF
#undef CI
}

template <int L>
DI void phaseA(const Ctx& c, const Params& p) {
    const int lane = c.lane, w = c.w, tid = w * 64 + lane;
    const int head = 4 * c.xg + (c.gi >> 3), qb = c.gi & 7;
    constexpr u32 OQ = L ? OFF_Q1 : OFF_Q0, OK_ = L ? OFF_K1 : OFF_K0, OV = L ? OFF_V1 : OFF_V0, OO = L ? OFF_O1 : OFF_O0;
    { const int t = tid >> 3, s = tid & 7; const u32 val = (s & 3) == 0 ? 0x3f803f80u : 0u;
      lds_st<u32x4>(L_V + (u32)t * VSTR + 1024u + (u32)s * 16u, (u32x4){val, val, val, val}); }
    if (w == 0) wait_flags(c, (L ? OFF_FQ1 : OFF_FQ0) + (u32)((head >> 1) * 16 + (lane & 15)) * 128u);
    BAR();
    const u32 koff = OK_ + (u32)((head * 64 + w) * 64 + lane) * 16u, voff = OV + (u32)((head * 64 + w) * 64 + lane) * 16u;
    u32x4 k0 = ldw(c.ws, koff), v0 = ldw(c.ws, voff);
    const u32x4 qraw = ldw(c.ws, OQ + (u32)((head * 64 + qb * 8 + w) * 64 + lane) * 16u);
    u32x4 rk = ldw(c.ws, koff + 8192u), rv = ldw(c.ws, voff + 8192u);
    lds_st<u32x4>(L_K + (u32)w * 1024u + (u32)lane * 16u, k0);
    lds_st<u32x4>(L_V + (u32)w * VSTR + (u32)lane * 16u, v0);
    BAR();
    const int r = lane & 31, h = lane >> 5;
    const u32 kp = L_K + (u32)lane * 16u;
    const u32 onesoff = 1024u + ((r >= 20 && r < 28) ? 64u : 0u) + ((r == 16 || r == 20) ? 0u : 16u);
    const u32 vp0 = L_V + (r < 16 ? (u32)(h * 256 + r * 16) : onesoff);
    const u32 vp1 = L_V + (r < 16 ? (u32)(512 + h * 256 + r * 16) : onesoff);
    const bf16x8 qf = __builtin_bit_cast(bf16x8, qraw);
    f32x16 O = {}; const f32x16 cz = {};
    attn_loop<false, true>(c, kp, vp0, vp1, qf, cz, O, koff, voff, rk, rv);
    float l = O[8];
    bool bad = !(l >= 0x1p-60f && l <= 0x1p60f);
#pragma unroll
    for (int j = 0; j < 8; ++j) bad = bad || !(__builtin_fabsf(O[j]) <= 0x1p126f);
    if (__any(bad)) {
        float m = -INFINITY; const f32x16 zero = {};
        for (int t = 0; t < 64; ++t) {
            f32x16 S = MFMA32(lds_ld<bf16x8>(kp + (u32)t * 1024u), qf, zero);
#pragma unroll
            for (int j = 0; j < 16; ++j) m = fmaxf(m, S[j]);
        }
        m = fmaxf(m, __shfl_xor(m, 32));
        f32x16 ci;
#pragma unroll
        for (int j = 0; j < 16; ++j) ci[j] = -m;
        O = f32x16{};
        attn_loop<true, false>(c, kp, vp0, vp1, qf, ci, O, 0, 0, rk, rv);
        l = O[8];
    }
    const float rl = 1.0f / l;
    u32x4 o;
#pragma unroll
    for (int j = 0; j < 4; ++j) o[j] = pk(O[2 * j] * rl, O[2 * j + 1] * rl);
    stw(c.ws, OO + (u32)((head * 64 + qb * 8 + w) * 64 + lane) * 16u, o);
    DRAIN(); BAR();
    if (w == 0 && lane == 0) set_flag(c, (L ? OFF_FA1 : OFF_FA0) + (u32)(qb * 32 + head) * 128u);
}

template <int L>
DI void phaseO(const Ctx& c, const Params& p, u32x4 (&x2)[2], u32& hsync) {
    const int lane = c.lane, w = c.w;
    const int tb = 4 * c.xg + (c.gi >> 3), fb = c.gi & 7;
    const int r = lane & 31, h = lane >> 5;
    constexpr u32 OO = L ? OFF_O1 : OFF_O0, GS = L ? OFF_GS1 : OFF_GS0, GC = L ? OFF_GC1 : OFF_GC0, GX = L ? OFF_GX1 : OFF_GX0;
    {
        const u32 vo = (u32)((lane >> 2) * 1024 + fb * 128 + (lane & 3) * 16);
#pragma unroll
        for (int j = 0; j < 8; ++j) {
            const int n = w * 8 + j, ft = n >> 5, g = n & 31;
            glds(p.wo, vo + (u32)ft * 64u + (u32)g * (16u * 1024u), L_WO + (u32)ft * 32768u + (u32)g * 1024u);
        }
    }
    if (w >= 4) {
        const int hw = w - 4;
        if (hw == 0) wait_flags(c, (L ? OFF_FA1 : OFF_FA0) + (u32)((tb >> 2) * 32 + (lane & 31)) * 128u);
        DRAIN(); BAR();
        BAR();
        if (L == 0) wqkv_dma(c, p, 8 * (c.xg >> 2) + (c.gi >> 2), 0, 2, hw, 4); else ffn_weights_dma<1>(c, p);
        BAR();
        if (L == 0) {
            const int hp = 8 * (c.xg >> 2) + (c.gi >> 2);
            wqkv_dma(c, p, hp, 2, 3, hw, 4);
            ffn_weights_dma<0>(c, p);
            BAR();
            DRAIN(); BAR();
        } else {
            DRAIN();
            helper_sync(c, hsync);
            ffn_chain<1>(c, p, hsync);
        }
        return;
    }
    const int tt = w & 1, kh = w >> 1;
    const int tile = tb * 2 + tt, tok = tile * 32 + r;
    const int f0 = fb * 64 + kh * 32;
    u32x2 res[4], wr[4];
#pragma unroll
    for (int g = 0; g < 4; ++g) {
        if (L == 0) res[g] = *(const u32x2*)(p.x0 + (size_t)tok * 512 + f0 + 8 * g + 4 * h);
        wr[g] = *(const u32x2*)(p.wr + f0 + 8 * g + 4 * h);
    }
    float wsum;
    { const u32x4 v = *(const u32x4*)(p.wr + lane * 8); float s = 0;
#pragma unroll
      for (int j = 0; j < 4; ++j) s += bflo(v[j]) + bfhi(v[j]);
      wsum = wave_sum(s); }
    if (L == 1) {
        const int sel0 = __builtin_amdgcn_readfirstlane((int)lds_ld<u32>(P_SEL)) & 2047;
        if ((sel0 >> 5) == tile) {
            const u32 ub = OFF_GY + (u32)(f0 / 4 + h) * 16u;
            u32 off[4] = {ub, ub + 32u, ub + 64u, ub + 96u}; u32x4 v[4];
            wait_units<4>(c, off, v);
            if (r == (sel0 & 31)) { x2[0] = (u32x4){v[0][0], v[0][2], v[1][0], v[1][2]}; x2[1] = (u32x4){v[2][0], v[2][2], v[3][0], v[3][2]}; }
        }
#pragma unroll
        for (int g = 0; g < 4; ++g) res[g] = (u32x2){x2[g >> 1][(g & 1) * 2], x2[g >> 1][(g & 1) * 2 + 1]};
    }
    DRAIN(); BAR();
    u32x4 bf[16];
    const u32 oo = OO + (u32)(((kh * 16) * 64 + tile) * 64 + lane) * 16u;
#pragma unroll
    for (int s = 0; s < 16; ++s) bf[s] = ldw(c.ws, oo + (u32)s * 65536u);
    const int i16 = lane & 15, blk = (lane >> 4) & 1;
    const u32 trA = L_WO + (u32)((kh * 256 + 4 * h + (i16 >> 2)) * 64 + blk * 32 + (i16 & 3) * 8);
    f32x16 a0 = {}, a1 = {};
#pragma unroll
    for (int s = 0; s < 16; ++s) {
        const u32 a_ = trA + (u32)s * 1024u;
        const bf16x8 bb = __builtin_bit_cast(bf16x8, bf[s]);
        a0 = MFMA32(frag_tr(a_, a_ + 512), bb, a0);
        a1 = MFMA32(frag_tr(a_ + 32768, a_ + 32768 + 512), bb, a1);
    }
    {
        const f32x16& snd = kh ? a0 : a1;
#pragma unroll
        for (int g = 0; g < 4; ++g) lds_st<f32x4>(L_XB + (u32)w * 4096u + (u32)g * 1024u + (u32)lane * 16u, (f32x4){snd[4 * g], snd[4 * g + 1], snd[4 * g + 2], snd[4 * g + 3]});
    }
    BAR();
    float x1[16];
    {
        const f32x16& keep = kh ? a1 : a0;
#pragma unroll
        for (int g = 0; g < 4; ++g) {
            const f32x4 o = lds_ld<f32x4>(L_XB + (u32)(w ^ 2) * 4096u + (u32)g * 1024u + (u32)lane * 16u);
            const u32 t0 = pk(keep[4 * g] + o[0], keep[4 * g + 1] + o[1]), t1 = pk(keep[4 * g + 2] + o[2], keep[4 * g + 3] + o[3]);
            x1[4 * g] = bflo(res[g][0]) + bflo(t0); x1[4 * g + 1] = bfhi(res[g][0]) + bfhi(t0);
            x1[4 * g + 2] = bflo(res[g][1]) + bflo(t1); x1[4 * g + 3] = bfhi(res[g][1]) + bfhi(t1);
        }
    }
    u32x4 xp[2];
#pragma unroll
    for (int s = 0; s < 2; ++s)
#pragma unroll
        for (int j = 0; j < 4; ++j) xp[s][j] = pk(x1[8 * s + 2 * j], x1[8 * s + 2 * j + 1]);
    if (L == 0) {
#pragma unroll
        for (int s = 0; s < 2; ++s) stw(c.ws, OFF_X1 + (u32)((tile * 32 + f0 / 16 + s) * 64 + lane) * 16u, xp[s]);
    }
    float s1 = 0, s2 = 0, s3 = 0;
#pragma unroll
    for (int i = 0; i < 16; ++i) {
        const u32 wd = xp[i >> 3][(i & 7) >> 1];
        x1[i] = (i & 1) ? bfhi(wd) : bflo(wd);
        const u32 ww = wr[i >> 2][(i & 3) >> 1];
        const float wv = (i & 1) ? bfhi(ww) : bflo(ww);
        s1 += x1[i]; s2 += x1[i] * x1[i]; s3 += x1[i] * wv;
    }
    s1 = swap32_add(s1); s2 = swap32_add(s2); s3 = swap32_add(s3);
    if (h == 0) { const u32 a = L_SB + (u32)w * 384u + (u32)r * 12u; lds_st<float>(a, s1); lds_st<float>(a + 4, s2); lds_st<float>(a + 8, s3); }
    BAR();
    if (kh == 0) {
        const u32 a = L_SB + (u32)(w ^ 2) * 384u + (u32)r * 12u;
        const float t1 = s1 + lds_ld<float>(a), t2 = s2 + lds_ld<float>(a + 4), t3 = s3 + lds_ld<float>(a + 8);
        stw(c.ws, GS + (u32)((tb * 8 + fb) * 2048 + h * 1024 + (tt * 32 + r) * 16),
            h ? mk_unit(__float_as_uint(t3), 0u, c.tag) : mk_unit(__float_as_uint(t1), __float_as_uint(t2), c.tag));
    }
    if (L == 0) {
        DRAIN(); BAR();
        if (w == 0 && lane == 0) set_flag(c, OFF_FO + (u32)(tb * 8 + fb) * 128u);
    }
    float mu, rs, var, rt;
    {
        u32 off[8]; u32x4 v[8];
#pragma unroll
        for (int k = 0; k < 4; ++k) { off[2 * k] = GS + (u32)((tb * 8 + 4 * h + k) * 2048 + (tt * 32 + r) * 16); off[2 * k + 1] = off[2 * k] + 1024u; }
        wait_units<8>(c, off, v);
        float t1 = 0, t2 = 0, t3 = 0;
#pragma unroll
        for (int k = 0; k < 4; ++k) { t1 += __uint_as_float(v[2 * k][0]); t2 += __uint_as_float(v[2 * k][2]); t3 += __uint_as_float(v[2 * k + 1][0]); }
        t1 = swap32_add(t1); t2 = swap32_add(t2); t3 = swap32_add(t3);
        mu = t1 * (1.0f / 512.0f); var = fmaxf(t2 * (1.0f / 512.0f) - mu * mu, 0.f);
        rs = 1.0f / sqrtf(var + 1e-5f);
        rt = rbf(rs * (t3 - mu * wsum));
    }
    float br = rt; int bt = r; float bmu = mu, brs = rs;
#pragma unroll
    for (int m = 1; m < 32; m <<= 1) {
        float r2 = __shfl_xor(br, m), mu2 = __shfl_xor(bmu, m), rs2 = __shfl_xor(brs, m); int t2 = __shfl_xor(bt, m);
        bool take = (r2 > br) || (r2 == br && t2 < bt);
        br = take ? r2 : br; bt = take ? t2 : bt; bmu = take ? mu2 : bmu; brs = take ? rs2 : brs;
    }
    if (fb == 0 && kh == 0) {
        if (lane < 16) {
            const u32 o = GC + (u32)(lane >> 1) * 2048u + (u32)tile * 32u + (u32)(lane & 1) * 16u;
            stw(c.ws, o, (lane & 1) ? mk_unit(__float_as_uint(brs), 0u, c.tag) : mk_unit((__float_as_uint(br) & 0xffff0000u) | (u32)bt, __float_as_uint(bmu), c.tag));
        }
        if (L == 0 && h == 0) {
            const float g = 1.0f + rs, A = g / sqrtf(var * g * g + 1e-5f);
            stw(c.ws, OFF_TS1 + (u32)tok * 16u, mk_unit(__float_as_uint(A), __float_as_uint(A * mu), c.tag));
        }
    }
    if (r == bt) {
#pragma unroll
        for (int g = 0; g < 4; ++g) stw(c.ws, GX + (u32)tile * 2048u + (u32)(f0 / 4 + 2 * g + h) * 16u, mk_unit(xp[g >> 1][(g & 1) * 2], xp[g >> 1][(g & 1) * 2 + 1], c.tag));
    }
#pragma unroll
    for (int s = 0; s < 2; ++s)
#pragma unroll
        for (int j = 0; j < 4; ++j) {
            const float a = x1[8 * s + 2 * j], b_ = x1[8 * s + 2 * j + 1];
            const u32 hh = pk((a - mu) * rs, (b_ - mu) * rs);
            x2[s][j] = pk(a + bflo(hh), b_ + bfhi(hh));
        }
    if (L == 0) {
        BAR();
    } else {
        const u32 tl = L_XB + (u32)w * 4096u;
#pragma unroll
        for (int g = 0; g < 4; ++g) lds_st<u32x2>(tl + (u32)(r * 80 + (8 * g + 4 * h) * 2), (u32x2){x2[g >> 1][(g & 1) * 2], x2[g >> 1][(g & 1) * 2 + 1]});
        asm volatile("s_waitcnt lgkmcnt(0)" ::: "memory");
        u32x4 rowv[2];
#pragma unroll
        for (int q = 0; q < 2; ++q) {
            const int row = q * 16 + (lane >> 2);
            rowv[q] = lds_ld<u32x4>(tl + (u32)(row * 80 + (lane & 3) * 16));
            if (row != bt) *(u32x4*)(p.out + (size_t)(tile * 32 + row) * 512 + f0 + (lane & 3) * 8) = rowv[q];
        }
        wait_sel(1);
        const int sel1 = __builtin_amdgcn_readfirstlane((int)lds_ld<u32>(P_SEL + 32)) & 2047;
        if (tile * 32 + bt != sel1) {
#pragma unroll
            for (int q = 0; q < 2; ++q) {
                const int row = q * 16 + (lane >> 2);
                if (row == bt) *(u32x4*)(p.out + (size_t)(tile * 32 + row) * 512 + f0 + (lane & 3) * 8) = rowv[q];
            }
        }
    }
}

__global__ __launch_bounds__(512, 2) void fused_layers_kernel(Params p) {
    extern __shared__ __attribute__((aligned(16))) char lds[];
    Ctx c;
    const int tid = threadIdx.x;
    c.lane = tid & 63; c.w = __builtin_amdgcn_readfirstlane(tid >> 6);
    c.xg = blockIdx.x & 7; c.gi = blockIdx.x >> 3;
    c.ws = __builtin_amdgcn_make_buffer_rsrc(p.ws, 0, WS_BYTES, 0x00020000);
    c.tag = (u32)__builtin_amdgcn_readfirstlane((int)__builtin_amdgcn_raw_buffer_load_b32(c.ws, OFF_EPOCH, 0, 16)) + 1u;
    if (tid < 20) lds_st<u32>(P_HCNT + (u32)tid * 4u, 0u);
    asm volatile("" :: "v"(lds));
    BAR();
    u32 hsync = 0;
    u32x4 x2[2] = {};
    phaseQ<0>(c, p, hsync);
    if (STOP_AFTER >= 1) phaseA<0>(c, p);
    if (STOP_AFTER >= 2) phaseO<0>(c, p, x2, hsync);
    if (STOP_AFTER >= 3) phaseQ<1>(c, p, hsync);
    if (STOP_AFTER >= 4) phaseA<1>(c, p);
    if (STOP_AFTER >= 5) phaseO<1>(c, p, x2, hsync);
    if (blockIdx.x == 14 * 8 && tid == 256) __builtin_amdgcn_raw_buffer_store_b32(c.tag, c.ws, OFF_EPOCH, 0, 16);
}

extern "C" void kernel_launch(void* const* d_in, const int* in_sizes, int n_in, void* d_out, int out_size, void* d_ws, size_t ws_size, hipStream_t stream) {
    Params p;
    p.x0 = (const uint16_t*)d_in[0]; p.wqkv = (const uint16_t*)d_in[1]; p.wo = (const uint16_t*)d_in[2];
    p.wr = (const uint16_t*)d_in[4]; p.wg = (const uint16_t*)d_in[5]; p.wu = (const uint16_t*)d_in[6]; p.wd = (const uint16_t*)d_in[7];
    p.out = (uint16_t*)d_out; p.ws = (char*)d_ws;
    (void)hipFuncSetAttribute((const void*)fused_layers_kernel, hipFuncAttributeMaxDynamicSharedMemorySize, LDS_BYTES);
    fused_layers_kernel<<<256, 512, LDS_BYTES, stream>>>(p);
}
```

```cpp
#include <hip/hip_runtime.h>
#include <stdint.h>

typedef unsigned u32;
typedef unsigned long long u64;
typedef __attribute__((ext_vector_type(2))) u32 u32x2;
typedef __attribute__((ext_vector_type(4))) u32 u32x4;
typedef __attribute__((ext_vector_type(2))) float f32x2;
typedef __attribute__((ext_vector_type(4))) float f32x4;
typedef __attribute__((ext_vector_type(16))) float f32x16;
typedef __attribute__((ext_vector_type(8))) __bf16 bf16x8;
typedef __attribute__((ext_vector_type(2))) __bf16 bf16x2;
typedef __attribute__((ext_vector_type(4))) short s16x4;
typedef __attribute__((ext_vector_type(8))) short s16x8;
#define LAS __attribute__((address_space(3)))
#define DI __device__ __forceinline__
#define MFMA32(a, b, c) __builtin_amdgcn_mfma_f32_32x32x16_bf16((a), (b), (c), 0, 0, 0)
#define MFMA16(a, b, c) __builtin_amdgcn_mfma_f32_16x16x32_bf16((a), (b), (c), 0, 0, 0)
#define SBAR() __builtin_amdgcn_sched_barrier(0)
#define BAR() asm volatile("s_waitcnt lgkmcnt(0)\n\ts_barrier" ::: "memory")
#define DRAIN() asm volatile("s_waitcnt vmcnt(0)" ::: "memory")
#define MEMFENCE() asm volatile("" ::: "memory")

#define STOP_AFTER 99

constexpr u32 OFF_EPOCH = 0, OFF_ABORT = 256;
constexpr u32 OFF_FQ0 = 0x10000, OFF_FA0 = 0x18000, OFF_FO = 0x20000, OFF_FQ1 = 0x28000, OFF_FA1 = 0x30000;
constexpr u32 OFF_TS0 = 0x40000, OFF_TS1 = 0x48000;
constexpr u32 OFF_GC0 = 0x50000, OFF_GC1 = 0x51000;
constexpr u32 OFF_GM0 = 0x52000, OFF_GM1 = 0x52400;
constexpr u32 OFF_GA0 = 0x58000, OFF_GA1 = 0x5A000;
constexpr u32 OFF_GY = 0x5C000;
constexpr u32 OFF_GS0 = 0x80000, OFF_GS1 = 0xC0000;
constexpr u32 OFF_GX0 = 0x180000, OFF_GX1 = 0x1A0000;
constexpr u32 OFF_Q0 = 0x200000, OFF_K0 = 0x400000, OFF_V0 = 0x600000, OFF_O0 = 0x800000, OFF_X1 = 0xA00000;
constexpr u32 OFF_Q1 = 0xC00000, OFF_K1 = 0xE00000, OFF_V1 = 0x1000000, OFF_O1 = 0x1200000;
constexpr u32 WS_BYTES = 0x1400000;

constexpr u32 LDS_BYTES = 163840;
constexpr u32 L_W = 0;
constexpr u32 L_XR = 98304;
constexpr u32 L_AX = 140288;
constexpr u32 L_TOK = 161792;
constexpr u32 L_K = 0, L_V = 65536, VSTR = 1152;
constexpr u32 L_WO = 0, WO_FT = 65536;
constexpr u32 L_XB = 32768;
constexpr u32 L_SB = 161792;
constexpr u32 L_P = 163328;
constexpr u32 P_CS = L_P, P_HCNT = L_P + 384, P_PATCH = L_P + 388, P_SEL = L_P + 400;
constexpr u32 FW0 = 98304, FS0 = 155648;
constexpr u32 FW1 = FW0, FS1 = FS0;
constexpr u32 SPIN_CAP = 40000;

struct Params {
    const uint16_t* x0; const uint16_t* wqkv; const uint16_t* wo; const uint16_t* wr;
    const uint16_t* wg; const uint16_t* wu; const uint16_t* wd; uint16_t* out; char* ws;
};

typedef __amdgpu_buffer_rsrc_t rsrc_t;

DI u32 pk(float a, float b) { f32x2 v = {a, b}; return __builtin_bit_cast(u32, __builtin_convertvector(v, bf16x2)); }
DI float bflo(u32 v) { return __uint_as_float(v << 16); }
DI float bfhi(u32 v) { return __uint_as_float(v & 0xffff0000u); }
DI float rbf(float a) { return bflo(pk(a, 0.f)); }
DI u32x4 ldw(rsrc_t r, u32 off) { return __builtin_amdgcn_raw_buffer_load_b128(r, off, 0, 16); }
DI void stw(rsrc_t r, u32 off, u32x4 v) { __builtin_amdgcn_raw_buffer_store_b128(v, r, off, 0, 16); }
template <class T> DI T lds_ld(u32 a) { return *(const LAS T*)(uintptr_t)a; }
template <class T> DI void lds_st(u32 a, T v) { *(LAS T*)(uintptr_t)a = v; }
DI s16x4 lds_tr(u32 a) { return __builtin_amdgcn_ds_read_tr16_b64_v4i16((LAS s16x4*)(uintptr_t)a); }
DI bf16x8 frag_tr(u32 a0, u32 a1) { s16x4 lo = lds_tr(a0), hi = lds_tr(a1); return __builtin_bit_cast(bf16x8, __builtin_shufflevector(lo, hi, 0, 1, 2, 3, 4, 5, 6, 7)); }
DI float swap32_add(float v) { auto r = __builtin_amdgcn_permlane32_swap(__float_as_uint(v), __float_as_uint(v), false, false); return __uint_as_float(r[0]) + __uint_as_float(r[1]); }
template <int CTRL> DI u32 dpp(u32 v) { return (u32)__builtin_amdgcn_update_dpp((int)v, (int)v, CTRL, 0xf, 0xf, true); }
DI float wave_sum(float v) {
    v += __uint_as_float(dpp<0xB1>(__float_as_uint(v)));
    v += __uint_as_float(dpp<0x4E>(__float_as_uint(v)));
    v += __uint_as_float(dpp<0x141>(__float_as_uint(v)));
    v += __uint_as_float(dpp<0x140>(__float_as_uint(v)));
    { auto r = __builtin_amdgcn_permlane16_swap(__float_as_uint(v), __float_as_uint(v), false, false); v = __uint_as_float(r[0]) + __uint_as_float(r[1]); }
    return swap32_add(v);
}
DI u32 half_umax(u32 v) {
    v = max(v, dpp<0xB1>(v)); v = max(v, dpp<0x4E>(v)); v = max(v, dpp<0x141>(v)); v = max(v, dpp<0x140>(v));
    auto r = __builtin_amdgcn_permlane16_swap(v, v, false, false);
    return max(r[0], r[1]);
}
DI u32 wave_umax(u32 v) { v = half_umax(v); auto r = __builtin_amdgcn_permlane32_swap(v, v, false, false); return max(r[0], r[1]); }
DI u32 score_key(u32 bf, u32 idx) { return ((bf ^ ((bf & 0x8000u) ? 0xFFFFu : 0x8000u)) << 16) | (0xFFFFu - idx); }
DI u32 key_bf(u32 k) { const u32 b = k >> 16; return b ^ ((b & 0x8000u) ? 0x8000u : 0xFFFFu); }
DI u32 key_idx(u32 k) { return 0xFFFFu - (k & 0xFFFFu); }
DI void glds(const void* sbase, u32 voff, u32 ldsdst) {
    u32 sv;
    asm volatile("s_mov_b32 %0, m0\n\ts_mov_b32 m0, %3\n\ts_nop 0\n\tglobal_load_lds_dwordx4 %1, %2\n\ts_mov_b32 m0, %0"
                 : "=&s"(sv) : "v"(voff), "s"(sbase), "s"(ldsdst) : "memory");
}
struct Ctx {
    rsrc_t ws; u32 tag; int lane, w, xg, gi;
};
#define FRESH(c) asm volatile("" : "+v"((c).lane))
DI u32 epoch_tag(u32 v) { const u32 t = (u32)__builtin_amdgcn_readfirstlane((int)v) + 1u; return t ? t : 1u; }
DI bool spin_fail(const Ctx& c, u32& n) {
    __builtin_amdgcn_s_sleep(1);
    ++n;
    if (n > SPIN_CAP) { if (c.lane == 0) __builtin_amdgcn_raw_buffer_store_b32(c.tag, c.ws, OFF_ABORT, 0, 16); return true; }
    if ((n & 127u) == 0u) { u32 a = __builtin_amdgcn_raw_buffer_load_b32(c.ws, OFF_ABORT, 0, 16); if (__builtin_amdgcn_readfirstlane(a) == c.tag) return true; }
    return false;
}
DI void wait_flags(const Ctx& c, u32 off) {
    u32 n = 0;
    for (;;) {
        u32x2 v = __builtin_amdgcn_raw_buffer_load_b64(c.ws, off, 0, 16);
        MEMFENCE();
        if (__all(v[0] == c.tag && v[1] == ~c.tag)) break;
        if (spin_fail(c, n)) break;
    }
    MEMFENCE();
}
DI void set_flag(const Ctx& c, u32 off) { u32x2 v = {c.tag, ~c.tag}; __builtin_amdgcn_raw_buffer_store_b64(v, c.ws, off, 0, 16); }
DI bool unit_ok(u32x4 v, u32 tag) { return v[1] == tag && v[3] == tag; }
DI u32x4 mk_unit(u32 a, u32 b, u32 tag) { return (u32x4){a, tag, b, tag}; }
template <int N> DI void wait_units(const Ctx& c, const u32 (&off)[N], u32x4 (&v)[N]) {
    u32 n = 0;
    for (;;) {
        bool ok = true;
#pragma unroll
        for (int k = 0; k < N; ++k) v[k] = ldw(c.ws, off[k]);
        MEMFENCE();
#pragma unroll
        for (int k = 0; k < N; ++k) ok = ok && unit_ok(v[k], c.tag);
        if (__all(ok)) break;
        if (spin_fail(c, n)) break;
    }
    MEMFENCE();
}
DI u32 lds_poll_ld(u32 a) { return __hip_atomic_load((LAS u32*)(uintptr_t)a, __ATOMIC_RELAXED, __HIP_MEMORY_SCOPE_WORKGROUP); }
DI void helper_sync(const Ctx& c, u32& target) {
    target += 4;
    asm volatile("s_waitcnt lgkmcnt(0)" ::: "memory");
    if (c.lane == 0) __hip_atomic_fetch_add((LAS u32*)(uintptr_t)P_HCNT, 1u, __ATOMIC_RELAXED, __HIP_MEMORY_SCOPE_WORKGROUP);
    u32 n = 0;
    while (lds_poll_ld(P_HCNT) < target) { if (++n > (1u << 24)) break; }
    MEMFENCE();
}
DI void wait_sel(int L) {
    u32 n = 0;
    while (lds_poll_ld(P_SEL + L * 32 + 16) == 0u) { __builtin_amdgcn_s_sleep(1); if (++n > (1u << 22)) break; }
    MEMFENCE();
}

#define STAMP(i) do { } while (0)
#define CSTAMP(i) do { } while (0)
#define FST(i) do { } while (0)

DI u64 cand_tag(u32 tag) { return ((u64)tag << 11) | (u64)((tag * 0x9E3779B1u) >> 21); }
template <int L>
DI void ffn_chain(const Ctx& c, const Params& p, u32& hsync) {
    const int lane = c.lane, hw = c.w - 4;
    constexpr u32 FW = FW0, FS = FS0;
    constexpr u32 GC = L ? OFF_GC1 : OFF_GC0, GM = L ? OFF_GM1 : OFF_GM0, GX = L ? OFF_GX1 : OFF_GX0, GA = L ? OFF_GA1 : OFF_GA0;
    FST(20 + 20 * L);
    __builtin_amdgcn_s_setprio(3);
    if (hw == 0) {
        const u64 want = cand_tag(c.tag);
        u64 v; u32 n = 0;
        for (;;) {
            const u32x2 t = __builtin_amdgcn_raw_buffer_load_b64(c.ws, GC + c.xg * 512u + lane * 8u, 0, 16);
            MEMFENCE();
            v = ((u64)t[1] << 32) | t[0];
            if (__all((v >> 21) == want)) break;
            if (spin_fail(c, n)) break;
        }
        FST(21 + 20 * L);
        const u32 key = wave_umax(score_key(((u32)v >> 5) & 0xFFFFu, (u32)lane * 32u + ((u32)v & 31u)));
        const u32 tok = key_idx(key); const float r = __uint_as_float(key_bf(key) << 16);
        if (lane == 0) {
            lds_st<u32x2>(P_SEL + L * 32, (u32x2){(u32)tok, __float_as_uint(r)});
            asm volatile("s_waitcnt lgkmcnt(0)" ::: "memory");
            __hip_atomic_store((LAS u32*)(uintptr_t)(P_SEL + L * 32 + 16), 1u, __ATOMIC_RELAXED, __HIP_MEMORY_SCOPE_WORKGROUP);
        }
    }
    FST(22 + 20 * L);
    STAMP(L ? 25 : 13);
    const bool is_gu = c.gi < 14, is_dn = c.gi >= 14 && c.gi < 18;
    if (!is_gu && !is_dn) return;
    wait_sel(L);
    const u32x2 si = lds_ld<u32x2>(P_SEL + L * 32);
    const int sel = __builtin_amdgcn_readfirstlane((int)si[0]) & 2047;
    const float rsel = __uint_as_float(si[1]);
    const u32 gxrow = GX + (u32)(sel >> 5) * 2048u, gm = GM + (u32)(sel >> 5) * 16u;
    const int g16 = lane >> 4, i16 = lane & 15;
    const u32 troff = (u32)((8 * g16 + (i16 >> 2)) * 32 + (i16 & 3) * 8);
    if (is_gu) {
        const int gidx = c.xg * 14 + c.gi;
        u32 off[2] = {gxrow + (u32)(hw * 32 + (lane & 31)) * 16u, gm};
        u32x4 v[2];
        wait_units<2>(c, off, v);
        FST(23 + 20 * L);
        const float mu = __uint_as_float(v[1][0]), rs = __uint_as_float(v[1][2]);
        if (lane < 32) {
            u32x2 h;
            h[0] = pk((bflo(v[0][0]) - mu) * rs, (bfhi(v[0][0]) - mu) * rs);
            h[1] = pk((bflo(v[0][2]) - mu) * rs, (bfhi(v[0][2]) - mu) * rs);
            lds_st<u32x2>(FS + (u32)(hw * 32 + lane) * 8u, h);
        }
        helper_sync(c, hsync);
        STAMP(L ? 26 : 14);
        FST(24 + 20 * L);
        f32x4 ag = {}, au = {};
#pragma unroll
        for (int s = 0; s < 4; ++s) {
            const int st = hw * 4 + s;
            bf16x8 a = lds_ld<bf16x8>(FS + (u32)(st * 64 + g16 * 16));
            u32 bo = FW + (u32)st * 1024u + troff;
            ag = MFMA16(a, frag_tr(bo, bo + 128), ag);
            au = MFMA16(a, frag_tr(bo + 16384, bo + 16384 + 128), au);
        }
        if (lane < 16) { lds_st<float>(FS + 4608 + (u32)(hw * 32 + lane) * 4u, ag[0]); lds_st<float>(FS + 4608 + (u32)(hw * 32 + 16 + lane) * 4u, au[0]); }
        FST(25 + 20 * L);
        helper_sync(c, hsync);
        FST(26 + 20 * L);
        if (hw == 0) {
            float g = 0.f, u = 0.f;
#pragma unroll
            for (int k = 0; k < 4; ++k) { g += lds_ld<float>(FS + 4608 + (u32)(k * 32 + i16) * 4u); u += lds_ld<float>(FS + 4608 + (u32)(k * 32 + 16 + i16) * 4u); }
            g = rbf(g); u = rbf(u);
            const float sg = rbf(g * __builtin_amdgcn_rcpf(1.0f + __expf(-g)));
            const float a = rbf(sg * u);
            const float a0 = __shfl(a, (lane & 3) * 4), a1 = __shfl(a, (lane & 3) * 4 + 1), a2 = __shfl(a, (lane & 3) * 4 + 2), a3 = __shfl(a, (lane & 3) * 4 + 3);
            if (lane < 4) stw(c.ws, GA + (u32)(gidx * 4 + lane) * 16u, mk_unit(pk(a0, a1), pk(a2, a3), c.tag));
            FST(27 + 20 * L);
        }
    } else {
        const int didx = c.xg * 4 + (c.gi - 14);
        float mu, rs;
        {
            const int u0 = hw * 112 + lane, u1 = hw * 112 + 64 + (lane < 48 ? lane : 47);
            u32 off[4] = {gxrow + (u32)(didx * 4 + (lane & 3)) * 16u, gm, GA + (u32)u0 * 16u, GA + (u32)u1 * 16u};
            u32x4 v[4];
            wait_units<4>(c, off, v);
            FST(23 + 20 * L);
            mu = __uint_as_float(v[1][0]); rs = __uint_as_float(v[1][2]);
            lds_st<u32x2>(FS + 1024 + (u32)u0 * 8u, (u32x2){v[2][0], v[2][2]});
            if (lane < 48) lds_st<u32x2>(FS + 1024 + (u32)u1 * 8u, (u32x2){v[3][0], v[3][2]});
            if (hw == 0 && lane < 4) lds_st<u32x2>(FS + (u32)lane * 8u, (u32x2){v[0][0], v[0][2]});
        }
        helper_sync(c, hsync);
        STAMP(L ? 26 : 14);
        FST(24 + 20 * L);
        f32x4 ay = {};
#pragma unroll
        for (int s = 0; s < 14; ++s) {
            const int st = hw * 14 + s;
            bf16x8 a = lds_ld<bf16x8>(FS + 1024 + (u32)(st * 64 + g16 * 16));
            u32 bo = FW + (u32)st * 1024u + troff;
            ay = MFMA16(a, frag_tr(bo, bo + 128), ay);
        }
        if (lane < 16) lds_st<float>(FS + 4608 + (u32)(hw * 16 + lane) * 4u, ay[0]);
        FST(25 + 20 * L);
        helper_sync(c, hsync);
        FST(26 + 20 * L);
        if (hw == 0) {
            float y = 0.f;
#pragma unroll
            for (int k = 0; k < 4; ++k) y += lds_ld<float>(FS + 4608 + (u32)(k * 16 + i16) * 4u);
            const float x1 = __uint_as_float((u32)lds_ld<uint16_t>(FS + (u32)i16 * 2u) << 16);
            const float sig = rbf(__builtin_amdgcn_rcpf(1.0f + __expf(-rsel)));
            y = rbf(rbf(y) * sig);
            const float h = rbf((x1 - mu) * rs);
            const float x2 = rbf(x1 + rbf(h + y));
            const float a0 = __shfl(x2, (lane & 3) * 4), a1 = __shfl(x2, (lane & 3) * 4 + 1), a2 = __shfl(x2, (lane & 3) * 4 + 2), a3 = __shfl(x2, (lane & 3) * 4 + 3);
            if (L == 0) { if (lane < 4) stw(c.ws, OFF_GY + (u32)(didx * 4 + lane) * 16u, mk_unit(pk(a0, a1), pk(a2, a3), c.tag)); }
            else if (lane < 4) *(u32x2*)(p.out + (size_t)sel * 512 + didx * 16 + lane * 4) = (u32x2){pk(a0, a1), pk(a2, a3)};
            FST(27 + 20 * L);
        }
    }
}

DI void qkv_patch(const Ctx& c, u32& hsync) {
    const int lane = c.lane, hw = c.w - 4;
    constexpr u32 FS = FS0;
    u32 off[2] = {OFF_GY + (u32)lane * 32u, OFF_GY + (u32)lane * 32u + 16u}; u32x4 v[2];
    wait_units<2>(c, off, v);
    float x[8], s = 0, q = 0;
#pragma unroll
    for (int k = 0; k < 2; ++k) { x[4 * k] = bflo(v[k][0]); x[4 * k + 1] = bfhi(v[k][0]); x[4 * k + 2] = bflo(v[k][2]); x[4 * k + 3] = bfhi(v[k][2]); }
#pragma unroll
    for (int k = 0; k < 8; ++k) { s += x[k]; q += x[k] * x[k]; }
    s = wave_sum(s); q = wave_sum(q);
    const float mu = s * (1.0f / 512.0f), var = fmaxf(q * (1.0f / 512.0f) - mu * mu, 0.f);
    const float A = __builtin_amdgcn_rsqf(var + 1e-5f);
    lds_st<u32x4>(FS + (u32)lane * 16u, (u32x4){pk((x[0] - mu) * A, (x[1] - mu) * A), pk((x[2] - mu) * A, (x[3] - mu) * A), pk((x[4] - mu) * A, (x[5] - mu) * A), pk((x[6] - mu) * A, (x[7] - mu) * A)});
    const int g16 = lane >> 4, i16 = lane & 15;
    const u32 tro = L_W + (u32)((8 * g16 + (i16 >> 2)) * 64 + (i16 & 3) * 8);
    f32x4 acc[6] = {};
#pragma unroll
    for (int s_ = 0; s_ < 4; ++s_) {
        const int st = hw * 4 + s_;
        const bf16x8 a = lds_ld<bf16x8>(FS + (u32)(st * 64 + g16 * 16));
#pragma unroll
        for (int ct = 0; ct < 6; ++ct) {
            const u32 bo = tro + (u32)st * 2048u + (u32)(ct >> 1) * 32768u + (u32)(ct & 1) * 32u;
            acc[ct] = MFMA16(a, frag_tr(bo, bo + 256), acc[ct]);
        }
    }
    if (lane < 16) {
#pragma unroll
        for (int ct = 0; ct < 6; ++ct) lds_st<float>(FS + 1024 + (u32)(hw * 96 + ct * 16 + lane) * 4u, acc[ct][0]);
    }
    helper_sync(c, hsync);
    if (hw == 0) {
#pragma unroll
        for (int k = 0; k < 2; ++k) {
            const int n = k * 64 + lane;
            if (n < 96) {
                float t = 0.f;
#pragma unroll
                for (int j = 0; j < 4; ++j) t += lds_ld<float>(FS + 1024 + (u32)(j * 96 + n) * 4u);
                lds_st<float>(FS + 4608 + (u32)n * 4u, n < 32 ? t * (0.25f * 1.4426950408889634f) : t);
            }
        }
        asm volatile("s_waitcnt lgkmcnt(0)" ::: "memory");
        if (lane == 0) __hip_atomic_store((LAS u32*)(uintptr_t)P_PATCH, 1u, __ATOMIC_RELAXED, __HIP_MEMORY_SCOPE_WORKGROUP);
    }
}

DI u32x4 gld(const void* base, u32 off) { return *(const u32x4*)((const char*)base + off); }
struct FfSrc { const uint16_t* m; u32 vo, step; bool on, gu; };
DI FfSrc ff_src(const Ctx& c, const Params& p) {
    int lane = c.lane; asm volatile("" : "+v"(lane));
    FfSrc f; const int w = c.w;
    f.on = c.gi < 18; f.gu = c.gi < 14;
    f.m = f.gu ? (w < 4 ? p.wg : p.wu) : p.wd;
    const u32 rowb = f.gu ? 3584u : 1024u, col = f.gu ? (u32)(c.xg * 14 + c.gi) * 32u : (u32)(c.xg * 4 + (c.gi - 14)) * 32u;
    const u32 first = f.gu ? (u32)(w & 3) * 4u : (u32)w * 7u;
    f.step = 32u * rowb;
    f.vo = (u32)(lane >> 1) * rowb + col + (u32)(lane & 1) * 16u + first * f.step;
    return f;
}
DI void keep_wq(const Ctx& c, u32x4 (&pq)[12]) {
#pragma unroll
    for (int j = 0; j < 12; ++j) pq[j] = lds_ld<u32x4>(L_W + (u32)(c.w * 12 + j) * 1024u + (u32)c.lane * 16u);
}
DI void pre_store_wq(const Ctx& c, const u32x4 (&pq)[12]) {
#pragma unroll
    for (int j = 0; j < 12; ++j) lds_st<u32x4>(L_W + (u32)(c.w * 12 + j) * 1024u + (u32)c.lane * 16u, pq[j]);
}
DI void pre_store_ff(const Ctx& c, const u32x4 (&pf)[7]) {
    if (c.gi < 18) {
        const bool gu = c.gi < 14;
        const u32 a = FW0 + (u32)c.w * (gu ? 4096u : 7168u) + (u32)c.lane * 16u;
#pragma unroll
        for (int j = 0; j < 7; ++j) if (j < 4 || !gu) lds_st<u32x4>(a + (u32)j * 1024u, pf[j]);
    }
}
DI void wo_dma(const Ctx& c, const Params& p, u32 vo, int j) {
    const int n = c.w * 8 + j, ft = n >> 5, g = n & 31;
    glds(p.wo, vo + (u32)ft * 64u + (u32)g * 16384u, L_WO + (u32)ft * WO_FT + (u32)g * 1024u);
}
DI u32 wo_lane_off(const Ctx& c) {
    int lane = c.lane; asm volatile("" : "+v"(lane));
    return (u32)((lane >> 2) * 1024 + (c.gi & 7) * 128 + (lane & 3) * 16);
}

struct WFrag { bf16x8 q, k, v; };
template <u32 TR2> DI void wfrag_load(WFrag& f, u32 a01, u32 a2) { f.q = frag_tr(a01, a01 + TR2); f.k = frag_tr(a01 + 32768, a01 + 32768 + TR2); f.v = frag_tr(a2, a2 + TR2); }
template <u32 TR2> DI void qstep(f32x16& aq, f32x16& ak, f32x16& av, bf16x8 x, WFrag& cur, WFrag& nxt, u32 a01, u32 a2, bool more) {
    if (more) nxt.q = frag_tr(a01, a01 + TR2);
    SBAR(); aq = MFMA32(cur.q, x, aq); SBAR();
    if (more) nxt.k = frag_tr(a01 + 32768, a01 + 32768 + TR2);
    SBAR(); ak = MFMA32(cur.k, x, ak); SBAR();
    if (more) nxt.v = frag_tr(a2, a2 + TR2);
    SBAR(); av = MFMA32(x, cur.v, av); SBAR();
}

template <int L>
DI void phaseQ(Ctx& c0, const Params& p, u32& hsync, u32x4 (&pq)[12]) {
    Ctx c = c0; FRESH(c);
    const int lane = c.lane, w = c.w;
    const int rb = 4 * (c.xg & 3) + (c.gi & 3), hp = 8 * (c.xg >> 2) + (c.gi >> 2);
    const int r = lane & 31, h = lane >> 5;
    const int i16 = lane & 15, blk = (lane >> 4) & 1;
    const u32 trA = L_W + (u32)(((L == 0 ? 8 : 4) * h + (i16 >> 2)) * 64 + blk * 32 + (i16 & 3) * 8);
    constexpr u32 TR2 = L == 0 ? 256 : 512;
    constexpr u32 OQ = L ? OFF_Q1 : OFF_Q0, OK_ = L ? OFF_K1 : OFF_K0, OV = L ? OFF_V1 : OFF_V0, TS = L ? OFF_TS1 : OFF_TS0;
    const int rot = ((c.gi >> 2) + 2 * (c.gi & 3)) & 7;

    if (w >= 4) {
        const int hw = w - 4;
        if (L == 0) {
            u32x4 r0, r1; u32 ep;
            asm volatile("global_load_dword %0, %1, %2 sc1" : "=v"(ep) : "v"(0u), "s"(p.ws));
            const u32 tok = (u32)(rb * 128 + hp * 8 + hw * 2);
            const u32 vo = tok * 1024u + (u32)lane * 16u;
            asm volatile("global_load_dwordx4 %0, %1, %2" : "=v"(r0) : "v"(vo), "s"(p.x0));
            asm volatile("global_load_dwordx4 %0, %1, %2 offset:1024" : "=v"(r1) : "v"(vo), "s"(p.x0));
            const u32 wv = (u32)((lane >> 2) * 3072 + hp * 64 + (lane & 3) * 16 + hw * 16 * 3072);
            u32 xv[4];
#pragma unroll
            for (int q = 0; q < 4; ++q) { const int row = 8 * q + (lane >> 3); xv[q] = (u32)(((rb * 4 + hw) * 32 + row) * 1024 + (((lane & 7) ^ ((row >> 1) & 7)) * 16)); }
#define WD(i) do { const u32 kc_ = (u32)(((i) + rot) & 7); _Pragma("unroll") for (int t = 0; t < 3; ++t) \
                glds(p.wqkv, wv + (u32)t * 1024u + kc_ * (64u * 3072u), L_W + (u32)t * 32768u + (kc_ * 64u + (u32)hw * 16u) * 64u); } while (0)
#define XD(i) do { const u32 kc_ = (u32)(((i) + rot) & 7); _Pragma("unroll") for (int q = 0; q < 4; ++q) \
                glds(p.x0, xv[q] + kc_ * 128u, L_XR + (u32)((i) % 3) * 16384u + (u32)hw * 4096u + (u32)q * 1024u); } while (0)
#define QWAIT(n) asm volatile("s_waitcnt vmcnt(" #n ")\n\ts_barrier" ::: "memory")
#define CS(i) if (hw < 3) { _Pragma("unroll") for (int s_ = 0; s_ < 4; ++s_) { \
                const u32 a_ = trA + (u32)hw * 32768u + (u32)((((i) + rot) & 7) * 4 + s_) * 1024u; cs = MFMA32(frag_tr(a_, a_ + TR2), ones, cs); } }
            WD(0); XD(0); WD(1); XD(1); WD(2); XD(2); WD(3);
            FST(1);
            f32x16 cs = {};
            const bf16x8 ones = __builtin_bit_cast(bf16x8, (u32x4){0x3f803f80u, 0x3f803f80u, 0x3f803f80u, 0x3f803f80u});
            asm volatile("s_waitcnt vmcnt(14)\n\ts_barrier" : "+v"(r0), "+v"(r1), "+v"(ep) :: "memory");
            c.tag = epoch_tag(ep); c0.tag = c.tag;
            FST(2);
            {
                float s0 = 0, q0 = 0, s1 = 0, q1 = 0;
#pragma unroll
                for (int j = 0; j < 4; ++j) {
                    float a = bflo(r0[j]), b_ = bfhi(r0[j]); s0 += a + b_; q0 += a * a + b_ * b_;
                    float d = bflo(r1[j]), e = bfhi(r1[j]); s1 += d + e; q1 += d * d + e * e;
                }
                s0 = wave_sum(s0); q0 = wave_sum(q0); s1 = wave_sum(s1); q1 = wave_sum(q1);
                const float s = lane ? s1 : s0, q = lane ? q1 : q0;
                const float mu = s * (1.0f / 512.0f), var = fmaxf(q * (1.0f / 512.0f) - mu * mu, 0.f);
                const float A = __builtin_amdgcn_rsqf(var + 1e-5f);
                if (lane < 2) stw(c.ws, TS + (tok + (u32)lane) * 16u, mk_unit(__float_as_uint(A), __float_as_uint(A * mu), c.tag));
            }
            CS(0);
            QWAIT(8); FST(3); XD(3); WD(4); CS(1);
            QWAIT(8); FST(4); XD(4); WD(5); CS(2);
            QWAIT(7); FST(5); XD(5); WD(6); CS(3);
            QWAIT(7); FST(6); XD(6); WD(7); CS(4);
            QWAIT(7); FST(7); XD(7); CS(5);
            QWAIT(4); FST(8); CS(6);
            QWAIT(0); FST(9); CS(7);
#undef WD
#undef XD
#undef QWAIT
#undef CS
            if (hw < 3 && r == 0) {
#pragma unroll
                for (int g = 0; g < 4; ++g) lds_st<f32x4>(P_CS + (u32)(hw * 32 + 8 * g + 4 * h) * 4u, (f32x4){cs[4 * g], cs[4 * g + 1], cs[4 * g + 2], cs[4 * g + 3]});
            }
            STAMP(1);
            BAR();
        } else {
            ffn_chain<0>(c, p, hsync);
            STAMP(15);
            wait_sel(0);
            const int sel = __builtin_amdgcn_readfirstlane((int)lds_ld<u32>(P_SEL)) & 2047;
            if ((sel >> 7) == rb) qkv_patch(c, hsync);
        }
        DRAIN(); BAR();
        STAMP(L ? 16 : 3);
        if (L == 0) keep_wq(c, pq);
    } else {
        const int T = rb * 4 + w;
        f32x16 aq = {}, ak = {}, av = {};
        float A, B;
        if (L == 0) {
            const u32 ep = __builtin_amdgcn_raw_buffer_load_b32(c.ws, OFF_EPOCH, 0, 16);
            u32 xa[4];
#pragma unroll
            for (int s_ = 0; s_ < 4; ++s_) xa[s_] = L_XR + (u32)w * 4096u + (u32)(r * 128 + (((2 * s_ + h) ^ ((r >> 1) & 7)) * 16));
            bf16x8 xf[2][4];
            WFrag f0, f1;
            asm volatile("s_barrier" ::: "memory");
            FST(2);
#pragma unroll
            for (int s_ = 0; s_ < 4; ++s_) xf[0][s_] = lds_ld<bf16x8>(xa[s_]);
            { const u32 a01 = trA + (u32)(rot & 7) * 4096u; wfrag_load<TR2>(f0, a01, a01 + 65536u); }
#pragma unroll
            for (int i = 0; i < 8; ++i) {
                const u32 a01 = trA + (u32)((i + rot) & 7) * 4096u, a2 = a01 + 65536u;
                const u32 n01 = trA + (u32)((i + 1 + rot) & 7) * 4096u, n2 = n01 + 65536u;
                qstep<TR2>(aq, ak, av, xf[i & 1][0], f0, f1, a01 + 1024, a2 + 1024, true);
                qstep<TR2>(aq, ak, av, xf[i & 1][1], f1, f0, a01 + 2048, a2 + 2048, true);
                qstep<TR2>(aq, ak, av, xf[i & 1][2], f0, f1, a01 + 3072, a2 + 3072, true);
                if (i < 7) {
                    asm volatile("s_barrier" ::: "memory");
                    FST(3 + i);
#pragma unroll
                    for (int s_ = 0; s_ < 4; ++s_) xf[(i + 1) & 1][s_] = lds_ld<bf16x8>(xa[s_] + (u32)((i + 1) % 3) * 16384u);
                }
                qstep<TR2>(aq, ak, av, xf[i & 1][3], f1, f0, n01, n2, i < 7);
            }
            FST(10);
            STAMP(1);
            c.tag = epoch_tag(ep); c0.tag = c.tag;
            u32 off[1] = {TS + (u32)(T * 32 + r) * 16u}; u32x4 v[1];
            wait_units<1>(c, off, v);
            A = __uint_as_float(v[0][0]); B = __uint_as_float(v[0][2]);
            BAR();
            STAMP(2);
        } else {
            wait_flags(c, OFF_FO + (u32)(rb * 16 + (lane & 15)) * 128u);
            STAMP(13);
            u32x4 xf[32];
            const u32 xo = OFF_X1 + (u32)T * 32768u + (u32)lane * 16u;
#pragma unroll
            for (int s = 0; s < 32; ++s) xf[s] = ldw(c.ws, xo + (u32)s * 1024u);
            STAMP(14);
            {
                u32 a01 = trA, a2 = trA + 65536u;
                asm volatile("" : "+v"(a01), "+v"(a2));
                WFrag f0, f1;
                wfrag_load<TR2>(f0, a01, a2);
#pragma unroll
                for (int st = 0; st < 32; st += 2) {
                    qstep<TR2>(aq, ak, av, __builtin_bit_cast(bf16x8, xf[st]), f0, f1, a01 + (u32)(st + 1) * 1024u, a2 + (u32)(st + 1) * 1024u, true);
                    qstep<TR2>(aq, ak, av, __builtin_bit_cast(bf16x8, xf[st + 1]), f1, f0, a01 + (u32)(st + 2) * 1024u, a2 + (u32)(st + 2) * 1024u, st + 2 < 32);
                }
            }
            u32 off[1] = {TS + (u32)(T * 32 + r) * 16u}; u32x4 v[1];
            wait_units<1>(c, off, v);
            A = __uint_as_float(v[0][0]); B = __uint_as_float(v[0][2]);
        }
        if (L == 1) STAMP(15);
        int ts = -1;
        if (L == 1) {
            wait_sel(0);
            const int sel = __builtin_amdgcn_readfirstlane((int)lds_ld<u32>(P_SEL)) & 2047;
            if ((sel >> 5) == T) {
                ts = sel & 31;
                u32 n = 0;
                while (lds_poll_ld(P_PATCH) == 0u) { __builtin_amdgcn_s_sleep(1); if (++n > (1u << 22)) break; }
                MEMFENCE();
            }
        }
        const u32 tokl = L_TOK + (u32)w * 256u;
        if (h == 0) lds_st<f32x2>(tokl + (u32)r * 8u, (f32x2){A, B});
        constexpr float QS = 0.25f * 1.4426950408889634f;
#pragma unroll
        for (int t = 0; t < 2; ++t) {
            const f32x16& acc = t ? ak : aq;
            const float sA = t ? A : A * QS, sB = t ? B : B * QS;
            float o[16];
#pragma unroll
            for (int g = 0; g < 4; ++g) {
                const f32x4 cs = lds_ld<f32x4>(P_CS + (u32)(t * 32 + 8 * g + 4 * h) * 4u);
#pragma unroll
                for (int e = 0; e < 4; ++e) o[4 * g + e] = sA * acc[4 * g + e] - sB * cs[e];
            }
            if (L == 1 && r == ts) {
#pragma unroll
                for (int g = 0; g < 4; ++g) {
                    const f32x4 pv = lds_ld<f32x4>(FS0 + 4608 + (u32)(t * 32 + 8 * g + 4 * h) * 4u);
#pragma unroll
                    for (int e = 0; e < 4; ++e) o[4 * g + e] = pv[e];
                }
            }
#pragma unroll
            for (int a = 0; a < 2; ++a) {
                u32x4 v = {pk(o[8 * a], o[8 * a + 1]), pk(o[8 * a + 2], o[8 * a + 3]), pk(o[8 * a + 4], o[8 * a + 5]), pk(o[8 * a + 6], o[8 * a + 7])};
                stw(c.ws, (t ? OK_ : OQ) + (u32)(((hp * 2 + a) * 64 + T) * 64 + lane) * 16u, v);
            }
        }
        {
            asm volatile("s_waitcnt lgkmcnt(0)" ::: "memory");
            const float cs = lds_ld<float>(P_CS + (u32)(64 + r) * 4u);
            float o[16];
#pragma unroll
            for (int g = 0; g < 4; ++g) {
                const f32x4 ab0 = lds_ld<f32x4>(tokl + (u32)(8 * g + 4 * h) * 8u), ab1 = lds_ld<f32x4>(tokl + (u32)(8 * g + 4 * h + 2) * 8u);
                o[4 * g] = ab0[0] * av[4 * g] - ab0[1] * cs; o[4 * g + 1] = ab0[2] * av[4 * g + 1] - ab0[3] * cs;
                o[4 * g + 2] = ab1[0] * av[4 * g + 2] - ab1[1] * cs; o[4 * g + 3] = ab1[2] * av[4 * g + 3] - ab1[3] * cs;
            }
            if (L == 1 && ts >= 0) {
                const float pv = lds_ld<float>(FS0 + 4608 + (u32)(64 + r) * 4u);
#pragma unroll
                for (int i = 0; i < 16; ++i) if ((i & 3) + 8 * (i >> 2) + 4 * h == ts) o[i] = pv;
            }
            const int head = hp * 2 + (r >> 4), d = r & 15;
#pragma unroll
            for (int s = 0; s < 2; ++s) {
                u32x4 v = {pk(o[8 * s], o[8 * s + 1]), pk(o[8 * s + 2], o[8 * s + 3]), pk(o[8 * s + 4], o[8 * s + 5]), pk(o[8 * s + 6], o[8 * s + 7])};
                stw(c.ws, OV + (u32)((head * 64 + T) * 1024 + s * 512 + h * 256 + d * 16), v);
            }
        }
        DRAIN(); BAR();
        STAMP(L ? 16 : 3);
        if (w == 0 && lane == 0) set_flag(c, (L ? OFF_FQ1 : OFF_FQ0) + (u32)(hp * 16 + rb) * 128u);
        if (L == 0) keep_wq(c, pq);
    }
}

struct AState { f32x16 Sa, Sb, Oa, Ob; bf16x8 kC, kN, v0, v1; u32x4 pb1; };
#define EXP2(x) __builtin_amdgcn_exp2f(x)
#define BC8(x) __builtin_bit_cast(bf16x8, x)
#define PVM(O, v, p) O = MFMA32(v, BC8(p), O)
template <bool USE_C, class HOOK>
DI void attn_step(AState& s, bf16x8 qa, bf16x8 qb, const f32x16& ca, const f32x16& cb, u32 kp, u32 vp0, u32 vp1, u32 knext, u32 voff, HOOK hook) {
    const f32x16 zero = {};
    u32x4 pa0, pa1, pb0;
    s.kN = lds_ld<bf16x8>(kp + knext); SBAR();
    s.v0 = lds_ld<bf16x8>(vp0 + voff); SBAR();
    PVM(s.Ob, s.v1, s.pb1); SBAR();
    s.v1 = lds_ld<bf16x8>(vp1 + voff); SBAR();
    float a0 = EXP2(s.Sa[0]); SBAR(); float a1 = EXP2(s.Sa[1]); SBAR(); float a2 = EXP2(s.Sa[2]); SBAR(); float a3 = EXP2(s.Sa[3]); SBAR();
    s.Sb = MFMA32(s.kC, qb, USE_C ? cb : zero); SBAR();
    float a4 = EXP2(s.Sa[4]); SBAR(); float a5 = EXP2(s.Sa[5]); SBAR(); float a6 = EXP2(s.Sa[6]); SBAR(); float a7 = EXP2(s.Sa[7]); SBAR();
    pa0[0] = pk(a0, a1); SBAR(); pa0[1] = pk(a2, a3); SBAR(); float a8 = EXP2(s.Sa[8]); SBAR(); pa0[2] = pk(a4, a5); SBAR(); pa0[3] = pk(a6, a7); SBAR();
    float a9 = EXP2(s.Sa[9]); SBAR();
    asm volatile("" : "+v"(s.v0), "+v"(s.v1), "+v"(s.kN)); SBAR();
    float a10 = EXP2(s.Sa[10]); SBAR();
    PVM(s.Oa, s.v0, pa0); SBAR();
    float a11 = EXP2(s.Sa[11]); SBAR(); float a12 = EXP2(s.Sa[12]); SBAR(); float a13 = EXP2(s.Sa[13]); SBAR(); float a14 = EXP2(s.Sa[14]); SBAR(); float a15 = EXP2(s.Sa[15]); SBAR();
    pa1[0] = pk(a8, a9); SBAR(); pa1[1] = pk(a10, a11); SBAR(); pa1[2] = pk(a12, a13); SBAR(); pa1[3] = pk(a14, a15); SBAR();
    float b0 = EXP2(s.Sb[0]); SBAR(); float b1 = EXP2(s.Sb[1]); SBAR();
    PVM(s.Oa, s.v1, pa1); SBAR();
    hook(); SBAR();
    float b2 = EXP2(s.Sb[2]); SBAR(); float b3 = EXP2(s.Sb[3]); SBAR(); float b4 = EXP2(s.Sb[4]); SBAR(); float b5 = EXP2(s.Sb[5]); SBAR();
    s.Sa = MFMA32(s.kN, qa, USE_C ? ca : zero); SBAR();
    float b6 = EXP2(s.Sb[6]); SBAR(); float b7 = EXP2(s.Sb[7]); SBAR();
    pb0[0] = pk(b0, b1); SBAR(); pb0[1] = pk(b2, b3); SBAR(); float b8 = EXP2(s.Sb[8]); SBAR(); pb0[2] = pk(b4, b5); SBAR(); pb0[3] = pk(b6, b7); SBAR();
    float b9 = EXP2(s.Sb[9]); SBAR(); float b10 = EXP2(s.Sb[10]); SBAR();
    PVM(s.Ob, s.v0, pb0); SBAR();
    float b11 = EXP2(s.Sb[11]); SBAR(); float b12 = EXP2(s.Sb[12]); SBAR(); float b13 = EXP2(s.Sb[13]); SBAR(); float b14 = EXP2(s.Sb[14]); SBAR(); float b15 = EXP2(s.Sb[15]); SBAR();
    s.pb1[0] = pk(b8, b9); SBAR(); s.pb1[1] = pk(b10, b11); SBAR(); s.pb1[2] = pk(b12, b13); SBAR(); s.pb1[3] = pk(b14, b15); SBAR();
    s.kC = s.kN;
}
struct NoHook { DI void operator()() const {} };
template <bool USE_C>
DI void attn_chunk(AState& s, bf16x8 qa, bf16x8 qb, const f32x16& ca, const f32x16& cb, u32 kp, u32 vp0, u32 vp1) {
    attn_step<USE_C>(s, qa, qb, ca, cb, kp, vp0, vp1, 1024, 0, NoHook());
    attn_step<USE_C>(s, qa, qb, ca, cb, kp, vp0, vp1, 2048, VSTR, NoHook());
    attn_step<USE_C>(s, qa, qb, ca, cb, kp, vp0, vp1, 3072, 2 * VSTR, NoHook());
    attn_step<USE_C>(s, qa, qb, ca, cb, kp, vp0, vp1, 8192, 3 * VSTR, NoHook());
}
DI void attn_begin(AState& s, bf16x8 qa, const f32x16& ca, u32 kp) {
    const f32x16 zero = {};
    s.Oa = zero; s.Ob = zero; s.pb1 = (u32x4){0u, 0u, 0u, 0u}; s.v1 = BC8(s.pb1);
    s.kC = lds_ld<bf16x8>(kp);
    s.Sa = MFMA32(s.kC, qa, ca);
}
DI void attn_combine(const Ctx& c, AState& s, float (&O)[9]) {
    const int kh = c.w >> 2;
    s.Ob = MFMA32(s.v1, BC8(s.pb1), s.Ob);
    const f32x16& snd = kh ? s.Oa : s.Ob; const f32x16& keep = kh ? s.Ob : s.Oa;
    const u32 xb = L_AX + (u32)c.w * 2304u;
    lds_st<f32x4>(xb + (u32)c.lane * 16u, (f32x4){snd[0], snd[1], snd[2], snd[3]});
    lds_st<f32x4>(xb + 1024u + (u32)c.lane * 16u, (f32x4){snd[4], snd[5], snd[6], snd[7]});
    lds_st<float>(xb + 2048u + (u32)c.lane * 4u, snd[8]);
    BAR();
    const u32 xp = L_AX + (u32)(c.w ^ 4) * 2304u;
    const f32x4 u0 = lds_ld<f32x4>(xp + (u32)c.lane * 16u), u1 = lds_ld<f32x4>(xp + 1024u + (u32)c.lane * 16u);
    const float l2 = lds_ld<float>(xp + 2048u + (u32)c.lane * 4u);
#pragma unroll
    for (int q = 0; q < 4; ++q) { O[q] = keep[q] + u0[q]; O[4 + q] = keep[4 + q] + u1[q]; }
    O[8] = keep[8] + l2;
}

template <int L>
DI void phaseA(const Ctx& c0, const Params& p) {
    Ctx c = c0; FRESH(c);
    u32x4 pf[7] = {};
    const int lane = c.lane, w = c.w, tid = w * 64 + lane;
    const int head = 4 * c.xg + (c.gi >> 3), qb = c.gi & 7;
    const int j = w & 3, kh = w >> 2;
    constexpr u32 OQ = L ? OFF_Q1 : OFF_Q0, OK_ = L ? OFF_K1 : OFF_K0, OV = L ? OFF_V1 : OFF_V0, OO = L ? OFF_O1 : OFF_O0;
    if (tid == 0) lds_st<u32>(L_AX + 8 * 2304u, 0u);
    if (w == 0) wait_flags(c, (L ? OFF_FQ1 : OFF_FQ0) + (u32)((head >> 1) * 16 + (lane & 15)) * 128u);
    BAR();
    STAMP(L ? 17 : 4);
    CSTAMP(9);
    const u32 koff = OK_ + (u32)((head * 64 + w) * 64 + lane) * 16u, voff = OV + (u32)((head * 64 + w) * 64 + lane) * 16u;
    u32x4 rk[8], rv[8];
    rk[0] = ldw(c.ws, koff); rv[0] = ldw(c.ws, voff);
    const u32 qo = OQ + (u32)((head * 64 + qb * 8 + 2 * j) * 64 + lane) * 16u;
    const u32x4 qar = ldw(c.ws, qo), qbr = ldw(c.ws, qo + 1024u);
    SBAR(); __builtin_amdgcn_s_barrier(); SBAR();
    rk[1] = ldw(c.ws, koff + 8192u); rv[1] = ldw(c.ws, voff + 8192u); rk[2] = ldw(c.ws, koff + 16384u); rv[2] = ldw(c.ws, voff + 16384u);
    SBAR(); __builtin_amdgcn_s_barrier(); SBAR();
    rk[3] = ldw(c.ws, koff + 24576u); rv[3] = ldw(c.ws, voff + 24576u); rk[4] = ldw(c.ws, koff + 32768u); rv[4] = ldw(c.ws, voff + 32768u);
    SBAR();
    { const int t = tid >> 3, s = tid & 7; const u32 val = (s & 3) == 0 ? 0x3f803f80u : 0u;
      lds_st<u32x4>(L_V + (u32)t * VSTR + 1024u + (u32)s * 16u, (u32x4){val, val, val, val}); }
    const u32 kw = L_K + (u32)w * 1024u + (u32)lane * 16u, vw = L_V + (u32)w * VSTR + (u32)lane * 16u;
    lds_st<u32x4>(kw, rk[0]); lds_st<u32x4>(vw, rv[0]);
    lds_st<u32x4>(kw + 8192u, rk[1]); lds_st<u32x4>(vw + 8u * VSTR, rv[1]);
    CSTAMP(10);
    BAR();
    const int r = lane & 31, h = lane >> 5;
    const u32 kp0 = L_K + (u32)kh * 4096u + (u32)lane * 16u;
    const u32 onesoff = 1024u + ((r >= 20 && r < 28) ? 64u : 0u) + ((r == 16 || r == 20) ? 0u : 16u);
    const u32 vp00 = L_V + (u32)kh * (4u * VSTR) + (r < 16 ? (u32)(h * 256 + r * 16) : onesoff);
    const u32 vp10 = L_V + (u32)kh * (4u * VSTR) + (r < 16 ? (u32)(512 + h * 256 + r * 16) : onesoff);
    const bf16x8 qa = BC8(qar), qbf = BC8(qbr);
    const f32x16 cz = {};
    AState s;
    STAMP(L ? 18 : 5);
#define PRIO(n) __builtin_amdgcn_s_setprio(n)
#define BAR2() BAR()
    PRIO(3);
    CSTAMP(0);
    attn_begin(s, qa, cz, kp0);
    {
#define PUT(ch) [&]() { lds_st<u32x4>(kw + (u32)(ch) * 8192u, rk[ch]); lds_st<u32x4>(vw + (u32)(ch) * (8u * VSTR), rv[ch]); }
#define GET(ch) [&]() { rk[ch] = ldw(c.ws, koff + (u32)(ch) * 8192u); rv[ch] = ldw(c.ws, voff + (u32)(ch) * 8192u); }
#define GETPUT(g, ch) [&]() { GET(g)(); PUT(ch)(); }
#define FF(j_) [&]() { if (ff.on && ((j_) < 4 || !ff.gu)) pf[j_] = gld(ff.m, ff.vo + (u32)(j_) * ff.step); }
#define WO(j_) [&]() { wo_dma(c, p, wv, j_); }
#define ST(ch, t, HOOK) attn_step<false>(s, qa, qbf, cz, cz, kp, vp0, vp1, ((t) == 3 ? 8192 : ((t) + 1) * 1024) + ((ch) & 1) * 8192, (((ch) & 1) * 8 + (t)) * VSTR, HOOK)
#define NEXT2() do { kp += 16384; vp0 += 16 * VSTR; vp1 += 16 * VSTR; asm volatile("" : "+v"(kp), "+v"(vp0), "+v"(vp1)); } while (0)
        u32 kp = kp0, vp0 = vp00, vp1 = vp10;
        ST(0, 0, GET(5)); ST(0, 1, GETPUT(6, 2)); ST(0, 2, GETPUT(7, 3)); ST(0, 3, PUT(4));
        CSTAMP(1);
        ST(1, 0, PUT(5)); ST(1, 1, PUT(6)); ST(1, 2, PUT(7));
        BAR();
        ST(1, 3, NoHook());
        const FfSrc ff = ff_src(c, p);
        NEXT2(); PRIO(2);
        CSTAMP(2);
        ST(2, 0, FF(0)); ST(2, 1, FF(1)); ST(2, 2, FF(2)); ST(2, 3, FF(3));
        CSTAMP(3);
        ST(3, 0, FF(4)); ST(3, 1, FF(5)); ST(3, 2, FF(6)); ST(3, 3, NoHook());
        NEXT2(); PRIO(1);
        CSTAMP(4);
        ST(4, 0, NoHook()); ST(4, 1, NoHook()); ST(4, 2, NoHook()); ST(4, 3, NoHook());
        CSTAMP(5);
        ST(5, 0, NoHook()); ST(5, 1, NoHook()); ST(5, 2, NoHook()); ST(5, 3, NoHook());
        const u32 wv = wo_lane_off(c);
        NEXT2(); PRIO(0);
        BAR2();
        CSTAMP(6);
        ST(6, 0, WO(0)); ST(6, 1, WO(1)); ST(6, 2, WO(2)); ST(6, 3, WO(3));
        CSTAMP(7);
        ST(7, 0, WO(4)); ST(7, 1, WO(5)); ST(7, 2, WO(6)); ST(7, 3, WO(7));
#undef PUT
#undef GET
#undef GETPUT
#undef FF
#undef WO
#undef ST
#undef NEXT2
    }
    CSTAMP(8);
    STAMP(L ? 19 : 6);
    FRESH(c);
    float O[9];
    attn_combine(c, s, O);
    const u32 oo = OO + (u32)((head * 64 + qb * 8 + 2 * j + kh) * 64 + c.lane) * 16u;
    {
        bool bad = !(O[8] >= 0x1p-60f && O[8] <= 0x1p60f);
#pragma unroll
        for (int q = 0; q < 8; ++q) bad = bad || !(__builtin_fabsf(O[q]) <= 0x1p126f);
        if (__any(bad) && c.lane == 0) lds_st<u32>(L_AX + 8 * 2304u, 1u);
        const float rl = __builtin_amdgcn_rcpf(O[8]);
        u32x4 o;
#pragma unroll
        for (int q = 0; q < 4; ++q) o[q] = pk(O[2 * q] * rl, O[2 * q + 1] * rl);
        stw(c.ws, oo, o);
    }
    DRAIN(); BAR();
    if (__builtin_amdgcn_readfirstlane((int)lds_ld<u32>(L_AX + 8 * 2304u)) != 0) {
        {
            const u32 ko = OK_ + (u32)((head * 64 + w) * 64 + c.lane) * 16u, vo = OV + (u32)((head * 64 + w) * 64 + c.lane) * 16u;
            for (int ch = 0; ch < 4; ++ch) {
                const u32x4 a = ldw(c.ws, ko + (u32)ch * 8192u), b_ = ldw(c.ws, vo + (u32)ch * 8192u);
                lds_st<u32x4>(L_K + (u32)(ch * 8 + w) * 1024u + (u32)c.lane * 16u, a);
                lds_st<u32x4>(L_V + (u32)(ch * 8 + w) * VSTR + (u32)c.lane * 16u, b_);
            }
            { const int t = (w * 64 + c.lane) >> 3, s_ = c.lane & 7; const u32 val = (s_ & 3) == 0 ? 0x3f803f80u : 0u;
              lds_st<u32x4>(L_V + (u32)t * VSTR + 1024u + (u32)s_ * 16u, (u32x4){val, val, val, val}); }
        }
        BAR();
        float ma = -INFINITY, mb = -INFINITY;
        for (int ch = 0; ch < 8; ++ch)
            for (int t = 0; t < 4; ++t) {
                const bf16x8 kf = lds_ld<bf16x8>(kp0 + (u32)(ch * 8192 + t * 1024));
                const f32x16 Sa = MFMA32(kf, qa, cz), Sb = MFMA32(kf, qbf, cz);
#pragma unroll
                for (int q = 0; q < 16; ++q) { ma = fmaxf(ma, Sa[q]); mb = fmaxf(mb, Sb[q]); }
            }
        ma = fmaxf(ma, __shfl_xor(ma, 32)); mb = fmaxf(mb, __shfl_xor(mb, 32));
        const u32 xb = L_AX + (u32)w * 2304u, xp = L_AX + (u32)(w ^ 4) * 2304u;
        lds_st<f32x2>(xb + (u32)c.lane * 8u, (f32x2){ma, mb});
        BAR();
        const f32x2 mo = lds_ld<f32x2>(xp + (u32)c.lane * 8u);
        ma = fmaxf(ma, mo[0]); mb = fmaxf(mb, mo[1]);
        BAR();
        f32x16 ca, cb;
#pragma unroll
        for (int q = 0; q < 16; ++q) { ca[q] = -ma; cb[q] = -mb; }
        attn_begin(s, qa, ca, kp0);
        u32 kp = kp0, vp0 = vp00, vp1 = vp10;
        for (int ch = 0; ch < 8; ++ch) {
            attn_chunk<true>(s, qa, qbf, ca, cb, kp, vp0, vp1);
            kp += 8192; vp0 += 8 * VSTR; vp1 += 8 * VSTR;
        }
        attn_combine(c, s, O);
        const float rl = 1.0f / O[8];
        u32x4 o;
#pragma unroll
        for (int q = 0; q < 4; ++q) o[q] = pk(O[2 * q] * rl, O[2 * q + 1] * rl);
        stw(c.ws, oo, o);
        BAR();
        const u32 wv2 = wo_lane_off(c);
        for (int q = 0; q < 8; ++q) wo_dma(c, p, wv2, q);
        DRAIN(); BAR();
    }
    STAMP(L ? 20 : 7);
    if (w == 0 && c.lane == 0) set_flag(c, (L ? OFF_FA1 : OFF_FA0) + (u32)(qb * 32 + head) * 128u);
    pre_store_ff(c, pf);
}

template <int L>
DI void phaseO(const Ctx& c0, const Params& p, u32x4 (&x2)[2], u32& hsync, const u32x4 (&pq)[12]) {
    Ctx c = c0; FRESH(c);
    const int lane = c.lane, w = c.w;
    const int tb = 4 * c.xg + (c.gi >> 3), fb = c.gi & 7;
    const int r = lane & 31, h = lane >> 5;
    constexpr u32 OO = L ? OFF_O1 : OFF_O0, GS = L ? OFF_GS1 : OFF_GS0, GC = L ? OFF_GC1 : OFF_GC0, GM = L ? OFF_GM1 : OFF_GM0, GX = L ? OFF_GX1 : OFF_GX0;
    constexpr u32 L_TS = L_SB, L_CAND = L_SB + 512;
    if (w >= 4) {
        const int hw = w - 4;
        if (hw == 0) wait_flags(c, (L ? OFF_FA1 : OFF_FA0) + (u32)((tb >> 2) * 32 + (lane & 31)) * 128u);
        float wsum = 0.f;
        if (hw == 0) {
            const u32x4 v = *(const u32x4*)(p.wr + lane * 8); float s = 0;
#pragma unroll
            for (int j = 0; j < 4; ++j) s += bflo(v[j]) + bfhi(v[j]);
            wsum = wave_sum(s);
        }
        BAR();
        STAMP(L ? 21 : 8);
        BAR();
        BAR();
        __builtin_amdgcn_s_setprio(3);
        FST(30 + 20 * L);
        if (hw == 0) {
            const u32 a = L_SB + (u32)h * 384u + (u32)r * 12u;
            const float t1 = lds_ld<float>(a) + lds_ld<float>(a + 768), t2 = lds_ld<float>(a + 4) + lds_ld<float>(a + 772), t3 = lds_ld<float>(a + 8) + lds_ld<float>(a + 776);
            stw(c.ws, GS + (u32)((tb * 8 + fb) * 1024 + lane * 16), (u32x4){__float_as_uint(t1), __float_as_uint(t2), __float_as_uint(t3), c.tag});
        }
        FST(31 + 20 * L);
        if (L == 0) {
            pre_store_wq(c, pq);
            BAR();
        }
        FST(32 + 20 * L);
        if (hw == 0) {
            u32x4 v[8]; u32 n = 0;
            for (;;) {
                bool ok = true;
#pragma unroll
                for (int k = 0; k < 8; ++k) v[k] = ldw(c.ws, GS + (u32)((tb * 8 + k) * 1024 + lane * 16));
                MEMFENCE();
#pragma unroll
                for (int k = 0; k < 8; ++k) ok = ok && v[k][3] == c.tag;
                if (__all(ok)) break;
                if (spin_fail(c, n)) break;
            }
            FST(33 + 20 * L);
            float t1 = 0, t2 = 0, t3 = 0;
#pragma unroll
            for (int k = 0; k < 8; ++k) { t1 += __uint_as_float(v[k][0]); t2 += __uint_as_float(v[k][1]); t3 += __uint_as_float(v[k][2]); }
            const float mu = t1 * (1.0f / 512.0f), var = fmaxf(t2 * (1.0f / 512.0f) - mu * mu, 0.f);
            const float rs = __builtin_amdgcn_rsqf(var + 1e-5f);
            const float rt = rbf(rs * (t3 - mu * wsum));
            const u32 key = half_umax(score_key(__float_as_uint(rt) >> 16, (u32)r));
            const int bt = (int)key_idx(key);
            if (fb == 0) {
                const u32 k0 = (u32)__builtin_amdgcn_readlane((int)key, 0), k1 = (u32)__builtin_amdgcn_readlane((int)key, 32);
                const u32 kT = (lane & 1) ? k1 : k0;
                const u64 g = (cand_tag(c.tag) << 21) | (u64)(key_bf(kT) << 5) | (u64)key_idx(kT);
                if (lane < 16) __builtin_amdgcn_raw_buffer_store_b64((u32x2){(u32)g, (u32)(g >> 32)}, c.ws, GC + (u32)(lane >> 1) * 512u + (u32)(tb * 2 + (lane & 1)) * 8u, 0, 16);
                if (r == bt) stw(c.ws, GM + (u32)(tb * 2 + h) * 16u, mk_unit(__float_as_uint(mu), __float_as_uint(rs), c.tag));
                if (L == 0) {
                    const float g1 = 1.0f + rs, A = g1 * __builtin_amdgcn_rsqf(var * g1 * g1 + 1e-5f);
                    stw(c.ws, OFF_TS1 + (u32)(tb * 64 + lane) * 16u, mk_unit(__float_as_uint(A), __float_as_uint(A * mu), c.tag));
                }
            }
            FST(34 + 20 * L);
            lds_st<f32x2>(L_TS + (u32)lane * 8u, (f32x2){mu, rs});
            if (r == 0) lds_st<u32>(L_CAND + (u32)h * 4u, (u32)bt);
        }
        BAR();
        STAMP(L ? 23 : 12);
        if (L == 1) ffn_chain<1>(c, p, hsync);
        return;
    }
    const int tt = w & 1, kh = w >> 1;
    const int tile = tb * 2 + tt, tok = tile * 32 + r;
    const int f0 = fb * 64 + kh * 32;
    u32x2 res[4], wr[4];
#pragma unroll
    for (int g = 0; g < 4; ++g) {
        if (L == 0) res[g] = *(const u32x2*)(p.x0 + (size_t)tok * 512 + f0 + 8 * g + 4 * h);
        wr[g] = *(const u32x2*)(p.wr + f0 + 8 * g + 4 * h);
    }
    if (L == 1) {
        const int sel0 = __builtin_amdgcn_readfirstlane((int)lds_ld<u32>(P_SEL)) & 2047;
        if ((sel0 >> 5) == tile) {
            const u32 ub = OFF_GY + (u32)(f0 / 4 + h) * 16u;
            u32 off[4] = {ub, ub + 32u, ub + 64u, ub + 96u}; u32x4 v[4];
            wait_units<4>(c, off, v);
            if (r == (sel0 & 31)) { x2[0] = (u32x4){v[0][0], v[0][2], v[1][0], v[1][2]}; x2[1] = (u32x4){v[2][0], v[2][2], v[3][0], v[3][2]}; }
        }
#pragma unroll
        for (int g = 0; g < 4; ++g) res[g] = (u32x2){x2[g >> 1][(g & 1) * 2], x2[g >> 1][(g & 1) * 2 + 1]};
    }
    BAR();
    STAMP(L ? 21 : 8);
    u32x4 bf[16];
    const u32 oo = OO + (u32)(((kh * 16) * 64 + tile) * 64 + lane) * 16u;
#pragma unroll
    for (int s = 0; s < 16; ++s) bf[s] = ldw(c.ws, oo + (u32)s * 65536u);
    const int i16 = lane & 15, blk = (lane >> 4) & 1;
    const u32 trA = L_WO + (u32)((kh * 256 + 4 * h + (i16 >> 2)) * 64 + blk * 32 + (i16 & 3) * 8);
    f32x16 a0 = {}, a1 = {};
    {
        u32 ta = trA, tb_ = trA + WO_FT;
        asm volatile("" : "+v"(ta), "+v"(tb_));
        bf16x8 f0_[2], f1_[2];
        f0_[0] = frag_tr(ta, ta + 512); f0_[1] = frag_tr(tb_, tb_ + 512);
#pragma unroll
        for (int s = 0; s < 16; s += 2) {
            f1_[0] = frag_tr(ta + (u32)(s + 1) * 1024u, ta + (u32)(s + 1) * 1024u + 512); SBAR();
            a0 = MFMA32(f0_[0], BC8(bf[s]), a0); SBAR();
            f1_[1] = frag_tr(tb_ + (u32)(s + 1) * 1024u, tb_ + (u32)(s + 1) * 1024u + 512); SBAR();
            a1 = MFMA32(f0_[1], BC8(bf[s]), a1); SBAR();
            if (s + 2 < 16) f0_[0] = frag_tr(ta + (u32)(s + 2) * 1024u, ta + (u32)(s + 2) * 1024u + 512);
            SBAR(); a0 = MFMA32(f1_[0], BC8(bf[s + 1]), a0); SBAR();
            if (s + 2 < 16) f0_[1] = frag_tr(tb_ + (u32)(s + 2) * 1024u, tb_ + (u32)(s + 2) * 1024u + 512);
            SBAR(); a1 = MFMA32(f1_[1], BC8(bf[s + 1]), a1); SBAR();
        }
    }
    {
        const f32x16& snd = kh ? a0 : a1;
#pragma unroll
        for (int g = 0; g < 4; ++g) lds_st<f32x4>(L_XB + (u32)w * 4096u + (u32)g * 1024u + (u32)lane * 16u, (f32x4){snd[4 * g], snd[4 * g + 1], snd[4 * g + 2], snd[4 * g + 3]});
    }
    BAR();
    STAMP(L ? 22 : 9);
    u32x4 xp[2];
    {
        const f32x16& keep = kh ? a1 : a0;
#pragma unroll
        for (int g = 0; g < 4; ++g) {
            const f32x4 o = lds_ld<f32x4>(L_XB + (u32)(w ^ 2) * 4096u + (u32)g * 1024u + (u32)lane * 16u);
            const u32 t0 = pk(keep[4 * g] + o[0], keep[4 * g + 1] + o[1]), t1 = pk(keep[4 * g + 2] + o[2], keep[4 * g + 3] + o[3]);
            xp[g >> 1][(g & 1) * 2] = pk(bflo(res[g][0]) + bflo(t0), bfhi(res[g][0]) + bfhi(t0));
            xp[g >> 1][(g & 1) * 2 + 1] = pk(bflo(res[g][1]) + bflo(t1), bfhi(res[g][1]) + bfhi(t1));
        }
    }
    if (L == 0) {
#pragma unroll
        for (int s = 0; s < 2; ++s) stw(c.ws, OFF_X1 + (u32)((tile * 32 + f0 / 16 + s) * 64 + lane) * 16u, xp[s]);
    }
    float x1[16];
    {
        float s1 = 0, s2 = 0, s3 = 0;
#pragma unroll
        for (int i = 0; i < 16; ++i) {
            const u32 wd = xp[i >> 3][(i & 7) >> 1];
            x1[i] = (i & 1) ? bfhi(wd) : bflo(wd);
            const u32 ww = wr[i >> 2][(i & 3) >> 1];
            const float wv = (i & 1) ? bfhi(ww) : bflo(ww);
            s1 += x1[i]; s2 += x1[i] * x1[i]; s3 += x1[i] * wv;
        }
        s1 = swap32_add(s1); s2 = swap32_add(s2); s3 = swap32_add(s3);
        if (h == 0) { const u32 a = L_SB + (u32)w * 384u + (u32)r * 12u; lds_st<float>(a, s1); lds_st<float>(a + 4, s2); lds_st<float>(a + 8, s3); }
    }
    BAR();
    if (L == 0) {
        DRAIN(); BAR();
        STAMP(10);
        if (w == 0 && lane == 0) set_flag(c, OFF_FO + (u32)(tb * 8 + fb) * 128u);
        pre_store_wq(c, pq);
    }
    BAR();
    STAMP(L ? 23 : 11);
    const f32x2 st = lds_ld<f32x2>(L_TS + (u32)(tt * 32 + r) * 8u);
    const int bt = (int)lds_ld<u32>(L_CAND + (u32)tt * 4u) & 31;
    if (r == bt) {
#pragma unroll
        for (int g = 0; g < 4; ++g) stw(c.ws, GX + (u32)tile * 2048u + (u32)(f0 / 4 + 2 * g + h) * 16u, mk_unit(xp[g >> 1][(g & 1) * 2], xp[g >> 1][(g & 1) * 2 + 1], c.tag));
    }
#pragma unroll
    for (int s = 0; s < 2; ++s)
#pragma unroll
        for (int j = 0; j < 4; ++j) {
            const float a = x1[8 * s + 2 * j], b_ = x1[8 * s + 2 * j + 1];
            const u32 hh = pk((a - st[0]) * st[1], (b_ - st[0]) * st[1]);
            x2[s][j] = pk(a + bflo(hh), b_ + bfhi(hh));
        }
    if (L == 1) {
        const u32 tl = L_XB + (u32)w * 4096u;
#pragma unroll
        for (int g = 0; g < 4; ++g) lds_st<u32x2>(tl + (u32)(r * 80 + (8 * g + 4 * h) * 2), (u32x2){x2[g >> 1][(g & 1) * 2], x2[g >> 1][(g & 1) * 2 + 1]});
        asm volatile("s_waitcnt lgkmcnt(0)" ::: "memory");
        u32x4 rowv[2];
#pragma unroll
        for (int q = 0; q < 2; ++q) {
            const int row = q * 16 + (lane >> 2);
            rowv[q] = lds_ld<u32x4>(tl + (u32)(row * 80 + (lane & 3) * 16));
            if (row != bt) *(u32x4*)(p.out + (size_t)(tile * 32 + row) * 512 + f0 + (lane & 3) * 8) = rowv[q];
        }
        wait_sel(1);
        const int sel1 = __builtin_amdgcn_readfirstlane((int)lds_ld<u32>(P_SEL + 32)) & 2047;
        if (tile * 32 + bt != sel1) {
#pragma unroll
            for (int q = 0; q < 2; ++q) {
                const int row = q * 16 + (lane >> 2);
                if (row == bt) *(u32x4*)(p.out + (size_t)(tile * 32 + row) * 512 + f0 + (lane & 3) * 8) = rowv[q];
            }
        }
    }
}

__global__ __launch_bounds__(512, 2) void fused_layers_kernel(Params p) {
    extern __shared__ __attribute__((aligned(16))) char lds[];
    Ctx c;
    const int tid = threadIdx.x;
    c.lane = tid & 63; c.w = __builtin_amdgcn_readfirstlane(tid >> 6);
    c.xg = blockIdx.x & 7; c.gi = blockIdx.x >> 3;
    c.ws = __builtin_amdgcn_make_buffer_rsrc(p.ws, 0, WS_BYTES, 0x00020000);
    c.tag = 0u;
    if (c.w == 6) {
        const u32 o = (u32)(((4 * c.xg + (c.gi >> 3)) * 8 + (c.gi & 7)) * 1024 + c.lane * 16);
        stw(c.ws, OFF_GS0 + o, (u32x4){0u, 0u, 0u, 0u}); stw(c.ws, OFF_GS1 + o, (u32x4){0u, 0u, 0u, 0u});
    }
    {
        u64 pc; u32 len;
        asm volatile("s_getpc_b64 %0\nfl_code_here:\n\ts_mov_b32 %1, fl_code_end-fl_code_here" : "=s"(pc), "=s"(len));
        if (c.w == 7) {
            const u32 o0 = min((u32)c.gi * 3072u + (u32)c.lane * 16u, len - 16u) & ~15u, o1 = min(o0 + 1024u, len - 16u) & ~15u, o2 = min(o0 + 2048u, len - 16u) & ~15u;
            const u64 base = pc & ~15ull;
            glds((const void*)base, o0, 147456u); glds((const void*)base, o1, 148480u); glds((const void*)base, o2, 149504u);
        }
    }
    if (c.w == 5) {
        const int l = c.lane;
        const char* a = p.ws + (size_t)(l < 10 ? l : 0) * 0x200000;
        a = l == 10 ? (const char*)p.wo : l == 11 ? (const char*)p.wr : l == 12 ? (const char*)p.wg : l == 13 ? (const char*)p.wu : l == 14 ? (const char*)p.wd : l == 15 ? (const char*)p.out : a;
        a = l == 16 ? (const char*)p.wg + 1835004 : l == 17 ? (const char*)p.wu + 1835004 : l == 18 ? (const char*)p.wd + 1835004 : l == 19 ? (const char*)p.out + 2097148 : a;
        u32 sv;
        asm volatile("s_mov_b32 %0, m0\n\ts_mov_b32 m0, %2\n\ts_nop 0\n\tglobal_load_lds_dword %1, off\n\ts_mov_b32 m0, %0" : "=&s"(sv) : "v"(a), "s"(150528u) : "memory");
    }
    if (tid < 20) lds_st<u32>(P_HCNT + (u32)tid * 4u, 0u);
    asm volatile("" :: "v"(lds));
    BAR();
    STAMP(0);
    FST(0);
    u32 hsync = 0;
    u32x4 x2[2] = {};
    u32x4 pq[12];
    phaseQ<0>(c, p, hsync, pq);
    if (STOP_AFTER >= 1) phaseA<0>(c, p);
    if (STOP_AFTER >= 2) phaseO<0>(c, p, x2, hsync, pq);
    if (STOP_AFTER >= 3) phaseQ<1>(c, p, hsync, pq);
    if (STOP_AFTER >= 4) phaseA<1>(c, p);
    if (STOP_AFTER >= 5) phaseO<1>(c, p, x2, hsync, pq);
    STAMP(24);
    if (blockIdx.x == 14 * 8 && tid == 256) __builtin_amdgcn_raw_buffer_store_b32(c.tag, c.ws, OFF_EPOCH, 0, 16);
    asm volatile("fl_code_end:" ::: "memory");
}

extern "C" void kernel_launch(void* const* d_in, const int* in_sizes, int n_in, void* d_out, int out_size, void* d_ws, size_t ws_size, hipStream_t stream) {
    Params p;
    p.x0 = (const uint16_t*)d_in[0]; p.wqkv = (const uint16_t*)d_in[1]; p.wo = (const uint16_t*)d_in[2];
    p.wr = (const uint16_t*)d_in[4]; p.wg = (const uint16_t*)d_in[5]; p.wu = (const uint16_t*)d_in[6]; p.wd = (const uint16_t*)d_in[7];
    p.out = (uint16_t*)d_out; p.ws = (char*)d_ws;
    (void)hipFuncSetAttribute((const void*)fused_layers_kernel, hipFuncAttributeMaxDynamicSharedMemorySize, LDS_BYTES);
    fused_layers_kernel<<<256, 512, LDS_BYTES, stream>>>(p);
}
```

```cpp
#include <hip/hip_runtime.h>
#include <stdint.h>

typedef unsigned u32;
typedef unsigned long long u64;
typedef __attribute__((ext_vector_type(2))) u32 u32x2;
typedef __attribute__((ext_vector_type(4))) u32 u32x4;
typedef __attribute__((ext_vector_type(2))) float f32x2;
typedef __attribute__((ext_vector_type(4))) float f32x4;
typedef __attribute__((ext_vector_type(16))) float f32x16;
typedef __attribute__((ext_vector_type(8))) __bf16 bf16x8;
typedef __attribute__((ext_vector_type(2))) __bf16 bf16x2;
typedef __attribute__((ext_vector_type(4))) short s16x4;
typedef __attribute__((ext_vector_type(8))) short s16x8;
#define LAS __attribute__((address_space(3)))
#define DI __device__ __forceinline__
#define MFMA32(a, b, c) __builtin_amdgcn_mfma_f32_32x32x16_bf16((a), (b), (c), 0, 0, 0)
#define MFMA16(a, b, c) __builtin_amdgcn_mfma_f32_16x16x32_bf16((a), (b), (c), 0, 0, 0)
#define SBAR() __builtin_amdgcn_sched_barrier(0)
#define BAR() asm volatile("s_waitcnt lgkmcnt(0)\n\ts_barrier" ::: "memory")
#define DRAIN() asm volatile("s_waitcnt vmcnt(0)" ::: "memory")
#define MEMFENCE() asm volatile("" ::: "memory")

#define STOP_AFTER 99

constexpr u32 OFF_EPOCH = 0, OFF_ABORT = 256;
constexpr u32 OFF_FQ0 = 0x10000, OFF_FA0 = 0x18000, OFF_FO = 0x20000, OFF_FQ1 = 0x28000, OFF_FA1 = 0x30000;
constexpr u32 OFF_TS0 = 0x40000, OFF_TS1 = 0x48000;
constexpr u32 OFF_GC0 = 0x50000, OFF_GC1 = 0x51000;
constexpr u32 OFF_GM0 = 0x52000, OFF_GM1 = 0x52400;
constexpr u32 OFF_GA0 = 0x58000, OFF_GA1 = 0x5A000;
constexpr u32 OFF_GY = 0x5C000;
constexpr u32 OFF_GS0 = 0x80000, OFF_GS1 = 0xC0000;
constexpr u32 OFF_GX0 = 0x180000, OFF_GX1 = 0x1A0000;
constexpr u32 OFF_Q0 = 0x200000, OFF_K0 = 0x400000, OFF_V0 = 0x600000, OFF_O0 = 0x800000, OFF_X1 = 0xA00000;
constexpr u32 OFF_Q1 = 0xC00000, OFF_K1 = 0xE00000, OFF_V1 = 0x1000000, OFF_O1 = 0x1200000;
constexpr u32 WS_BYTES = 0x1400000;

constexpr u32 LDS_BYTES = 163840;
constexpr u32 L_W = 0;
constexpr u32 L_XR = 98304;
constexpr u32 L_AX = 140288;
constexpr u32 L_TOK = 161792 + 512;
constexpr u32 L_K = 0, L_V = 65536, VSTR = 1152;
constexpr u32 L_WO = 0, WO_FT = 65536;
constexpr u32 L_XB = 32768;
constexpr u32 L_SB = 161792;
constexpr u32 L_P = 163328;
constexpr u32 P_CS = L_P, P_HCNT = L_P + 384, P_PATCH = L_P + 388, P_WQ = L_P + 392, P_XGO = L_P + 396, P_SEL = L_P + 400, P_ST = L_P + 464;
constexpr u32 L_X1T = 49152;
constexpr u32 FW0 = 98304, FS0 = 155648;
constexpr u32 FW1 = FW0, FS1 = FS0;
constexpr u32 SPIN_CAP = 40000;

struct Params {
    const uint16_t* x0; const uint16_t* wqkv; const uint16_t* wo; const uint16_t* wr;
    const uint16_t* wg; const uint16_t* wu; const uint16_t* wd; uint16_t* out; char* ws;
};

typedef __amdgpu_buffer_rsrc_t rsrc_t;

DI u32 pk(float a, float b) { f32x2 v = {a, b}; return __builtin_bit_cast(u32, __builtin_convertvector(v, bf16x2)); }
DI float bflo(u32 v) { return __uint_as_float(v << 16); }
DI float bfhi(u32 v) { return __uint_as_float(v & 0xffff0000u); }
DI float rbf(float a) { return bflo(pk(a, 0.f)); }
DI u32x4 ldw(rsrc_t r, u32 off) { return __builtin_amdgcn_raw_buffer_load_b128(r, off, 0, 16); }
DI void stw(rsrc_t r, u32 off, u32x4 v) { __builtin_amdgcn_raw_buffer_store_b128(v, r, off, 0, 16); }
template <class T> DI T lds_ld(u32 a) { return *(const LAS T*)(uintptr_t)a; }
template <class T> DI void lds_st(u32 a, T v) { *(LAS T*)(uintptr_t)a = v; }
DI s16x4 lds_tr(u32 a) { return __builtin_amdgcn_ds_read_tr16_b64_v4i16((LAS s16x4*)(uintptr_t)a); }
DI bf16x8 frag_tr(u32 a0, u32 a1) { s16x4 lo = lds_tr(a0), hi = lds_tr(a1); return __builtin_bit_cast(bf16x8, __builtin_shufflevector(lo, hi, 0, 1, 2, 3, 4, 5, 6, 7)); }
DI float swap32_add(float v) { auto r = __builtin_amdgcn_permlane32_swap(__float_as_uint(v), __float_as_uint(v), false, false); return __uint_as_float(r[0]) + __uint_as_float(r[1]); }
template <int CTRL> DI u32 dpp(u32 v) { return (u32)__builtin_amdgcn_update_dpp((int)v, (int)v, CTRL, 0xf, 0xf, true); }
DI float wave_sum(float v) {
    v += __uint_as_float(dpp<0xB1>(__float_as_uint(v)));
    v += __uint_as_float(dpp<0x4E>(__float_as_uint(v)));
    v += __uint_as_float(dpp<0x141>(__float_as_uint(v)));
    v += __uint_as_float(dpp<0x140>(__float_as_uint(v)));
    { auto r = __builtin_amdgcn_permlane16_swap(__float_as_uint(v), __float_as_uint(v), false, false); v = __uint_as_float(r[0]) + __uint_as_float(r[1]); }
    return swap32_add(v);
}
DI u32 half_umax(u32 v) {
    v = max(v, dpp<0xB1>(v)); v = max(v, dpp<0x4E>(v)); v = max(v, dpp<0x141>(v)); v = max(v, dpp<0x140>(v));
    auto r = __builtin_amdgcn_permlane16_swap(v, v, false, false);
    return max(r[0], r[1]);
}
DI u32 wave_umax(u32 v) { v = half_umax(v); auto r = __builtin_amdgcn_permlane32_swap(v, v, false, false); return max(r[0], r[1]); }
DI u32 score_key(u32 bf, u32 idx) { return ((bf ^ ((bf & 0x8000u) ? 0xFFFFu : 0x8000u)) << 16) | (0xFFFFu - idx); }
DI u32 key_bf(u32 k) { const u32 b = k >> 16; return b ^ ((b & 0x8000u) ? 0x8000u : 0xFFFFu); }
DI u32 key_idx(u32 k) { return 0xFFFFu - (k & 0xFFFFu); }
DI void glds(const void* sbase, u32 voff, u32 ldsdst) {
    u32 sv;
    asm volatile("s_mov_b32 %0, m0\n\ts_mov_b32 m0, %3\n\ts_nop 0\n\tglobal_load_lds_dwordx4 %1, %2\n\ts_mov_b32 m0, %0"
                 : "=&s"(sv) : "v"(voff), "s"(sbase), "s"(ldsdst) : "memory");
}
struct Ctx {
    rsrc_t ws; u32 tag; int lane, w, xg, gi;
};
#define FRESH(c) asm volatile("" : "+v"((c).lane))
DI u32 epoch_tag(u32 v) { const u32 t = (u32)__builtin_amdgcn_readfirstlane((int)v) + 1u; return t ? t : 1u; }
DI bool spin_fail(const Ctx& c, u32& n) {
    __builtin_amdgcn_s_sleep(1);
    ++n;
    if (n > SPIN_CAP) { if (c.lane == 0) __builtin_amdgcn_raw_buffer_store_b32(c.tag, c.ws, OFF_ABORT, 0, 16); return true; }
    if ((n & 127u) == 0u) { u32 a = __builtin_amdgcn_raw_buffer_load_b32(c.ws, OFF_ABORT, 0, 16); if (__builtin_amdgcn_readfirstlane(a) == c.tag) return true; }
    return false;
}
DI void wait_flags(const Ctx& c, u32 off) {
    u32 n = 0;
    for (;;) {
        u32x2 v = __builtin_amdgcn_raw_buffer_load_b64(c.ws, off, 0, 16);
        MEMFENCE();
        if (__all(v[0] == c.tag && v[1] == ~c.tag)) break;
        if (spin_fail(c, n)) break;
    }
    MEMFENCE();
}
DI void set_flag(const Ctx& c, u32 off) { u32x2 v = {c.tag, ~c.tag}; __builtin_amdgcn_raw_buffer_store_b64(v, c.ws, off, 0, 16); }
DI bool unit_ok(u32x4 v, u32 tag) { return v[1] == tag && v[3] == tag; }
DI u32x4 mk_unit(u32 a, u32 b, u32 tag) { return (u32x4){a, tag, b, tag}; }
template <int N> DI void wait_units(const Ctx& c, const u32 (&off)[N], u32x4 (&v)[N]) {
    u32 n = 0;
    for (;;) {
        bool ok = true;
#pragma unroll
        for (int k = 0; k < N; ++k) v[k] = ldw(c.ws, off[k]);
        MEMFENCE();
#pragma unroll
        for (int k = 0; k < N; ++k) ok = ok && unit_ok(v[k], c.tag);
        if (__all(ok)) break;
        if (spin_fail(c, n)) break;
    }
    MEMFENCE();
}
DI u32 lds_poll_ld(u32 a) { return __hip_atomic_load((LAS u32*)(uintptr_t)a, __ATOMIC_RELAXED, __HIP_MEMORY_SCOPE_WORKGROUP); }
DI void helper_sync(const Ctx& c, u32& target) {
    target += 4;
    asm volatile("s_waitcnt lgkmcnt(0)" ::: "memory");
    if (c.lane == 0) __hip_atomic_fetch_add((LAS u32*)(uintptr_t)P_HCNT, 1u, __ATOMIC_RELAXED, __HIP_MEMORY_SCOPE_WORKGROUP);
    u32 n = 0;
    while (lds_poll_ld(P_HCNT) < target) { if (++n > (1u << 24)) break; }
    MEMFENCE();
}
DI void lds_wait_ge(u32 a, u32 v) {
    u32 n = 0;
    while (lds_poll_ld(a) < v) { __builtin_amdgcn_s_sleep(1); if (++n > (1u << 22)) break; }
    MEMFENCE();
}
DI void lds_signal_add(const Ctx& c, u32 a) {
    asm volatile("s_waitcnt lgkmcnt(0)" ::: "memory");
    if (c.lane == 0) __hip_atomic_fetch_add((LAS u32*)(uintptr_t)a, 1u, __ATOMIC_RELAXED, __HIP_MEMORY_SCOPE_WORKGROUP);
}
DI void wait_sel(int L) {
    u32 n = 0;
    while (lds_poll_ld(P_SEL + L * 32 + 16) == 0u) { __builtin_amdgcn_s_sleep(1); if (++n > (1u << 22)) break; }
    MEMFENCE();
}

#define STAMP(i) do { } while (0)
#define CSTAMP(i) do { } while (0)
#define FST(i) do { } while (0)

DI u64 cand_tag(u32 tag) { return ((u64)tag << 11) | (u64)((tag * 0x9E3779B1u) >> 21); }
template <int L>
DI void ffn_chain(const Ctx& c, const Params& p, u32& hsync) {
    const int lane = c.lane, hw = c.w - 4;
    constexpr u32 FW = FW0, FS = FS0;
    constexpr u32 GC = L ? OFF_GC1 : OFF_GC0, GM = L ? OFF_GM1 : OFF_GM0, GX = L ? OFF_GX1 : OFF_GX0, GA = L ? OFF_GA1 : OFF_GA0;
    FST(20 + 20 * L);
    __builtin_amdgcn_s_setprio(3);
    const bool is_gu = c.gi < 14, is_dn = c.gi >= 14 && c.gi < 18;
    const int g16 = lane >> 4, i16 = lane & 15;
    bf16x8 wf[14];
    {
        const u32 tro = FW + (u32)((8 * g16 + (i16 >> 2)) * 32 + (i16 & 3) * 8) + (u32)hw * (is_gu ? 4096u : 14336u);
#pragma unroll
        for (int s = 0; s < 14; ++s) {
            const u32 bo = tro + (is_gu ? (u32)(s & 3) * 1024u + (u32)((s >> 2) & 1) * 16384u : (u32)s * 1024u);
            if (is_gu || is_dn) wf[s] = frag_tr(bo, bo + 128);
        }
    }
    if (hw == 0) {
        const u64 want = cand_tag(c.tag);
        u64 v; u32 n = 0;
        for (;;) {
            const u32x2 t = __builtin_amdgcn_raw_buffer_load_b64(c.ws, GC + c.xg * 512u + lane * 8u, 0, 16);
            MEMFENCE();
            v = ((u64)t[1] << 32) | t[0];
            if (__all((v >> 21) == want)) break;
            if (spin_fail(c, n)) break;
        }
        FST(21 + 20 * L);
        const u32 key = wave_umax(score_key(((u32)v >> 5) & 0xFFFFu, (u32)lane * 32u + ((u32)v & 31u)));
        const u32 tok = key_idx(key); const float r = __uint_as_float(key_bf(key) << 16);
        if (lane == 0) {
            lds_st<u32x2>(P_SEL + L * 32, (u32x2){(u32)tok, __float_as_uint(r)});
            asm volatile("s_waitcnt lgkmcnt(0)" ::: "memory");
            __hip_atomic_store((LAS u32*)(uintptr_t)(P_SEL + L * 32 + 16), 1u, __ATOMIC_RELAXED, __HIP_MEMORY_SCOPE_WORKGROUP);
        }
    }
    FST(22 + 20 * L);
    STAMP(L ? 25 : 13);
    if (L == 0 && hw == 0 && !is_gu) lds_signal_add(c, P_XGO);
    if (!is_gu && !is_dn) return;
    wait_sel(L);
    const u32x2 si = lds_ld<u32x2>(P_SEL + L * 32);
    const int sel = __builtin_amdgcn_readfirstlane((int)si[0]) & 2047;
    const float rsel = __uint_as_float(si[1]);
    const u32 gxrow = GX + (u32)(sel >> 5) * 2048u, gm = GM + (u32)(sel >> 5) * 16u;
    if (is_gu) {
        const int gidx = c.xg * 14 + c.gi;
        u32 off[2] = {gxrow + (u32)(hw * 32 + (lane & 31)) * 16u, gm};
        u32x4 v[2];
        FST(29 + 20 * L);
        wait_units<2>(c, off, v);
        if (L == 0) lds_signal_add(c, P_XGO);
        FST(23 + 20 * L);
        const float mu = __uint_as_float(v[1][0]), rs = __uint_as_float(v[1][2]);
        if (lane < 32) {
            u32x2 h;
            h[0] = pk((bflo(v[0][0]) - mu) * rs, (bfhi(v[0][0]) - mu) * rs);
            h[1] = pk((bflo(v[0][2]) - mu) * rs, (bfhi(v[0][2]) - mu) * rs);
            lds_st<u32x2>(FS + (u32)(hw * 32 + lane) * 8u, h);
        }
        STAMP(L ? 26 : 14);
        FST(24 + 20 * L);
        f32x4 ag = {}, au = {};
#pragma unroll
        for (int s = 0; s < 4; ++s) {
            const bf16x8 a = lds_ld<bf16x8>(FS + (u32)((hw * 4 + s) * 64 + g16 * 16));
            ag = MFMA16(a, wf[s], ag);
            au = MFMA16(a, wf[4 + s], au);
        }
        if (lane < 16) { lds_st<float>(FS + 4608 + (u32)(hw * 32 + lane) * 4u, ag[0]); lds_st<float>(FS + 4608 + (u32)(hw * 32 + 16 + lane) * 4u, au[0]); }
        FST(25 + 20 * L);
        helper_sync(c, hsync);
        FST(26 + 20 * L);
        if (hw == 0) {
            float g = 0.f, u = 0.f;
#pragma unroll
            for (int k = 0; k < 4; ++k) { g += lds_ld<float>(FS + 4608 + (u32)(k * 32 + i16) * 4u); u += lds_ld<float>(FS + 4608 + (u32)(k * 32 + 16 + i16) * 4u); }
            g = rbf(g); u = rbf(u);
            const float sg = rbf(g * __builtin_amdgcn_rcpf(1.0f + __expf(-g)));
            const float a = rbf(sg * u);
            const float a0 = __shfl(a, (lane & 3) * 4), a1 = __shfl(a, (lane & 3) * 4 + 1), a2 = __shfl(a, (lane & 3) * 4 + 2), a3 = __shfl(a, (lane & 3) * 4 + 3);
            if (lane < 4) stw(c.ws, GA + (u32)(gidx * 4 + lane) * 16u, mk_unit(pk(a0, a1), pk(a2, a3), c.tag));
            FST(27 + 20 * L);
        }
    } else {
        const int didx = c.xg * 4 + (c.gi - 14);
        float mu, rs;
        {
            const int u0 = hw * 112 + lane, u1 = hw * 112 + 64 + (lane < 48 ? lane : 47);
            u32 off[4] = {gxrow + (u32)(didx * 4 + (lane & 3)) * 16u, gm, GA + (u32)u0 * 16u, GA + (u32)u1 * 16u};
            u32x4 v[4];
            wait_units<4>(c, off, v);
            FST(23 + 20 * L);
            mu = __uint_as_float(v[1][0]); rs = __uint_as_float(v[1][2]);
            lds_st<u32x2>(FS + 1024 + (u32)u0 * 8u, (u32x2){v[2][0], v[2][2]});
            if (lane < 48) lds_st<u32x2>(FS + 1024 + (u32)u1 * 8u, (u32x2){v[3][0], v[3][2]});
            if (hw == 0 && lane < 4) lds_st<u32x2>(FS + (u32)lane * 8u, (u32x2){v[0][0], v[0][2]});
        }
        STAMP(L ? 26 : 14);
        FST(24 + 20 * L);
        f32x4 ay = {}, az = {};
        bf16x8 af[14];
#pragma unroll
        for (int s = 0; s < 14; ++s) af[s] = lds_ld<bf16x8>(FS + 1024 + (u32)((hw * 14 + s) * 64 + g16 * 16));
#pragma unroll
        for (int s = 0; s < 14; s += 2) { ay = MFMA16(af[s], wf[s], ay); az = MFMA16(af[s + 1], wf[s + 1], az); }
        if (lane < 16) lds_st<float>(FS + 4608 + (u32)(hw * 16 + lane) * 4u, ay[0] + az[0]);
        FST(25 + 20 * L);
        helper_sync(c, hsync);
        FST(26 + 20 * L);
        if (hw == 0) {
            float y = 0.f;
#pragma unroll
            for (int k = 0; k < 4; ++k) y += lds_ld<float>(FS + 4608 + (u32)(k * 16 + i16) * 4u);
            const float x1 = __uint_as_float((u32)lds_ld<uint16_t>(FS + (u32)i16 * 2u) << 16);
            const float sig = rbf(__builtin_amdgcn_rcpf(1.0f + __expf(-rsel)));
            y = rbf(rbf(y) * sig);
            const float h = rbf((x1 - mu) * rs);
            const float x2 = rbf(x1 + rbf(h + y));
            const float a0 = __shfl(x2, (lane & 3) * 4), a1 = __shfl(x2, (lane & 3) * 4 + 1), a2 = __shfl(x2, (lane & 3) * 4 + 2), a3 = __shfl(x2, (lane & 3) * 4 + 3);
            if (L == 0) { if (lane < 4) stw(c.ws, OFF_GY + (u32)(didx * 4 + lane) * 16u, mk_unit(pk(a0, a1), pk(a2, a3), c.tag)); }
            else if (lane < 4) *(u32x2*)(p.out + (size_t)sel * 512 + didx * 16 + lane * 4) = (u32x2){pk(a0, a1), pk(a2, a3)};
            FST(27 + 20 * L);
        }
    }
}

DI void qkv_patch(const Ctx& c, u32& hsync) {
    const int lane = c.lane, hw = c.w - 4;
    constexpr u32 FS = FS0;
    const int g16 = lane >> 4, i16 = lane & 15;
    bf16x8 wf[24];
    {
        const u32 tro = L_W + (u32)((8 * g16 + (i16 >> 2)) * 64 + (i16 & 3) * 8) + (u32)hw * 8192u;
#pragma unroll
        for (int s_ = 0; s_ < 4; ++s_)
#pragma unroll
            for (int ct = 0; ct < 6; ++ct) {
                const u32 bo = tro + (u32)s_ * 2048u + (u32)(ct >> 1) * 32768u + (u32)(ct & 1) * 32u;
                wf[s_ * 6 + ct] = frag_tr(bo, bo + 256);
            }
    }
    u32 off[2] = {OFF_GY + (u32)lane * 32u, OFF_GY + (u32)lane * 32u + 16u}; u32x4 v[2];
    wait_units<2>(c, off, v);
    float x[8], s = 0, q = 0;
#pragma unroll
    for (int k = 0; k < 2; ++k) { x[4 * k] = bflo(v[k][0]); x[4 * k + 1] = bfhi(v[k][0]); x[4 * k + 2] = bflo(v[k][2]); x[4 * k + 3] = bfhi(v[k][2]); }
#pragma unroll
    for (int k = 0; k < 8; ++k) { s += x[k]; q += x[k] * x[k]; }
    s = wave_sum(s); q = wave_sum(q);
    const float mu = s * (1.0f / 512.0f), var = fmaxf(q * (1.0f / 512.0f) - mu * mu, 0.f);
    const float A = __builtin_amdgcn_rsqf(var + 1e-5f);
    lds_st<u32x4>(FS + (u32)lane * 16u, (u32x4){pk((x[0] - mu) * A, (x[1] - mu) * A), pk((x[2] - mu) * A, (x[3] - mu) * A), pk((x[4] - mu) * A, (x[5] - mu) * A), pk((x[6] - mu) * A, (x[7] - mu) * A)});
    f32x4 acc[6] = {};
#pragma unroll
    for (int s_ = 0; s_ < 4; ++s_) {
        const bf16x8 a = lds_ld<bf16x8>(FS + (u32)((hw * 4 + s_) * 64 + g16 * 16));
#pragma unroll
        for (int ct = 0; ct < 6; ++ct) acc[ct] = MFMA16(a, wf[s_ * 6 + ct], acc[ct]);
    }
    if (lane < 16) {
#pragma unroll
        for (int ct = 0; ct < 6; ++ct) lds_st<float>(FS + 1024 + (u32)(hw * 96 + ct * 16 + lane) * 4u, acc[ct][0]);
    }
    helper_sync(c, hsync);
    if (hw == 0) {
#pragma unroll
        for (int k = 0; k < 2; ++k) {
            const int n = k * 64 + lane;
            if (n < 96) {
                float t = 0.f;
#pragma unroll
                for (int j = 0; j < 4; ++j) t += lds_ld<float>(FS + 1024 + (u32)(j * 96 + n) * 4u);
                lds_st<float>(FS + 4608 + (u32)n * 4u, n < 32 ? t * (0.25f * 1.4426950408889634f) : t);
            }
        }
        asm volatile("s_waitcnt lgkmcnt(0)" ::: "memory");
        if (lane == 0) __hip_atomic_store((LAS u32*)(uintptr_t)P_PATCH, 1u, __ATOMIC_RELAXED, __HIP_MEMORY_SCOPE_WORKGROUP);
    }
}

DI u32x4 gld(const void* base, u32 off) { return *(const u32x4*)((const char*)base + off); }
struct FfSrc { const uint16_t* m; u32 vo, step; bool on, gu; };
DI FfSrc ff_src(const Ctx& c, const Params& p) {
    int lane = c.lane; asm volatile("" : "+v"(lane));
    FfSrc f; const int w = c.w;
    f.on = c.gi < 18; f.gu = c.gi < 14;
    f.m = f.gu ? (w < 4 ? p.wg : p.wu) : p.wd;
    const u32 rowb = f.gu ? 3584u : 1024u, col = f.gu ? (u32)(c.xg * 14 + c.gi) * 32u : (u32)(c.xg * 4 + (c.gi - 14)) * 32u;
    const u32 first = f.gu ? (u32)(w & 3) * 4u : (u32)w * 7u;
    f.step = 32u * rowb;
    f.vo = (u32)(lane >> 1) * rowb + col + (u32)(lane & 1) * 16u + first * f.step;
    return f;
}
DI void keep_wq(const Ctx& c, u32x4 (&pq)[12]) {
#pragma unroll
    for (int j = 0; j < 12; ++j) pq[j] = lds_ld<u32x4>(L_W + (u32)(c.w * 12 + j) * 1024u + (u32)c.lane * 16u);
}
DI void pre_store_wq(const Ctx& c, const u32x4 (&pq)[12]) {
#pragma unroll
    for (int j = 0; j < 12; ++j) lds_st<u32x4>(L_W + (u32)(c.w * 12 + j) * 1024u + (u32)c.lane * 16u, pq[j]);
}
DI void pre_store_ff(const Ctx& c, const u32x4 (&pf)[7]) {
    if (c.gi < 18) {
        const bool gu = c.gi < 14;
        const u32 a = FW0 + (u32)c.w * (gu ? 4096u : 7168u) + (u32)c.lane * 16u;
#pragma unroll
        for (int j = 0; j < 7; ++j) if (j < 4 || !gu) lds_st<u32x4>(a + (u32)j * 1024u, pf[j]);
    }
}
DI void wo_dma(const Ctx& c, const Params& p, u32 vo, int j) {
    const int n = c.w * 8 + j, ft = n >> 5, g = n & 31;
    glds(p.wo, vo + (u32)ft * 64u + (u32)g * 16384u, L_WO + (u32)ft * WO_FT + (u32)g * 1024u);
}
DI u32 wo_lane_off(const Ctx& c) {
    int lane = c.lane; asm volatile("" : "+v"(lane));
    return (u32)((lane >> 2) * 1024 + (c.gi & 7) * 128 + (lane & 3) * 16);
}

struct WFrag { bf16x8 q, k, v; };
template <u32 TR2> DI void wfrag_load(WFrag& f, u32 a01, u32 a2) { f.q = frag_tr(a01, a01 + TR2); f.k = frag_tr(a01 + 32768, a01 + 32768 + TR2); f.v = frag_tr(a2, a2 + TR2); }
template <u32 TR2> DI void qstep(f32x16& aq, f32x16& ak, f32x16& av, bf16x8 x, WFrag& cur, WFrag& nxt, u32 a01, u32 a2, bool more) {
    if (more) nxt.q = frag_tr(a01, a01 + TR2);
    SBAR(); aq = MFMA32(cur.q, x, aq); SBAR();
    if (more) nxt.k = frag_tr(a01 + 32768, a01 + 32768 + TR2);
    SBAR(); ak = MFMA32(cur.k, x, ak); SBAR();
    if (more) nxt.v = frag_tr(a2, a2 + TR2);
    SBAR(); av = MFMA32(x, cur.v, av); SBAR();
}

template <int L>
DI void phaseQ(Ctx& c0, const Params& p, u32& hsync, u32x4 (&pq)[12], const u32x4 (&xf)[32]) {
    Ctx c = c0; FRESH(c);
    const int lane = c.lane, w = c.w;
    const int rb = 4 * (c.xg & 3) + (c.gi & 3), hp = 8 * (c.xg >> 2) + (c.gi >> 2);
    const int r = lane & 31, h = lane >> 5;
    const int i16 = lane & 15, blk = (lane >> 4) & 1;
    const u32 trA = L_W + (u32)(((L == 0 ? 8 : 4) * h + (i16 >> 2)) * 64 + blk * 32 + (i16 & 3) * 8);
    constexpr u32 TR2 = L == 0 ? 256 : 512;
    constexpr u32 OQ = L ? OFF_Q1 : OFF_Q0, OK_ = L ? OFF_K1 : OFF_K0, OV = L ? OFF_V1 : OFF_V0, TS = L ? OFF_TS1 : OFF_TS0;
    const int rot = ((c.gi >> 2) + 2 * (c.gi & 3)) & 7;

    if (w >= 4) {
        const int hw = w - 4;
        if (L == 0) {
            u32x4 r0, r1; u32 ep;
            asm volatile("global_load_dword %0, %1, %2 sc1" : "=v"(ep) : "v"(0u), "s"(p.ws));
            const u32 tok = (u32)(rb * 128 + hp * 8 + hw * 2);
            const u32 vo = tok * 1024u + (u32)lane * 16u;
            asm volatile("global_load_dwordx4 %0, %1, %2" : "=v"(r0) : "v"(vo), "s"(p.x0));
            asm volatile("global_load_dwordx4 %0, %1, %2 offset:1024" : "=v"(r1) : "v"(vo), "s"(p.x0));
            const u32 wv = (u32)((lane >> 2) * 3072 + hp * 64 + (lane & 3) * 16 + hw * 16 * 3072);
            u32 xv[4];
#pragma unroll
            for (int q = 0; q < 4; ++q) { const int row = 8 * q + (lane >> 3); xv[q] = (u32)(((rb * 4 + hw) * 32 + row) * 1024 + (((lane & 7) ^ ((row >> 1) & 7)) * 16)); }
#define WD(i) do { const u32 kc_ = (u32)(((i) + rot) & 7); _Pragma("unroll") for (int t = 0; t < 3; ++t) \
                glds(p.wqkv, wv + (u32)t * 1024u + kc_ * (64u * 3072u), L_W + (u32)t * 32768u + (kc_ * 64u + (u32)hw * 16u) * 64u); } while (0)
#define XD(i) do { const u32 kc_ = (u32)(((i) + rot) & 7); _Pragma("unroll") for (int q = 0; q < 4; ++q) \
                glds(p.x0, xv[q] + kc_ * 128u, L_XR + (u32)((i) % 3) * 16384u + (u32)hw * 4096u + (u32)q * 1024u); } while (0)
#define QWAIT(n) asm volatile("s_waitcnt vmcnt(" #n ")\n\ts_barrier" ::: "memory")
#define CS(i) if (hw < 3) { _Pragma("unroll") for (int s_ = 0; s_ < 4; ++s_) { \
                const u32 a_ = trA + (u32)hw * 32768u + (u32)((((i) + rot) & 7) * 4 + s_) * 1024u; cs = MFMA32(frag_tr(a_, a_ + TR2), ones, cs); } }
            WD(0); XD(0); WD(1); XD(1); WD(2); XD(2); WD(3);
            FST(1);
            f32x16 cs = {};
            const bf16x8 ones = __builtin_bit_cast(bf16x8, (u32x4){0x3f803f80u, 0x3f803f80u, 0x3f803f80u, 0x3f803f80u});
            asm volatile("s_waitcnt vmcnt(14)\n\ts_barrier" : "+v"(r0), "+v"(r1), "+v"(ep) :: "memory");
            c.tag = epoch_tag(ep); c0.tag = c.tag;
            FST(2);
            {
                float s0 = 0, q0 = 0, s1 = 0, q1 = 0;
#pragma unroll
                for (int j = 0; j < 4; ++j) {
                    float a = bflo(r0[j]), b_ = bfhi(r0[j]); s0 += a + b_; q0 += a * a + b_ * b_;
                    float d = bflo(r1[j]), e = bfhi(r1[j]); s1 += d + e; q1 += d * d + e * e;
                }
                s0 = wave_sum(s0); q0 = wave_sum(q0); s1 = wave_sum(s1); q1 = wave_sum(q1);
                const float s = lane ? s1 : s0, q = lane ? q1 : q0;
                const float mu = s * (1.0f / 512.0f), var = fmaxf(q * (1.0f / 512.0f) - mu * mu, 0.f);
                const float A = __builtin_amdgcn_rsqf(var + 1e-5f);
                if (lane < 2) stw(c.ws, TS + (tok + (u32)lane) * 16u, mk_unit(__float_as_uint(A), __float_as_uint(A * mu), c.tag));
            }
            CS(0);
            QWAIT(8); FST(3); XD(3); WD(4); CS(1);
            QWAIT(8); FST(4); XD(4); WD(5); CS(2);
            QWAIT(7); FST(5); XD(5); WD(6); CS(3);
            QWAIT(7); FST(6); XD(6); WD(7); CS(4);
            QWAIT(7); FST(7); XD(7); CS(5);
            QWAIT(4); FST(8); CS(6);
            QWAIT(0); FST(9); CS(7);
#undef WD
#undef XD
#undef QWAIT
#undef CS
            if (hw < 3 && r == 0) {
#pragma unroll
                for (int g = 0; g < 4; ++g) lds_st<f32x4>(P_CS + (u32)(hw * 32 + 8 * g + 4 * h) * 4u, (f32x4){cs[4 * g], cs[4 * g + 1], cs[4 * g + 2], cs[4 * g + 3]});
            }
            STAMP(1);
            BAR();
        } else {
            ffn_chain<0>(c, p, hsync);
            STAMP(15);
            wait_sel(0);
            const int sel = __builtin_amdgcn_readfirstlane((int)lds_ld<u32>(P_SEL)) & 2047;
            if ((sel >> 7) == rb) { lds_wait_ge(P_WQ, 8u); qkv_patch(c, hsync); }
        }
        DRAIN(); BAR();
        STAMP(L ? 16 : 3);
        if (L == 0) keep_wq(c, pq);
    } else {
        const int T = rb * 4 + w;
        f32x16 aq = {}, ak = {}, av = {};
        float A, B;
        if (L == 0) {
            const u32 ep = __builtin_amdgcn_raw_buffer_load_b32(c.ws, OFF_EPOCH, 0, 16);
            u32 xa[4];
#pragma unroll
            for (int s_ = 0; s_ < 4; ++s_) xa[s_] = L_XR + (u32)w * 4096u + (u32)(r * 128 + (((2 * s_ + h) ^ ((r >> 1) & 7)) * 16));
            bf16x8 xr[2][4];
            WFrag f0, f1;
            asm volatile("s_barrier" ::: "memory");
            FST(2);
#pragma unroll
            for (int s_ = 0; s_ < 4; ++s_) xr[0][s_] = lds_ld<bf16x8>(xa[s_]);
            { const u32 a01 = trA + (u32)(rot & 7) * 4096u; wfrag_load<TR2>(f0, a01, a01 + 65536u); }
#pragma unroll
            for (int i = 0; i < 8; ++i) {
                const u32 a01 = trA + (u32)((i + rot) & 7) * 4096u, a2 = a01 + 65536u;
                const u32 n01 = trA + (u32)((i + 1 + rot) & 7) * 4096u, n2 = n01 + 65536u;
                qstep<TR2>(aq, ak, av, xr[i & 1][0], f0, f1, a01 + 1024, a2 + 1024, true);
                qstep<TR2>(aq, ak, av, xr[i & 1][1], f1, f0, a01 + 2048, a2 + 2048, true);
                qstep<TR2>(aq, ak, av, xr[i & 1][2], f0, f1, a01 + 3072, a2 + 3072, true);
                if (i < 7) {
                    asm volatile("s_barrier" ::: "memory");
                    FST(3 + i);
#pragma unroll
                    for (int s_ = 0; s_ < 4; ++s_) xr[(i + 1) & 1][s_] = lds_ld<bf16x8>(xa[s_] + (u32)((i + 1) % 3) * 16384u);
                }
                qstep<TR2>(aq, ak, av, xr[i & 1][3], f1, f0, n01, n2, i < 7);
            }
            FST(10);
            STAMP(1);
            c.tag = epoch_tag(ep); c0.tag = c.tag;
            u32 off[1] = {TS + (u32)(T * 32 + r) * 16u}; u32x4 v[1];
            wait_units<1>(c, off, v);
            A = __uint_as_float(v[0][0]); B = __uint_as_float(v[0][2]);
            BAR();
            STAMP(2);
        } else {
            lds_wait_ge(P_WQ, 8u);
            STAMP(14);
            {
                u32 a01 = trA, a2 = trA + 65536u;
                asm volatile("" : "+v"(a01), "+v"(a2));
                WFrag f0, f1;
                wfrag_load<TR2>(f0, a01, a2);
#pragma unroll
                for (int st = 0; st < 32; st += 2) {
                    qstep<TR2>(aq, ak, av, __builtin_bit_cast(bf16x8, xf[st]), f0, f1, a01 + (u32)(st + 1) * 1024u, a2 + (u32)(st + 1) * 1024u, true);
                    qstep<TR2>(aq, ak, av, __builtin_bit_cast(bf16x8, xf[st + 1]), f1, f0, a01 + (u32)(st + 2) * 1024u, a2 + (u32)(st + 2) * 1024u, st + 2 < 32);
                }
            }
            u32 off[1] = {TS + (u32)(T * 32 + r) * 16u}; u32x4 v[1];
            wait_units<1>(c, off, v);
            A = __uint_as_float(v[0][0]); B = __uint_as_float(v[0][2]);
        }
        if (L == 1) STAMP(15);
        const u32 tokl = L_TOK + (u32)w * 256u;
        if (h == 0) lds_st<f32x2>(tokl + (u32)r * 8u, (f32x2){A, B});
        constexpr float QS = 0.25f * 1.4426950408889634f;
#pragma unroll
        for (int t = 0; t < 2; ++t) {
            const f32x16& acc = t ? ak : aq;
            const float sA = t ? A : A * QS, sB = t ? B : B * QS;
            float o[16];
#pragma unroll
            for (int g = 0; g < 4; ++g) {
                const f32x4 cs = lds_ld<f32x4>(P_CS + (u32)(t * 32 + 8 * g + 4 * h) * 4u);
#pragma unroll
                for (int e = 0; e < 4; ++e) o[4 * g + e] = sA * acc[4 * g + e] - sB * cs[e];
            }
#pragma unroll
            for (int a = 0; a < 2; ++a) {
                u32x4 v = {pk(o[8 * a], o[8 * a + 1]), pk(o[8 * a + 2], o[8 * a + 3]), pk(o[8 * a + 4], o[8 * a + 5]), pk(o[8 * a + 6], o[8 * a + 7])};
                stw(c.ws, (t ? OK_ : OQ) + (u32)(((hp * 2 + a) * 64 + T) * 64 + lane) * 16u, v);
            }
        }
        {
            asm volatile("s_waitcnt lgkmcnt(0)" ::: "memory");
            const float cs = lds_ld<float>(P_CS + (u32)(64 + r) * 4u);
            float o[16];
#pragma unroll
            for (int g = 0; g < 4; ++g) {
                const f32x4 ab0 = lds_ld<f32x4>(tokl + (u32)(8 * g + 4 * h) * 8u), ab1 = lds_ld<f32x4>(tokl + (u32)(8 * g + 4 * h + 2) * 8u);
                o[4 * g] = ab0[0] * av[4 * g] - ab0[1] * cs; o[4 * g + 1] = ab0[2] * av[4 * g + 1] - ab0[3] * cs;
                o[4 * g + 2] = ab1[0] * av[4 * g + 2] - ab1[1] * cs; o[4 * g + 3] = ab1[2] * av[4 * g + 3] - ab1[3] * cs;
            }
            const int head = hp * 2 + (r >> 4), d = r & 15;
#pragma unroll
            for (int s = 0; s < 2; ++s) {
                u32x4 v = {pk(o[8 * s], o[8 * s + 1]), pk(o[8 * s + 2], o[8 * s + 3]), pk(o[8 * s + 4], o[8 * s + 5]), pk(o[8 * s + 6], o[8 * s + 7])};
                stw(c.ws, OV + (u32)((head * 64 + T) * 1024 + s * 512 + h * 256 + d * 16), v);
            }
        }
        if (L == 1) {
            wait_sel(0);
            const int sel = __builtin_amdgcn_readfirstlane((int)lds_ld<u32>(P_SEL)) & 2047;
            if ((sel >> 5) == T) {
                const int ts = sel & 31;
                DRAIN();
                lds_wait_ge(P_PATCH, 1u);
                if (r == ts) {
#pragma unroll
                    for (int t = 0; t < 2; ++t)
#pragma unroll
                        for (int a = 0; a < 2; ++a) {
                            const f32x4 p0 = lds_ld<f32x4>(FS0 + 4608 + (u32)(t * 32 + 16 * a + 4 * h) * 4u), p1 = lds_ld<f32x4>(FS0 + 4608 + (u32)(t * 32 + 16 * a + 8 + 4 * h) * 4u);
                            stw(c.ws, (t ? OK_ : OQ) + (u32)(((hp * 2 + a) * 64 + T) * 64 + lane) * 16u, (u32x4){pk(p0[0], p0[1]), pk(p0[2], p0[3]), pk(p1[0], p1[1]), pk(p1[2], p1[3])});
                        }
                }
                if (h == 0) {
                    const float pv = lds_ld<float>(FS0 + 4608 + (u32)(64 + r) * 4u);
                    const u32 o = OV + (u32)(((hp * 2 + (r >> 4)) * 64 + T) * 1024 + (ts >> 4) * 512 + ((ts >> 2) & 1) * 256 + (r & 15) * 16 + ((ts & 3) + 4 * ((ts >> 3) & 1)) * 2);
                    __builtin_amdgcn_raw_buffer_store_b16((uint16_t)(pk(pv, 0.f) & 0xFFFFu), c.ws, o, 0, 16);
                }
            }
        }
        DRAIN(); BAR();
        STAMP(L ? 16 : 3);
        if (w == 0 && lane == 0) set_flag(c, (L ? OFF_FQ1 : OFF_FQ0) + (u32)(hp * 16 + rb) * 128u);
        if (L == 0) keep_wq(c, pq);
    }
}

struct AState { f32x16 Sa, Sb, Oa, Ob; bf16x8 kC, kN, v0, v1; u32x4 pb1; };
#define EXP2(x) __builtin_amdgcn_exp2f(x)
#define BC8(x) __builtin_bit_cast(bf16x8, x)
#define PVM(O, v, p) O = MFMA32(v, BC8(p), O)
template <bool USE_C, class HOOK>
DI void attn_step(AState& s, bf16x8 qa, bf16x8 qb, const f32x16& ca, const f32x16& cb, u32 kp, u32 vp0, u32 vp1, u32 knext, u32 voff, HOOK hook) {
    const f32x16 zero = {};
    u32x4 pa0, pa1, pb0;
    s.kN = lds_ld<bf16x8>(kp + knext); SBAR();
    s.v0 = lds_ld<bf16x8>(vp0 + voff); SBAR();
    PVM(s.Ob, s.v1, s.pb1); SBAR();
    s.v1 = lds_ld<bf16x8>(vp1 + voff); SBAR();
    float a0 = EXP2(s.Sa[0]); SBAR(); float a1 = EXP2(s.Sa[1]); SBAR(); float a2 = EXP2(s.Sa[2]); SBAR(); float a3 = EXP2(s.Sa[3]); SBAR();
    s.Sb = MFMA32(s.kC, qb, USE_C ? cb : zero); SBAR();
    float a4 = EXP2(s.Sa[4]); SBAR(); float a5 = EXP2(s.Sa[5]); SBAR(); float a6 = EXP2(s.Sa[6]); SBAR(); float a7 = EXP2(s.Sa[7]); SBAR();
    pa0[0] = pk(a0, a1); SBAR(); pa0[1] = pk(a2, a3); SBAR(); float a8 = EXP2(s.Sa[8]); SBAR(); pa0[2] = pk(a4, a5); SBAR(); pa0[3] = pk(a6, a7); SBAR();
    float a9 = EXP2(s.Sa[9]); SBAR();
    asm volatile("" : "+v"(s.v0), "+v"(s.v1), "+v"(s.kN)); SBAR();
    float a10 = EXP2(s.Sa[10]); SBAR();
    PVM(s.Oa, s.v0, pa0); SBAR();
    float a11 = EXP2(s.Sa[11]); SBAR(); float a12 = EXP2(s.Sa[12]); SBAR(); float a13 = EXP2(s.Sa[13]); SBAR(); float a14 = EXP2(s.Sa[14]); SBAR(); float a15 = EXP2(s.Sa[15]); SBAR();
    pa1[0] = pk(a8, a9); SBAR(); pa1[1] = pk(a10, a11); SBAR(); pa1[2] = pk(a12, a13); SBAR(); pa1[3] = pk(a14, a15); SBAR();
    float b0 = EXP2(s.Sb[0]); SBAR(); float b1 = EXP2(s.Sb[1]); SBAR();
    PVM(s.Oa, s.v1, pa1); SBAR();
    hook(); SBAR();
    float b2 = EXP2(s.Sb[2]); SBAR(); float b3 = EXP2(s.Sb[3]); SBAR(); float b4 = EXP2(s.Sb[4]); SBAR(); float b5 = EXP2(s.Sb[5]); SBAR();
    s.Sa = MFMA32(s.kN, qa, USE_C ? ca : zero); SBAR();
    float b6 = EXP2(s.Sb[6]); SBAR(); float b7 = EXP2(s.Sb[7]); SBAR();
    pb0[0] = pk(b0, b1); SBAR(); pb0[1] = pk(b2, b3); SBAR(); float b8 = EXP2(s.Sb[8]); SBAR(); pb0[2] = pk(b4, b5); SBAR(); pb0[3] = pk(b6, b7); SBAR();
    float b9 = EXP2(s.Sb[9]); SBAR(); float b10 = EXP2(s.Sb[10]); SBAR();
    PVM(s.Ob, s.v0, pb0); SBAR();
    float b11 = EXP2(s.Sb[11]); SBAR(); float b12 = EXP2(s.Sb[12]); SBAR(); float b13 = EXP2(s.Sb[13]); SBAR(); float b14 = EXP2(s.Sb[14]); SBAR(); float b15 = EXP2(s.Sb[15]); SBAR();
    s.pb1[0] = pk(b8, b9); SBAR(); s.pb1[1] = pk(b10, b11); SBAR(); s.pb1[2] = pk(b12, b13); SBAR(); s.pb1[3] = pk(b14, b15); SBAR();
    s.kC = s.kN;
}
struct NoHook { DI void operator()() const {} };
template <bool USE_C>
DI void attn_chunk(AState& s, bf16x8 qa, bf16x8 qb, const f32x16& ca, const f32x16& cb, u32 kp, u32 vp0, u32 vp1) {
    attn_step<USE_C>(s, qa, qb, ca, cb, kp, vp0, vp1, 1024, 0, NoHook());
    attn_step<USE_C>(s, qa, qb, ca, cb, kp, vp0, vp1, 2048, VSTR, NoHook());
    attn_step<USE_C>(s, qa, qb, ca, cb, kp, vp0, vp1, 3072, 2 * VSTR, NoHook());
    attn_step<USE_C>(s, qa, qb, ca, cb, kp, vp0, vp1, 8192, 3 * VSTR, NoHook());
}
DI void attn_begin(AState& s, bf16x8 qa, const f32x16& ca, u32 kp) {
    const f32x16 zero = {};
    s.Oa = zero; s.Ob = zero; s.pb1 = (u32x4){0u, 0u, 0u, 0u}; s.v1 = BC8(s.pb1);
    s.kC = lds_ld<bf16x8>(kp);
    s.Sa = MFMA32(s.kC, qa, ca);
}
DI void attn_combine(const Ctx& c, AState& s, float (&O)[9]) {
    const int kh = c.w >> 2;
    s.Ob = MFMA32(s.v1, BC8(s.pb1), s.Ob);
    const f32x16& snd = kh ? s.Oa : s.Ob; const f32x16& keep = kh ? s.Ob : s.Oa;
    const u32 xb = L_AX + (u32)c.w * 2304u;
    lds_st<f32x4>(xb + (u32)c.lane * 16u, (f32x4){snd[0], snd[1], snd[2], snd[3]});
    lds_st<f32x4>(xb + 1024u + (u32)c.lane * 16u, (f32x4){snd[4], snd[5], snd[6], snd[7]});
    lds_st<float>(xb + 2048u + (u32)c.lane * 4u, snd[8]);
    BAR();
    const u32 xp = L_AX + (u32)(c.w ^ 4) * 2304u;
    const f32x4 u0 = lds_ld<f32x4>(xp + (u32)c.lane * 16u), u1 = lds_ld<f32x4>(xp + 1024u + (u32)c.lane * 16u);
    const float l2 = lds_ld<float>(xp + 2048u + (u32)c.lane * 4u);
#pragma unroll
    for (int q = 0; q < 4; ++q) { O[q] = keep[q] + u0[q]; O[4 + q] = keep[4 + q] + u1[q]; }
    O[8] = keep[8] + l2;
}

template <int L>
DI void phaseA(const Ctx& c0, const Params& p) {
    Ctx c = c0; FRESH(c);
    u32x4 pf[7] = {};
    const int lane = c.lane, w = c.w, tid = w * 64 + lane;
    const int head = 4 * c.xg + (c.gi >> 3), qb = c.gi & 7;
    const int j = w & 3, kh = w >> 2;
    constexpr u32 OQ = L ? OFF_Q1 : OFF_Q0, OK_ = L ? OFF_K1 : OFF_K0, OV = L ? OFF_V1 : OFF_V0, OO = L ? OFF_O1 : OFF_O0;
    if (tid == 0) lds_st<u32>(L_AX + 8 * 2304u, 0u);
    if (w == 0) wait_flags(c, (L ? OFF_FQ1 : OFF_FQ0) + (u32)((head >> 1) * 16 + (lane & 15)) * 128u);
    BAR();
    STAMP(L ? 17 : 4);
    CSTAMP(9);
    const u32 koff = OK_ + (u32)((head * 64 + w) * 64 + lane) * 16u, voff = OV + (u32)((head * 64 + w) * 64 + lane) * 16u;
    u32x4 rk[8], rv[8];
    rk[0] = ldw(c.ws, koff); rv[0] = ldw(c.ws, voff);
    const u32 qo = OQ + (u32)((head * 64 + qb * 8 + 2 * j) * 64 + lane) * 16u;
    const u32x4 qar = ldw(c.ws, qo), qbr = ldw(c.ws, qo + 1024u);
    SBAR(); __builtin_amdgcn_s_barrier(); SBAR();
    rk[1] = ldw(c.ws, koff + 8192u); rv[1] = ldw(c.ws, voff + 8192u); rk[2] = ldw(c.ws, koff + 16384u); rv[2] = ldw(c.ws, voff + 16384u);
    SBAR(); __builtin_amdgcn_s_barrier(); SBAR();
    rk[3] = ldw(c.ws, koff + 24576u); rv[3] = ldw(c.ws, voff + 24576u); rk[4] = ldw(c.ws, koff + 32768u); rv[4] = ldw(c.ws, voff + 32768u);
    SBAR();
    { const int t = tid >> 3, s = tid & 7; const u32 val = (s & 3) == 0 ? 0x3f803f80u : 0u;
      lds_st<u32x4>(L_V + (u32)t * VSTR + 1024u + (u32)s * 16u, (u32x4){val, val, val, val}); }
    const u32 kw = L_K + (u32)w * 1024u + (u32)lane * 16u, vw = L_V + (u32)w * VSTR + (u32)lane * 16u;
    lds_st<u32x4>(kw, rk[0]); lds_st<u32x4>(vw, rv[0]);
    lds_st<u32x4>(kw + 8192u, rk[1]); lds_st<u32x4>(vw + 8u * VSTR, rv[1]);
    CSTAMP(10);
    BAR();
    const int r = lane & 31, h = lane >> 5;
    const u32 kp0 = L_K + (u32)kh * 4096u + (u32)lane * 16u;
    const u32 onesoff = 1024u + ((r >= 20 && r < 28) ? 64u : 0u) + ((r == 16 || r == 20) ? 0u : 16u);
    const u32 vp00 = L_V + (u32)kh * (4u * VSTR) + (r < 16 ? (u32)(h * 256 + r * 16) : onesoff);
    const u32 vp10 = L_V + (u32)kh * (4u * VSTR) + (r < 16 ? (u32)(512 + h * 256 + r * 16) : onesoff);
    const bf16x8 qa = BC8(qar), qbf = BC8(qbr);
    const f32x16 cz = {};
    AState s;
    STAMP(L ? 18 : 5);
#define PRIO(n) __builtin_amdgcn_s_setprio(n)
#define BAR2() BAR()
    PRIO(3);
    CSTAMP(0);
    attn_begin(s, qa, cz, kp0);
    {
#define PUT(ch) [&]() { lds_st<u32x4>(kw + (u32)(ch) * 8192u, rk[ch]); lds_st<u32x4>(vw + (u32)(ch) * (8u * VSTR), rv[ch]); }
#define GET(ch) [&]() { rk[ch] = ldw(c.ws, koff + (u32)(ch) * 8192u); rv[ch] = ldw(c.ws, voff + (u32)(ch) * 8192u); }
#define GETPUT(g, ch) [&]() { GET(g)(); PUT(ch)(); }
#define FF(j_) [&]() { if (ff.on && ((j_) < 4 || !ff.gu)) pf[j_] = gld(ff.m, ff.vo + (u32)(j_) * ff.step); }
#define WO(j_) [&]() { wo_dma(c, p, wv, j_); }
#define ST(ch, t, HOOK) attn_step<false>(s, qa, qbf, cz, cz, kp, vp0, vp1, ((t) == 3 ? 8192 : ((t) + 1) * 1024) + ((ch) & 1) * 8192, (((ch) & 1) * 8 + (t)) * VSTR, HOOK)
#define NEXT2() do { kp += 16384; vp0 += 16 * VSTR; vp1 += 16 * VSTR; asm volatile("" : "+v"(kp), "+v"(vp0), "+v"(vp1)); } while (0)
        u32 kp = kp0, vp0 = vp00, vp1 = vp10;
        ST(0, 0, GET(5)); ST(0, 1, GETPUT(6, 2)); ST(0, 2, GETPUT(7, 3)); ST(0, 3, PUT(4));
        CSTAMP(1);
        ST(1, 0, PUT(5)); ST(1, 1, PUT(6)); ST(1, 2, PUT(7));
        BAR();
        ST(1, 3, NoHook());
        const FfSrc ff = ff_src(c, p);
        NEXT2(); PRIO(2);
        CSTAMP(2);
        ST(2, 0, FF(0)); ST(2, 1, FF(1)); ST(2, 2, FF(2)); ST(2, 3, FF(3));
        CSTAMP(3);
        ST(3, 0, FF(4)); ST(3, 1, FF(5)); ST(3, 2, FF(6)); ST(3, 3, NoHook());
        NEXT2(); PRIO(1);
        CSTAMP(4);
        ST(4, 0, NoHook()); ST(4, 1, NoHook()); ST(4, 2, NoHook()); ST(4, 3, NoHook());
        CSTAMP(5);
        ST(5, 0, NoHook()); ST(5, 1, NoHook()); ST(5, 2, NoHook()); ST(5, 3, NoHook());
        const u32 wv = wo_lane_off(c);
        NEXT2(); PRIO(0);
        BAR2();
        CSTAMP(6);
        ST(6, 0, WO(0)); ST(6, 1, WO(1)); ST(6, 2, WO(2)); ST(6, 3, WO(3));
        CSTAMP(7);
        ST(7, 0, WO(4)); ST(7, 1, WO(5)); ST(7, 2, WO(6)); ST(7, 3, WO(7));
#undef PUT
#undef GET
#undef GETPUT
#undef FF
#undef WO
#undef ST
#undef NEXT2
    }
    CSTAMP(8);
    STAMP(L ? 19 : 6);
    FRESH(c);
    float O[9];
    attn_combine(c, s, O);
    const u32 oo = OO + (u32)((head * 64 + qb * 8 + 2 * j + kh) * 64 + c.lane) * 16u;
    {
        bool bad = !(O[8] >= 0x1p-60f && O[8] <= 0x1p60f);
#pragma unroll
        for (int q = 0; q < 8; ++q) bad = bad || !(__builtin_fabsf(O[q]) <= 0x1p126f);
        if (__any(bad) && c.lane == 0) lds_st<u32>(L_AX + 8 * 2304u, 1u);
        const float rl = __builtin_amdgcn_rcpf(O[8]);
        u32x4 o;
#pragma unroll
        for (int q = 0; q < 4; ++q) o[q] = pk(O[2 * q] * rl, O[2 * q + 1] * rl);
        stw(c.ws, oo, o);
    }
    DRAIN(); BAR();
    if (__builtin_amdgcn_readfirstlane((int)lds_ld<u32>(L_AX + 8 * 2304u)) != 0) {
        {
            const u32 ko = OK_ + (u32)((head * 64 + w) * 64 + c.lane) * 16u, vo = OV + (u32)((head * 64 + w) * 64 + c.lane) * 16u;
            for (int ch = 0; ch < 4; ++ch) {
                const u32x4 a = ldw(c.ws, ko + (u32)ch * 8192u), b_ = ldw(c.ws, vo + (u32)ch * 8192u);
                lds_st<u32x4>(L_K + (u32)(ch * 8 + w) * 1024u + (u32)c.lane * 16u, a);
                lds_st<u32x4>(L_V + (u32)(ch * 8 + w) * VSTR + (u32)c.lane * 16u, b_);
            }
            { const int t = (w * 64 + c.lane) >> 3, s_ = c.lane & 7; const u32 val = (s_ & 3) == 0 ? 0x3f803f80u : 0u;
              lds_st<u32x4>(L_V + (u32)t * VSTR + 1024u + (u32)s_ * 16u, (u32x4){val, val, val, val}); }
        }
        BAR();
        float ma = -INFINITY, mb = -INFINITY;
        for (int ch = 0; ch < 8; ++ch)
            for (int t = 0; t < 4; ++t) {
                const bf16x8 kf = lds_ld<bf16x8>(kp0 + (u32)(ch * 8192 + t * 1024));
                const f32x16 Sa = MFMA32(kf, qa, cz), Sb = MFMA32(kf, qbf, cz);
#pragma unroll
                for (int q = 0; q < 16; ++q) { ma = fmaxf(ma, Sa[q]); mb = fmaxf(mb, Sb[q]); }
            }
        ma = fmaxf(ma, __shfl_xor(ma, 32)); mb = fmaxf(mb, __shfl_xor(mb, 32));
        const u32 xb = L_AX + (u32)w * 2304u, xp = L_AX + (u32)(w ^ 4) * 2304u;
        lds_st<f32x2>(xb + (u32)c.lane * 8u, (f32x2){ma, mb});
        BAR();
        const f32x2 mo = lds_ld<f32x2>(xp + (u32)c.lane * 8u);
        ma = fmaxf(ma, mo[0]); mb = fmaxf(mb, mo[1]);
        BAR();
        f32x16 ca, cb;
#pragma unroll
        for (int q = 0; q < 16; ++q) { ca[q] = -ma; cb[q] = -mb; }
        attn_begin(s, qa, ca, kp0);
        u32 kp = kp0, vp0 = vp00, vp1 = vp10;
        for (int ch = 0; ch < 8; ++ch) {
            attn_chunk<true>(s, qa, qbf, ca, cb, kp, vp0, vp1);
            kp += 8192; vp0 += 8 * VSTR; vp1 += 8 * VSTR;
        }
        attn_combine(c, s, O);
        const float rl = 1.0f / O[8];
        u32x4 o;
#pragma unroll
        for (int q = 0; q < 4; ++q) o[q] = pk(O[2 * q] * rl, O[2 * q + 1] * rl);
        stw(c.ws, oo, o);
        BAR();
        const u32 wv2 = wo_lane_off(c);
        for (int q = 0; q < 8; ++q) wo_dma(c, p, wv2, q);
        DRAIN(); BAR();
    }
    STAMP(L ? 20 : 7);
    if (w == 0 && c.lane == 0) set_flag(c, (L ? OFF_FA1 : OFF_FA0) + (u32)(qb * 32 + head) * 128u);
    pre_store_ff(c, pf);
}

template <int L>
DI void phaseO(const Ctx& c0, const Params& p, u32x4 (&x2)[2], u32& hsync, const u32x4 (&pq)[12], u32x4 (&xf)[32]) {
    Ctx c = c0; FRESH(c);
    const int lane = c.lane, w = c.w;
    const int tb = 4 * c.xg + (c.gi >> 3), fb = c.gi & 7;
    const int r = lane & 31, h = lane >> 5;
    constexpr u32 OO = L ? OFF_O1 : OFF_O0, GS = L ? OFF_GS1 : OFF_GS0, GC = L ? OFF_GC1 : OFF_GC0, GM = L ? OFF_GM1 : OFF_GM0, GX = L ? OFF_GX1 : OFF_GX0;
    constexpr u32 L_TS = L_SB, L_CAND = P_ST + 8;
    if (w >= 4) {
        const int hw = w - 4;
        if (hw == 0) wait_flags(c, (L ? OFF_FA1 : OFF_FA0) + (u32)((tb >> 2) * 32 + (lane & 31)) * 128u);
        float wsum = 0.f;
        if (hw == 0) {
            const u32x4 v = *(const u32x4*)(p.wr + lane * 8); float s = 0;
#pragma unroll
            for (int j = 0; j < 4; ++j) s += bflo(v[j]) + bfhi(v[j]);
            wsum = wave_sum(s);
        }
        BAR();
        STAMP(L ? 21 : 8);
        BAR();
        BAR();
        __builtin_amdgcn_s_setprio(3);
        FST(30 + 20 * L);
        if (hw == 0) {
            const u32 a = L_SB + (u32)h * 384u + (u32)r * 12u;
            const float t1 = lds_ld<float>(a) + lds_ld<float>(a + 768), t2 = lds_ld<float>(a + 4) + lds_ld<float>(a + 772), t3 = lds_ld<float>(a + 8) + lds_ld<float>(a + 776);
            stw(c.ws, GS + (u32)((tb * 8 + fb) * 1024 + lane * 16), (u32x4){__float_as_uint(t1), __float_as_uint(t2), __float_as_uint(t3), c.tag});
        }
        FST(31 + 20 * L);
        if (L == 0) {
            if (hw != 0) { pre_store_wq(c, pq); lds_signal_add(c, P_WQ); }
            BAR();
        }
        FST(32 + 20 * L);
        if (hw == 0) {
            u32x4 v[8]; u32 n = 0;
            for (;;) {
                bool ok = true;
#pragma unroll
                for (int k = 0; k < 8; ++k) v[k] = ldw(c.ws, GS + (u32)((tb * 8 + k) * 1024 + lane * 16));
                MEMFENCE();
#pragma unroll
                for (int k = 0; k < 8; ++k) ok = ok && v[k][3] == c.tag;
                if (__all(ok)) break;
                if (spin_fail(c, n)) break;
            }
            FST(33 + 20 * L);
            float t1 = 0, t2 = 0, t3 = 0;
#pragma unroll
            for (int k = 0; k < 8; ++k) { t1 += __uint_as_float(v[k][0]); t2 += __uint_as_float(v[k][1]); t3 += __uint_as_float(v[k][2]); }
            const float mu = t1 * (1.0f / 512.0f), var = fmaxf(t2 * (1.0f / 512.0f) - mu * mu, 0.f);
            const float rs = __builtin_amdgcn_rsqf(var + 1e-5f);
            const float rt = rbf(rs * (t3 - mu * wsum));
            const u32 key = half_umax(score_key(__float_as_uint(rt) >> 16, (u32)r));
            const int bt = (int)key_idx(key);
            if (fb == 0) {
                const u32 k0 = (u32)__builtin_amdgcn_readlane((int)key, 0), k1 = (u32)__builtin_amdgcn_readlane((int)key, 32);
                const u32 kT = (lane & 1) ? k1 : k0;
                const u64 g = (cand_tag(c.tag) << 21) | (u64)(key_bf(kT) << 5) | (u64)key_idx(kT);
                if (lane < 16) __builtin_amdgcn_raw_buffer_store_b64((u32x2){(u32)g, (u32)(g >> 32)}, c.ws, GC + (u32)(lane >> 1) * 512u + (u32)(tb * 2 + (lane & 1)) * 8u, 0, 16);
                if (r == bt) stw(c.ws, GM + (u32)(tb * 2 + h) * 16u, mk_unit(__float_as_uint(mu), __float_as_uint(rs), c.tag));
                if (L == 0) {
                    const float g1 = 1.0f + rs, A = g1 * __builtin_amdgcn_rsqf(var * g1 * g1 + 1e-5f);
                    stw(c.ws, OFF_TS1 + (u32)(tb * 64 + lane) * 16u, mk_unit(__float_as_uint(A), __float_as_uint(A * mu), c.tag));
                }
            }
            FST(34 + 20 * L);
            lds_st<f32x2>(L_TS + (u32)lane * 8u, (f32x2){mu, rs});
            if (r == 0) lds_st<u32>(L_CAND + (u32)h * 4u, (u32)bt);
            lds_signal_add(c, P_ST + L * 4);
            {
                const int ct = (lane >> 4) & 1, u = lane & 15;
                const int cb = __builtin_amdgcn_readlane(bt, 0), cb1 = __builtin_amdgcn_readlane(bt, 32);
                const int row = ct ? cb1 : cb;
                const u32x2 d = lds_ld<u32x2>(L_X1T + (u32)(ct + 2 * (u >> 3)) * 2048u + (u32)((u >> 2) & 1) * 1024u + (u32)((u & 1) * 32 + row) * 16u + (u32)((u >> 1) & 1) * 8u);
                if (lane < 32) stw(c.ws, GX + (u32)(tb * 2 + ct) * 2048u + (u32)(fb * 16 + u) * 16u, mk_unit(d[0], d[1], c.tag));
            }
            if (L == 0) { pre_store_wq(c, pq); lds_signal_add(c, P_WQ); }
        }
        STAMP(L ? 23 : 12);
        if (L == 1) ffn_chain<1>(c, p, hsync);
        return;
    }
    const int tt = w & 1, kh = w >> 1;
    const int tile = tb * 2 + tt, tok = tile * 32 + r;
    const int f0 = fb * 64 + kh * 32;
    u32x2 res[4], wr[4];
#pragma unroll
    for (int g = 0; g < 4; ++g) {
        if (L == 0) res[g] = *(const u32x2*)(p.x0 + (size_t)tok * 512 + f0 + 8 * g + 4 * h);
        wr[g] = *(const u32x2*)(p.wr + f0 + 8 * g + 4 * h);
    }
    if (L == 1) {
        const int sel0 = __builtin_amdgcn_readfirstlane((int)lds_ld<u32>(P_SEL)) & 2047;
        if ((sel0 >> 5) == tile) {
            const u32 ub = OFF_GY + (u32)(f0 / 4 + h) * 16u;
            u32 off[4] = {ub, ub + 32u, ub + 64u, ub + 96u}; u32x4 v[4];
            wait_units<4>(c, off, v);
            if (r == (sel0 & 31)) { x2[0] = (u32x4){v[0][0], v[0][2], v[1][0], v[1][2]}; x2[1] = (u32x4){v[2][0], v[2][2], v[3][0], v[3][2]}; }
        }
#pragma unroll
        for (int g = 0; g < 4; ++g) res[g] = (u32x2){x2[g >> 1][(g & 1) * 2], x2[g >> 1][(g & 1) * 2 + 1]};
    }
    BAR();
    STAMP(L ? 21 : 8);
    u32x4 bf[16];
    const u32 oo = OO + (u32)(((kh * 16) * 64 + tile) * 64 + lane) * 16u;
#pragma unroll
    for (int s = 0; s < 16; ++s) bf[s] = ldw(c.ws, oo + (u32)s * 65536u);
    const int i16 = lane & 15, blk = (lane >> 4) & 1;
    const u32 trA = L_WO + (u32)((kh * 256 + 4 * h + (i16 >> 2)) * 64 + blk * 32 + (i16 & 3) * 8);
    f32x16 a0 = {}, a1 = {};
    {
        u32 ta = trA, tb_ = trA + WO_FT;
        asm volatile("" : "+v"(ta), "+v"(tb_));
        bf16x8 f0_[2], f1_[2];
        f0_[0] = frag_tr(ta, ta + 512); f0_[1] = frag_tr(tb_, tb_ + 512);
#pragma unroll
        for (int s = 0; s < 16; s += 2) {
            f1_[0] = frag_tr(ta + (u32)(s + 1) * 1024u, ta + (u32)(s + 1) * 1024u + 512); SBAR();
            a0 = MFMA32(f0_[0], BC8(bf[s]), a0); SBAR();
            f1_[1] = frag_tr(tb_ + (u32)(s + 1) * 1024u, tb_ + (u32)(s + 1) * 1024u + 512); SBAR();
            a1 = MFMA32(f0_[1], BC8(bf[s]), a1); SBAR();
            if (s + 2 < 16) f0_[0] = frag_tr(ta + (u32)(s + 2) * 1024u, ta + (u32)(s + 2) * 1024u + 512);
            SBAR(); a0 = MFMA32(f1_[0], BC8(bf[s + 1]), a0); SBAR();
            if (s + 2 < 16) f0_[1] = frag_tr(tb_ + (u32)(s + 2) * 1024u, tb_ + (u32)(s + 2) * 1024u + 512);
            SBAR(); a1 = MFMA32(f1_[1], BC8(bf[s + 1]), a1); SBAR();
        }
    }
    {
        const f32x16& snd = kh ? a0 : a1;
#pragma unroll
        for (int g = 0; g < 4; ++g) lds_st<f32x4>(L_XB + (u32)w * 4096u + (u32)g * 1024u + (u32)lane * 16u, (f32x4){snd[4 * g], snd[4 * g + 1], snd[4 * g + 2], snd[4 * g + 3]});
    }
    BAR();
    STAMP(L ? 22 : 9);
    u32x4 xp[2];
    {
        const f32x16& keep = kh ? a1 : a0;
#pragma unroll
        for (int g = 0; g < 4; ++g) {
            const f32x4 o = lds_ld<f32x4>(L_XB + (u32)(w ^ 2) * 4096u + (u32)g * 1024u + (u32)lane * 16u);
            const u32 t0 = pk(keep[4 * g] + o[0], keep[4 * g + 1] + o[1]), t1 = pk(keep[4 * g + 2] + o[2], keep[4 * g + 3] + o[3]);
            xp[g >> 1][(g & 1) * 2] = pk(bflo(res[g][0]) + bflo(t0), bfhi(res[g][0]) + bfhi(t0));
            xp[g >> 1][(g & 1) * 2 + 1] = pk(bflo(res[g][1]) + bflo(t1), bfhi(res[g][1]) + bfhi(t1));
        }
    }
    if (L == 0) {
#pragma unroll
        for (int s = 0; s < 2; ++s) stw(c.ws, OFF_X1 + (u32)((tile * 32 + f0 / 16 + s) * 64 + lane) * 16u, xp[s]);
    }
#pragma unroll
    for (int s = 0; s < 2; ++s) lds_st<u32x4>(L_X1T + (u32)w * 2048u + (u32)s * 1024u + (u32)lane * 16u, xp[s]);
    float x1[16];
    {
        float s1 = 0, s2 = 0, s3 = 0;
#pragma unroll
        for (int i = 0; i < 16; ++i) {
            const u32 wd = xp[i >> 3][(i & 7) >> 1];
            x1[i] = (i & 1) ? bfhi(wd) : bflo(wd);
            const u32 ww = wr[i >> 2][(i & 3) >> 1];
            const float wv = (i & 1) ? bfhi(ww) : bflo(ww);
            s1 += x1[i]; s2 += x1[i] * x1[i]; s3 += x1[i] * wv;
        }
        s1 = swap32_add(s1); s2 = swap32_add(s2); s3 = swap32_add(s3);
        if (h == 0) { const u32 a = L_SB + (u32)w * 384u + (u32)r * 12u; lds_st<float>(a, s1); lds_st<float>(a + 4, s2); lds_st<float>(a + 8, s3); }
    }
    BAR();
    if (L == 0) {
        DRAIN(); BAR();
        STAMP(10);
        if (w == 0 && lane == 0) set_flag(c, OFF_FO + (u32)(tb * 8 + fb) * 128u);
        pre_store_wq(c, pq); lds_signal_add(c, P_WQ);
        const int rbq = 4 * (c.xg & 3) + (c.gi & 3);
        wait_flags(c, OFF_FO + (u32)(rbq * 16 + (lane & 15)) * 128u);
        STAMP(13);
    }
    lds_wait_ge(P_ST + L * 4, 1u);
    STAMP(L ? 23 : 11);
    const f32x2 st = lds_ld<f32x2>(L_TS + (u32)(tt * 32 + r) * 8u);
    const int bt = (int)lds_ld<u32>(L_CAND + (u32)tt * 4u) & 31;
#pragma unroll
    for (int s = 0; s < 2; ++s)
#pragma unroll
        for (int j = 0; j < 4; ++j) {
            const float a = x1[8 * s + 2 * j], b_ = x1[8 * s + 2 * j + 1];
            const u32 hh = pk((a - st[0]) * st[1], (b_ - st[0]) * st[1]);
            x2[s][j] = pk(a + bflo(hh), b_ + bfhi(hh));
        }
    if (L == 0) {
        lds_wait_ge(P_XGO, c.gi < 14 ? 4u : 1u);
        const int rbq = 4 * (c.xg & 3) + (c.gi & 3);
        const u32 xo = OFF_X1 + (u32)(rbq * 4 + w) * 32768u + (u32)lane * 16u;
#pragma unroll
        for (int s = 0; s < 32; ++s) xf[s] = ldw(c.ws, xo + (u32)s * 1024u);
    }
    if (L == 1) {
        const u32 tl = L_XB + (u32)w * 4096u;
#pragma unroll
        for (int g = 0; g < 4; ++g) lds_st<u32x2>(tl + (u32)(r * 80 + (8 * g + 4 * h) * 2), (u32x2){x2[g >> 1][(g & 1) * 2], x2[g >> 1][(g & 1) * 2 + 1]});
        asm volatile("s_waitcnt lgkmcnt(0)" ::: "memory");
        u32x4 rowv[2];
#pragma unroll
        for (int q = 0; q < 2; ++q) {
            const int row = q * 16 + (lane >> 2);
            rowv[q] = lds_ld<u32x4>(tl + (u32)(row * 80 + (lane & 3) * 16));
            if (row != bt) *(u32x4*)(p.out + (size_t)(tile * 32 + row) * 512 + f0 + (lane & 3) * 8) = rowv[q];
        }
        wait_sel(1);
        const int sel1 = __builtin_amdgcn_readfirstlane((int)lds_ld<u32>(P_SEL + 32)) & 2047;
        if (tile * 32 + bt != sel1) {
#pragma unroll
            for (int q = 0; q < 2; ++q) {
                const int row = q * 16 + (lane >> 2);
                if (row == bt) *(u32x4*)(p.out + (size_t)(tile * 32 + row) * 512 + f0 + (lane & 3) * 8) = rowv[q];
            }
        }
    }
}

#if defined(__HIP_DEVICE_COMPILE__)
__attribute__((target("no-packed-fp32-ops")))
#endif
__global__ __launch_bounds__(512, 2) void fused_layers_kernel(Params p) {
    extern __shared__ __attribute__((aligned(16))) char lds[];
    Ctx c;
    const int tid = threadIdx.x;
    c.lane = tid & 63; c.w = __builtin_amdgcn_readfirstlane(tid >> 6);
    c.xg = blockIdx.x & 7; c.gi = blockIdx.x >> 3;
    c.ws = __builtin_amdgcn_make_buffer_rsrc(p.ws, 0, WS_BYTES, 0x00020000);
    c.tag = 0u;
    if (c.w == 6) {
        const u32 o = (u32)(((4 * c.xg + (c.gi >> 3)) * 8 + (c.gi & 7)) * 1024 + c.lane * 16);
        stw(c.ws, OFF_GS0 + o, (u32x4){0u, 0u, 0u, 0u}); stw(c.ws, OFF_GS1 + o, (u32x4){0u, 0u, 0u, 0u});
    }
    {
        u64 pc; u32 len;
        asm volatile("s_getpc_b64 %0\nfl_code_here:\n\ts_mov_b32 %1, fl_code_end-fl_code_here" : "=s"(pc), "=s"(len));
        if (c.w == 7) {
            const u32 o0 = min((u32)c.gi * 3072u + (u32)c.lane * 16u, len - 16u) & ~15u, o1 = min(o0 + 1024u, len - 16u) & ~15u, o2 = min(o0 + 2048u, len - 16u) & ~15u;
            const u64 base = pc & ~15ull;
            glds((const void*)base, o0, 147456u); glds((const void*)base, o1, 148480u); glds((const void*)base, o2, 149504u);
        }
    }
    if (c.w == 5) {
        const int l = c.lane;
        const char* a = p.ws + (size_t)(l < 10 ? l : 0) * 0x200000;
        a = l == 10 ? (const char*)p.wo : l == 11 ? (const char*)p.wr : l == 12 ? (const char*)p.wg : l == 13 ? (const char*)p.wu : l == 14 ? (const char*)p.wd : l == 15 ? (const char*)p.out : a;
        a = l == 16 ? (const char*)p.wg + 1835004 : l == 17 ? (const char*)p.wu + 1835004 : l == 18 ? (const char*)p.wd + 1835004 : l == 19 ? (const char*)p.out + 2097148 : a;
        u32 sv;
        asm volatile("s_mov_b32 %0, m0\n\ts_mov_b32 m0, %2\n\ts_nop 0\n\tglobal_load_lds_dword %1, off\n\ts_mov_b32 m0, %0" : "=&s"(sv) : "v"(a), "s"(150528u) : "memory");
    }
    if (tid < 24) lds_st<u32>(P_HCNT + (u32)tid * 4u, 0u);
    asm volatile("" :: "v"(lds));
    BAR();
    STAMP(0);
    FST(0);
    u32 hsync = 0;
    u32x4 x2[2] = {};
    u32x4 pq[12], xf[32];
    phaseQ<0>(c, p, hsync, pq, xf);
    if (STOP_AFTER >= 1) phaseA<0>(c, p);
    if (STOP_AFTER >= 2) phaseO<0>(c, p, x2, hsync, pq, xf);
    if (STOP_AFTER >= 3) phaseQ<1>(c, p, hsync, pq, xf);
    if (STOP_AFTER >= 4) phaseA<1>(c, p);
    if (STOP_AFTER >= 5) phaseO<1>(c, p, x2, hsync, pq, xf);
    STAMP(24);
    if (blockIdx.x == 14 * 8 && tid == 256) __builtin_amdgcn_raw_buffer_store_b32(c.tag, c.ws, OFF_EPOCH, 0, 16);
    asm volatile("fl_code_end:" ::: "memory");
}

extern "C" void kernel_launch(void* const* d_in, const int* in_sizes, int n_in, void* d_out, int out_size, void* d_ws, size_t ws_size, hipStream_t stream) {
    Params p;
    p.x0 = (const uint16_t*)d_in[0]; p.wqkv = (const uint16_t*)d_in[1]; p.wo = (const uint16_t*)d_in[2];
    p.wr = (const uint16_t*)d_in[4]; p.wg = (const uint16_t*)d_in[5]; p.wu = (const uint16_t*)d_in[6]; p.wd = (const uint16_t*)d_in[7];
    p.out = (uint16_t*)d_out; p.ws = (char*)d_ws;
    (void)hipFuncSetAttribute((const void*)fused_layers_kernel, hipFuncAttributeMaxDynamicSharedMemorySize, LDS_BYTES);
    fused_layers_kernel<<<256, 512, LDS_BYTES, stream>>>(p);
}
```

```cpp
#include <hip/hip_runtime.h>
#include <stdint.h>

typedef unsigned u32;
typedef unsigned long long u64;
typedef __attribute__((ext_vector_type(2))) u32 u32x2;
typedef __attribute__((ext_vector_type(4))) u32 u32x4;
typedef __attribute__((ext_vector_type(2))) float f32x2;
typedef __attribute__((ext_vector_type(4))) float f32x4;
typedef __attribute__((ext_vector_type(16))) float f32x16;
typedef __attribute__((ext_vector_type(8))) __bf16 bf16x8;
typedef __attribute__((ext_vector_type(2))) __bf16 bf16x2;
typedef __attribute__((ext_vector_type(4))) short s16x4;
typedef __attribute__((ext_vector_type(8))) short s16x8;
#define LAS __attribute__((address_space(3)))
#define DI __device__ __forceinline__
#define MFMA32(a, b, c) __builtin_amdgcn_mfma_f32_32x32x16_bf16((a), (b), (c), 0, 0, 0)
#define MFMA16(a, b, c) __builtin_amdgcn_mfma_f32_16x16x32_bf16((a), (b), (c), 0, 0, 0)
#define SBAR() __builtin_amdgcn_sched_barrier(0)
#define BAR() asm volatile("s_waitcnt lgkmcnt(0)\n\ts_barrier" ::: "memory")
#define DRAIN() asm volatile("s_waitcnt vmcnt(0)" ::: "memory")
#define MEMFENCE() asm volatile("" ::: "memory")

#define STOP_AFTER 99

constexpr u32 OFF_EPOCH = 0, OFF_ABORT = 256;
constexpr u32 OFF_FQ0 = 0x10000, OFF_FA0 = 0x18000, OFF_FO = 0x20000, OFF_FQ1 = 0x28000, OFF_FA1 = 0x30000;
constexpr u32 OFF_TS0 = 0x40000, OFF_TS1 = 0x48000;
constexpr u32 OFF_GC0 = 0x50000, OFF_GC1 = 0x51000;
constexpr u32 OFF_GM0 = 0x52000, OFF_GM1 = 0x52400;
constexpr u32 OFF_GA0 = 0x58000, OFF_GA1 = 0x5A000;
constexpr u32 OFF_GY = 0x5C000;
constexpr u32 OFF_GS0 = 0x80000, OFF_GS1 = 0xC0000;
constexpr u32 OFF_GX0 = 0x180000, OFF_GX1 = 0x1A0000;
constexpr u32 OFF_Q0 = 0x200000, OFF_K0 = 0x400000, OFF_V0 = 0x600000, OFF_O0 = 0x800000, OFF_X1 = 0xA00000;
constexpr u32 OFF_Q1 = 0xC00000, OFF_K1 = 0xE00000, OFF_V1 = 0x1000000, OFF_O1 = 0x1200000;
constexpr u32 WS_BYTES = 0x1400000;

constexpr u32 LDS_BYTES = 163840;
constexpr u32 L_W = 0;
constexpr u32 L_XR = 98304;
constexpr u32 L_AX = 140288;
constexpr u32 L_TOK = 161792 + 512;
constexpr u32 L_K = 0, L_V = 65536, VSTR = 1152;
constexpr u32 L_WO = 0, WO_FT = 65536;
constexpr u32 L_XB = 32768;
constexpr u32 L_SB = 161792;
constexpr u32 L_P = 163328;
constexpr u32 P_CS = L_P, P_HCNT = L_P + 384, P_PATCH = L_P + 388, P_WQ = L_P + 392, P_XGO = L_P + 396, P_SEL = L_P + 400, P_ST = L_P + 464;
constexpr u32 L_X1T = 49152;
constexpr u32 FW0 = 98304, FS0 = 155648;
constexpr u32 FW1 = FW0, FS1 = FS0;
constexpr u32 SPIN_CAP = 40000;

struct Params {
    const uint16_t* x0; const uint16_t* wqkv; const uint16_t* wo; const uint16_t* wr;
    const uint16_t* wg; const uint16_t* wu; const uint16_t* wd; uint16_t* out; char* ws;
};

typedef __amdgpu_buffer_rsrc_t rsrc_t;

DI u32 pk(float a, float b) { f32x2 v = {a, b}; return __builtin_bit_cast(u32, __builtin_convertvector(v, bf16x2)); }
DI float bflo(u32 v) { return __uint_as_float(v << 16); }
DI float bfhi(u32 v) { return __uint_as_float(v & 0xffff0000u); }
DI float rbf(float a) { return bflo(pk(a, 0.f)); }
DI u32x4 ldw(rsrc_t r, u32 off) { return __builtin_amdgcn_raw_buffer_load_b128(r, off, 0, 16); }
DI void stw(rsrc_t r, u32 off, u32x4 v) { __builtin_amdgcn_raw_buffer_store_b128(v, r, off, 0, 16); }
template <class T> DI T lds_ld(u32 a) { return *(const LAS T*)(uintptr_t)a; }
template <class T> DI void lds_st(u32 a, T v) { *(LAS T*)(uintptr_t)a = v; }
DI s16x4 lds_tr(u32 a) { return __builtin_amdgcn_ds_read_tr16_b64_v4i16((LAS s16x4*)(uintptr_t)a); }
DI bf16x8 frag_tr(u32 a0, u32 a1) { s16x4 lo = lds_tr(a0), hi = lds_tr(a1); return __builtin_bit_cast(bf16x8, __builtin_shufflevector(lo, hi, 0, 1, 2, 3, 4, 5, 6, 7)); }
DI float swap32_add(float v) { auto r = __builtin_amdgcn_permlane32_swap(__float_as_uint(v), __float_as_uint(v), false, false); return __uint_as_float(r[0]) + __uint_as_float(r[1]); }
template <int CTRL> DI u32 dpp(u32 v) { return (u32)__builtin_amdgcn_update_dpp((int)v, (int)v, CTRL, 0xf, 0xf, true); }
DI float wave_sum(float v) {
    v += __uint_as_float(dpp<0xB1>(__float_as_uint(v)));
    v += __uint_as_float(dpp<0x4E>(__float_as_uint(v)));
    v += __uint_as_float(dpp<0x141>(__float_as_uint(v)));
    v += __uint_as_float(dpp<0x140>(__float_as_uint(v)));
    { auto r = __builtin_amdgcn_permlane16_swap(__float_as_uint(v), __float_as_uint(v), false, false); v = __uint_as_float(r[0]) + __uint_as_float(r[1]); }
    return swap32_add(v);
}
DI u32 half_umax(u32 v) {
    v = max(v, dpp<0xB1>(v)); v = max(v, dpp<0x4E>(v)); v = max(v, dpp<0x141>(v)); v = max(v, dpp<0x140>(v));
    auto r = __builtin_amdgcn_permlane16_swap(v, v, false, false);
    return max(r[0], r[1]);
}
DI u32 wave_umax(u32 v) { v = half_umax(v); auto r = __builtin_amdgcn_permlane32_swap(v, v, false, false); return max(r[0], r[1]); }
DI u32 score_key(u32 bf, u32 idx) { return ((bf ^ ((bf & 0x8000u) ? 0xFFFFu : 0x8000u)) << 16) | (0xFFFFu - idx); }
DI u32 key_bf(u32 k) { const u32 b = k >> 16; return b ^ ((b & 0x8000u) ? 0x8000u : 0xFFFFu); }
DI u32 key_idx(u32 k) { return 0xFFFFu - (k & 0xFFFFu); }
DI void glds(const void* sbase, u32 voff, u32 ldsdst) {
    u32 sv;
    asm volatile("s_mov_b32 %0, m0\n\ts_mov_b32 m0, %3\n\ts_nop 0\n\tglobal_load_lds_dwordx4 %1, %2\n\ts_mov_b32 m0, %0"
                 : "=&s"(sv) : "v"(voff), "s"(sbase), "s"(ldsdst) : "memory");
}
struct Ctx {
    rsrc_t ws; u32 tag; int lane, w, xg, gi;
};
#define FRESH(c) asm volatile("" : "+v"((c).lane))
DI u32 epoch_tag(u32 v) { const u32 t = (u32)__builtin_amdgcn_readfirstlane((int)v) + 1u; return t ? t : 1u; }
DI bool spin_fail(const Ctx& c, u32& n) {
    __builtin_amdgcn_s_sleep(1);
    ++n;
    if (n > SPIN_CAP) { if (c.lane == 0) __builtin_amdgcn_raw_buffer_store_b32(c.tag, c.ws, OFF_ABORT, 0, 16); return true; }
    if ((n & 127u) == 0u) { u32 a = __builtin_amdgcn_raw_buffer_load_b32(c.ws, OFF_ABORT, 0, 16); if (__builtin_amdgcn_readfirstlane(a) == c.tag) return true; }
    return false;
}
DI void wait_flags(const Ctx& c, u32 off) {
    u32 n = 0;
    for (;;) {
        u32x2 v = __builtin_amdgcn_raw_buffer_load_b64(c.ws, off, 0, 16);
        MEMFENCE();
        if (__all(v[0] == c.tag && v[1] == ~c.tag)) break;
        if (spin_fail(c, n)) break;
    }
    MEMFENCE();
}
DI void set_flag(const Ctx& c, u32 off) { u32x2 v = {c.tag, ~c.tag}; __builtin_amdgcn_raw_buffer_store_b64(v, c.ws, off, 0, 16); }
DI bool unit_ok(u32x4 v, u32 tag) { return v[1] == tag && v[3] == tag; }
DI u32x4 mk_unit(u32 a, u32 b, u32 tag) { return (u32x4){a, tag, b, tag}; }
template <int N> DI void wait_units(const Ctx& c, const u32 (&off)[N], u32x4 (&v)[N]) {
    u32 n = 0;
    for (;;) {
        bool ok = true;
#pragma unroll
        for (int k = 0; k < N; ++k) v[k] = ldw(c.ws, off[k]);
        MEMFENCE();
#pragma unroll
        for (int k = 0; k < N; ++k) ok = ok && unit_ok(v[k], c.tag);
        if (__all(ok)) break;
        if (spin_fail(c, n)) break;
    }
    MEMFENCE();
}
DI u32 lds_poll_ld(u32 a) { return __hip_atomic_load((LAS u32*)(uintptr_t)a, __ATOMIC_RELAXED, __HIP_MEMORY_SCOPE_WORKGROUP); }
DI void helper_sync(const Ctx& c, u32& target) {
    target += 4;
    asm volatile("s_waitcnt lgkmcnt(0)" ::: "memory");
    if (c.lane == 0) __hip_atomic_fetch_add((LAS u32*)(uintptr_t)P_HCNT, 1u, __ATOMIC_RELAXED, __HIP_MEMORY_SCOPE_WORKGROUP);
    u32 n = 0;
    while (lds_poll_ld(P_HCNT) < target) { if (++n > (1u << 24)) break; }
    MEMFENCE();
}
DI void lds_wait_ge(u32 a, u32 v) {
    u32 n = 0;
    while (lds_poll_ld(a) < v) { __builtin_amdgcn_s_sleep(1); if (++n > (1u << 22)) break; }
    MEMFENCE();
}
DI void lds_signal_add(const Ctx& c, u32 a) {
    asm volatile("s_waitcnt lgkmcnt(0)" ::: "memory");
    if (c.lane == 0) __hip_atomic_fetch_add((LAS u32*)(uintptr_t)a, 1u, __ATOMIC_RELAXED, __HIP_MEMORY_SCOPE_WORKGROUP);
}
DI void wait_sel(int L) {
    u32 n = 0;
    while (lds_poll_ld(P_SEL + L * 32 + 16) == 0u) { __builtin_amdgcn_s_sleep(1); if (++n > (1u << 22)) break; }
    MEMFENCE();
}

#define STAMP(i) do { } while (0)
#define CSTAMP(i) do { } while (0)
#define FST(i) do { } while (0)

DI u64 cand_tag(u32 tag) { return ((u64)tag << 11) | (u64)((tag * 0x9E3779B1u) >> 21); }
template <int L>
DI void ffn_chain(const Ctx& c, const Params& p, u32& hsync) {
    const int lane = c.lane, hw = c.w - 4;
    constexpr u32 FW = FW0, FS = FS0;
    constexpr u32 GC = L ? OFF_GC1 : OFF_GC0, GM = L ? OFF_GM1 : OFF_GM0, GX = L ? OFF_GX1 : OFF_GX0, GA = L ? OFF_GA1 : OFF_GA0;
    FST(20 + 20 * L);
    __builtin_amdgcn_s_setprio(3);
    const bool is_gu = c.gi < 14, is_dn = c.gi >= 14 && c.gi < 18;
    const int g16 = lane >> 4, i16 = lane & 15;
    bf16x8 wf[14];
    {
        const u32 tro = FW + (u32)((8 * g16 + (i16 >> 2)) * 32 + (i16 & 3) * 8) + (u32)hw * (is_gu ? 4096u : 14336u);
#pragma unroll
        for (int s = 0; s < 14; ++s) {
            const u32 bo = tro + (is_gu ? (u32)(s & 3) * 1024u + (u32)((s >> 2) & 1) * 16384u : (u32)s * 1024u);
            if (is_gu || is_dn) wf[s] = frag_tr(bo, bo + 128);
        }
    }
    if (hw == 0) {
        const u64 want = cand_tag(c.tag);
        u64 v; u32 n = 0;
        for (;;) {
            const u32x2 t = __builtin_amdgcn_raw_buffer_load_b64(c.ws, GC + c.xg * 512u + lane * 8u, 0, 16);
            MEMFENCE();
            v = ((u64)t[1] << 32) | t[0];
            if (__all((v >> 21) == want)) break;
            if (spin_fail(c, n)) break;
        }
        FST(21 + 20 * L);
        const u32 key = wave_umax(score_key(((u32)v >> 5) & 0xFFFFu, (u32)lane * 32u + ((u32)v & 31u)));
        const u32 tok = key_idx(key); const float r = __uint_as_float(key_bf(key) << 16);
        if (lane == 0) {
            lds_st<u32x2>(P_SEL + L * 32, (u32x2){(u32)tok, __float_as_uint(r)});
            asm volatile("s_waitcnt lgkmcnt(0)" ::: "memory");
            __hip_atomic_store((LAS u32*)(uintptr_t)(P_SEL + L * 32 + 16), 1u, __ATOMIC_RELAXED, __HIP_MEMORY_SCOPE_WORKGROUP);
        }
    }
    FST(22 + 20 * L);
    STAMP(L ? 25 : 13);
    if (L == 0 && hw == 0 && !is_gu) lds_signal_add(c, P_XGO);
    if (!is_gu && !is_dn) return;
    wait_sel(L);
    const u32x2 si = lds_ld<u32x2>(P_SEL + L * 32);
    const int sel = __builtin_amdgcn_readfirstlane((int)si[0]) & 2047;
    const float rsel = __uint_as_float(si[1]);
    const u32 gxrow = GX + (u32)(sel >> 5) * 2048u, gm = GM + (u32)(sel >> 5) * 16u;
    if (is_gu) {
        const int gidx = c.xg * 14 + c.gi;
        u32 off[2] = {gxrow + (u32)(hw * 32 + (lane & 31)) * 16u, gm};
        u32x4 v[2];
        FST(29 + 20 * L);
        wait_units<2>(c, off, v);
        if (L == 0) lds_signal_add(c, P_XGO);
        FST(23 + 20 * L);
        const float mu = __uint_as_float(v[1][0]), rs = __uint_as_float(v[1][2]);
        if (lane < 32) {
            u32x2 h;
            h[0] = pk((bflo(v[0][0]) - mu) * rs, (bfhi(v[0][0]) - mu) * rs);
            h[1] = pk((bflo(v[0][2]) - mu) * rs, (bfhi(v[0][2]) - mu) * rs);
            lds_st<u32x2>(FS + (u32)(hw * 32 + lane) * 8u, h);
        }
        STAMP(L ? 26 : 14);
        FST(24 + 20 * L);
        f32x4 ag = {}, au = {};
#pragma unroll
        for (int s = 0; s < 4; ++s) {
            const bf16x8 a = lds_ld<bf16x8>(FS + (u32)((hw * 4 + s) * 64 + g16 * 16));
            ag = MFMA16(a, wf[s], ag);
            au = MFMA16(a, wf[4 + s], au);
        }
        if (lane < 16) { lds_st<float>(FS + 4608 + (u32)(hw * 32 + lane) * 4u, ag[0]); lds_st<float>(FS + 4608 + (u32)(hw * 32 + 16 + lane) * 4u, au[0]); }
        FST(25 + 20 * L);
        helper_sync(c, hsync);
        FST(26 + 20 * L);
        if (hw == 0) {
            float g = 0.f, u = 0.f;
#pragma unroll
            for (int k = 0; k < 4; ++k) { g += lds_ld<float>(FS + 4608 + (u32)(k * 32 + i16) * 4u); u += lds_ld<float>(FS + 4608 + (u32)(k * 32 + 16 + i16) * 4u); }
            g = rbf(g); u = rbf(u);
            const float sg = rbf(g * __builtin_amdgcn_rcpf(1.0f + __expf(-g)));
            const float a = rbf(sg * u);
            const float a0 = __shfl(a, (lane & 3) * 4), a1 = __shfl(a, (lane & 3) * 4 + 1), a2 = __shfl(a, (lane & 3) * 4 + 2), a3 = __shfl(a, (lane & 3) * 4 + 3);
            if (lane < 4) stw(c.ws, GA + (u32)(gidx * 4 + lane) * 16u, mk_unit(pk(a0, a1), pk(a2, a3), c.tag));
            FST(27 + 20 * L);
        }
    } else {
        const int didx = c.xg * 4 + (c.gi - 14);
        float mu, rs;
        {
            const int u0 = hw * 112 + lane, u1 = hw * 112 + 64 + (lane < 48 ? lane : 47);
            u32 off[4] = {gxrow + (u32)(didx * 4 + (lane & 3)) * 16u, gm, GA + (u32)u0 * 16u, GA + (u32)u1 * 16u};
            u32x4 v[4];
            wait_units<4>(c, off, v);
            FST(23 + 20 * L);
            mu = __uint_as_float(v[1][0]); rs = __uint_as_float(v[1][2]);
            lds_st<u32x2>(FS + 1024 + (u32)u0 * 8u, (u32x2){v[2][0], v[2][2]});
            if (lane < 48) lds_st<u32x2>(FS + 1024 + (u32)u1 * 8u, (u32x2){v[3][0], v[3][2]});
            if (hw == 0 && lane < 4) lds_st<u32x2>(FS + (u32)lane * 8u, (u32x2){v[0][0], v[0][2]});
        }
        STAMP(L ? 26 : 14);
        FST(24 + 20 * L);
        f32x4 ay = {}, az = {};
        bf16x8 af[14];
#pragma unroll
        for (int s = 0; s < 14; ++s) af[s] = lds_ld<bf16x8>(FS + 1024 + (u32)((hw * 14 + s) * 64 + g16 * 16));
#pragma unroll
        for (int s = 0; s < 14; s += 2) { ay = MFMA16(af[s], wf[s], ay); az = MFMA16(af[s + 1], wf[s + 1], az); }
        if (lane < 16) lds_st<float>(FS + 4608 + (u32)(hw * 16 + lane) * 4u, ay[0] + az[0]);
        FST(25 + 20 * L);
        helper_sync(c, hsync);
        FST(26 + 20 * L);
        if (hw == 0) {
            float y = 0.f;
#pragma unroll
            for (int k = 0; k < 4; ++k) y += lds_ld<float>(FS + 4608 + (u32)(k * 16 + i16) * 4u);
            const float x1 = __uint_as_float((u32)lds_ld<uint16_t>(FS + (u32)i16 * 2u) << 16);
            const float sig = rbf(__builtin_amdgcn_rcpf(1.0f + __expf(-rsel)));
            y = rbf(rbf(y) * sig);
            const float h = rbf((x1 - mu) * rs);
            const float x2 = rbf(x1 + rbf(h + y));
            const float a0 = __shfl(x2, (lane & 3) * 4), a1 = __shfl(x2, (lane & 3) * 4 + 1), a2 = __shfl(x2, (lane & 3) * 4 + 2), a3 = __shfl(x2, (lane & 3) * 4 + 3);
            if (L == 0) { if (lane < 4) stw(c.ws, OFF_GY + (u32)(didx * 4 + lane) * 16u, mk_unit(pk(a0, a1), pk(a2, a3), c.tag)); }
            else if (lane < 4) *(u32x2*)(p.out + (size_t)sel * 512 + didx * 16 + lane * 4) = (u32x2){pk(a0, a1), pk(a2, a3)};
            FST(27 + 20 * L);
        }
    }
}

DI void qkv_patch(const Ctx& c, u32& hsync) {
    const int lane = c.lane, hw = c.w - 4;
    constexpr u32 FS = FS0;
    const int g16 = lane >> 4, i16 = lane & 15;
    bf16x8 wf[24];
    {
        const u32 tro = L_W + (u32)((8 * g16 + (i16 >> 2)) * 64 + (i16 & 3) * 8) + (u32)hw * 8192u;
#pragma unroll
        for (int s_ = 0; s_ < 4; ++s_)
#pragma unroll
            for (int ct = 0; ct < 6; ++ct) {
                const u32 bo = tro + (u32)s_ * 2048u + (u32)(ct >> 1) * 32768u + (u32)(ct & 1) * 32u;
                wf[s_ * 6 + ct] = frag_tr(bo, bo + 256);
            }
    }
    u32 off[2] = {OFF_GY + (u32)lane * 32u, OFF_GY + (u32)lane * 32u + 16u}; u32x4 v[2];
    wait_units<2>(c, off, v);
    float x[8], s = 0, q = 0;
#pragma unroll
    for (int k = 0; k < 2; ++k) { x[4 * k] = bflo(v[k][0]); x[4 * k + 1] = bfhi(v[k][0]); x[4 * k + 2] = bflo(v[k][2]); x[4 * k + 3] = bfhi(v[k][2]); }
#pragma unroll
    for (int k = 0; k < 8; ++k) { s += x[k]; q += x[k] * x[k]; }
    s = wave_sum(s); q = wave_sum(q);
    const float mu = s * (1.0f / 512.0f), var = fmaxf(q * (1.0f / 512.0f) - mu * mu, 0.f);
    const float A = __builtin_amdgcn_rsqf(var + 1e-5f);
    lds_st<u32x4>(FS + (u32)lane * 16u, (u32x4){pk((x[0] - mu) * A, (x[1] - mu) * A), pk((x[2] - mu) * A, (x[3] - mu) * A), pk((x[4] - mu) * A, (x[5] - mu) * A), pk((x[6] - mu) * A, (x[7] - mu) * A)});
    f32x4 acc[6] = {};
#pragma unroll
    for (int s_ = 0; s_ < 4; ++s_) {
        const bf16x8 a = lds_ld<bf16x8>(FS + (u32)((hw * 4 + s_) * 64 + g16 * 16));
#pragma unroll
        for (int ct = 0; ct < 6; ++ct) acc[ct] = MFMA16(a, wf[s_ * 6 + ct], acc[ct]);
    }
    if (lane < 16) {
#pragma unroll
        for (int ct = 0; ct < 6; ++ct) lds_st<float>(FS + 1024 + (u32)(hw * 96 + ct * 16 + lane) * 4u, acc[ct][0]);
    }
    helper_sync(c, hsync);
    if (hw == 0) {
#pragma unroll
        for (int k = 0; k < 2; ++k) {
            const int n = k * 64 + lane;
            if (n < 96) {
                float t = 0.f;
#pragma unroll
                for (int j = 0; j < 4; ++j) t += lds_ld<float>(FS + 1024 + (u32)(j * 96 + n) * 4u);
                lds_st<float>(FS + 4608 + (u32)n * 4u, n < 32 ? t * (0.25f * 1.4426950408889634f) : t);
            }
        }
        asm volatile("s_waitcnt lgkmcnt(0)" ::: "memory");
        if (lane == 0) __hip_atomic_store((LAS u32*)(uintptr_t)P_PATCH, 1u, __ATOMIC_RELAXED, __HIP_MEMORY_SCOPE_WORKGROUP);
    }
}

DI u32x4 gld(const void* base, u32 off) { return *(const u32x4*)((const char*)base + off); }
struct FfSrc { const uint16_t* m; u32 vo, step; bool on, gu; };
DI FfSrc ff_src(const Ctx& c, const Params& p) {
    int lane = c.lane; asm volatile("" : "+v"(lane));
    FfSrc f; const int w = c.w;
    f.on = c.gi < 18; f.gu = c.gi < 14;
    f.m = f.gu ? (w < 4 ? p.wg : p.wu) : p.wd;
    const u32 rowb = f.gu ? 3584u : 1024u, col = f.gu ? (u32)(c.xg * 14 + c.gi) * 32u : (u32)(c.xg * 4 + (c.gi - 14)) * 32u;
    const u32 first = f.gu ? (u32)(w & 3) * 4u : (u32)w * 7u;
    f.step = 32u * rowb;
    f.vo = (u32)(lane >> 1) * rowb + col + (u32)(lane & 1) * 16u + first * f.step;
    return f;
}
DI void keep_wq(const Ctx& c, u32x4 (&pq)[12]) {
#pragma unroll
    for (int j = 0; j < 12; ++j) pq[j] = lds_ld<u32x4>(L_W + (u32)(c.w * 12 + j) * 1024u + (u32)c.lane * 16u);
}
DI void pre_store_wq(const Ctx& c, const u32x4 (&pq)[12]) {
#pragma unroll
    for (int j = 0; j < 12; ++j) lds_st<u32x4>(L_W + (u32)(c.w * 12 + j) * 1024u + (u32)c.lane * 16u, pq[j]);
}
DI void pre_store_ff(const Ctx& c, const u32x4 (&pf)[7]) {
    if (c.gi < 18) {
        const bool gu = c.gi < 14;
        const u32 a = FW0 + (u32)c.w * (gu ? 4096u : 7168u) + (u32)c.lane * 16u;
#pragma unroll
        for (int j = 0; j < 7; ++j) if (j < 4 || !gu) lds_st<u32x4>(a + (u32)j * 1024u, pf[j]);
    }
}
DI void wo_dma(const Ctx& c, const Params& p, u32 vo, int j) {
    const int n = c.w * 8 + j, ft = n >> 5, g = n & 31;
    glds(p.wo, vo + (u32)ft * 64u + (u32)g * 16384u, L_WO + (u32)ft * WO_FT + (u32)g * 1024u);
}
DI u32 wo_lane_off(const Ctx& c) {
    int lane = c.lane; asm volatile("" : "+v"(lane));
    return (u32)((lane >> 2) * 1024 + (c.gi & 7) * 128 + (lane & 3) * 16);
}

struct WFrag { bf16x8 q, k, v; };
template <u32 TR2> DI void wfrag_load(WFrag& f, u32 a01, u32 a2) { f.q = frag_tr(a01, a01 + TR2); f.k = frag_tr(a01 + 32768, a01 + 32768 + TR2); f.v = frag_tr(a2, a2 + TR2); }
template <u32 TR2> DI void qstep(f32x16& aq, f32x16& ak, f32x16& av, bf16x8 x, WFrag& cur, WFrag& nxt, u32 a01, u32 a2, bool more) {
    if (more) nxt.q = frag_tr(a01, a01 + TR2);
    SBAR(); aq = MFMA32(cur.q, x, aq); SBAR();
    if (more) nxt.k = frag_tr(a01 + 32768, a01 + 32768 + TR2);
    SBAR(); ak = MFMA32(cur.k, x, ak); SBAR();
    if (more) nxt.v = frag_tr(a2, a2 + TR2);
    SBAR(); av = MFMA32(x, cur.v, av); SBAR();
}

template <int L>
DI void phaseQ(Ctx& c0, const Params& p, u32& hsync, u32x4 (&pq)[12], const u32x4 (&xf)[32]) {
    Ctx c = c0; FRESH(c);
    const int lane = c.lane, w = c.w;
    const int rb = 4 * (c.xg & 3) + (c.gi & 3), hp = 8 * (c.xg >> 2) + (c.gi >> 2);
    const int r = lane & 31, h = lane >> 5;
    const int i16 = lane & 15, blk = (lane >> 4) & 1;
    const u32 trA = L_W + (u32)(((L == 0 ? 8 : 4) * h + (i16 >> 2)) * 64 + blk * 32 + (i16 & 3) * 8);
    constexpr u32 TR2 = L == 0 ? 256 : 512;
    constexpr u32 OQ = L ? OFF_Q1 : OFF_Q0, OK_ = L ? OFF_K1 : OFF_K0, OV = L ? OFF_V1 : OFF_V0, TS = L ? OFF_TS1 : OFF_TS0;
    const int rot = ((c.gi >> 2) + 2 * (c.gi & 3)) & 7;

    if (w >= 4) {
        const int hw = w - 4;
        if (L == 0) {
            u32x4 r0, r1; u32 ep;
            asm volatile("global_load_dword %0, %1, %2 sc1" : "=v"(ep) : "v"(0u), "s"(p.ws));
            const u32 tok = (u32)(rb * 128 + hp * 8 + hw * 2);
            const u32 vo = tok * 1024u + (u32)lane * 16u;
            asm volatile("global_load_dwordx4 %0, %1, %2" : "=v"(r0) : "v"(vo), "s"(p.x0));
            asm volatile("global_load_dwordx4 %0, %1, %2 offset:1024" : "=v"(r1) : "v"(vo), "s"(p.x0));
            const u32 wv = (u32)((lane >> 2) * 3072 + hp * 64 + (lane & 3) * 16 + hw * 16 * 3072);
            u32 xv[4];
#pragma unroll
            for (int q = 0; q < 4; ++q) { const int row = 8 * q + (lane >> 3); xv[q] = (u32)(((rb * 4 + hw) * 32 + row) * 1024 + (((lane & 7) ^ ((row >> 1) & 7)) * 16)); }
#define WD(i) do { const u32 kc_ = (u32)(((i) + rot) & 7); _Pragma("unroll") for (int t = 0; t < 3; ++t) \
                glds(p.wqkv, wv + (u32)t * 1024u + kc_ * (64u * 3072u), L_W + (u32)t * 32768u + (kc_ * 64u + (u32)hw * 16u) * 64u); } while (0)
#define XD(i) do { const u32 kc_ = (u32)(((i) + rot) & 7); _Pragma("unroll") for (int q = 0; q < 4; ++q) \
                glds(p.x0, xv[q] + kc_ * 128u, L_XR + (u32)((i) % 3) * 16384u + (u32)hw * 4096u + (u32)q * 1024u); } while (0)
#define QWAIT(n) asm volatile("s_waitcnt vmcnt(" #n ")\n\ts_barrier" ::: "memory")
#define CS(i) if (hw < 3) { _Pragma("unroll") for (int s_ = 0; s_ < 4; ++s_) { \
                const u32 a_ = trA + (u32)hw * 32768u + (u32)((((i) + rot) & 7) * 4 + s_) * 1024u; cs = MFMA32(frag_tr(a_, a_ + TR2), ones, cs); } }
            WD(0); XD(0); WD(1); XD(1); WD(2); XD(2); WD(3);
            FST(1);
            f32x16 cs = {};
            const bf16x8 ones = __builtin_bit_cast(bf16x8, (u32x4){0x3f803f80u, 0x3f803f80u, 0x3f803f80u, 0x3f803f80u});
            asm volatile("s_waitcnt vmcnt(14)\n\ts_barrier" : "+v"(r0), "+v"(r1), "+v"(ep) :: "memory");
            c.tag = epoch_tag(ep); c0.tag = c.tag;
            FST(2);
            {
                float s0 = 0, q0 = 0, s1 = 0, q1 = 0;
#pragma unroll
                for (int j = 0; j < 4; ++j) {
                    float a = bflo(r0[j]), b_ = bfhi(r0[j]); s0 += a + b_; q0 += a * a + b_ * b_;
                    float d = bflo(r1[j]), e = bfhi(r1[j]); s1 += d + e; q1 += d * d + e * e;
                }
                s0 = wave_sum(s0); q0 = wave_sum(q0); s1 = wave_sum(s1); q1 = wave_sum(q1);
                const float s = lane ? s1 : s0, q = lane ? q1 : q0;
                const float mu = s * (1.0f / 512.0f), var = fmaxf(q * (1.0f / 512.0f) - mu * mu, 0.f);
                const float A = __builtin_amdgcn_rsqf(var + 1e-5f);
                if (lane < 2) stw(c.ws, TS + (tok + (u32)lane) * 16u, mk_unit(__float_as_uint(A), __float_as_uint(A * mu), c.tag));
            }
            CS(0);
            QWAIT(8); FST(3); XD(3); WD(4); CS(1);
            QWAIT(8); FST(4); XD(4); WD(5); CS(2);
            QWAIT(7); FST(5); XD(5); WD(6); CS(3);
            QWAIT(7); FST(6); XD(6); WD(7); CS(4);
            QWAIT(7); FST(7); XD(7); CS(5);
            QWAIT(4); FST(8); CS(6);
            QWAIT(0); FST(9); CS(7);
#undef WD
#undef XD
#undef QWAIT
#undef CS
            if (hw < 3 && r == 0) {
#pragma unroll
                for (int g = 0; g < 4; ++g) lds_st<f32x4>(P_CS + (u32)(hw * 32 + 8 * g + 4 * h) * 4u, (f32x4){cs[4 * g], cs[4 * g + 1], cs[4 * g + 2], cs[4 * g + 3]});
            }
            STAMP(1);
            BAR();
        } else {
            ffn_chain<0>(c, p, hsync);
            STAMP(15);
            wait_sel(0);
            const int sel = __builtin_amdgcn_readfirstlane((int)lds_ld<u32>(P_SEL)) & 2047;
            if ((sel >> 7) == rb) { lds_wait_ge(P_WQ, 8u); qkv_patch(c, hsync); }
        }
        DRAIN(); BAR();
        STAMP(L ? 16 : 3);
        if (L == 0) keep_wq(c, pq);
    } else {
        const int T = rb * 4 + w;
        f32x16 aq = {}, ak = {}, av = {};
        float A, B;
        if (L == 0) {
            const u32 ep = __builtin_amdgcn_raw_buffer_load_b32(c.ws, OFF_EPOCH, 0, 16);
            u32 xa[4];
#pragma unroll
            for (int s_ = 0; s_ < 4; ++s_) xa[s_] = L_XR + (u32)w * 4096u + (u32)(r * 128 + (((2 * s_ + h) ^ ((r >> 1) & 7)) * 16));
            bf16x8 xr[2][4];
            WFrag f0, f1;
            asm volatile("s_barrier" ::: "memory");
            FST(2);
#pragma unroll
            for (int s_ = 0; s_ < 4; ++s_) xr[0][s_] = lds_ld<bf16x8>(xa[s_]);
            { const u32 a01 = trA + (u32)(rot & 7) * 4096u; wfrag_load<TR2>(f0, a01, a01 + 65536u); }
#pragma unroll
            for (int i = 0; i < 8; ++i) {
                const u32 a01 = trA + (u32)((i + rot) & 7) * 4096u, a2 = a01 + 65536u;
                const u32 n01 = trA + (u32)((i + 1 + rot) & 7) * 4096u, n2 = n01 + 65536u;
                qstep<TR2>(aq, ak, av, xr[i & 1][0], f0, f1, a01 + 1024, a2 + 1024, true);
                qstep<TR2>(aq, ak, av, xr[i & 1][1], f1, f0, a01 + 2048, a2 + 2048, true);
                qstep<TR2>(aq, ak, av, xr[i & 1][2], f0, f1, a01 + 3072, a2 + 3072, true);
                if (i < 7) {
                    asm volatile("s_barrier" ::: "memory");
                    FST(3 + i);
#pragma unroll
                    for (int s_ = 0; s_ < 4; ++s_) xr[(i + 1) & 1][s_] = lds_ld<bf16x8>(xa[s_] + (u32)((i + 1) % 3) * 16384u);
                }
                qstep<TR2>(aq, ak, av, xr[i & 1][3], f1, f0, n01, n2, i < 7);
            }
            FST(10);
            STAMP(1);
            c.tag = epoch_tag(ep); c0.tag = c.tag;
            u32 off[1] = {TS + (u32)(T * 32 + r) * 16u}; u32x4 v[1];
            wait_units<1>(c, off, v);
            A = __uint_as_float(v[0][0]); B = __uint_as_float(v[0][2]);
            BAR();
            STAMP(2);
        } else {
            lds_wait_ge(P_WQ, 8u);
            STAMP(14);
            {
                u32 a01 = trA, a2 = trA + 65536u;
                asm volatile("" : "+v"(a01), "+v"(a2));
                WFrag f0, f1;
                wfrag_load<TR2>(f0, a01, a2);
#pragma unroll
                for (int st = 0; st < 32; st += 2) {
                    qstep<TR2>(aq, ak, av, __builtin_bit_cast(bf16x8, xf[st]), f0, f1, a01 + (u32)(st + 1) * 1024u, a2 + (u32)(st + 1) * 1024u, true);
                    qstep<TR2>(aq, ak, av, __builtin_bit_cast(bf16x8, xf[st + 1]), f1, f0, a01 + (u32)(st + 2) * 1024u, a2 + (u32)(st + 2) * 1024u, st + 2 < 32);
                }
            }
            u32 off[1] = {TS + (u32)(T * 32 + r) * 16u}; u32x4 v[1];
            wait_units<1>(c, off, v);
            A = __uint_as_float(v[0][0]); B = __uint_as_float(v[0][2]);
        }
        if (L == 1) STAMP(15);
        const u32 tokl = L_TOK + (u32)w * 256u;
        if (h == 0) lds_st<f32x2>(tokl + (u32)r * 8u, (f32x2){A, B});
        constexpr float QS = 0.25f * 1.4426950408889634f;
#pragma unroll
        for (int t = 0; t < 2; ++t) {
            const f32x16& acc = t ? ak : aq;
            const float sA = t ? A : A * QS, sB = t ? B : B * QS;
            float o[16];
#pragma unroll
            for (int g = 0; g < 4; ++g) {
                const f32x4 cs = lds_ld<f32x4>(P_CS + (u32)(t * 32 + 8 * g + 4 * h) * 4u);
#pragma unroll
                for (int e = 0; e < 4; ++e) o[4 * g + e] = sA * acc[4 * g + e] - sB * cs[e];
            }
#pragma unroll
            for (int a = 0; a < 2; ++a) {
                u32x4 v = {pk(o[8 * a], o[8 * a + 1]), pk(o[8 * a + 2], o[8 * a + 3]), pk(o[8 * a + 4], o[8 * a + 5]), pk(o[8 * a + 6], o[8 * a + 7])};
                stw(c.ws, (t ? OK_ : OQ) + (u32)(((hp * 2 + a) * 64 + T) * 64 + lane) * 16u, v);
            }
        }
        {
            asm volatile("s_waitcnt lgkmcnt(0)" ::: "memory");
            const float cs = lds_ld<float>(P_CS + (u32)(64 + r) * 4u);
            float o[16];
#pragma unroll
            for (int g = 0; g < 4; ++g) {
                const f32x4 ab0 = lds_ld<f32x4>(tokl + (u32)(8 * g + 4 * h) * 8u), ab1 = lds_ld<f32x4>(tokl + (u32)(8 * g + 4 * h + 2) * 8u);
                o[4 * g] = ab0[0] * av[4 * g] - ab0[1] * cs; o[4 * g + 1] = ab0[2] * av[4 * g + 1] - ab0[3] * cs;
                o[4 * g + 2] = ab1[0] * av[4 * g + 2] - ab1[1] * cs; o[4 * g + 3] = ab1[2] * av[4 * g + 3] - ab1[3] * cs;
            }
            const int head = hp * 2 + (r >> 4), d = r & 15;
#pragma unroll
            for (int s = 0; s < 2; ++s) {
                u32x4 v = {pk(o[8 * s], o[8 * s + 1]), pk(o[8 * s + 2], o[8 * s + 3]), pk(o[8 * s + 4], o[8 * s + 5]), pk(o[8 * s + 6], o[8 * s + 7])};
                stw(c.ws, OV + (u32)((head * 64 + T) * 1024 + s * 512 + h * 256 + d * 16), v);
            }
        }
        if (L == 1) {
            wait_sel(0);
            const int sel = __builtin_amdgcn_readfirstlane((int)lds_ld<u32>(P_SEL)) & 2047;
            if ((sel >> 5) == T) {
                const int ts = sel & 31;
                DRAIN();
                lds_wait_ge(P_PATCH, 1u);
                if (r == ts) {
#pragma unroll
                    for (int t = 0; t < 2; ++t)
#pragma unroll
                        for (int a = 0; a < 2; ++a) {
                            const f32x4 p0 = lds_ld<f32x4>(FS0 + 4608 + (u32)(t * 32 + 16 * a + 4 * h) * 4u), p1 = lds_ld<f32x4>(FS0 + 4608 + (u32)(t * 32 + 16 * a + 8 + 4 * h) * 4u);
                            stw(c.ws, (t ? OK_ : OQ) + (u32)(((hp * 2 + a) * 64 + T) * 64 + lane) * 16u, (u32x4){pk(p0[0], p0[1]), pk(p0[2], p0[3]), pk(p1[0], p1[1]), pk(p1[2], p1[3])});
                        }
                }
                if (h == 0) {
                    const float pv = lds_ld<float>(FS0 + 4608 + (u32)(64 + r) * 4u);
                    const u32 o = OV + (u32)(((hp * 2 + (r >> 4)) * 64 + T) * 1024 + (ts >> 4) * 512 + ((ts >> 2) & 1) * 256 + (r & 15) * 16 + ((ts & 3) + 4 * ((ts >> 3) & 1)) * 2);
                    __builtin_amdgcn_raw_buffer_store_b16((uint16_t)(pk(pv, 0.f) & 0xFFFFu), c.ws, o, 0, 16);
                }
            }
        }
        DRAIN(); BAR();
        STAMP(L ? 16 : 3);
        if (w == 0 && lane == 0) set_flag(c, (L ? OFF_FQ1 : OFF_FQ0) + (u32)(hp * 16 + rb) * 128u);
        if (L == 0) keep_wq(c, pq);
    }
}

struct AState { f32x16 Sa, Sb, Oa, Ob; bf16x8 kC, kN, v0, v1; u32x4 pb1; };
#define EXP2(x) __builtin_amdgcn_exp2f(x)
#define BC8(x) __builtin_bit_cast(bf16x8, x)
#define PVM(O, v, p) O = MFMA32(v, BC8(p), O)
template <bool USE_C, class HOOK>
DI void attn_step(AState& s, bf16x8 qa, bf16x8 qb, const f32x16& ca, const f32x16& cb, u32 kp, u32 vp0, u32 vp1, u32 knext, u32 voff, HOOK hook) {
    const f32x16 zero = {};
    u32x4 pa0, pa1, pb0;
    s.kN = lds_ld<bf16x8>(kp + knext); SBAR();
    s.v0 = lds_ld<bf16x8>(vp0 + voff); SBAR();
    PVM(s.Ob, s.v1, s.pb1); SBAR();
    s.v1 = lds_ld<bf16x8>(vp1 + voff); SBAR();
    float a0 = EXP2(s.Sa[0]); SBAR(); float a1 = EXP2(s.Sa[1]); SBAR(); float a2 = EXP2(s.Sa[2]); SBAR(); float a3 = EXP2(s.Sa[3]); SBAR();
    s.Sb = MFMA32(s.kC, qb, USE_C ? cb : zero); SBAR();
    float a4 = EXP2(s.Sa[4]); SBAR(); float a5 = EXP2(s.Sa[5]); SBAR(); float a6 = EXP2(s.Sa[6]); SBAR(); float a7 = EXP2(s.Sa[7]); SBAR();
    pa0[0] = pk(a0, a1); SBAR(); pa0[1] = pk(a2, a3); SBAR(); float a8 = EXP2(s.Sa[8]); SBAR(); pa0[2] = pk(a4, a5); SBAR(); pa0[3] = pk(a6, a7); SBAR();
    float a9 = EXP2(s.Sa[9]); SBAR();
    asm volatile("" : "+v"(s.v0), "+v"(s.v1), "+v"(s.kN)); SBAR();
    float a10 = EXP2(s.Sa[10]); SBAR();
    PVM(s.Oa, s.v0, pa0); SBAR();
    float a11 = EXP2(s.Sa[11]); SBAR(); float a12 = EXP2(s.Sa[12]); SBAR(); float a13 = EXP2(s.Sa[13]); SBAR(); float a14 = EXP2(s.Sa[14]); SBAR(); float a15 = EXP2(s.Sa[15]); SBAR();
    pa1[0] = pk(a8, a9); SBAR(); pa1[1] = pk(a10, a11); SBAR(); pa1[2] = pk(a12, a13); SBAR(); pa1[3] = pk(a14, a15); SBAR();
    float b0 = EXP2(s.Sb[0]); SBAR(); float b1 = EXP2(s.Sb[1]); SBAR();
    PVM(s.Oa, s.v1, pa1); SBAR();
    hook(); SBAR();
    float b2 = EXP2(s.Sb[2]); SBAR(); float b3 = EXP2(s.Sb[3]); SBAR(); float b4 = EXP2(s.Sb[4]); SBAR(); float b5 = EXP2(s.Sb[5]); SBAR();
    s.Sa = MFMA32(s.kN, qa, USE_C ? ca : zero); SBAR();
    float b6 = EXP2(s.Sb[6]); SBAR(); float b7 = EXP2(s.Sb[7]); SBAR();
    pb0[0] = pk(b0, b1); SBAR(); pb0[1] = pk(b2, b3); SBAR(); float b8 = EXP2(s.Sb[8]); SBAR(); pb0[2] = pk(b4, b5); SBAR(); pb0[3] = pk(b6, b7); SBAR();
    float b9 = EXP2(s.Sb[9]); SBAR(); float b10 = EXP2(s.Sb[10]); SBAR();
    PVM(s.Ob, s.v0, pb0); SBAR();
    float b11 = EXP2(s.Sb[11]); SBAR(); float b12 = EXP2(s.Sb[12]); SBAR(); float b13 = EXP2(s.Sb[13]); SBAR(); float b14 = EXP2(s.Sb[14]); SBAR(); float b15 = EXP2(s.Sb[15]); SBAR();
    s.pb1[0] = pk(b8, b9); SBAR(); s.pb1[1] = pk(b10, b11); SBAR(); s.pb1[2] = pk(b12, b13); SBAR(); s.pb1[3] = pk(b14, b15); SBAR();
    s.kC = s.kN;
}
struct NoHook { DI void operator()() const {} };
template <bool USE_C>
DI void attn_chunk(AState& s, bf16x8 qa, bf16x8 qb, const f32x16& ca, const f32x16& cb, u32 kp, u32 vp0, u32 vp1) {
    attn_step<USE_C>(s, qa, qb, ca, cb, kp, vp0, vp1, 1024, 0, NoHook());
    attn_step<USE_C>(s, qa, qb, ca, cb, kp, vp0, vp1, 2048, VSTR, NoHook());
    attn_step<USE_C>(s, qa, qb, ca, cb, kp, vp0, vp1, 3072, 2 * VSTR, NoHook());
    attn_step<USE_C>(s, qa, qb, ca, cb, kp, vp0, vp1, 8192, 3 * VSTR, NoHook());
}
DI void attn_begin(AState& s, bf16x8 qa, const f32x16& ca, u32 kp) {
    const f32x16 zero = {};
    s.Oa = zero; s.Ob = zero; s.pb1 = (u32x4){0u, 0u, 0u, 0u}; s.v1 = BC8(s.pb1);
    s.kC = lds_ld<bf16x8>(kp);
    s.Sa = MFMA32(s.kC, qa, ca);
}
DI void attn_combine(const Ctx& c, AState& s, float (&O)[9]) {
    const int kh = c.w >> 2;
    s.Ob = MFMA32(s.v1, BC8(s.pb1), s.Ob);
    const f32x16& snd = kh ? s.Oa : s.Ob; const f32x16& keep = kh ? s.Ob : s.Oa;
    const u32 xb = L_AX + (u32)c.w * 2304u;
    lds_st<f32x4>(xb + (u32)c.lane * 16u, (f32x4){snd[0], snd[1], snd[2], snd[3]});
    lds_st<f32x4>(xb + 1024u + (u32)c.lane * 16u, (f32x4){snd[4], snd[5], snd[6], snd[7]});
    lds_st<float>(xb + 2048u + (u32)c.lane * 4u, snd[8]);
    BAR();
    const u32 xp = L_AX + (u32)(c.w ^ 4) * 2304u;
    const f32x4 u0 = lds_ld<f32x4>(xp + (u32)c.lane * 16u), u1 = lds_ld<f32x4>(xp + 1024u + (u32)c.lane * 16u);
    const float l2 = lds_ld<float>(xp + 2048u + (u32)c.lane * 4u);
#pragma unroll
    for (int q = 0; q < 4; ++q) { O[q] = keep[q] + u0[q]; O[4 + q] = keep[4 + q] + u1[q]; }
    O[8] = keep[8] + l2;
}

template <int L>
DI void phaseA(const Ctx& c0, const Params& p) {
    Ctx c = c0; FRESH(c);
    u32x4 pf[7] = {};
    const int lane = c.lane, w = c.w, tid = w * 64 + lane;
    const int head = 4 * c.xg + (c.gi >> 3), qb = c.gi & 7;
    const int j = w & 3, kh = w >> 2;
    constexpr u32 OQ = L ? OFF_Q1 : OFF_Q0, OK_ = L ? OFF_K1 : OFF_K0, OV = L ? OFF_V1 : OFF_V0, OO = L ? OFF_O1 : OFF_O0;
    if (tid == 0) lds_st<u32>(L_AX + 8 * 2304u, 0u);
    if (w == 0) wait_flags(c, (L ? OFF_FQ1 : OFF_FQ0) + (u32)((head >> 1) * 16 + (lane & 15)) * 128u);
    BAR();
    STAMP(L ? 17 : 4);
    CSTAMP(9);
    const u32 koff = OK_ + (u32)((head * 64 + w) * 64 + lane) * 16u, voff = OV + (u32)((head * 64 + w) * 64 + lane) * 16u;
    u32x4 rk[8], rv[8];
    rk[0] = ldw(c.ws, koff); rv[0] = ldw(c.ws, voff);
    const u32 qo = OQ + (u32)((head * 64 + qb * 8 + 2 * j) * 64 + lane) * 16u;
    const u32x4 qar = ldw(c.ws, qo), qbr = ldw(c.ws, qo + 1024u);
    SBAR(); __builtin_amdgcn_s_barrier(); SBAR();
    rk[1] = ldw(c.ws, koff + 8192u); rv[1] = ldw(c.ws, voff + 8192u); rk[2] = ldw(c.ws, koff + 16384u); rv[2] = ldw(c.ws, voff + 16384u);
    SBAR(); __builtin_amdgcn_s_barrier(); SBAR();
    rk[3] = ldw(c.ws, koff + 24576u); rv[3] = ldw(c.ws, voff + 24576u); rk[4] = ldw(c.ws, koff + 32768u); rv[4] = ldw(c.ws, voff + 32768u);
    SBAR();
    { const int t = tid >> 3, s = tid & 7; const u32 val = (s & 3) == 0 ? 0x3f803f80u : 0u;
      lds_st<u32x4>(L_V + (u32)t * VSTR + 1024u + (u32)s * 16u, (u32x4){val, val, val, val}); }
    const u32 kw = L_K + (u32)w * 1024u + (u32)lane * 16u, vw = L_V + (u32)w * VSTR + (u32)lane * 16u;
    lds_st<u32x4>(kw, rk[0]); lds_st<u32x4>(vw, rv[0]);
    lds_st<u32x4>(kw + 8192u, rk[1]); lds_st<u32x4>(vw + 8u * VSTR, rv[1]);
    CSTAMP(10);
    BAR();
    const int r = lane & 31, h = lane >> 5;
    const u32 kp0 = L_K + (u32)kh * 4096u + (u32)lane * 16u;
    const u32 onesoff = 1024u + ((r >= 20 && r < 28) ? 64u : 0u) + ((r == 16 || r == 20) ? 0u : 16u);
    const u32 vp00 = L_V + (u32)kh * (4u * VSTR) + (r < 16 ? (u32)(h * 256 + r * 16) : onesoff);
    const u32 vp10 = L_V + (u32)kh * (4u * VSTR) + (r < 16 ? (u32)(512 + h * 256 + r * 16) : onesoff);
    const bf16x8 qa = BC8(qar), qbf = BC8(qbr);
    const f32x16 cz = {};
    AState s;
    STAMP(L ? 18 : 5);
#define PRIO(n) __builtin_amdgcn_s_setprio(n)
#define BAR2() BAR()
    PRIO(3);
    CSTAMP(0);
    attn_begin(s, qa, cz, kp0);
    {
#define PUT(ch) [&]() { lds_st<u32x4>(kw + (u32)(ch) * 8192u, rk[ch]); lds_st<u32x4>(vw + (u32)(ch) * (8u * VSTR), rv[ch]); }
#define GET(ch) [&]() { rk[ch] = ldw(c.ws, koff + (u32)(ch) * 8192u); rv[ch] = ldw(c.ws, voff + (u32)(ch) * 8192u); }
#define GETPUT(g, ch) [&]() { GET(g)(); PUT(ch)(); }
#define FF(j_) [&]() { if (ff.on && ((j_) < 4 || !ff.gu)) pf[j_] = gld(ff.m, ff.vo + (u32)(j_) * ff.step); }
#define WO(j_) [&]() { wo_dma(c, p, wv, j_); }
#define ST(ch, t, HOOK) attn_step<false>(s, qa, qbf, cz, cz, kp, vp0, vp1, ((t) == 3 ? 8192 : ((t) + 1) * 1024) + ((ch) & 1) * 8192, (((ch) & 1) * 8 + (t)) * VSTR, HOOK)
#define NEXT2() do { kp += 16384; vp0 += 16 * VSTR; vp1 += 16 * VSTR; asm volatile("" : "+v"(kp), "+v"(vp0), "+v"(vp1)); } while (0)
        u32 kp = kp0, vp0 = vp00, vp1 = vp10;
        ST(0, 0, GET(5)); ST(0, 1, GETPUT(6, 2)); ST(0, 2, GETPUT(7, 3)); ST(0, 3, PUT(4));
        CSTAMP(1);
        ST(1, 0, PUT(5)); ST(1, 1, PUT(6)); ST(1, 2, PUT(7));
        BAR();
        ST(1, 3, NoHook());
        const FfSrc ff = ff_src(c, p);
        NEXT2(); PRIO(2);
        CSTAMP(2);
        ST(2, 0, FF(0)); ST(2, 1, FF(1)); ST(2, 2, FF(2)); ST(2, 3, FF(3));
        CSTAMP(3);
        ST(3, 0, FF(4)); ST(3, 1, FF(5)); ST(3, 2, FF(6)); ST(3, 3, NoHook());
        NEXT2(); PRIO(1);
        CSTAMP(4);
        ST(4, 0, NoHook()); ST(4, 1, NoHook()); ST(4, 2, NoHook()); ST(4, 3, NoHook());
        CSTAMP(5);
        ST(5, 0, NoHook()); ST(5, 1, NoHook()); ST(5, 2, NoHook()); ST(5, 3, NoHook());
        const u32 wv = wo_lane_off(c);
        NEXT2(); PRIO(0);
        BAR2();
        CSTAMP(6);
        ST(6, 0, WO(0)); ST(6, 1, WO(1)); ST(6, 2, WO(2)); ST(6, 3, WO(3));
        CSTAMP(7);
        ST(7, 0, WO(4)); ST(7, 1, WO(5)); ST(7, 2, WO(6)); ST(7, 3, WO(7));
#undef PUT
#undef GET
#undef GETPUT
#undef FF
#undef WO
#undef ST
#undef NEXT2
    }
    CSTAMP(8);
    STAMP(L ? 19 : 6);
    FRESH(c);
    float O[9];
    attn_combine(c, s, O);
    const u32 oo = OO + (u32)((head * 64 + qb * 8 + 2 * j + kh) * 64 + c.lane) * 16u;
    {
        bool bad = !(O[8] >= 0x1p-60f && O[8] <= 0x1p60f);
#pragma unroll
        for (int q = 0; q < 8; ++q) bad = bad || !(__builtin_fabsf(O[q]) <= 0x1p126f);
        if (__any(bad) && c.lane == 0) lds_st<u32>(L_AX + 8 * 2304u, 1u);
        const float rl = __builtin_amdgcn_rcpf(O[8]);
        u32x4 o;
#pragma unroll
        for (int q = 0; q < 4; ++q) o[q] = pk(O[2 * q] * rl, O[2 * q + 1] * rl);
        stw(c.ws, oo, o);
    }
    DRAIN(); BAR();
    if (__builtin_amdgcn_readfirstlane((int)lds_ld<u32>(L_AX + 8 * 2304u)) != 0) {
        {
            const u32 ko = OK_ + (u32)((head * 64 + w) * 64 + c.lane) * 16u, vo = OV + (u32)((head * 64 + w) * 64 + c.lane) * 16u;
            for (int ch = 0; ch < 4; ++ch) {
                const u32x4 a = ldw(c.ws, ko + (u32)ch * 8192u), b_ = ldw(c.ws, vo + (u32)ch * 8192u);
                lds_st<u32x4>(L_K + (u32)(ch * 8 + w) * 1024u + (u32)c.lane * 16u, a);
                lds_st<u32x4>(L_V + (u32)(ch * 8 + w) * VSTR + (u32)c.lane * 16u, b_);
            }
            { const int t = (w * 64 + c.lane) >> 3, s_ = c.lane & 7; const u32 val = (s_ & 3) == 0 ? 0x3f803f80u : 0u;
              lds_st<u32x4>(L_V + (u32)t * VSTR + 1024u + (u32)s_ * 16u, (u32x4){val, val, val, val}); }
        }
        BAR();
        float ma = -INFINITY, mb = -INFINITY;
        for (int ch = 0; ch < 8; ++ch)
            for (int t = 0; t < 4; ++t) {
                const bf16x8 kf = lds_ld<bf16x8>(kp0 + (u32)(ch * 8192 + t * 1024));
                const f32x16 Sa = MFMA32(kf, qa, cz), Sb = MFMA32(kf, qbf, cz);
#pragma unroll
                for (int q = 0; q < 16; ++q) { ma = fmaxf(ma, Sa[q]); mb = fmaxf(mb, Sb[q]); }
            }
        ma = fmaxf(ma, __shfl_xor(ma, 32)); mb = fmaxf(mb, __shfl_xor(mb, 32));
        const u32 xb = L_AX + (u32)w * 2304u, xp = L_AX + (u32)(w ^ 4) * 2304u;
        lds_st<f32x2>(xb + (u32)c.lane * 8u, (f32x2){ma, mb});
        BAR();
        const f32x2 mo = lds_ld<f32x2>(xp + (u32)c.lane * 8u);
        ma = fmaxf(ma, mo[0]); mb = fmaxf(mb, mo[1]);
        BAR();
        f32x16 ca, cb;
#pragma unroll
        for (int q = 0; q < 16; ++q) { ca[q] = -ma; cb[q] = -mb; }
        attn_begin(s, qa, ca, kp0);
        u32 kp = kp0, vp0 = vp00, vp1 = vp10;
        for (int ch = 0; ch < 8; ++ch) {
            attn_chunk<true>(s, qa, qbf, ca, cb, kp, vp0, vp1);
            kp += 8192; vp0 += 8 * VSTR; vp1 += 8 * VSTR;
        }
        attn_combine(c, s, O);
        const float rl = 1.0f / O[8];
        u32x4 o;
#pragma unroll
        for (int q = 0; q < 4; ++q) o[q] = pk(O[2 * q] * rl, O[2 * q + 1] * rl);
        stw(c.ws, oo, o);
        BAR();
        const u32 wv2 = wo_lane_off(c);
        for (int q = 0; q < 8; ++q) wo_dma(c, p, wv2, q);
        DRAIN(); BAR();
    }
    STAMP(L ? 20 : 7);
    if (w == 0 && c.lane == 0) set_flag(c, (L ? OFF_FA1 : OFF_FA0) + (u32)(qb * 32 + head) * 128u);
    pre_store_ff(c, pf);
}

template <int L>
DI void phaseO(const Ctx& c0, const Params& p, u32x4 (&x2)[2], u32& hsync, const u32x4 (&pq)[12], u32x4 (&xf)[32]) {
    Ctx c = c0; FRESH(c);
    const int lane = c.lane, w = c.w;
    const int tb = 4 * c.xg + (c.gi >> 3), fb = c.gi & 7;
    const int r = lane & 31, h = lane >> 5;
    constexpr u32 OO = L ? OFF_O1 : OFF_O0, GS = L ? OFF_GS1 : OFF_GS0, GC = L ? OFF_GC1 : OFF_GC0, GM = L ? OFF_GM1 : OFF_GM0, GX = L ? OFF_GX1 : OFF_GX0;
    constexpr u32 L_TS = L_SB, L_CAND = P_ST + 8;
    if (w >= 4) {
        const int hw = w - 4;
        if (hw == 0) wait_flags(c, (L ? OFF_FA1 : OFF_FA0) + (u32)((tb >> 2) * 32 + (lane & 31)) * 128u);
        float wsum = 0.f;
        if (hw == 0) {
            const u32x4 v = *(const u32x4*)(p.wr + lane * 8); float s = 0;
#pragma unroll
            for (int j = 0; j < 4; ++j) s += bflo(v[j]) + bfhi(v[j]);
            wsum = wave_sum(s);
        }
        BAR();
        STAMP(L ? 21 : 8);
        BAR();
        BAR();
        __builtin_amdgcn_s_setprio(3);
        FST(30 + 20 * L);
        if (hw == 0) {
            const u32 a = L_SB + (u32)h * 384u + (u32)r * 12u;
            const float t1 = lds_ld<float>(a) + lds_ld<float>(a + 768), t2 = lds_ld<float>(a + 4) + lds_ld<float>(a + 772), t3 = lds_ld<float>(a + 8) + lds_ld<float>(a + 776);
            stw(c.ws, GS + (u32)((tb * 8 + fb) * 1024 + lane * 16), (u32x4){__float_as_uint(t1), __float_as_uint(t2), __float_as_uint(t3), c.tag});
        }
        FST(31 + 20 * L);
        if (L == 0) {
            if (hw != 0) { pre_store_wq(c, pq); lds_signal_add(c, P_WQ); }
            BAR();
        }
        FST(32 + 20 * L);
        if (hw == 0) {
            u32x4 v[8]; u32 n = 0;
            for (;;) {
                bool ok = true;
#pragma unroll
                for (int k = 0; k < 8; ++k) v[k] = ldw(c.ws, GS + (u32)((tb * 8 + k) * 1024 + lane * 16));
                MEMFENCE();
#pragma unroll
                for (int k = 0; k < 8; ++k) ok = ok && v[k][3] == c.tag;
                if (__all(ok)) break;
                if (spin_fail(c, n)) break;
            }
            FST(33 + 20 * L);
            float t1 = 0, t2 = 0, t3 = 0;
#pragma unroll
            for (int k = 0; k < 8; ++k) { t1 += __uint_as_float(v[k][0]); t2 += __uint_as_float(v[k][1]); t3 += __uint_as_float(v[k][2]); }
            const float mu = t1 * (1.0f / 512.0f), var = fmaxf(t2 * (1.0f / 512.0f) - mu * mu, 0.f);
            const float rs = __builtin_amdgcn_rsqf(var + 1e-5f);
            const float rt = rbf(rs * (t3 - mu * wsum));
            const u32 key = half_umax(score_key(__float_as_uint(rt) >> 16, (u32)r));
            const int bt = (int)key_idx(key);
            if (fb == 0) {
                const u32 k0 = (u32)__builtin_amdgcn_readlane((int)key, 0), k1 = (u32)__builtin_amdgcn_readlane((int)key, 32);
                const u32 kT = (lane & 1) ? k1 : k0;
                const u64 g = (cand_tag(c.tag) << 21) | (u64)(key_bf(kT) << 5) | (u64)key_idx(kT);
                if (lane < 16) __builtin_amdgcn_raw_buffer_store_b64((u32x2){(u32)g, (u32)(g >> 32)}, c.ws, GC + (u32)(lane >> 1) * 512u + (u32)(tb * 2 + (lane & 1)) * 8u, 0, 16);
                if (r == bt) stw(c.ws, GM + (u32)(tb * 2 + h) * 16u, mk_unit(__float_as_uint(mu), __float_as_uint(rs), c.tag));
                if (L == 0) {
                    const float g1 = 1.0f + rs, A = g1 * __builtin_amdgcn_rsqf(var * g1 * g1 + 1e-5f);
                    stw(c.ws, OFF_TS1 + (u32)(tb * 64 + lane) * 16u, mk_unit(__float_as_uint(A), __float_as_uint(A * mu), c.tag));
                }
            }
            FST(34 + 20 * L);
            lds_st<f32x2>(L_TS + (u32)lane * 8u, (f32x2){mu, rs});
            if (r == 0) lds_st<u32>(L_CAND + (u32)h * 4u, (u32)bt);
            lds_signal_add(c, P_ST + L * 4);
            {
                const int ct = (lane >> 4) & 1, u = lane & 15;
                const int cb = __builtin_amdgcn_readlane(bt, 0), cb1 = __builtin_amdgcn_readlane(bt, 32);
                const int row = ct ? cb1 : cb;
                const u32x2 d = lds_ld<u32x2>(L_X1T + (u32)(ct + 2 * (u >> 3)) * 2048u + (u32)((u >> 2) & 1) * 1024u + (u32)((u & 1) * 32 + row) * 16u + (u32)((u >> 1) & 1) * 8u);
                if (lane < 32) stw(c.ws, GX + (u32)(tb * 2 + ct) * 2048u + (u32)(fb * 16 + u) * 16u, mk_unit(d[0], d[1], c.tag));
            }
            if (L == 0) { pre_store_wq(c, pq); lds_signal_add(c, P_WQ); }
        }
        STAMP(L ? 23 : 12);
        if (L == 1) ffn_chain<1>(c, p, hsync);
        return;
    }
    const int tt = w & 1, kh = w >> 1;
    const int tile = tb * 2 + tt, tok = tile * 32 + r;
    const int f0 = fb * 64 + kh * 32;
    u32x2 res[4], wr[4];
#pragma unroll
    for (int g = 0; g < 4; ++g) {
        if (L == 0) res[g] = *(const u32x2*)(p.x0 + (size_t)tok * 512 + f0 + 8 * g + 4 * h);
        wr[g] = *(const u32x2*)(p.wr + f0 + 8 * g + 4 * h);
    }
    if (L == 1) {
        const int sel0 = __builtin_amdgcn_readfirstlane((int)lds_ld<u32>(P_SEL)) & 2047;
        if ((sel0 >> 5) == tile) {
            const u32 ub = OFF_GY + (u32)(f0 / 4 + h) * 16u;
            u32 off[4] = {ub, ub + 32u, ub + 64u, ub + 96u}; u32x4 v[4];
            wait_units<4>(c, off, v);
            if (r == (sel0 & 31)) { x2[0] = (u32x4){v[0][0], v[0][2], v[1][0], v[1][2]}; x2[1] = (u32x4){v[2][0], v[2][2], v[3][0], v[3][2]}; }
        }
#pragma unroll
        for (int g = 0; g < 4; ++g) res[g] = (u32x2){x2[g >> 1][(g & 1) * 2], x2[g >> 1][(g & 1) * 2 + 1]};
    }
    BAR();
    STAMP(L ? 21 : 8);
    u32x4 bf[16];
    const u32 oo = OO + (u32)(((kh * 16) * 64 + tile) * 64 + lane) * 16u;
#pragma unroll
    for (int s = 0; s < 16; ++s) bf[s] = ldw(c.ws, oo + (u32)s * 65536u);
    const int i16 = lane & 15, blk = (lane >> 4) & 1;
    const u32 trA = L_WO + (u32)((kh * 256 + 4 * h + (i16 >> 2)) * 64 + blk * 32 + (i16 & 3) * 8);
    f32x16 a0 = {}, a1 = {};
    {
        u32 ta = trA, tb_ = trA + WO_FT;
        asm volatile("" : "+v"(ta), "+v"(tb_));
        bf16x8 f0_[2], f1_[2];
        f0_[0] = frag_tr(ta, ta + 512); f0_[1] = frag_tr(tb_, tb_ + 512);
#pragma unroll
        for (int s = 0; s < 16; s += 2) {
            f1_[0] = frag_tr(ta + (u32)(s + 1) * 1024u, ta + (u32)(s + 1) * 1024u + 512); SBAR();
            a0 = MFMA32(f0_[0], BC8(bf[s]), a0); SBAR();
            f1_[1] = frag_tr(tb_ + (u32)(s + 1) * 1024u, tb_ + (u32)(s + 1) * 1024u + 512); SBAR();
            a1 = MFMA32(f0_[1], BC8(bf[s]), a1); SBAR();
            if (s + 2 < 16) f0_[0] = frag_tr(ta + (u32)(s + 2) * 1024u, ta + (u32)(s + 2) * 1024u + 512);
            SBAR(); a0 = MFMA32(f1_[0], BC8(bf[s + 1]), a0); SBAR();
            if (s + 2 < 16) f0_[1] = frag_tr(tb_ + (u32)(s + 2) * 1024u, tb_ + (u32)(s + 2) * 1024u + 512);
            SBAR(); a1 = MFMA32(f1_[1], BC8(bf[s + 1]), a1); SBAR();
        }
    }
    {
        const f32x16& snd = kh ? a0 : a1;
#pragma unroll
        for (int g = 0; g < 4; ++g) lds_st<f32x4>(L_XB + (u32)w * 4096u + (u32)g * 1024u + (u32)lane * 16u, (f32x4){snd[4 * g], snd[4 * g + 1], snd[4 * g + 2], snd[4 * g + 3]});
    }
    BAR();
    STAMP(L ? 22 : 9);
    u32x4 xp[2];
    {
        const f32x16& keep = kh ? a1 : a0;
#pragma unroll
        for (int g = 0; g < 4; ++g) {
            const f32x4 o = lds_ld<f32x4>(L_XB + (u32)(w ^ 2) * 4096u + (u32)g * 1024u + (u32)lane * 16u);
            const u32 t0 = pk(keep[4 * g] + o[0], keep[4 * g + 1] + o[1]), t1 = pk(keep[4 * g + 2] + o[2], keep[4 * g + 3] + o[3]);
            xp[g >> 1][(g & 1) * 2] = pk(bflo(res[g][0]) + bflo(t0), bfhi(res[g][0]) + bfhi(t0));
            xp[g >> 1][(g & 1) * 2 + 1] = pk(bflo(res[g][1]) + bflo(t1), bfhi(res[g][1]) + bfhi(t1));
        }
    }
    if (L == 0) {
#pragma unroll
        for (int s = 0; s < 2; ++s) stw(c.ws, OFF_X1 + (u32)((tile * 32 + f0 / 16 + s) * 64 + lane) * 16u, xp[s]);
    }
#pragma unroll
    for (int s = 0; s < 2; ++s) lds_st<u32x4>(L_X1T + (u32)w * 2048u + (u32)s * 1024u + (u32)lane * 16u, xp[s]);
    float x1[16];
    {
        float s1 = 0, s2 = 0, s3 = 0;
#pragma unroll
        for (int i = 0; i < 16; ++i) {
            const u32 wd = xp[i >> 3][(i & 7) >> 1];
            x1[i] = (i & 1) ? bfhi(wd) : bflo(wd);
            const u32 ww = wr[i >> 2][(i & 3) >> 1];
            const float wv = (i & 1) ? bfhi(ww) : bflo(ww);
            s1 += x1[i]; s2 += x1[i] * x1[i]; s3 += x1[i] * wv;
        }
        s1 = swap32_add(s1); s2 = swap32_add(s2); s3 = swap32_add(s3);
        if (h == 0) { const u32 a = L_SB + (u32)w * 384u + (u32)r * 12u; lds_st<float>(a, s1); lds_st<float>(a + 4, s2); lds_st<float>(a + 8, s3); }
    }
    BAR();
    if (L == 0) {
        DRAIN(); BAR();
        STAMP(10);
        if (w == 0 && lane == 0) set_flag(c, OFF_FO + (u32)(tb * 8 + fb) * 128u);
        pre_store_wq(c, pq); lds_signal_add(c, P_WQ);
        const int rbq = 4 * (c.xg & 3) + (c.gi & 3);
        wait_flags(c, OFF_FO + (u32)(rbq * 16 + (lane & 15)) * 128u);
        STAMP(13);
    }
    lds_wait_ge(P_ST + L * 4, 1u);
    STAMP(L ? 23 : 11);
    const f32x2 st = lds_ld<f32x2>(L_TS + (u32)(tt * 32 + r) * 8u);
    const int bt = (int)lds_ld<u32>(L_CAND + (u32)tt * 4u) & 31;
#pragma unroll
    for (int s = 0; s < 2; ++s)
#pragma unroll
        for (int j = 0; j < 4; ++j) {
            const float a = x1[8 * s + 2 * j], b_ = x1[8 * s + 2 * j + 1];
            const u32 hh = pk((a - st[0]) * st[1], (b_ - st[0]) * st[1]);
            x2[s][j] = pk(a + bflo(hh), b_ + bfhi(hh));
        }
    if (L == 0) {
        lds_wait_ge(P_XGO, c.gi < 14 ? 4u : 1u);
        const int rbq = 4 * (c.xg & 3) + (c.gi & 3);
        const u32 xo = OFF_X1 + (u32)(rbq * 4 + w) * 32768u + (u32)lane * 16u;
#pragma unroll
        for (int s = 0; s < 32; ++s) xf[s] = ldw(c.ws, xo + (u32)s * 1024u);
    }
    if (L == 1) {
        const u32 tl = L_XB + (u32)w * 4096u;
#pragma unroll
        for (int g = 0; g < 4; ++g) lds_st<u32x2>(tl + (u32)(r * 80 + (8 * g + 4 * h) * 2), (u32x2){x2[g >> 1][(g & 1) * 2], x2[g >> 1][(g & 1) * 2 + 1]});
        asm volatile("s_waitcnt lgkmcnt(0)" ::: "memory");
        u32x4 rowv[2];
#pragma unroll
        for (int q = 0; q < 2; ++q) {
            const int row = q * 16 + (lane >> 2);
            rowv[q] = lds_ld<u32x4>(tl + (u32)(row * 80 + (lane & 3) * 16));
            if (row != bt) *(u32x4*)(p.out + (size_t)(tile * 32 + row) * 512 + f0 + (lane & 3) * 8) = rowv[q];
        }
        wait_sel(1);
        const int sel1 = __builtin_amdgcn_readfirstlane((int)lds_ld<u32>(P_SEL + 32)) & 2047;
        if (tile * 32 + bt != sel1) {
#pragma unroll
            for (int q = 0; q < 2; ++q) {
                const int row = q * 16 + (lane >> 2);
                if (row == bt) *(u32x4*)(p.out + (size_t)(tile * 32 + row) * 512 + f0 + (lane & 3) * 8) = rowv[q];
            }
        }
    }
}

#if defined(__HIP_DEVICE_COMPILE__)
__attribute__((target("no-packed-fp32-ops")))
#endif
__global__ __launch_bounds__(512, 2) void fused_layers_kernel(Params p) {
    extern __shared__ __attribute__((aligned(16))) char lds[];
    Ctx c;
    const int tid = threadIdx.x;
    c.lane = tid & 63; c.w = __builtin_amdgcn_readfirstlane(tid >> 6);
    c.xg = blockIdx.x & 7; c.gi = blockIdx.x >> 3;
    c.ws = __builtin_amdgcn_make_buffer_rsrc(p.ws, 0, WS_BYTES, 0x00020000);
    c.tag = 0u;
    if (c.w == 6) {
        const u32 o = (u32)(((4 * c.xg + (c.gi >> 3)) * 8 + (c.gi & 7)) * 1024 + c.lane * 16);
        stw(c.ws, OFF_GS0 + o, (u32x4){0u, 0u, 0u, 0u}); stw(c.ws, OFF_GS1 + o, (u32x4){0u, 0u, 0u, 0u});
    }
    {
        u64 pc; u32 len;
        asm volatile("s_getpc_b64 %0\nfl_code_here:\n\ts_mov_b32 %1, fl_code_end-fl_code_here" : "=s"(pc), "=s"(len));
        if (c.w == 7) {
            const u32 o0 = min((u32)c.gi * 3072u + (u32)c.lane * 16u, len - 16u) & ~15u, o1 = min(o0 + 1024u, len - 16u) & ~15u, o2 = min(o0 + 2048u, len - 16u) & ~15u;
            const u64 base = pc & ~15ull;
            glds((const void*)base, o0, 147456u); glds((const void*)base, o1, 148480u); glds((const void*)base, o2, 149504u);
        }
    }
    if (tid < 24) lds_st<u32>(P_HCNT + (u32)tid * 4u, 0u);
    asm volatile("" :: "v"(lds));
    BAR();
    STAMP(0);
    FST(0);
    u32 hsync = 0;
    u32x4 x2[2] = {};
    u32x4 pq[12], xf[32];
    phaseQ<0>(c, p, hsync, pq, xf);
    if (STOP_AFTER >= 1) phaseA<0>(c, p);
    if (STOP_AFTER >= 2) phaseO<0>(c, p, x2, hsync, pq, xf);
    if (STOP_AFTER >= 3) phaseQ<1>(c, p, hsync, pq, xf);
    if (STOP_AFTER >= 4) phaseA<1>(c, p);
    if (STOP_AFTER >= 5) phaseO<1>(c, p, x2, hsync, pq, xf);
    STAMP(24);
    if (blockIdx.x == 14 * 8 && tid == 256) __builtin_amdgcn_raw_buffer_store_b32(c.tag, c.ws, OFF_EPOCH, 0, 16);
    asm volatile("fl_code_end:" ::: "memory");
}

extern "C" void kernel_launch(void* const* d_in, const int* in_sizes, int n_in, void* d_out, int out_size, void* d_ws, size_t ws_size, hipStream_t stream) {
    Params p;
    p.x0 = (const uint16_t*)d_in[0]; p.wqkv = (const uint16_t*)d_in[1]; p.wo = (const uint16_t*)d_in[2];
    p.wr = (const uint16_t*)d_in[4]; p.wg = (const uint16_t*)d_in[5]; p.wu = (const uint16_t*)d_in[6]; p.wd = (const uint16_t*)d_in[7];
    p.out = (uint16_t*)d_out; p.ws = (char*)d_ws;
    (void)hipFuncSetAttribute((const void*)fused_layers_kernel, hipFuncAttributeMaxDynamicSharedMemorySize, LDS_BYTES);
    fused_layers_kernel<<<256, 512, LDS_BYTES, stream>>>(p);
}
```

```cpp
#include <hip/hip_runtime.h>
#include <stdint.h>

typedef unsigned u32;
typedef unsigned long long u64;
typedef __attribute__((ext_vector_type(2))) u32 u32x2;
typedef __attribute__((ext_vector_type(4))) u32 u32x4;
typedef __attribute__((ext_vector_type(2))) float f32x2;
typedef __attribute__((ext_vector_type(4))) float f32x4;
typedef __attribute__((ext_vector_type(16))) float f32x16;
typedef __attribute__((ext_vector_type(8))) __bf16 bf16x8;
typedef __attribute__((ext_vector_type(2))) __bf16 bf16x2;
typedef __attribute__((ext_vector_type(4))) short s16x4;
typedef __attribute__((ext_vector_type(8))) short s16x8;
#define LAS __attribute__((address_space(3)))
#define DI __device__ __forceinline__
#define MFMA32(a, b, c) __builtin_amdgcn_mfma_f32_32x32x16_bf16((a), (b), (c), 0, 0, 0)
#define MFMA16(a, b, c) __builtin_amdgcn_mfma_f32_16x16x32_bf16((a), (b), (c), 0, 0, 0)
#define SBAR() __builtin_amdgcn_sched_barrier(0)
#define BAR() asm volatile("s_waitcnt lgkmcnt(0)\n\ts_barrier" ::: "memory")
#define DRAIN() asm volatile("s_waitcnt vmcnt(0)" ::: "memory")
#define MEMFENCE() asm volatile("" ::: "memory")

#define STOP_AFTER 99

constexpr u32 OFF_EPOCH = 0, OFF_ABORT = 256;
constexpr u32 OFF_FQ0 = 0x10000, OFF_FO = 0x20000, OFF_FQ1 = 0x28000;
constexpr u32 OFF_TS0 = 0x40000, OFF_TS1 = 0x48000;
constexpr u32 OFF_GC0 = 0x50000, OFF_GC1 = 0x51000;
constexpr u32 OFF_GM0 = 0x52000, OFF_GM1 = 0x52400;
constexpr u32 OFF_GA0 = 0x58000, OFF_GA1 = 0x5A000;
constexpr u32 OFF_GY = 0x5C000;
constexpr u32 OFF_GS0 = 0x80000, OFF_GS1 = 0xC0000;
constexpr u32 OFF_GX0 = 0x180000, OFF_GX1 = 0x1A0000;
constexpr u32 OFF_Q0 = 0x200000, OFF_K0 = 0x400000, OFF_V0 = 0x600000, OFF_O0 = 0x800000, OFF_X1 = 0xA00000;
constexpr u32 OFF_Q1 = 0xC00000, OFF_K1 = 0xE00000, OFF_V1 = 0x1000000, OFF_O1 = 0x1200000;
constexpr u32 WS_BYTES = 0x1400000;

constexpr u32 LDS_BYTES = 163840;
constexpr u32 L_W = 0;
constexpr u32 L_XR = 98304;
constexpr u32 L_AX = 140288;
constexpr u32 L_TOK = 161792 + 512;
constexpr u32 L_K = 0, L_V = 65536, VSTR = 1152;
constexpr u32 L_WO = 0, WO_FT = 65536;
constexpr u32 L_XB = 32768;
constexpr u32 L_SB = 161792;
constexpr u32 L_P = 163328;
constexpr u32 P_CS = L_P, P_HCNT = L_P + 384, P_PATCH = L_P + 388, P_WQ = L_P + 392, P_WSUM = L_P + 396, P_SEL = L_P + 400, P_ST = L_P + 464;
constexpr u32 L_X1T = 49152;
constexpr u32 FW0 = 98304, FS0 = 155648;
constexpr u32 FW1 = FW0, FS1 = FS0;
constexpr u32 SPIN_CAP = 40000;

struct Params {
    const uint16_t* x0; const uint16_t* wqkv; const uint16_t* wo; const uint16_t* wr;
    const uint16_t* wg; const uint16_t* wu; const uint16_t* wd; uint16_t* out; char* ws;
};

typedef __amdgpu_buffer_rsrc_t rsrc_t;

DI u32 pk(float a, float b) { f32x2 v = {a, b}; return __builtin_bit_cast(u32, __builtin_convertvector(v, bf16x2)); }
DI float bflo(u32 v) { return __uint_as_float(v << 16); }
DI float bfhi(u32 v) { return __uint_as_float(v & 0xffff0000u); }
DI float rbf(float a) { return bflo(pk(a, 0.f)); }
DI u32x4 ldw(rsrc_t r, u32 off) { return __builtin_amdgcn_raw_buffer_load_b128(r, off, 0, 16); }
DI void stw(rsrc_t r, u32 off, u32x4 v) { __builtin_amdgcn_raw_buffer_store_b128(v, r, off, 0, 16); }
template <class T> DI T lds_ld(u32 a) { return *(const LAS T*)(uintptr_t)a; }
template <class T> DI void lds_st(u32 a, T v) { *(LAS T*)(uintptr_t)a = v; }
DI s16x4 lds_tr(u32 a) { return __builtin_amdgcn_ds_read_tr16_b64_v4i16((LAS s16x4*)(uintptr_t)a); }
DI bf16x8 frag_tr(u32 a0, u32 a1) { s16x4 lo = lds_tr(a0), hi = lds_tr(a1); return __builtin_bit_cast(bf16x8, __builtin_shufflevector(lo, hi, 0, 1, 2, 3, 4, 5, 6, 7)); }
DI float swap32_add(float v) { auto r = __builtin_amdgcn_permlane32_swap(__float_as_uint(v), __float_as_uint(v), false, false); return __uint_as_float(r[0]) + __uint_as_float(r[1]); }
template <int CTRL> DI u32 dpp(u32 v) { return (u32)__builtin_amdgcn_update_dpp((int)v, (int)v, CTRL, 0xf, 0xf, true); }
DI float wave_sum(float v) {
    v += __uint_as_float(dpp<0xB1>(__float_as_uint(v)));
    v += __uint_as_float(dpp<0x4E>(__float_as_uint(v)));
    v += __uint_as_float(dpp<0x141>(__float_as_uint(v)));
    v += __uint_as_float(dpp<0x140>(__float_as_uint(v)));
    { auto r = __builtin_amdgcn_permlane16_swap(__float_as_uint(v), __float_as_uint(v), false, false); v = __uint_as_float(r[0]) + __uint_as_float(r[1]); }
    return swap32_add(v);
}
DI u32 half_umax(u32 v) {
    v = max(v, dpp<0xB1>(v)); v = max(v, dpp<0x4E>(v)); v = max(v, dpp<0x141>(v)); v = max(v, dpp<0x140>(v));
    auto r = __builtin_amdgcn_permlane16_swap(v, v, false, false);
    return max(r[0], r[1]);
}
DI u32 wave_umax(u32 v) { v = half_umax(v); auto r = __builtin_amdgcn_permlane32_swap(v, v, false, false); return max(r[0], r[1]); }
DI u32 score_key(u32 bf, u32 idx) { return ((bf ^ ((bf & 0x8000u) ? 0xFFFFu : 0x8000u)) << 16) | (0xFFFFu - idx); }
DI u32 key_bf(u32 k) { const u32 b = k >> 16; return b ^ ((b & 0x8000u) ? 0x8000u : 0xFFFFu); }
DI u32 key_idx(u32 k) { return 0xFFFFu - (k & 0xFFFFu); }
DI void glds(const void* sbase, u32 voff, u32 ldsdst) {
    u32 sv;
    asm volatile("s_mov_b32 %0, m0\n\ts_mov_b32 m0, %3\n\ts_nop 0\n\tglobal_load_lds_dwordx4 %1, %2\n\ts_mov_b32 m0, %0"
                 : "=&s"(sv) : "v"(voff), "s"(sbase), "s"(ldsdst) : "memory");
}
struct Ctx {
    rsrc_t ws; u32 tag; int lane, w, xg, gi;
};
#define FRESH(c) asm volatile("" : "+v"((c).lane))
DI u32 epoch_tag(u32 v) { const u32 t = (u32)__builtin_amdgcn_readfirstlane((int)v) + 1u; return t ? t : 1u; }
DI bool spin_fail(const Ctx& c, u32& n) {
    __builtin_amdgcn_s_sleep(1);
    ++n;
    if (n > SPIN_CAP) { if (c.lane == 0) __builtin_amdgcn_raw_buffer_store_b32(c.tag, c.ws, OFF_ABORT, 0, 16); return true; }
    if ((n & 127u) == 0u) { u32 a = __builtin_amdgcn_raw_buffer_load_b32(c.ws, OFF_ABORT, 0, 16); if (__builtin_amdgcn_readfirstlane(a) == c.tag) return true; }
    return false;
}
DI void wait_flags(const Ctx& c, u32 off) {
    u32 n = 0;
    for (;;) {
        u32x2 v = __builtin_amdgcn_raw_buffer_load_b64(c.ws, off, 0, 16);
        MEMFENCE();
        if (__all(v[0] == c.tag && v[1] == ~c.tag)) break;
        if (spin_fail(c, n)) break;
    }
    MEMFENCE();
}
DI void set_flag(const Ctx& c, u32 off) { u32x2 v = {c.tag, ~c.tag}; __builtin_amdgcn_raw_buffer_store_b64(v, c.ws, off, 0, 16); }
constexpr u32 POISON = 0xFFFFFFFFu;
DI bool poisoned(u32x4 v) { return max(max(v[0], v[1]), max(v[2], v[3])) == POISON; }
DI u32x4 not_poison(u32x4 v) { return (u32x4){min(v[0], POISON - 1u), min(v[1], POISON - 1u), min(v[2], POISON - 1u), min(v[3], POISON - 1u)}; }
DI void wait_piece(const Ctx& c, u32 off, u32x4& v) {
    u32 n = 0;
    while (__any(poisoned(v))) { if (spin_fail(c, n)) break; v = ldw(c.ws, off); MEMFENCE(); }
}
DI bool unit_ok(u32x4 v, u32 tag) { return v[1] == tag && v[3] == tag; }
DI u32x4 mk_unit(u32 a, u32 b, u32 tag) { return (u32x4){a, tag, b, tag}; }
template <int N> DI void wait_units(const Ctx& c, const u32 (&off)[N], u32x4 (&v)[N]) {
    u32 n = 0;
    for (;;) {
        bool ok = true;
#pragma unroll
        for (int k = 0; k < N; ++k) v[k] = ldw(c.ws, off[k]);
        MEMFENCE();
#pragma unroll
        for (int k = 0; k < N; ++k) ok = ok && unit_ok(v[k], c.tag);
        if (__all(ok)) break;
        if (spin_fail(c, n)) break;
    }
    MEMFENCE();
}
DI u32 lds_poll_ld(u32 a) { return __hip_atomic_load((LAS u32*)(uintptr_t)a, __ATOMIC_RELAXED, __HIP_MEMORY_SCOPE_WORKGROUP); }
DI void helper_sync(const Ctx& c, u32& target) {
    target += 4;
    asm volatile("s_waitcnt lgkmcnt(0)" ::: "memory");
    if (c.lane == 0) __hip_atomic_fetch_add((LAS u32*)(uintptr_t)P_HCNT, 1u, __ATOMIC_RELAXED, __HIP_MEMORY_SCOPE_WORKGROUP);
    u32 n = 0;
    while (lds_poll_ld(P_HCNT) < target) { if (++n > (1u << 24)) break; }
    MEMFENCE();
}
DI void lds_wait_ge(u32 a, u32 v) {
    u32 n = 0;
    while (lds_poll_ld(a) < v) { __builtin_amdgcn_s_sleep(1); if (++n > (1u << 22)) break; }
    MEMFENCE();
}
DI void lds_signal_add(const Ctx& c, u32 a) {
    asm volatile("s_waitcnt lgkmcnt(0)" ::: "memory");
    if (c.lane == 0) __hip_atomic_fetch_add((LAS u32*)(uintptr_t)a, 1u, __ATOMIC_RELAXED, __HIP_MEMORY_SCOPE_WORKGROUP);
}
DI void wait_sel(int L) {
    u32 n = 0;
    while (lds_poll_ld(P_SEL + L * 32 + 16) == 0u) { __builtin_amdgcn_s_sleep(1); if (++n > (1u << 22)) break; }
    MEMFENCE();
}

#define STAMP(i) do { } while (0)
#define CSTAMP(i) do { } while (0)
#define FST(i) do { } while (0)

DI u64 cand_tag(u32 tag) { return ((u64)tag << 11) | (u64)((tag * 0x9E3779B1u) >> 21); }
template <int L>
DI void ffn_chain(const Ctx& c, const Params& p, u32& hsync) {
    const int lane = c.lane, hw = c.w - 4;
    constexpr u32 FW = FW0, FS = FS0;
    constexpr u32 GC = L ? OFF_GC1 : OFF_GC0, GM = L ? OFF_GM1 : OFF_GM0, GX = L ? OFF_GX1 : OFF_GX0, GA = L ? OFF_GA1 : OFF_GA0;
    FST(20 + 20 * L);
    __builtin_amdgcn_s_setprio(3);
    const bool is_gu = c.gi < 14, is_dn = c.gi >= 14 && c.gi < 18;
    const int g16 = lane >> 4, i16 = lane & 15;
    bf16x8 wf[14];
    {
        const u32 tro = FW + (u32)((8 * g16 + (i16 >> 2)) * 32 + (i16 & 3) * 8) + (u32)hw * (is_gu ? 4096u : 14336u);
#pragma unroll
        for (int s = 0; s < 14; ++s) {
            const u32 bo = tro + (is_gu ? (u32)(s & 3) * 1024u + (u32)((s >> 2) & 1) * 16384u : (u32)s * 1024u);
            if (is_gu || is_dn) wf[s] = frag_tr(bo, bo + 128);
        }
    }
    if (hw == 0) {
        const u64 want = cand_tag(c.tag);
        u64 v; u32 n = 0;
        for (;;) {
            const u32x2 t = __builtin_amdgcn_raw_buffer_load_b64(c.ws, GC + c.xg * 512u + lane * 8u, 0, 16);
            MEMFENCE();
            v = ((u64)t[1] << 32) | t[0];
            if (__all((v >> 21) == want)) break;
            if (spin_fail(c, n)) break;
        }
        FST(21 + 20 * L);
        const u32 key = wave_umax(score_key(((u32)v >> 5) & 0xFFFFu, (u32)lane * 32u + ((u32)v & 31u)));
        const u32 tok = key_idx(key); const float r = __uint_as_float(key_bf(key) << 16);
        if (lane == 0) {
            lds_st<u32x2>(P_SEL + L * 32, (u32x2){(u32)tok, __float_as_uint(r)});
            asm volatile("s_waitcnt lgkmcnt(0)" ::: "memory");
            __hip_atomic_store((LAS u32*)(uintptr_t)(P_SEL + L * 32 + 16), 1u, __ATOMIC_RELAXED, __HIP_MEMORY_SCOPE_WORKGROUP);
        }
    }
    FST(22 + 20 * L);
    STAMP(L ? 25 : 13);
    if (!is_gu && !is_dn) return;
    wait_sel(L);
    const u32x2 si = lds_ld<u32x2>(P_SEL + L * 32);
    const int sel = __builtin_amdgcn_readfirstlane((int)si[0]) & 2047;
    const float rsel = __uint_as_float(si[1]);
    const u32 gxrow = GX + (u32)(sel >> 5) * 2048u, gm = GM + (u32)(sel >> 5) * 16u;
    if (is_gu) {
        const int gidx = c.xg * 14 + c.gi;
        u32 off[2] = {gxrow + (u32)(hw * 32 + (lane & 31)) * 16u, gm};
        u32x4 v[2];
        FST(29 + 20 * L);
        wait_units<2>(c, off, v);
        FST(23 + 20 * L);
        const float mu = __uint_as_float(v[1][0]), rs = __uint_as_float(v[1][2]);
        if (lane < 32) {
            u32x2 h;
            h[0] = pk((bflo(v[0][0]) - mu) * rs, (bfhi(v[0][0]) - mu) * rs);
            h[1] = pk((bflo(v[0][2]) - mu) * rs, (bfhi(v[0][2]) - mu) * rs);
            lds_st<u32x2>(FS + (u32)(hw * 32 + lane) * 8u, h);
        }
        STAMP(L ? 26 : 14);
        FST(24 + 20 * L);
        f32x4 ag = {}, au = {};
#pragma unroll
        for (int s = 0; s < 4; ++s) {
            const bf16x8 a = lds_ld<bf16x8>(FS + (u32)((hw * 4 + s) * 64 + g16 * 16));
            ag = MFMA16(a, wf[s], ag);
            au = MFMA16(a, wf[4 + s], au);
        }
        if (lane < 16) { lds_st<float>(FS + 4608 + (u32)(hw * 32 + lane) * 4u, ag[0]); lds_st<float>(FS + 4608 + (u32)(hw * 32 + 16 + lane) * 4u, au[0]); }
        FST(25 + 20 * L);
        helper_sync(c, hsync);
        FST(26 + 20 * L);
        if (hw == 0) {
            float g = 0.f, u = 0.f;
#pragma unroll
            for (int k = 0; k < 4; ++k) { g += lds_ld<float>(FS + 4608 + (u32)(k * 32 + i16) * 4u); u += lds_ld<float>(FS + 4608 + (u32)(k * 32 + 16 + i16) * 4u); }
            g = rbf(g); u = rbf(u);
            const float sg = rbf(g * __builtin_amdgcn_rcpf(1.0f + __expf(-g)));
            const float a = rbf(sg * u);
            const float a0 = __shfl(a, (lane & 3) * 4), a1 = __shfl(a, (lane & 3) * 4 + 1), a2 = __shfl(a, (lane & 3) * 4 + 2), a3 = __shfl(a, (lane & 3) * 4 + 3);
            if (lane < 4) stw(c.ws, GA + (u32)(gidx * 4 + lane) * 16u, mk_unit(pk(a0, a1), pk(a2, a3), c.tag));
            FST(27 + 20 * L);
        }
    } else {
        const int didx = c.xg * 4 + (c.gi - 14);
        float mu, rs;
        {
            const int u0 = hw * 112 + lane, u1 = hw * 112 + 64 + (lane < 48 ? lane : 47);
            u32 off[4] = {gxrow + (u32)(didx * 4 + (lane & 3)) * 16u, gm, GA + (u32)u0 * 16u, GA + (u32)u1 * 16u};
            u32x4 v[4];
            wait_units<4>(c, off, v);
            FST(23 + 20 * L);
            mu = __uint_as_float(v[1][0]); rs = __uint_as_float(v[1][2]);
            lds_st<u32x2>(FS + 1024 + (u32)u0 * 8u, (u32x2){v[2][0], v[2][2]});
            if (lane < 48) lds_st<u32x2>(FS + 1024 + (u32)u1 * 8u, (u32x2){v[3][0], v[3][2]});
            if (hw == 0 && lane < 4) lds_st<u32x2>(FS + (u32)lane * 8u, (u32x2){v[0][0], v[0][2]});
        }
        STAMP(L ? 26 : 14);
        FST(24 + 20 * L);
        f32x4 ay = {}, az = {};
        bf16x8 af[14];
#pragma unroll
        for (int s = 0; s < 14; ++s) af[s] = lds_ld<bf16x8>(FS + 1024 + (u32)((hw * 14 + s) * 64 + g16 * 16));
#pragma unroll
        for (int s = 0; s < 14; s += 2) { ay = MFMA16(af[s], wf[s], ay); az = MFMA16(af[s + 1], wf[s + 1], az); }
        if (lane < 16) lds_st<float>(FS + 4608 + (u32)(hw * 16 + lane) * 4u, ay[0] + az[0]);
        FST(25 + 20 * L);
        helper_sync(c, hsync);
        FST(26 + 20 * L);
        if (hw == 0) {
            float y = 0.f;
#pragma unroll
            for (int k = 0; k < 4; ++k) y += lds_ld<float>(FS + 4608 + (u32)(k * 16 + i16) * 4u);
            const float x1 = __uint_as_float((u32)lds_ld<uint16_t>(FS + (u32)i16 * 2u) << 16);
            const float sig = rbf(__builtin_amdgcn_rcpf(1.0f + __expf(-rsel)));
            y = rbf(rbf(y) * sig);
            const float h = rbf((x1 - mu) * rs);
            const float x2 = rbf(x1 + rbf(h + y));
            const float a0 = __shfl(x2, (lane & 3) * 4), a1 = __shfl(x2, (lane & 3) * 4 + 1), a2 = __shfl(x2, (lane & 3) * 4 + 2), a3 = __shfl(x2, (lane & 3) * 4 + 3);
            if (L == 0) { if (lane < 4) stw(c.ws, OFF_GY + (u32)(didx * 4 + lane) * 16u, mk_unit(pk(a0, a1), pk(a2, a3), c.tag)); }
            else if (lane < 4) *(u32x2*)(p.out + (size_t)sel * 512 + didx * 16 + lane * 4) = (u32x2){pk(a0, a1), pk(a2, a3)};
            FST(27 + 20 * L);
        }
    }
}

DI void qkv_patch(const Ctx& c, u32& hsync) {
    const int lane = c.lane, hw = c.w - 4;
    constexpr u32 FS = FS0;
    const int g16 = lane >> 4, i16 = lane & 15;
    bf16x8 wf[24];
    {
        const u32 tro = L_W + (u32)((8 * g16 + (i16 >> 2)) * 64 + (i16 & 3) * 8) + (u32)hw * 8192u;
#pragma unroll
        for (int s_ = 0; s_ < 4; ++s_)
#pragma unroll
            for (int ct = 0; ct < 6; ++ct) {
                const u32 bo = tro + (u32)s_ * 2048u + (u32)(ct >> 1) * 32768u + (u32)(ct & 1) * 32u;
                wf[s_ * 6 + ct] = frag_tr(bo, bo + 256);
            }
    }
    u32 off[2] = {OFF_GY + (u32)lane * 32u, OFF_GY + (u32)lane * 32u + 16u}; u32x4 v[2];
    wait_units<2>(c, off, v);
    float x[8], s = 0, q = 0;
#pragma unroll
    for (int k = 0; k < 2; ++k) { x[4 * k] = bflo(v[k][0]); x[4 * k + 1] = bfhi(v[k][0]); x[4 * k + 2] = bflo(v[k][2]); x[4 * k + 3] = bfhi(v[k][2]); }
#pragma unroll
    for (int k = 0; k < 8; ++k) { s += x[k]; q += x[k] * x[k]; }
    s = wave_sum(s); q = wave_sum(q);
    const float mu = s * (1.0f / 512.0f), var = fmaxf(q * (1.0f / 512.0f) - mu * mu, 0.f);
    const float A = __builtin_amdgcn_rsqf(var + 1e-5f);
    lds_st<u32x4>(FS + (u32)lane * 16u, (u32x4){pk((x[0] - mu) * A, (x[1] - mu) * A), pk((x[2] - mu) * A, (x[3] - mu) * A), pk((x[4] - mu) * A, (x[5] - mu) * A), pk((x[6] - mu) * A, (x[7] - mu) * A)});
    f32x4 acc[6] = {};
#pragma unroll
    for (int s_ = 0; s_ < 4; ++s_) {
        const bf16x8 a = lds_ld<bf16x8>(FS + (u32)((hw * 4 + s_) * 64 + g16 * 16));
#pragma unroll
        for (int ct = 0; ct < 6; ++ct) acc[ct] = MFMA16(a, wf[s_ * 6 + ct], acc[ct]);
    }
    if (lane < 16) {
#pragma unroll
        for (int ct = 0; ct < 6; ++ct) lds_st<float>(FS + 1024 + (u32)(hw * 96 + ct * 16 + lane) * 4u, acc[ct][0]);
    }
    helper_sync(c, hsync);
    if (hw == 0) {
#pragma unroll
        for (int k = 0; k < 2; ++k) {
            const int n = k * 64 + lane;
            if (n < 96) {
                float t = 0.f;
#pragma unroll
                for (int j = 0; j < 4; ++j) t += lds_ld<float>(FS + 1024 + (u32)(j * 96 + n) * 4u);
                lds_st<float>(FS + 4608 + (u32)n * 4u, n < 32 ? t * (0.25f * 1.4426950408889634f) : t);
            }
        }
        asm volatile("s_waitcnt lgkmcnt(0)" ::: "memory");
        if (lane == 0) __hip_atomic_store((LAS u32*)(uintptr_t)P_PATCH, 1u, __ATOMIC_RELAXED, __HIP_MEMORY_SCOPE_WORKGROUP);
    }
}

DI u32x4 gld(const void* base, u32 off) { return *(const u32x4*)((const char*)base + off); }
struct FfSrc { const uint16_t* m; u32 vo, step; bool on, gu; };
DI FfSrc ff_src(const Ctx& c, const Params& p) {
    int lane = c.lane; asm volatile("" : "+v"(lane));
    FfSrc f; const int w = c.w;
    f.on = c.gi < 18; f.gu = c.gi < 14;
    f.m = f.gu ? (w < 4 ? p.wg : p.wu) : p.wd;
    const u32 rowb = f.gu ? 3584u : 1024u, col = f.gu ? (u32)(c.xg * 14 + c.gi) * 32u : (u32)(c.xg * 4 + (c.gi - 14)) * 32u;
    const u32 first = f.gu ? (u32)(w & 3) * 4u : (u32)w * 7u;
    f.step = 32u * rowb;
    f.vo = (u32)(lane >> 1) * rowb + col + (u32)(lane & 1) * 16u + first * f.step;
    return f;
}
DI void keep_wq(const Ctx& c, u32x4 (&pq)[12]) {
#pragma unroll
    for (int j = 0; j < 12; ++j) pq[j] = lds_ld<u32x4>(L_W + (u32)(c.w * 12 + j) * 1024u + (u32)c.lane * 16u);
}
DI void pre_store_wq(const Ctx& c, const u32x4 (&pq)[12]) {
#pragma unroll
    for (int j = 0; j < 12; ++j) lds_st<u32x4>(L_W + (u32)(c.w * 12 + j) * 1024u + (u32)c.lane * 16u, pq[j]);
}
DI void pre_store_ff(const Ctx& c, const u32x4 (&pf)[7]) {
    if (c.gi < 18) {
        const bool gu = c.gi < 14;
        const u32 a = FW0 + (u32)c.w * (gu ? 4096u : 7168u) + (u32)c.lane * 16u;
#pragma unroll
        for (int j = 0; j < 7; ++j) if (j < 4 || !gu) lds_st<u32x4>(a + (u32)j * 1024u, pf[j]);
    }
}
DI u32 wo_lane_off(const Ctx& c) {
    int lane = c.lane; asm volatile("" : "+v"(lane));
    return (u32)((lane >> 2) * 1024 + (c.gi & 7) * 128 + (lane & 3) * 16 + (c.w >> 2) * 64 + (c.w & 3) * 8 * 16384);
}
DI void pre_store_wo(const Ctx& c, const u32x4 (&po)[8]) {
#pragma unroll
    for (int j = 0; j < 8; ++j) lds_st<u32x4>(L_WO + (u32)(c.w >> 2) * WO_FT + (u32)((c.w & 3) * 8 + j) * 1024u + (u32)c.lane * 16u, po[j]);
}

struct WFrag { bf16x8 q, k, v; };
template <u32 TR2> DI void wfrag_load(WFrag& f, u32 a01, u32 a2) { f.q = frag_tr(a01, a01 + TR2); f.k = frag_tr(a01 + 32768, a01 + 32768 + TR2); f.v = frag_tr(a2, a2 + TR2); }
template <u32 TR2> DI void qstep(f32x16& aq, f32x16& ak, f32x16& av, bf16x8 x, WFrag& cur, WFrag& nxt, u32 a01, u32 a2, bool more) {
    if (more) nxt.q = frag_tr(a01, a01 + TR2);
    SBAR(); aq = MFMA32(cur.q, x, aq); SBAR();
    if (more) nxt.k = frag_tr(a01 + 32768, a01 + 32768 + TR2);
    SBAR(); ak = MFMA32(cur.k, x, ak); SBAR();
    if (more) nxt.v = frag_tr(a2, a2 + TR2);
    SBAR(); av = MFMA32(x, cur.v, av); SBAR();
}

template <int L>
DI void phaseQ(Ctx& c0, const Params& p, u32& hsync, u32x4 (&pq)[12], const u32x4 (&xf)[32]) {
    Ctx c = c0; FRESH(c);
    const int lane = c.lane, w = c.w;
    const int rb = 4 * (c.xg & 3) + (c.gi & 3), hp = 8 * (c.xg >> 2) + (c.gi >> 2);
    const int r = lane & 31, h = lane >> 5;
    const int i16 = lane & 15, blk = (lane >> 4) & 1;
    const u32 trA = L_W + (u32)(((L == 0 ? 8 : 4) * h + (i16 >> 2)) * 64 + blk * 32 + (i16 & 3) * 8);
    constexpr u32 TR2 = L == 0 ? 256 : 512;
    constexpr u32 OQ = L ? OFF_Q1 : OFF_Q0, OK_ = L ? OFF_K1 : OFF_K0, OV = L ? OFF_V1 : OFF_V0, TS = L ? OFF_TS1 : OFF_TS0;
    const int rot = ((c.gi >> 2) + 2 * (c.gi & 3)) & 7;

    if (w >= 4) {
        const int hw = w - 4;
        if (L == 0) {
            u32x4 r0, r1; u32 ep;
            asm volatile("global_load_dword %0, %1, %2 sc1" : "=v"(ep) : "v"(0u), "s"(p.ws));
            const u32 tok = (u32)(rb * 128 + hp * 8 + hw * 2);
            const u32 vo = tok * 1024u + (u32)lane * 16u;
            asm volatile("global_load_dwordx4 %0, %1, %2" : "=v"(r0) : "v"(vo), "s"(p.x0));
            asm volatile("global_load_dwordx4 %0, %1, %2 offset:1024" : "=v"(r1) : "v"(vo), "s"(p.x0));
            const u32 wv = (u32)((lane >> 2) * 3072 + hp * 64 + (lane & 3) * 16 + hw * 16 * 3072);
            u32 xv[4];
#pragma unroll
            for (int q = 0; q < 4; ++q) { const int row = 8 * q + (lane >> 3); xv[q] = (u32)(((rb * 4 + hw) * 32 + row) * 1024 + (((lane & 7) ^ ((row >> 1) & 7)) * 16)); }
#define WD(i) do { const u32 kc_ = (u32)(((i) + rot) & 7); _Pragma("unroll") for (int t = 0; t < 3; ++t) \
                glds(p.wqkv, wv + (u32)t * 1024u + kc_ * (64u * 3072u), L_W + (u32)t * 32768u + (kc_ * 64u + (u32)hw * 16u) * 64u); } while (0)
#define XD(i) do { const u32 kc_ = (u32)(((i) + rot) & 7); _Pragma("unroll") for (int q = 0; q < 4; ++q) \
                glds(p.x0, xv[q] + kc_ * 128u, L_XR + (u32)((i) % 3) * 16384u + (u32)hw * 4096u + (u32)q * 1024u); } while (0)
#define QWAIT(n) asm volatile("s_waitcnt vmcnt(" #n ")\n\ts_barrier" ::: "memory")
#define CS(i) if (hw < 3) { _Pragma("unroll") for (int s_ = 0; s_ < 4; ++s_) { \
                const u32 a_ = trA + (u32)hw * 32768u + (u32)((((i) + rot) & 7) * 4 + s_) * 1024u; cs = MFMA32(frag_tr(a_, a_ + TR2), ones, cs); } }
            WD(0); XD(0); WD(1); XD(1); WD(2); XD(2); WD(3);
            FST(1);
            f32x16 cs = {};
            const bf16x8 ones = __builtin_bit_cast(bf16x8, (u32x4){0x3f803f80u, 0x3f803f80u, 0x3f803f80u, 0x3f803f80u});
            asm volatile("s_waitcnt vmcnt(17)\n\ts_barrier" : "+v"(r0), "+v"(r1), "+v"(ep) :: "memory");
            c.tag = epoch_tag(ep); c0.tag = c.tag;
            FST(2);
            {
                float s0 = 0, q0 = 0, s1 = 0, q1 = 0;
#pragma unroll
                for (int j = 0; j < 4; ++j) {
                    float a = bflo(r0[j]), b_ = bfhi(r0[j]); s0 += a + b_; q0 += a * a + b_ * b_;
                    float d = bflo(r1[j]), e = bfhi(r1[j]); s1 += d + e; q1 += d * d + e * e;
                }
                s0 = wave_sum(s0); q0 = wave_sum(q0); s1 = wave_sum(s1); q1 = wave_sum(q1);
                const float s = lane ? s1 : s0, q = lane ? q1 : q0;
                const float mu = s * (1.0f / 512.0f), var = fmaxf(q * (1.0f / 512.0f) - mu * mu, 0.f);
                const float A = __builtin_amdgcn_rsqf(var + 1e-5f);
                if (lane < 2) stw(c.ws, TS + (tok + (u32)lane) * 16u, mk_unit(__float_as_uint(A), __float_as_uint(A * mu), c.tag));
            }
            CS(0);
            QWAIT(11); FST(3); XD(3); WD(4); CS(1);
            QWAIT(11); FST(4); XD(4); WD(5); CS(2);
            QWAIT(10); FST(5); XD(5); WD(6); CS(3);
            QWAIT(10); FST(6); XD(6); WD(7); CS(4);
            QWAIT(10); FST(7); XD(7); CS(5);
            QWAIT(7); FST(8); CS(6);
            QWAIT(0); FST(9); CS(7);
#undef WD
#undef XD
#undef QWAIT
#undef CS
            if (hw < 3 && r == 0) {
#pragma unroll
                for (int g = 0; g < 4; ++g) lds_st<f32x4>(P_CS + (u32)(hw * 32 + 8 * g + 4 * h) * 4u, (f32x4){cs[4 * g], cs[4 * g + 1], cs[4 * g + 2], cs[4 * g + 3]});
            }
            STAMP(1);
            BAR();
        } else {
            ffn_chain<0>(c, p, hsync);
            STAMP(15);
        }
        DRAIN(); BAR();
        STAMP(L ? 16 : 3);
        if (L == 0) keep_wq(c, pq);
        else {
            wait_sel(0);
            const int sel = __builtin_amdgcn_readfirstlane((int)lds_ld<u32>(P_SEL)) & 2047;
            if ((sel >> 7) == rb) { lds_wait_ge(P_WQ, 8u); qkv_patch(c, hsync); }
        }
    } else {
        const int T = rb * 4 + w;
        f32x16 aq = {}, ak = {}, av = {};
        float A, B;
        if (L == 0) {
            const u32 ep = __builtin_amdgcn_raw_buffer_load_b32(c.ws, OFF_EPOCH, 0, 16);
            u32 xa[4];
#pragma unroll
            for (int s_ = 0; s_ < 4; ++s_) xa[s_] = L_XR + (u32)w * 4096u + (u32)(r * 128 + (((2 * s_ + h) ^ ((r >> 1) & 7)) * 16));
            bf16x8 xr[2][4];
            WFrag f0, f1;
            asm volatile("s_barrier" ::: "memory");
            FST(2);
#pragma unroll
            for (int s_ = 0; s_ < 4; ++s_) xr[0][s_] = lds_ld<bf16x8>(xa[s_]);
            { const u32 a01 = trA + (u32)(rot & 7) * 4096u; wfrag_load<TR2>(f0, a01, a01 + 65536u); }
#pragma unroll
            for (int i = 0; i < 8; ++i) {
                const u32 a01 = trA + (u32)((i + rot) & 7) * 4096u, a2 = a01 + 65536u;
                const u32 n01 = trA + (u32)((i + 1 + rot) & 7) * 4096u, n2 = n01 + 65536u;
                qstep<TR2>(aq, ak, av, xr[i & 1][0], f0, f1, a01 + 1024, a2 + 1024, true);
                qstep<TR2>(aq, ak, av, xr[i & 1][1], f1, f0, a01 + 2048, a2 + 2048, true);
                qstep<TR2>(aq, ak, av, xr[i & 1][2], f0, f1, a01 + 3072, a2 + 3072, true);
                if (i < 7) {
                    asm volatile("s_barrier" ::: "memory");
                    FST(3 + i);
#pragma unroll
                    for (int s_ = 0; s_ < 4; ++s_) xr[(i + 1) & 1][s_] = lds_ld<bf16x8>(xa[s_] + (u32)((i + 1) % 3) * 16384u);
                }
                qstep<TR2>(aq, ak, av, xr[i & 1][3], f1, f0, n01, n2, i < 7);
            }
            FST(10);
            STAMP(1);
            c.tag = epoch_tag(ep); c0.tag = c.tag;
            if (w == 3) {
                const u32x4 v = *(const u32x4*)(p.wr + lane * 8); float s = 0;
#pragma unroll
                for (int j = 0; j < 4; ++j) s += bflo(v[j]) + bfhi(v[j]);
                s = wave_sum(s);
                if (lane == 0) lds_st<float>(P_WSUM, s);
            }
            u32 off[1] = {TS + (u32)(T * 32 + r) * 16u}; u32x4 v[1];
            wait_units<1>(c, off, v);
            A = __uint_as_float(v[0][0]); B = __uint_as_float(v[0][2]);
            BAR();
            STAMP(2);
        } else {
            lds_wait_ge(P_WQ, 8u);
            STAMP(14);
            {
                u32 a01 = trA, a2 = trA + 65536u;
                asm volatile("" : "+v"(a01), "+v"(a2));
                WFrag f0, f1;
                wfrag_load<TR2>(f0, a01, a2);
#pragma unroll
                for (int st = 0; st < 32; st += 2) {
                    qstep<TR2>(aq, ak, av, __builtin_bit_cast(bf16x8, xf[st]), f0, f1, a01 + (u32)(st + 1) * 1024u, a2 + (u32)(st + 1) * 1024u, true);
                    qstep<TR2>(aq, ak, av, __builtin_bit_cast(bf16x8, xf[st + 1]), f1, f0, a01 + (u32)(st + 2) * 1024u, a2 + (u32)(st + 2) * 1024u, st + 2 < 32);
                }
            }
            u32 off[1] = {TS + (u32)(T * 32 + r) * 16u}; u32x4 v[1];
            wait_units<1>(c, off, v);
            A = __uint_as_float(v[0][0]); B = __uint_as_float(v[0][2]);
        }
        if (L == 1) STAMP(15);
        int ts = -1;
        if (L == 1) {
            wait_sel(0);
            const int sel = __builtin_amdgcn_readfirstlane((int)lds_ld<u32>(P_SEL)) & 2047;
            if ((sel >> 5) == T) ts = sel & 31;
        }
        const u32x4 pois = {POISON, POISON, POISON, POISON};
        const u32 tokl = L_TOK + (u32)w * 256u;
        if (h == 0) lds_st<f32x2>(tokl + (u32)r * 8u, (f32x2){A, B});
        constexpr float QS = 0.25f * 1.4426950408889634f;
#pragma unroll
        for (int t = 0; t < 2; ++t) {
            const f32x16& acc = t ? ak : aq;
            const float sA = t ? A : A * QS, sB = t ? B : B * QS;
            float o[16];
#pragma unroll
            for (int g = 0; g < 4; ++g) {
                const f32x4 cs = lds_ld<f32x4>(P_CS + (u32)(t * 32 + 8 * g + 4 * h) * 4u);
#pragma unroll
                for (int e = 0; e < 4; ++e) o[4 * g + e] = sA * acc[4 * g + e] - sB * cs[e];
            }
#pragma unroll
            for (int a = 0; a < 2; ++a) {
                u32x4 v = {pk(o[8 * a], o[8 * a + 1]), pk(o[8 * a + 2], o[8 * a + 3]), pk(o[8 * a + 4], o[8 * a + 5]), pk(o[8 * a + 6], o[8 * a + 7])};
                if (L == 1 && r == ts) v = pois;
                stw(c.ws, (t ? OK_ : OQ) + (u32)(((hp * 2 + a) * 64 + T) * 64 + lane) * 16u, v);
            }
        }
        u32x4 vlate = pois;
        const u32 vbase = OV + (u32)(((hp * 2 + (r >> 4)) * 64 + T) * 1024 + h * 256 + (r & 15) * 16);
        {
            asm volatile("s_waitcnt lgkmcnt(0)" ::: "memory");
            const float cs = lds_ld<float>(P_CS + (u32)(64 + r) * 4u);
            float o[16];
#pragma unroll
            for (int g = 0; g < 4; ++g) {
                const f32x4 ab0 = lds_ld<f32x4>(tokl + (u32)(8 * g + 4 * h) * 8u), ab1 = lds_ld<f32x4>(tokl + (u32)(8 * g + 4 * h + 2) * 8u);
                o[4 * g] = ab0[0] * av[4 * g] - ab0[1] * cs; o[4 * g + 1] = ab0[2] * av[4 * g + 1] - ab0[3] * cs;
                o[4 * g + 2] = ab1[0] * av[4 * g + 2] - ab1[1] * cs; o[4 * g + 3] = ab1[2] * av[4 * g + 3] - ab1[3] * cs;
            }
#pragma unroll
            for (int s = 0; s < 2; ++s) {
                u32x4 v = {pk(o[8 * s], o[8 * s + 1]), pk(o[8 * s + 2], o[8 * s + 3]), pk(o[8 * s + 4], o[8 * s + 5]), pk(o[8 * s + 6], o[8 * s + 7])};
                if (L == 1 && ts >= 0 && s == (ts >> 4) && h == ((ts >> 2) & 1)) { vlate = v; v = pois; }
                stw(c.ws, vbase + (u32)s * 512u, v);
            }
        }
        DRAIN(); BAR();
        STAMP(L ? 16 : 3);
        if (w == 0 && lane == 0) set_flag(c, (L ? OFF_FQ1 : OFF_FQ0) + (u32)(hp * 16 + rb) * 128u);
        if (L == 1 && ts >= 0) {
            lds_wait_ge(P_PATCH, 1u);
            if (r == ts) {
#pragma unroll
                for (int t = 0; t < 2; ++t)
#pragma unroll
                    for (int a = 0; a < 2; ++a) {
                        const f32x4 p0 = lds_ld<f32x4>(FS0 + 4608 + (u32)(t * 32 + 16 * a + 4 * h) * 4u), p1 = lds_ld<f32x4>(FS0 + 4608 + (u32)(t * 32 + 16 * a + 8 + 4 * h) * 4u);
                        stw(c.ws, (t ? OK_ : OQ) + (u32)(((hp * 2 + a) * 64 + T) * 64 + lane) * 16u, not_poison((u32x4){pk(p0[0], p0[1]), pk(p0[2], p0[3]), pk(p1[0], p1[1]), pk(p1[2], p1[3])}));
                    }
            }
            if (h == ((ts >> 2) & 1)) {
                const u32 pv = pk(lds_ld<float>(FS0 + 4608 + (u32)(64 + r) * 4u), 0.f) & 0xFFFFu;
                const int jj = (ts & 3) + 4 * ((ts >> 3) & 1);
#pragma unroll
                for (int k = 0; k < 4; ++k) if (k == (jj >> 1)) vlate[k] = (jj & 1) ? ((vlate[k] & 0xFFFFu) | (pv << 16)) : ((vlate[k] & 0xFFFF0000u) | pv);
                stw(c.ws, vbase + (u32)(ts >> 4) * 512u, not_poison(vlate));
            }
        }
        if (L == 0) keep_wq(c, pq);
    }
}

struct AState { f32x16 Sa, Sb, Oa, Ob; bf16x8 kC, kN, v0, v1; u32x4 pb1; };
#define EXP2(x) __builtin_amdgcn_exp2f(x)
#define BC8(x) __builtin_bit_cast(bf16x8, x)
#define PVM(O, v, p) O = MFMA32(v, BC8(p), O)
template <bool USE_C, class HOOK>
DI void attn_step(AState& s, bf16x8 qa, bf16x8 qb, const f32x16& ca, const f32x16& cb, u32 kp, u32 vp0, u32 vp1, u32 knext, u32 voff, HOOK hook) {
    const f32x16 zero = {};
    u32x4 pa0, pa1, pb0;
    s.kN = lds_ld<bf16x8>(kp + knext); SBAR();
    s.v0 = lds_ld<bf16x8>(vp0 + voff); SBAR();
    PVM(s.Ob, s.v1, s.pb1); SBAR();
    s.v1 = lds_ld<bf16x8>(vp1 + voff); SBAR();
    float a0 = EXP2(s.Sa[0]); SBAR(); float a1 = EXP2(s.Sa[1]); SBAR(); float a2 = EXP2(s.Sa[2]); SBAR(); float a3 = EXP2(s.Sa[3]); SBAR();
    s.Sb = MFMA32(s.kC, qb, USE_C ? cb : zero); SBAR();
    float a4 = EXP2(s.Sa[4]); SBAR(); float a5 = EXP2(s.Sa[5]); SBAR(); float a6 = EXP2(s.Sa[6]); SBAR(); float a7 = EXP2(s.Sa[7]); SBAR();
    pa0[0] = pk(a0, a1); SBAR(); pa0[1] = pk(a2, a3); SBAR(); float a8 = EXP2(s.Sa[8]); SBAR(); pa0[2] = pk(a4, a5); SBAR(); pa0[3] = pk(a6, a7); SBAR();
    float a9 = EXP2(s.Sa[9]); SBAR();
    asm volatile("" : "+v"(s.v0), "+v"(s.v1), "+v"(s.kN)); SBAR();
    float a10 = EXP2(s.Sa[10]); SBAR();
    PVM(s.Oa, s.v0, pa0); SBAR();
    float a11 = EXP2(s.Sa[11]); SBAR(); float a12 = EXP2(s.Sa[12]); SBAR(); float a13 = EXP2(s.Sa[13]); SBAR(); float a14 = EXP2(s.Sa[14]); SBAR(); float a15 = EXP2(s.Sa[15]); SBAR();
    pa1[0] = pk(a8, a9); SBAR(); pa1[1] = pk(a10, a11); SBAR(); pa1[2] = pk(a12, a13); SBAR(); pa1[3] = pk(a14, a15); SBAR();
    float b0 = EXP2(s.Sb[0]); SBAR(); float b1 = EXP2(s.Sb[1]); SBAR();
    PVM(s.Oa, s.v1, pa1); SBAR();
    hook(); SBAR();
    float b2 = EXP2(s.Sb[2]); SBAR(); float b3 = EXP2(s.Sb[3]); SBAR(); float b4 = EXP2(s.Sb[4]); SBAR(); float b5 = EXP2(s.Sb[5]); SBAR();
    s.Sa = MFMA32(s.kN, qa, USE_C ? ca : zero); SBAR();
    float b6 = EXP2(s.Sb[6]); SBAR(); float b7 = EXP2(s.Sb[7]); SBAR();
    pb0[0] = pk(b0, b1); SBAR(); pb0[1] = pk(b2, b3); SBAR(); float b8 = EXP2(s.Sb[8]); SBAR(); pb0[2] = pk(b4, b5); SBAR(); pb0[3] = pk(b6, b7); SBAR();
    float b9 = EXP2(s.Sb[9]); SBAR(); float b10 = EXP2(s.Sb[10]); SBAR();
    PVM(s.Ob, s.v0, pb0); SBAR();
    float b11 = EXP2(s.Sb[11]); SBAR(); float b12 = EXP2(s.Sb[12]); SBAR(); float b13 = EXP2(s.Sb[13]); SBAR(); float b14 = EXP2(s.Sb[14]); SBAR(); float b15 = EXP2(s.Sb[15]); SBAR();
    s.pb1[0] = pk(b8, b9); SBAR(); s.pb1[1] = pk(b10, b11); SBAR(); s.pb1[2] = pk(b12, b13); SBAR(); s.pb1[3] = pk(b14, b15); SBAR();
    s.kC = s.kN;
}
struct NoHook { DI void operator()() const {} };
template <bool USE_C>
DI void attn_chunk(AState& s, bf16x8 qa, bf16x8 qb, const f32x16& ca, const f32x16& cb, u32 kp, u32 vp0, u32 vp1) {
    attn_step<USE_C>(s, qa, qb, ca, cb, kp, vp0, vp1, 1024, 0, NoHook());
    attn_step<USE_C>(s, qa, qb, ca, cb, kp, vp0, vp1, 2048, VSTR, NoHook());
    attn_step<USE_C>(s, qa, qb, ca, cb, kp, vp0, vp1, 3072, 2 * VSTR, NoHook());
    attn_step<USE_C>(s, qa, qb, ca, cb, kp, vp0, vp1, 8192, 3 * VSTR, NoHook());
}
DI void attn_begin(AState& s, bf16x8 qa, const f32x16& ca, u32 kp) {
    const f32x16 zero = {};
    s.Oa = zero; s.Ob = zero; s.pb1 = (u32x4){0u, 0u, 0u, 0u}; s.v1 = BC8(s.pb1);
    s.kC = lds_ld<bf16x8>(kp);
    s.Sa = MFMA32(s.kC, qa, ca);
}
DI void attn_combine(const Ctx& c, AState& s, float (&O)[9]) {
    const int kh = c.w >> 2;
    s.Ob = MFMA32(s.v1, BC8(s.pb1), s.Ob);
    const f32x16& snd = kh ? s.Oa : s.Ob; const f32x16& keep = kh ? s.Ob : s.Oa;
    const u32 xb = L_AX + (u32)c.w * 2304u;
    lds_st<f32x4>(xb + (u32)c.lane * 16u, (f32x4){snd[0], snd[1], snd[2], snd[3]});
    lds_st<f32x4>(xb + 1024u + (u32)c.lane * 16u, (f32x4){snd[4], snd[5], snd[6], snd[7]});
    lds_st<float>(xb + 2048u + (u32)c.lane * 4u, snd[8]);
    BAR();
    const u32 xp = L_AX + (u32)(c.w ^ 4) * 2304u;
    const f32x4 u0 = lds_ld<f32x4>(xp + (u32)c.lane * 16u), u1 = lds_ld<f32x4>(xp + 1024u + (u32)c.lane * 16u);
    const float l2 = lds_ld<float>(xp + 2048u + (u32)c.lane * 4u);
#pragma unroll
    for (int q = 0; q < 4; ++q) { O[q] = keep[q] + u0[q]; O[4 + q] = keep[4 + q] + u1[q]; }
    O[8] = keep[8] + l2;
}

template <int L>
DI void phaseA(const Ctx& c0, const Params& p) {
    Ctx c = c0; FRESH(c);
    u32x4 pf[7] = {}, po[8];
    const int lane = c.lane, w = c.w, tid = w * 64 + lane;
    const int head = 4 * c.xg + (c.gi >> 3), qb = c.gi & 7;
    const int j = w & 3, kh = w >> 2;
    constexpr u32 OQ = L ? OFF_Q1 : OFF_Q0, OK_ = L ? OFF_K1 : OFF_K0, OV = L ? OFF_V1 : OFF_V0, OO = L ? OFF_O1 : OFF_O0;
    if (tid == 0) lds_st<u32>(L_AX + 8 * 2304u, 0u);
    if (w == 0) {
        wait_flags(c, (L ? OFF_FQ1 : OFF_FQ0) + (u32)((head >> 1) * 16 + (lane & 15)) * 128u);
        if (L == 1) {
            const int sel = __builtin_amdgcn_readfirstlane((int)lds_ld<u32>(P_SEL)) & 2047, T = sel >> 5, ts = sel & 31;
            const u32 tk = (u32)((head * 64 + T) * 64 + (lane & 1) * 32 + ts) * 16u;
            u32 off = OK_ + tk;
            if (lane >= 2 && lane < 18) off = OV + (u32)((head * 64 + T) * 1024 + (ts >> 4) * 512 + ((ts >> 2) & 1) * 256 + (lane - 2) * 16);
            if (lane >= 18 && lane < 20 && (T >> 3) == qb) off = OQ + tk;
            u32x4 v = ldw(c.ws, off);
            wait_piece(c, off, v);
        }
    }
    if (L == 0 && w == 1) {
        const int qo = (4 * c.xg + (c.gi >> 3)) >> 2, hd = lane & 31, xg = hd >> 2, gi = (hd & 3) * 8 + qo;
        wait_flags(c, OFF_FQ0 + (u32)((8 * (xg >> 2) + (gi >> 2)) * 16 + 4 * (xg & 3) + (gi & 3)) * 128u);
    }
    BAR();
    STAMP(L ? 17 : 4);
    CSTAMP(9);
    const u32 koff = OK_ + (u32)((head * 64 + w) * 64 + lane) * 16u, voff = OV + (u32)((head * 64 + w) * 64 + lane) * 16u;
    u32x4 rk[8], rv[8];
    rk[0] = ldw(c.ws, koff); rv[0] = ldw(c.ws, voff);
    const u32 qo = OQ + (u32)((head * 64 + qb * 8 + 2 * j) * 64 + lane) * 16u;
    const u32x4 qar = ldw(c.ws, qo), qbr = ldw(c.ws, qo + 1024u);
    SBAR(); __builtin_amdgcn_s_barrier(); SBAR();
    rk[1] = ldw(c.ws, koff + 8192u); rv[1] = ldw(c.ws, voff + 8192u); rk[2] = ldw(c.ws, koff + 16384u); rv[2] = ldw(c.ws, voff + 16384u);
    SBAR(); __builtin_amdgcn_s_barrier(); SBAR();
    rk[3] = ldw(c.ws, koff + 24576u); rv[3] = ldw(c.ws, voff + 24576u); rk[4] = ldw(c.ws, koff + 32768u); rv[4] = ldw(c.ws, voff + 32768u);
    SBAR();
    { const int t = tid >> 3, s = tid & 7; const u32 val = (s & 3) == 0 ? 0x3f803f80u : 0u;
      lds_st<u32x4>(L_V + (u32)t * VSTR + 1024u + (u32)s * 16u, (u32x4){val, val, val, val}); }
    const u32 kw = L_K + (u32)w * 1024u + (u32)lane * 16u, vw = L_V + (u32)w * VSTR + (u32)lane * 16u;
    lds_st<u32x4>(kw, rk[0]); lds_st<u32x4>(vw, rv[0]);
    lds_st<u32x4>(kw + 8192u, rk[1]); lds_st<u32x4>(vw + 8u * VSTR, rv[1]);
    CSTAMP(10);
    BAR();
    const int r = lane & 31, h = lane >> 5;
    const u32 kp0 = L_K + (u32)kh * 4096u + (u32)lane * 16u;
    const u32 onesoff = 1024u + ((r >= 20 && r < 28) ? 64u : 0u) + ((r == 16 || r == 20) ? 0u : 16u);
    const u32 vp00 = L_V + (u32)kh * (4u * VSTR) + (r < 16 ? (u32)(h * 256 + r * 16) : onesoff);
    const u32 vp10 = L_V + (u32)kh * (4u * VSTR) + (r < 16 ? (u32)(512 + h * 256 + r * 16) : onesoff);
    const bf16x8 qa = BC8(qar), qbf = BC8(qbr);
    const f32x16 cz = {};
    AState s;
    STAMP(L ? 18 : 5);
#define PRIO(n) __builtin_amdgcn_s_setprio(n)
    PRIO(3);
    CSTAMP(0);
    attn_begin(s, qa, cz, kp0);
    {
#define PUT(ch) [&]() { lds_st<u32x4>(kw + (u32)(ch) * 8192u, rk[ch]); lds_st<u32x4>(vw + (u32)(ch) * (8u * VSTR), rv[ch]); }
#define GET(ch) [&]() { rk[ch] = ldw(c.ws, koff + (u32)(ch) * 8192u); rv[ch] = ldw(c.ws, voff + (u32)(ch) * 8192u); }
#define GETPUT(g, ch) [&]() { GET(g)(); PUT(ch)(); }
#define FF(j_) [&]() { if (ff.on && ((j_) < 4 || !ff.gu)) pf[j_] = gld(ff.m, ff.vo + (u32)(j_) * ff.step); }
#define WO(j_) [&]() { po[j_] = gld(p.wo, wv + (u32)(j_) * 16384u); }
#define ST(ch, t, HOOK) attn_step<false>(s, qa, qbf, cz, cz, kp, vp0, vp1, ((t) == 3 ? 8192 : ((t) + 1) * 1024) + ((ch) & 1) * 8192, (((ch) & 1) * 8 + (t)) * VSTR, HOOK)
#define NEXT2() do { kp += 16384; vp0 += 16 * VSTR; vp1 += 16 * VSTR; asm volatile("" : "+v"(kp), "+v"(vp0), "+v"(vp1)); } while (0)
        u32 kp = kp0, vp0 = vp00, vp1 = vp10;
        ST(0, 0, GET(5)); ST(0, 1, GETPUT(6, 2)); ST(0, 2, GETPUT(7, 3)); ST(0, 3, PUT(4));
        CSTAMP(1);
        ST(1, 0, PUT(5)); ST(1, 1, PUT(6)); ST(1, 2, PUT(7));
        BAR();
        ST(1, 3, NoHook());
        const FfSrc ff = ff_src(c, p);
        NEXT2(); PRIO(2);
        CSTAMP(2);
        ST(2, 0, FF(0)); ST(2, 1, FF(1)); ST(2, 2, FF(2)); ST(2, 3, FF(3));
        CSTAMP(3);
        ST(3, 0, FF(4)); ST(3, 1, FF(5)); ST(3, 2, FF(6)); ST(3, 3, NoHook());
        const u32 wv = wo_lane_off(c);
        NEXT2(); PRIO(1);
        CSTAMP(4);
        ST(4, 0, WO(0)); ST(4, 1, WO(1)); ST(4, 2, WO(2)); ST(4, 3, WO(3));
        CSTAMP(5);
        ST(5, 0, WO(4)); ST(5, 1, WO(5)); ST(5, 2, WO(6)); ST(5, 3, WO(7));
        NEXT2(); PRIO(0);
        CSTAMP(6);
        for (int ch = 6; ch < 8; ++ch) {
            attn_chunk<false>(s, qa, qbf, cz, cz, kp, vp0, vp1);
            kp += 8192; vp0 += 8 * VSTR; vp1 += 8 * VSTR;
            asm volatile("" : "+v"(kp), "+v"(vp0), "+v"(vp1));
        }
#undef PUT
#undef GET
#undef GETPUT
#undef FF
#undef WO
#undef ST
#undef NEXT2
    }
    CSTAMP(8);
    STAMP(L ? 19 : 6);
    FRESH(c);
    float O[9];
    attn_combine(c, s, O);
    const u32 oo = OO + (u32)((head * 64 + qb * 8 + 2 * j + kh) * 64 + c.lane) * 16u;
    bool redo;
    {
        bool bad = !(O[8] >= 0x1p-60f && O[8] <= 0x1p60f);
#pragma unroll
        for (int q = 0; q < 8; ++q) bad = bad || !(__builtin_fabsf(O[q]) <= 0x1p126f);
        redo = __any(bad);
        if (redo) { if (c.lane == 0) lds_st<u32>(L_AX + 8 * 2304u, 1u); }
        else {
            const float rl = __builtin_amdgcn_rcpf(O[8]);
            u32x4 o;
#pragma unroll
            for (int q = 0; q < 4; ++q) o[q] = pk(O[2 * q] * rl, O[2 * q + 1] * rl);
            stw(c.ws, oo, not_poison(o));
        }
    }
    BAR();
    if (__builtin_amdgcn_readfirstlane((int)lds_ld<u32>(L_AX + 8 * 2304u)) != 0) {
        float ma = -INFINITY, mb = -INFINITY;
        for (int ch = 0; ch < 8; ++ch)
            for (int t = 0; t < 4; ++t) {
                const bf16x8 kf = lds_ld<bf16x8>(kp0 + (u32)(ch * 8192 + t * 1024));
                const f32x16 Sa = MFMA32(kf, qa, cz), Sb = MFMA32(kf, qbf, cz);
#pragma unroll
                for (int q = 0; q < 16; ++q) { ma = fmaxf(ma, Sa[q]); mb = fmaxf(mb, Sb[q]); }
            }
        ma = fmaxf(ma, __shfl_xor(ma, 32)); mb = fmaxf(mb, __shfl_xor(mb, 32));
        const u32 xb = L_AX + (u32)w * 2304u, xp = L_AX + (u32)(w ^ 4) * 2304u;
        lds_st<f32x2>(xb + (u32)c.lane * 8u, (f32x2){ma, mb});
        BAR();
        const f32x2 mo = lds_ld<f32x2>(xp + (u32)c.lane * 8u);
        ma = fmaxf(ma, mo[0]); mb = fmaxf(mb, mo[1]);
        BAR();
        f32x16 ca, cb;
#pragma unroll
        for (int q = 0; q < 16; ++q) { ca[q] = -ma; cb[q] = -mb; }
        attn_begin(s, qa, ca, kp0);
        u32 kp = kp0, vp0 = vp00, vp1 = vp10;
        for (int ch = 0; ch < 8; ++ch) {
            attn_chunk<true>(s, qa, qbf, ca, cb, kp, vp0, vp1);
            kp += 8192; vp0 += 8 * VSTR; vp1 += 8 * VSTR;
        }
        attn_combine(c, s, O);
        if (redo) {
            const float rl = 1.0f / O[8];
            u32x4 o;
#pragma unroll
            for (int q = 0; q < 4; ++q) o[q] = pk(O[2 * q] * rl, O[2 * q + 1] * rl);
            stw(c.ws, oo, not_poison(o));
        }
        BAR();
    }
    STAMP(L ? 20 : 7);
    pre_store_wo(c, po);
    pre_store_ff(c, pf);
}

template <int L>
DI void phaseO(const Ctx& c0, const Params& p, u32x4 (&x2)[2], u32& hsync, const u32x4 (&pq)[12], u32x4 (&xf)[32]) {
    Ctx c = c0; FRESH(c);
    const int lane = c.lane, w = c.w;
    const int tb = 4 * c.xg + (c.gi >> 3), fb = c.gi & 7;
    const int r = lane & 31, h = lane >> 5;
    constexpr u32 OO = L ? OFF_O1 : OFF_O0, GS = L ? OFF_GS1 : OFF_GS0, GC = L ? OFF_GC1 : OFF_GC0, GM = L ? OFF_GM1 : OFF_GM0, GX = L ? OFF_GX1 : OFF_GX0;
    constexpr u32 L_TS = L_SB, L_CAND = P_ST + 8;
    if (w >= 4) {
        const int hw = w - 4;
        BAR();
        STAMP(L ? 21 : 8);
        BAR();
        BAR();
        __builtin_amdgcn_s_setprio(3);
        FST(30 + 20 * L);
        if (hw == 0) {
            const u32 a = L_SB + (u32)h * 384u + (u32)r * 12u;
            const float t1 = lds_ld<float>(a) + lds_ld<float>(a + 768), t2 = lds_ld<float>(a + 4) + lds_ld<float>(a + 772), t3 = lds_ld<float>(a + 8) + lds_ld<float>(a + 776);
            stw(c.ws, GS + (u32)((tb * 8 + fb) * 1024 + lane * 16), (u32x4){__float_as_uint(t1), __float_as_uint(t2), __float_as_uint(t3), c.tag});
        }
        FST(31 + 20 * L);
        if (L == 0) {
            if (hw != 0) { pre_store_wq(c, pq); lds_signal_add(c, P_WQ); }
            BAR();
        }
        FST(32 + 20 * L);
        if (hw == 0) {
            u32x4 v[8]; u32 n = 0;
            for (;;) {
                bool ok = true;
#pragma unroll
                for (int k = 0; k < 8; ++k) v[k] = ldw(c.ws, GS + (u32)((tb * 8 + k) * 1024 + lane * 16));
                MEMFENCE();
#pragma unroll
                for (int k = 0; k < 8; ++k) ok = ok && v[k][3] == c.tag;
                if (__all(ok)) break;
                if (spin_fail(c, n)) break;
            }
            FST(33 + 20 * L);
            float t1 = 0, t2 = 0, t3 = 0;
#pragma unroll
            for (int k = 0; k < 8; ++k) { t1 += __uint_as_float(v[k][0]); t2 += __uint_as_float(v[k][1]); t3 += __uint_as_float(v[k][2]); }
            const float mu = t1 * (1.0f / 512.0f), var = fmaxf(t2 * (1.0f / 512.0f) - mu * mu, 0.f);
            const float rs = __builtin_amdgcn_rsqf(var + 1e-5f);
            const float rt = rbf(rs * (t3 - mu * lds_ld<float>(P_WSUM)));
            const u32 key = half_umax(score_key(__float_as_uint(rt) >> 16, (u32)r));
            const int bt = (int)key_idx(key);
            if (fb == 0) {
                const u32 k0 = (u32)__builtin_amdgcn_readlane((int)key, 0), k1 = (u32)__builtin_amdgcn_readlane((int)key, 32);
                const u32 kT = (lane & 1) ? k1 : k0;
                const u64 g = (cand_tag(c.tag) << 21) | (u64)(key_bf(kT) << 5) | (u64)key_idx(kT);
                if (lane < 16) __builtin_amdgcn_raw_buffer_store_b64((u32x2){(u32)g, (u32)(g >> 32)}, c.ws, GC + (u32)(lane >> 1) * 512u + (u32)(tb * 2 + (lane & 1)) * 8u, 0, 16);
                if (r == bt) stw(c.ws, GM + (u32)(tb * 2 + h) * 16u, mk_unit(__float_as_uint(mu), __float_as_uint(rs), c.tag));
                if (L == 0) {
                    const float g1 = 1.0f + rs, A = g1 * __builtin_amdgcn_rsqf(var * g1 * g1 + 1e-5f);
                    stw(c.ws, OFF_TS1 + (u32)(tb * 64 + lane) * 16u, mk_unit(__float_as_uint(A), __float_as_uint(A * mu), c.tag));
                }
            }
            FST(34 + 20 * L);
            lds_st<f32x2>(L_TS + (u32)lane * 8u, (f32x2){mu, rs});
            if (r == 0) lds_st<u32>(L_CAND + (u32)h * 4u, (u32)bt);
            lds_signal_add(c, P_ST + L * 4);
            {
                const int ct = (lane >> 4) & 1, u = lane & 15;
                const int cb = __builtin_amdgcn_readlane(bt, 0), cb1 = __builtin_amdgcn_readlane(bt, 32);
                const int row = ct ? cb1 : cb;
                const u32x2 d = lds_ld<u32x2>(L_X1T + (u32)(ct + 2 * (u >> 3)) * 2048u + (u32)((u >> 2) & 1) * 1024u + (u32)((u & 1) * 32 + row) * 16u + (u32)((u >> 1) & 1) * 8u);
                if (lane < 32) stw(c.ws, GX + (u32)(tb * 2 + ct) * 2048u + (u32)(fb * 16 + u) * 16u, mk_unit(d[0], d[1], c.tag));
            }
            if (L == 0) { pre_store_wq(c, pq); lds_signal_add(c, P_WQ); }
        }
        STAMP(L ? 23 : 12);
        if (L == 1) ffn_chain<1>(c, p, hsync);
        return;
    }
    const int tt = w & 1, kh = w >> 1;
    const int tile = tb * 2 + tt, tok = tile * 32 + r;
    const int f0 = fb * 64 + kh * 32;
    u32x2 res[4], wr[4];
#pragma unroll
    for (int g = 0; g < 4; ++g) {
        if (L == 0) res[g] = *(const u32x2*)(p.x0 + (size_t)tok * 512 + f0 + 8 * g + 4 * h);
        wr[g] = *(const u32x2*)(p.wr + f0 + 8 * g + 4 * h);
    }
    if (L == 1) {
        const int sel0 = __builtin_amdgcn_readfirstlane((int)lds_ld<u32>(P_SEL)) & 2047;
        if ((sel0 >> 5) == tile) {
            const u32 ub = OFF_GY + (u32)(f0 / 4 + h) * 16u;
            u32 off[4] = {ub, ub + 32u, ub + 64u, ub + 96u}; u32x4 v[4];
            wait_units<4>(c, off, v);
            if (r == (sel0 & 31)) { x2[0] = (u32x4){v[0][0], v[0][2], v[1][0], v[1][2]}; x2[1] = (u32x4){v[2][0], v[2][2], v[3][0], v[3][2]}; }
        }
#pragma unroll
        for (int g = 0; g < 4; ++g) res[g] = (u32x2){x2[g >> 1][(g & 1) * 2], x2[g >> 1][(g & 1) * 2 + 1]};
    }
    BAR();
    STAMP(L ? 21 : 8);
    u32x4 bf[16];
    const u32 oo = OO + (u32)(((kh * 16) * 64 + tile) * 64 + lane) * 16u;
    {
        u32 n = 0;
        for (;;) {
#pragma unroll
            for (int s = 0; s < 16; ++s) bf[s] = ldw(c.ws, oo + (u32)s * 65536u);
            MEMFENCE();
            u32 m = 0;
#pragma unroll
            for (int s = 0; s < 16; ++s) m = max(m, max(max(bf[s][0], bf[s][1]), max(bf[s][2], bf[s][3])));
            if (!__any(m == POISON)) break;
            if (spin_fail(c, n)) break;
        }
    }
    const int i16 = lane & 15, blk = (lane >> 4) & 1;
    const u32 trA = L_WO + (u32)((kh * 256 + 4 * h + (i16 >> 2)) * 64 + blk * 32 + (i16 & 3) * 8);
    f32x16 a0 = {}, a1 = {};
    {
        u32 ta = trA, tb_ = trA + WO_FT;
        asm volatile("" : "+v"(ta), "+v"(tb_));
        bf16x8 f0_[2], f1_[2];
        f0_[0] = frag_tr(ta, ta + 512); f0_[1] = frag_tr(tb_, tb_ + 512);
#pragma unroll
        for (int s = 0; s < 16; s += 2) {
            f1_[0] = frag_tr(ta + (u32)(s + 1) * 1024u, ta + (u32)(s + 1) * 1024u + 512); SBAR();
            a0 = MFMA32(f0_[0], BC8(bf[s]), a0); SBAR();
            f1_[1] = frag_tr(tb_ + (u32)(s + 1) * 1024u, tb_ + (u32)(s + 1) * 1024u + 512); SBAR();
            a1 = MFMA32(f0_[1], BC8(bf[s]), a1); SBAR();
            if (s + 2 < 16) f0_[0] = frag_tr(ta + (u32)(s + 2) * 1024u, ta + (u32)(s + 2) * 1024u + 512);
            SBAR(); a0 = MFMA32(f1_[0], BC8(bf[s + 1]), a0); SBAR();
            if (s + 2 < 16) f0_[1] = frag_tr(tb_ + (u32)(s + 2) * 1024u, tb_ + (u32)(s + 2) * 1024u + 512);
            SBAR(); a1 = MFMA32(f1_[1], BC8(bf[s + 1]), a1); SBAR();
        }
    }
    {
        const f32x16& snd = kh ? a0 : a1;
#pragma unroll
        for (int g = 0; g < 4; ++g) lds_st<f32x4>(L_XB + (u32)w * 4096u + (u32)g * 1024u + (u32)lane * 16u, (f32x4){snd[4 * g], snd[4 * g + 1], snd[4 * g + 2], snd[4 * g + 3]});
    }
    BAR();
    STAMP(L ? 22 : 9);
    u32x4 xp[2];
    {
        const f32x16& keep = kh ? a1 : a0;
#pragma unroll
        for (int g = 0; g < 4; ++g) {
            const f32x4 o = lds_ld<f32x4>(L_XB + (u32)(w ^ 2) * 4096u + (u32)g * 1024u + (u32)lane * 16u);
            const u32 t0 = pk(keep[4 * g] + o[0], keep[4 * g + 1] + o[1]), t1 = pk(keep[4 * g + 2] + o[2], keep[4 * g + 3] + o[3]);
            xp[g >> 1][(g & 1) * 2] = pk(bflo(res[g][0]) + bflo(t0), bfhi(res[g][0]) + bfhi(t0));
            xp[g >> 1][(g & 1) * 2 + 1] = pk(bflo(res[g][1]) + bflo(t1), bfhi(res[g][1]) + bfhi(t1));
        }
    }
    if (L == 0) {
#pragma unroll
        for (int s = 0; s < 2; ++s) stw(c.ws, OFF_X1 + (u32)((tile * 32 + f0 / 16 + s) * 64 + lane) * 16u, xp[s]);
    }
#pragma unroll
    for (int s = 0; s < 2; ++s) lds_st<u32x4>(L_X1T + (u32)w * 2048u + (u32)s * 1024u + (u32)lane * 16u, xp[s]);
    float x1[16];
    {
        float s1 = 0, s2 = 0, s3 = 0;
#pragma unroll
        for (int i = 0; i < 16; ++i) {
            const u32 wd = xp[i >> 3][(i & 7) >> 1];
            x1[i] = (i & 1) ? bfhi(wd) : bflo(wd);
            const u32 ww = wr[i >> 2][(i & 3) >> 1];
            const float wv = (i & 1) ? bfhi(ww) : bflo(ww);
            s1 += x1[i]; s2 += x1[i] * x1[i]; s3 += x1[i] * wv;
        }
        s1 = swap32_add(s1); s2 = swap32_add(s2); s3 = swap32_add(s3);
        if (h == 0) { const u32 a = L_SB + (u32)w * 384u + (u32)r * 12u; lds_st<float>(a, s1); lds_st<float>(a + 4, s2); lds_st<float>(a + 8, s3); }
    }
    BAR();
    if (L == 0) {
        DRAIN(); BAR();
        STAMP(10);
        if (w == 0 && lane == 0) set_flag(c, OFF_FO + (u32)(tb * 8 + fb) * 128u);
        pre_store_wq(c, pq); lds_signal_add(c, P_WQ);
        const int rbq = 4 * (c.xg & 3) + (c.gi & 3);
        wait_flags(c, OFF_FO + (u32)(rbq * 16 + (lane & 15)) * 128u);
        STAMP(13);
    }
    lds_wait_ge(P_ST + L * 4, 1u);
    STAMP(L ? 23 : 11);
    const f32x2 st = lds_ld<f32x2>(L_TS + (u32)(tt * 32 + r) * 8u);
    const int bt = (int)lds_ld<u32>(L_CAND + (u32)tt * 4u) & 31;
#pragma unroll
    for (int s = 0; s < 2; ++s)
#pragma unroll
        for (int j = 0; j < 4; ++j) {
            const float a = x1[8 * s + 2 * j], b_ = x1[8 * s + 2 * j + 1];
            const u32 hh = pk((a - st[0]) * st[1], (b_ - st[0]) * st[1]);
            x2[s][j] = pk(a + bflo(hh), b_ + bfhi(hh));
        }
    if (L == 0) {
        const int rbq = 4 * (c.xg & 3) + (c.gi & 3);
        const u32 xo = OFF_X1 + (u32)(rbq * 4 + w) * 32768u + (u32)lane * 16u;
#pragma unroll
        for (int s = 0; s < 32; ++s) xf[s] = ldw(c.ws, xo + (u32)s * 1024u);
    }
    if (L == 1) {
        const u32 tl = L_XB + (u32)w * 4096u;
#pragma unroll
        for (int g = 0; g < 4; ++g) lds_st<u32x2>(tl + (u32)(r * 80 + (8 * g + 4 * h) * 2), (u32x2){x2[g >> 1][(g & 1) * 2], x2[g >> 1][(g & 1) * 2 + 1]});
        asm volatile("s_waitcnt lgkmcnt(0)" ::: "memory");
        u32x4 rowv[2];
#pragma unroll
        for (int q = 0; q < 2; ++q) {
            const int row = q * 16 + (lane >> 2);
            rowv[q] = lds_ld<u32x4>(tl + (u32)(row * 80 + (lane & 3) * 16));
            if (row != bt) *(u32x4*)(p.out + (size_t)(tile * 32 + row) * 512 + f0 + (lane & 3) * 8) = rowv[q];
        }
        wait_sel(1);
        const int sel1 = __builtin_amdgcn_readfirstlane((int)lds_ld<u32>(P_SEL + 32)) & 2047;
        if (tile * 32 + bt != sel1) {
#pragma unroll
            for (int q = 0; q < 2; ++q) {
                const int row = q * 16 + (lane >> 2);
                if (row == bt) *(u32x4*)(p.out + (size_t)(tile * 32 + row) * 512 + f0 + (lane & 3) * 8) = rowv[q];
            }
        }
    }
}

#if defined(__HIP_DEVICE_COMPILE__)
__attribute__((target("no-packed-fp32-ops")))
#endif
__global__ __launch_bounds__(512, 2) void fused_layers_kernel(Params p) {
    extern __shared__ __attribute__((aligned(16))) char lds[];
    Ctx c;
    const int tid = threadIdx.x;
    c.lane = tid & 63; c.w = __builtin_amdgcn_readfirstlane(tid >> 6);
    c.xg = blockIdx.x & 7; c.gi = blockIdx.x >> 3;
    c.ws = __builtin_amdgcn_make_buffer_rsrc(p.ws, 0, WS_BYTES, 0x00020000);
    c.tag = 0u;
    {
        const u32 o = (u32)(((4 * c.xg + (c.gi >> 3)) * 64 + (c.gi & 7) * 8 + 2 * (c.w & 3) + (c.w >> 2)) * 64 + c.lane) * 16u;
        const u32x4 pois = {POISON, POISON, POISON, POISON};
        stw(c.ws, OFF_O0 + o, pois); stw(c.ws, OFF_O1 + o, pois);
    }
    if (c.w == 6) {
        const u32 o = (u32)(((4 * c.xg + (c.gi >> 3)) * 8 + (c.gi & 7)) * 1024 + c.lane * 16);
        stw(c.ws, OFF_GS0 + o, (u32x4){0u, 0u, 0u, 0u}); stw(c.ws, OFF_GS1 + o, (u32x4){0u, 0u, 0u, 0u});
    }
    {
        u64 pc; u32 len;
        asm volatile("s_getpc_b64 %0\nfl_code_here:\n\ts_mov_b32 %1, fl_code_end-fl_code_here" : "=s"(pc), "=s"(len));
        if (c.w == 7) {
            const u32 o0 = min((u32)c.gi * 3072u + (u32)c.lane * 16u, len - 16u) & ~15u, o1 = min(o0 + 1024u, len - 16u) & ~15u, o2 = min(o0 + 2048u, len - 16u) & ~15u;
            const u64 base = pc & ~15ull;
            glds((const void*)base, o0, 147456u); glds((const void*)base, o1, 148480u); glds((const void*)base, o2, 149504u);
        }
    }
    if (c.w == 5) {
        const int l = c.lane;
        const char* a = p.ws + (size_t)(l < 10 ? l : 0) * 0x200000;
        a = l == 10 ? (const char*)p.wo : l == 11 ? (const char*)p.wr : l == 12 ? (const char*)p.wg : l == 13 ? (const char*)p.wu : l == 14 ? (const char*)p.wd : l == 15 ? (const char*)p.out : a;
        a = l == 16 ? (const char*)p.wg + 1835004 : l == 17 ? (const char*)p.wu + 1835004 : l == 18 ? (const char*)p.wd + 1835004 : l == 19 ? (const char*)p.out + 2097148 : a;
        u32 sv;
        asm volatile("s_mov_b32 %0, m0\n\ts_mov_b32 m0, %2\n\ts_nop 0\n\tglobal_load_lds_dword %1, off\n\ts_mov_b32 m0, %0" : "=&s"(sv) : "v"(a), "s"(150528u) : "memory");
    }
    if (tid < 24 && tid != 3) lds_st<u32>(P_HCNT + (u32)tid * 4u, 0u);
    asm volatile("" :: "v"(lds));
    BAR();
    STAMP(0);
    FST(0);
    u32 hsync = 0;
    u32x4 x2[2] = {};
    u32x4 pq[12], xf[32];
    phaseQ<0>(c, p, hsync, pq, xf);
    if (STOP_AFTER >= 1) phaseA<0>(c, p);
    if (STOP_AFTER >= 2) phaseO<0>(c, p, x2, hsync, pq, xf);
    if (STOP_AFTER >= 3) phaseQ<1>(c, p, hsync, pq, xf);
    if (STOP_AFTER >= 4) phaseA<1>(c, p);
    if (STOP_AFTER >= 5) phaseO<1>(c, p, x2, hsync, pq, xf);
    STAMP(24);
    if (blockIdx.x == 14 * 8 && tid == 256) __builtin_amdgcn_raw_buffer_store_b32(c.tag, c.ws, OFF_EPOCH, 0, 16);
    asm volatile("fl_code_end:" ::: "memory");
}

extern "C" void kernel_launch(void* const* d_in, const int* in_sizes, int n_in, void* d_out, int out_size, void* d_ws, size_t ws_size, hipStream_t stream) {
    Params p;
    p.x0 = (const uint16_t*)d_in[0]; p.wqkv = (const uint16_t*)d_in[1]; p.wo = (const uint16_t*)d_in[2];
    p.wr = (const uint16_t*)d_in[4]; p.wg = (const uint16_t*)d_in[5]; p.wu = (const uint16_t*)d_in[6]; p.wd = (const uint16_t*)d_in[7];
    p.out = (uint16_t*)d_out; p.ws = (char*)d_ws;
    (void)hipFuncSetAttribute((const void*)fused_layers_kernel, hipFuncAttributeMaxDynamicSharedMemorySize, LDS_BYTES);
    fused_layers_kernel<<<256, 512, LDS_BYTES, stream>>>(p);
}
```

```cpp
#include <hip/hip_runtime.h>
#include <stdint.h>

typedef unsigned u32;
typedef unsigned long long u64;
typedef __attribute__((ext_vector_type(2))) u32 u32x2;
typedef __attribute__((ext_vector_type(4))) u32 u32x4;
typedef __attribute__((ext_vector_type(2))) float f32x2;
typedef __attribute__((ext_vector_type(4))) float f32x4;
typedef __attribute__((ext_vector_type(16))) float f32x16;
typedef __attribute__((ext_vector_type(8))) __bf16 bf16x8;
typedef __attribute__((ext_vector_type(2))) __bf16 bf16x2;
typedef __attribute__((ext_vector_type(4))) short s16x4;
typedef __attribute__((ext_vector_type(8))) short s16x8;
#define LAS __attribute__((address_space(3)))
#define DI __device__ __forceinline__
#define MFMA32(a, b, c) __builtin_amdgcn_mfma_f32_32x32x16_bf16((a), (b), (c), 0, 0, 0)
#define MFMA16(a, b, c) __builtin_amdgcn_mfma_f32_16x16x32_bf16((a), (b), (c), 0, 0, 0)
#define SBAR() __builtin_amdgcn_sched_barrier(0)
#define BAR() asm volatile("s_waitcnt lgkmcnt(0)\n\ts_barrier" ::: "memory")
#define DRAIN() asm volatile("s_waitcnt vmcnt(0)" ::: "memory")
#define MEMFENCE() asm volatile("" ::: "memory")

#define STOP_AFTER 99

constexpr u32 OFF_EPOCH = 0, OFF_ABORT = 256;
constexpr u32 OFF_FQ0 = 0x10000, OFF_FO = 0x20000, OFF_FQ1 = 0x28000;
constexpr u32 OFF_TS0 = 0x40000, OFF_TS1 = 0x48000;
constexpr u32 OFF_GC0 = 0x50000, OFF_GC1 = 0x51000;
constexpr u32 OFF_GM0 = 0x52000, OFF_GM1 = 0x52400;
constexpr u32 OFF_GA0 = 0x58000, OFF_GA1 = 0x5A000;
constexpr u32 OFF_GY = 0x5C000;
constexpr u32 OFF_GS0 = 0x80000, OFF_GS1 = 0xC0000;
constexpr u32 OFF_GX0 = 0x180000, OFF_GX1 = 0x1A0000;
constexpr u32 OFF_Q0 = 0x200000, OFF_K0 = 0x400000, OFF_V0 = 0x600000, OFF_O0 = 0x800000, OFF_X1 = 0xA00000;
constexpr u32 OFF_Q1 = 0xC00000, OFF_K1 = 0xE00000, OFF_V1 = 0x1000000, OFF_O1 = 0x1200000;
constexpr u32 WS_BYTES = 0x1400000;

constexpr u32 LDS_BYTES = 163840;
constexpr u32 L_W = 0;
constexpr u32 L_XR = 98304;
constexpr u32 L_AX = 140288;
constexpr u32 L_TOK = 161792 + 512;
constexpr u32 L_K = 0, L_V = 65536, VSTR = 1152;
constexpr u32 L_WO = 0, WO_FT = 65536;
constexpr u32 L_XB = 32768;
constexpr u32 L_SB = 161792;
constexpr u32 L_P = 163328;
constexpr u32 P_CS = L_P, P_HCNT = L_P + 384, P_PATCH = L_P + 388, P_WQ = L_P + 392, P_WSUM = L_P + 396, P_SEL = L_P + 400, P_ST = L_P + 464;
constexpr u32 L_X1T = 49152;
constexpr u32 FW0 = 98304, FS0 = 155648;
constexpr u32 FW1 = FW0, FS1 = FS0;
constexpr u32 SPIN_CAP = 40000;

struct Params {
    const uint16_t* x0; const uint16_t* wqkv; const uint16_t* wo; const uint16_t* wr;
    const uint16_t* wg; const uint16_t* wu; const uint16_t* wd; uint16_t* out; char* ws;
};

typedef __amdgpu_buffer_rsrc_t rsrc_t;

DI u32 pk(float a, float b) { f32x2 v = {a, b}; return __builtin_bit_cast(u32, __builtin_convertvector(v, bf16x2)); }
DI float bflo(u32 v) { return __uint_as_float(v << 16); }
DI float bfhi(u32 v) { return __uint_as_float(v & 0xffff0000u); }
DI float rbf(float a) { return bflo(pk(a, 0.f)); }
DI u32x4 ldw(rsrc_t r, u32 off) { return __builtin_amdgcn_raw_buffer_load_b128(r, off, 0, 16); }
DI void stw(rsrc_t r, u32 off, u32x4 v) { __builtin_amdgcn_raw_buffer_store_b128(v, r, off, 0, 16); }
template <class T> DI T lds_ld(u32 a) { return *(const LAS T*)(uintptr_t)a; }
template <class T> DI void lds_st(u32 a, T v) { *(LAS T*)(uintptr_t)a = v; }
DI s16x4 lds_tr(u32 a) { return __builtin_amdgcn_ds_read_tr16_b64_v4i16((LAS s16x4*)(uintptr_t)a); }
DI bf16x8 frag_tr(u32 a0, u32 a1) { s16x4 lo = lds_tr(a0), hi = lds_tr(a1); return __builtin_bit_cast(bf16x8, __builtin_shufflevector(lo, hi, 0, 1, 2, 3, 4, 5, 6, 7)); }
DI float swap32_add(float v) { auto r = __builtin_amdgcn_permlane32_swap(__float_as_uint(v), __float_as_uint(v), false, false); return __uint_as_float(r[0]) + __uint_as_float(r[1]); }
template <int CTRL> DI u32 dpp(u32 v) { return (u32)__builtin_amdgcn_update_dpp((int)v, (int)v, CTRL, 0xf, 0xf, true); }
DI float wave_sum(float v) {
    v += __uint_as_float(dpp<0xB1>(__float_as_uint(v)));
    v += __uint_as_float(dpp<0x4E>(__float_as_uint(v)));
    v += __uint_as_float(dpp<0x141>(__float_as_uint(v)));
    v += __uint_as_float(dpp<0x140>(__float_as_uint(v)));
    { auto r = __builtin_amdgcn_permlane16_swap(__float_as_uint(v), __float_as_uint(v), false, false); v = __uint_as_float(r[0]) + __uint_as_float(r[1]); }
    return swap32_add(v);
}
DI u32 half_umax(u32 v) {
    v = max(v, dpp<0xB1>(v)); v = max(v, dpp<0x4E>(v)); v = max(v, dpp<0x141>(v)); v = max(v, dpp<0x140>(v));
    auto r = __builtin_amdgcn_permlane16_swap(v, v, false, false);
    return max(r[0], r[1]);
}
DI u32 wave_umax(u32 v) { v = half_umax(v); auto r = __builtin_amdgcn_permlane32_swap(v, v, false, false); return max(r[0], r[1]); }
DI u32 score_key(u32 bf, u32 idx) { return ((bf ^ ((bf & 0x8000u) ? 0xFFFFu : 0x8000u)) << 16) | (0xFFFFu - idx); }
DI u32 key_bf(u32 k) { const u32 b = k >> 16; return b ^ ((b & 0x8000u) ? 0x8000u : 0xFFFFu); }
DI u32 key_idx(u32 k) { return 0xFFFFu - (k & 0xFFFFu); }
DI void glds(const void* sbase, u32 voff, u32 ldsdst) {
    u32 sv;
    asm volatile("s_mov_b32 %0, m0\n\ts_mov_b32 m0, %3\n\ts_nop 0\n\tglobal_load_lds_dwordx4 %1, %2\n\ts_mov_b32 m0, %0"
                 : "=&s"(sv) : "v"(voff), "s"(sbase), "s"(ldsdst) : "memory");
}
struct Ctx {
    rsrc_t ws; u32 tag; int lane, w, xg, gi;
};
#define FRESH(c) asm volatile("" : "+v"((c).lane), "+s"((c).w), "+s"((c).xg), "+s"((c).gi))
DI u32 epoch_tag(u32 v) { const u32 t = (u32)__builtin_amdgcn_readfirstlane((int)v) + 1u; return t ? t : 1u; }
DI bool spin_fail(const Ctx& c, u32& n) {
    __builtin_amdgcn_s_sleep(1);
    ++n;
    if (n > SPIN_CAP) { if (c.lane == 0) __builtin_amdgcn_raw_buffer_store_b32(c.tag, c.ws, OFF_ABORT, 0, 16); return true; }
    if ((n & 127u) == 0u) { u32 a = __builtin_amdgcn_raw_buffer_load_b32(c.ws, OFF_ABORT, 0, 16); if (__builtin_amdgcn_readfirstlane(a) == c.tag) return true; }
    return false;
}
DI void wait_flags(const Ctx& c, u32 off) {
    u32 n = 0;
    for (;;) {
        u32x2 v = __builtin_amdgcn_raw_buffer_load_b64(c.ws, off, 0, 16);
        MEMFENCE();
        if (__all(v[0] == c.tag && v[1] == ~c.tag)) break;
        if (spin_fail(c, n)) break;
    }
    MEMFENCE();
}
DI void set_flag(const Ctx& c, u32 off) { u32x2 v = {c.tag, ~c.tag}; __builtin_amdgcn_raw_buffer_store_b64(v, c.ws, off, 0, 16); }
constexpr u32 POISON = 0xFFFFFFFFu;
DI bool poisoned(u32x4 v) { return max(max(v[0], v[1]), max(v[2], v[3])) == POISON; }
DI u32x4 not_poison(u32x4 v) { return (u32x4){min(v[0], POISON - 1u), min(v[1], POISON - 1u), min(v[2], POISON - 1u), min(v[3], POISON - 1u)}; }
DI void wait_piece(const Ctx& c, u32 off, u32x4& v) {
    u32 n = 0;
    while (__any(poisoned(v))) { if (spin_fail(c, n)) break; v = ldw(c.ws, off); MEMFENCE(); }
}
DI bool unit_ok(u32x4 v, u32 tag) { return v[1] == tag && v[3] == tag; }
DI u32x4 mk_unit(u32 a, u32 b, u32 tag) { return (u32x4){a, tag, b, tag}; }
template <int N> DI void wait_units(const Ctx& c, const u32 (&off)[N], u32x4 (&v)[N]) {
    u32 n = 0;
    for (;;) {
        bool ok = true;
#pragma unroll
        for (int k = 0; k < N; ++k) v[k] = ldw(c.ws, off[k]);
        MEMFENCE();
#pragma unroll
        for (int k = 0; k < N; ++k) ok = ok && unit_ok(v[k], c.tag);
        if (__all(ok)) break;
        if (spin_fail(c, n)) break;
    }
    MEMFENCE();
}
DI u32 lds_poll_ld(u32 a) { return __hip_atomic_load((LAS u32*)(uintptr_t)a, __ATOMIC_RELAXED, __HIP_MEMORY_SCOPE_WORKGROUP); }
DI void helper_sync(const Ctx& c, u32& target) {
    target += 4;
    asm volatile("s_waitcnt lgkmcnt(0)" ::: "memory");
    if (c.lane == 0) __hip_atomic_fetch_add((LAS u32*)(uintptr_t)P_HCNT, 1u, __ATOMIC_RELAXED, __HIP_MEMORY_SCOPE_WORKGROUP);
    u32 n = 0;
    while (lds_poll_ld(P_HCNT) < target) { if (++n > (1u << 24)) break; }
    MEMFENCE();
}
DI void lds_wait_ge(u32 a, u32 v) {
    u32 n = 0;
    while (lds_poll_ld(a) < v) { __builtin_amdgcn_s_sleep(1); if (++n > (1u << 22)) break; }
    MEMFENCE();
}
DI void lds_signal_add(const Ctx& c, u32 a) {
    asm volatile("s_waitcnt lgkmcnt(0)" ::: "memory");
    if (c.lane == 0) __hip_atomic_fetch_add((LAS u32*)(uintptr_t)a, 1u, __ATOMIC_RELAXED, __HIP_MEMORY_SCOPE_WORKGROUP);
}
DI void wait_sel(int L) {
    u32 n = 0;
    while (lds_poll_ld(P_SEL + L * 32 + 16) == 0u) { __builtin_amdgcn_s_sleep(1); if (++n > (1u << 22)) break; }
    MEMFENCE();
}

#define STAMP(i) do { } while (0)
#define CSTAMP(i) do { } while (0)
#define FST(i) do { } while (0)

DI u64 cand_tag(u32 tag) { return ((u64)tag << 11) | (u64)((tag * 0x9E3779B1u) >> 21); }
DI void ffn_chain(const Ctx& c, const Params& p, u32& hsync, const int L) {
    const int lane = c.lane, hw = c.w - 4;
    constexpr u32 FW = FW0, FS = FS0;
    const u32 GC = L ? OFF_GC1 : OFF_GC0, GM = L ? OFF_GM1 : OFF_GM0, GX = L ? OFF_GX1 : OFF_GX0, GA = L ? OFF_GA1 : OFF_GA0;
    FST(20 + 20 * L);
    __builtin_amdgcn_s_setprio(3);
    const bool is_gu = c.gi < 14, is_dn = c.gi >= 14 && c.gi < 18;
    const int g16 = lane >> 4, i16 = lane & 15;
    bf16x8 wf[14];
    {
        const u32 tro = FW + (u32)((8 * g16 + (i16 >> 2)) * 32 + (i16 & 3) * 8) + (u32)hw * (is_gu ? 4096u : 14336u);
#pragma unroll
        for (int s = 0; s < 14; ++s) {
            const u32 bo = tro + (is_gu ? (u32)(s & 3) * 1024u + (u32)((s >> 2) & 1) * 16384u : (u32)s * 1024u);
            if (is_gu || is_dn) wf[s] = frag_tr(bo, bo + 128);
        }
    }
    if (hw == 0) {
        const u64 want = cand_tag(c.tag);
        u64 v; u32 n = 0;
        for (;;) {
            const u32x2 t = __builtin_amdgcn_raw_buffer_load_b64(c.ws, GC + c.xg * 512u + lane * 8u, 0, 16);
            MEMFENCE();
            v = ((u64)t[1] << 32) | t[0];
            if (__all((v >> 21) == want)) break;
            if (spin_fail(c, n)) break;
        }
        FST(21 + 20 * L);
        const u32 key = wave_umax(score_key(((u32)v >> 5) & 0xFFFFu, (u32)lane * 32u + ((u32)v & 31u)));
        const u32 tok = key_idx(key); const float r = __uint_as_float(key_bf(key) << 16);
        if (lane == 0) {
            lds_st<u32x2>(P_SEL + L * 32, (u32x2){(u32)tok, __float_as_uint(r)});
            asm volatile("s_waitcnt lgkmcnt(0)" ::: "memory");
            __hip_atomic_store((LAS u32*)(uintptr_t)(P_SEL + L * 32 + 16), 1u, __ATOMIC_RELAXED, __HIP_MEMORY_SCOPE_WORKGROUP);
        }
    }
    FST(22 + 20 * L);
    STAMP(L ? 25 : 13);
    if (!is_gu && !is_dn) return;
    wait_sel(L);
    const u32x2 si = lds_ld<u32x2>(P_SEL + L * 32);
    const int sel = __builtin_amdgcn_readfirstlane((int)si[0]) & 2047;
    const float rsel = __uint_as_float(si[1]);
    const u32 gxrow = GX + (u32)(sel >> 5) * 2048u, gm = GM + (u32)(sel >> 5) * 16u;
    if (is_gu) {
        const int gidx = c.xg * 14 + c.gi;
        u32 off[2] = {gxrow + (u32)(hw * 32 + (lane & 31)) * 16u, gm};
        u32x4 v[2];
        FST(29 + 20 * L);
        wait_units<2>(c, off, v);
        FST(23 + 20 * L);
        const float mu = __uint_as_float(v[1][0]), rs = __uint_as_float(v[1][2]);
        if (lane < 32) {
            u32x2 h;
            h[0] = pk((bflo(v[0][0]) - mu) * rs, (bfhi(v[0][0]) - mu) * rs);
            h[1] = pk((bflo(v[0][2]) - mu) * rs, (bfhi(v[0][2]) - mu) * rs);
            lds_st<u32x2>(FS + (u32)(hw * 32 + lane) * 8u, h);
        }
        STAMP(L ? 26 : 14);
        FST(24 + 20 * L);
        f32x4 ag = {}, au = {};
#pragma unroll
        for (int s = 0; s < 4; ++s) {
            const bf16x8 a = lds_ld<bf16x8>(FS + (u32)((hw * 4 + s) * 64 + g16 * 16));
            ag = MFMA16(a, wf[s], ag);
            au = MFMA16(a, wf[4 + s], au);
        }
        if (lane < 16) { lds_st<float>(FS + 4608 + (u32)(hw * 32 + lane) * 4u, ag[0]); lds_st<float>(FS + 4608 + (u32)(hw * 32 + 16 + lane) * 4u, au[0]); }
        FST(25 + 20 * L);
        helper_sync(c, hsync);
        FST(26 + 20 * L);
        if (hw == 0) {
            float g = 0.f, u = 0.f;
#pragma unroll
            for (int k = 0; k < 4; ++k) { g += lds_ld<float>(FS + 4608 + (u32)(k * 32 + i16) * 4u); u += lds_ld<float>(FS + 4608 + (u32)(k * 32 + 16 + i16) * 4u); }
            g = rbf(g); u = rbf(u);
            const float sg = rbf(g * __builtin_amdgcn_rcpf(1.0f + __expf(-g)));
            const float a = rbf(sg * u);
            const float a0 = __shfl(a, (lane & 3) * 4), a1 = __shfl(a, (lane & 3) * 4 + 1), a2 = __shfl(a, (lane & 3) * 4 + 2), a3 = __shfl(a, (lane & 3) * 4 + 3);
            if (lane < 4) stw(c.ws, GA + (u32)(gidx * 4 + lane) * 16u, mk_unit(pk(a0, a1), pk(a2, a3), c.tag));
            FST(27 + 20 * L);
        }
    } else {
        const int didx = c.xg * 4 + (c.gi - 14);
        float mu, rs;
        {
            const int u0 = hw * 112 + lane, u1 = hw * 112 + 64 + (lane < 48 ? lane : 47);
            u32 off[4] = {gxrow + (u32)(didx * 4 + (lane & 3)) * 16u, gm, GA + (u32)u0 * 16u, GA + (u32)u1 * 16u};
            u32x4 v[4];
            wait_units<4>(c, off, v);
            FST(23 + 20 * L);
            mu = __uint_as_float(v[1][0]); rs = __uint_as_float(v[1][2]);
            lds_st<u32x2>(FS + 1024 + (u32)u0 * 8u, (u32x2){v[2][0], v[2][2]});
            if (lane < 48) lds_st<u32x2>(FS + 1024 + (u32)u1 * 8u, (u32x2){v[3][0], v[3][2]});
            if (hw == 0 && lane < 4) lds_st<u32x2>(FS + (u32)lane * 8u, (u32x2){v[0][0], v[0][2]});
        }
        STAMP(L ? 26 : 14);
        FST(24 + 20 * L);
        f32x4 ay = {}, az = {};
        bf16x8 af[14];
#pragma unroll
        for (int s = 0; s < 14; ++s) af[s] = lds_ld<bf16x8>(FS + 1024 + (u32)((hw * 14 + s) * 64 + g16 * 16));
#pragma unroll
        for (int s = 0; s < 14; s += 2) { ay = MFMA16(af[s], wf[s], ay); az = MFMA16(af[s + 1], wf[s + 1], az); }
        if (lane < 16) lds_st<float>(FS + 4608 + (u32)(hw * 16 + lane) * 4u, ay[0] + az[0]);
        FST(25 + 20 * L);
        helper_sync(c, hsync);
        FST(26 + 20 * L);
        if (hw == 0) {
            float y = 0.f;
#pragma unroll
            for (int k = 0; k < 4; ++k) y += lds_ld<float>(FS + 4608 + (u32)(k * 16 + i16) * 4u);
            const float x1 = __uint_as_float((u32)lds_ld<uint16_t>(FS + (u32)i16 * 2u) << 16);
            const float sig = rbf(__builtin_amdgcn_rcpf(1.0f + __expf(-rsel)));
            y = rbf(rbf(y) * sig);
            const float h = rbf((x1 - mu) * rs);
            const float x2 = rbf(x1 + rbf(h + y));
            const float a0 = __shfl(x2, (lane & 3) * 4), a1 = __shfl(x2, (lane & 3) * 4 + 1), a2 = __shfl(x2, (lane & 3) * 4 + 2), a3 = __shfl(x2, (lane & 3) * 4 + 3);
            if (L == 0) { if (lane < 4) stw(c.ws, OFF_GY + (u32)(didx * 4 + lane) * 16u, mk_unit(pk(a0, a1), pk(a2, a3), c.tag)); }
            else if (lane < 4) *(u32x2*)(p.out + (size_t)sel * 512 + didx * 16 + lane * 4) = (u32x2){pk(a0, a1), pk(a2, a3)};
            FST(27 + 20 * L);
        }
    }
}

DI void qkv_patch(const Ctx& c, u32& hsync) {
    const int lane = c.lane, hw = c.w - 4;
    constexpr u32 FS = FS0;
    const int g16 = lane >> 4, i16 = lane & 15;
    bf16x8 wf[24];
    {
        const u32 tro = L_W + (u32)((8 * g16 + (i16 >> 2)) * 64 + (i16 & 3) * 8) + (u32)hw * 8192u;
#pragma unroll
        for (int s_ = 0; s_ < 4; ++s_)
#pragma unroll
            for (int ct = 0; ct < 6; ++ct) {
                const u32 bo = tro + (u32)s_ * 2048u + (u32)(ct >> 1) * 32768u + (u32)(ct & 1) * 32u;
                wf[s_ * 6 + ct] = frag_tr(bo, bo + 256);
            }
    }
    u32 off[2] = {OFF_GY + (u32)lane * 32u, OFF_GY + (u32)lane * 32u + 16u}; u32x4 v[2];
    wait_units<2>(c, off, v);
    float x[8], s = 0, q = 0;
#pragma unroll
    for (int k = 0; k < 2; ++k) { x[4 * k] = bflo(v[k][0]); x[4 * k + 1] = bfhi(v[k][0]); x[4 * k + 2] = bflo(v[k][2]); x[4 * k + 3] = bfhi(v[k][2]); }
#pragma unroll
    for (int k = 0; k < 8; ++k) { s += x[k]; q += x[k] * x[k]; }
    s = wave_sum(s); q = wave_sum(q);
    const float mu = s * (1.0f / 512.0f), var = fmaxf(q * (1.0f / 512.0f) - mu * mu, 0.f);
    const float A = __builtin_amdgcn_rsqf(var + 1e-5f);
    lds_st<u32x4>(FS + (u32)lane * 16u, (u32x4){pk((x[0] - mu) * A, (x[1] - mu) * A), pk((x[2] - mu) * A, (x[3] - mu) * A), pk((x[4] - mu) * A, (x[5] - mu) * A), pk((x[6] - mu) * A, (x[7] - mu) * A)});
    f32x4 acc[6] = {};
#pragma unroll
    for (int s_ = 0; s_ < 4; ++s_) {
        const bf16x8 a = lds_ld<bf16x8>(FS + (u32)((hw * 4 + s_) * 64 + g16 * 16));
#pragma unroll
        for (int ct = 0; ct < 6; ++ct) acc[ct] = MFMA16(a, wf[s_ * 6 + ct], acc[ct]);
    }
    if (lane < 16) {
#pragma unroll
        for (int ct = 0; ct < 6; ++ct) lds_st<float>(FS + 1024 + (u32)(hw * 96 + ct * 16 + lane) * 4u, acc[ct][0]);
    }
    helper_sync(c, hsync);
    if (hw == 0) {
#pragma unroll
        for (int k = 0; k < 2; ++k) {
            const int n = k * 64 + lane;
            if (n < 96) {
                float t = 0.f;
#pragma unroll
                for (int j = 0; j < 4; ++j) t += lds_ld<float>(FS + 1024 + (u32)(j * 96 + n) * 4u);
                lds_st<float>(FS + 4608 + (u32)n * 4u, n < 32 ? t * (0.25f * 1.4426950408889634f) : t);
            }
        }
        asm volatile("s_waitcnt lgkmcnt(0)" ::: "memory");
        if (lane == 0) __hip_atomic_store((LAS u32*)(uintptr_t)P_PATCH, 1u, __ATOMIC_RELAXED, __HIP_MEMORY_SCOPE_WORKGROUP);
    }
}

DI u32x4 gld(const void* base, u32 off) { return *(const u32x4*)((const char*)base + off); }
struct FfSrc { const uint16_t* m; u32 vo, step; bool on, gu; };
DI FfSrc ff_src(const Ctx& c, const Params& p) {
    int lane = c.lane; asm volatile("" : "+v"(lane));
    FfSrc f; const int w = c.w;
    f.on = c.gi < 18; f.gu = c.gi < 14;
    f.m = f.gu ? (w < 4 ? p.wg : p.wu) : p.wd;
    const u32 rowb = f.gu ? 3584u : 1024u, col = f.gu ? (u32)(c.xg * 14 + c.gi) * 32u : (u32)(c.xg * 4 + (c.gi - 14)) * 32u;
    const u32 first = f.gu ? (u32)(w & 3) * 4u : (u32)w * 7u;
    f.step = 32u * rowb;
    f.vo = (u32)(lane >> 1) * rowb + col + (u32)(lane & 1) * 16u + first * f.step;
    return f;
}
DI void keep_wq(const Ctx& c, u32x4 (&pq)[12]) {
#pragma unroll
    for (int j = 0; j < 12; ++j) pq[j] = lds_ld<u32x4>(L_W + (u32)(c.w * 12 + j) * 1024u + (u32)c.lane * 16u);
}
DI void pre_store_wq(const Ctx& c, const u32x4 (&pq)[12]) {
#pragma unroll
    for (int j = 0; j < 12; ++j) lds_st<u32x4>(L_W + (u32)(c.w * 12 + j) * 1024u + (u32)c.lane * 16u, pq[j]);
}
DI void pre_store_ff(const Ctx& c, const u32x4 (&pf)[7]) {
    if (c.gi < 18) {
        const bool gu = c.gi < 14;
        const u32 a = FW0 + (u32)c.w * (gu ? 4096u : 7168u) + (u32)c.lane * 16u;
#pragma unroll
        for (int j = 0; j < 7; ++j) if (j < 4 || !gu) lds_st<u32x4>(a + (u32)j * 1024u, pf[j]);
    }
}
DI u32 wo_lane_off(const Ctx& c) {
    int lane = c.lane; asm volatile("" : "+v"(lane));
    return (u32)((lane >> 2) * 1024 + (c.gi & 7) * 128 + (lane & 3) * 16 + (c.w >> 2) * 64 + (c.w & 3) * 8 * 16384);
}
DI void pre_store_wo(const Ctx& c, const u32x4 (&po)[8]) {
#pragma unroll
    for (int j = 0; j < 8; ++j) lds_st<u32x4>(L_WO + (u32)(c.w >> 2) * WO_FT + (u32)((c.w & 3) * 8 + j) * 1024u + (u32)c.lane * 16u, po[j]);
}

struct WFrag { bf16x8 q, k, v; };
template <u32 TR2> DI void wfrag_load(WFrag& f, u32 a01, u32 a2) { f.q = frag_tr(a01, a01 + TR2); f.k = frag_tr(a01 + 32768, a01 + 32768 + TR2); f.v = frag_tr(a2, a2 + TR2); }
template <u32 TR2> DI void qstep(f32x16& aq, f32x16& ak, f32x16& av, bf16x8 x, WFrag& cur, WFrag& nxt, u32 a01, u32 a2, bool more) {
    if (more) nxt.q = frag_tr(a01, a01 + TR2);
    SBAR(); aq = MFMA32(cur.q, x, aq); SBAR();
    if (more) nxt.k = frag_tr(a01 + 32768, a01 + 32768 + TR2);
    SBAR(); ak = MFMA32(cur.k, x, ak); SBAR();
    if (more) nxt.v = frag_tr(a2, a2 + TR2);
    SBAR(); av = MFMA32(x, cur.v, av); SBAR();
}

template <int L>
DI void phaseQ(Ctx& c0, const Params& p, u32& hsync, u32x4 (&pq)[12]) {
    Ctx c = c0; FRESH(c);
    const int lane = c.lane, w = c.w;
    const int rb = 4 * (c.xg & 3) + (c.gi & 3), hp = 8 * (c.xg >> 2) + (c.gi >> 2);
    const int r = lane & 31, h = lane >> 5;
    const int i16 = lane & 15, blk = (lane >> 4) & 1;
    const u32 trA = L_W + (u32)(((L == 0 ? 8 : 4) * h + (i16 >> 2)) * 64 + blk * 32 + (i16 & 3) * 8);
    constexpr u32 TR2 = L == 0 ? 256 : 512;
    constexpr u32 OQ = L ? OFF_Q1 : OFF_Q0, OK_ = L ? OFF_K1 : OFF_K0, OV = L ? OFF_V1 : OFF_V0, TS = L ? OFF_TS1 : OFF_TS0;
    const int rot = ((c.gi >> 2) + 2 * (c.gi & 3)) & 7;

    if (w >= 4) {
        const int hw = w - 4;
        if (L == 0) {
            u32x4 r0, r1; u32 ep;
            asm volatile("global_load_dword %0, %1, %2 sc1" : "=v"(ep) : "v"(0u), "s"(p.ws));
            const u32 tok = (u32)(rb * 128 + hp * 8 + hw * 2);
            const u32 vo = tok * 1024u + (u32)lane * 16u;
            asm volatile("global_load_dwordx4 %0, %1, %2" : "=v"(r0) : "v"(vo), "s"(p.x0));
            asm volatile("global_load_dwordx4 %0, %1, %2 offset:1024" : "=v"(r1) : "v"(vo), "s"(p.x0));
            const u32 wv = (u32)((lane >> 2) * 3072 + hp * 64 + (lane & 3) * 16 + hw * 16 * 3072);
            u32 xv[4];
#pragma unroll
            for (int q = 0; q < 4; ++q) { const int row = 8 * q + (lane >> 3); xv[q] = (u32)(((rb * 4 + hw) * 32 + row) * 1024 + (((lane & 7) ^ ((row >> 1) & 7)) * 16)); }
#define WD(i) do { const u32 kc_ = (u32)(((i) + rot) & 7); _Pragma("unroll") for (int t = 0; t < 3; ++t) \
                glds(p.wqkv, wv + (u32)t * 1024u + kc_ * (64u * 3072u), L_W + (u32)t * 32768u + (kc_ * 64u + (u32)hw * 16u) * 64u); } while (0)
#define XD(i) do { const u32 kc_ = (u32)(((i) + rot) & 7); _Pragma("unroll") for (int q = 0; q < 4; ++q) \
                glds(p.x0, xv[q] + kc_ * 128u, L_XR + (u32)((i) % 3) * 16384u + (u32)hw * 4096u + (u32)q * 1024u); } while (0)
#define QWAIT(n) asm volatile("s_waitcnt vmcnt(" #n ")\n\ts_barrier" ::: "memory")
#define CS(i) if (hw < 3) { _Pragma("unroll") for (int s_ = 0; s_ < 4; ++s_) { \
                const u32 a_ = trA + (u32)hw * 32768u + (u32)((((i) + rot) & 7) * 4 + s_) * 1024u; cs = MFMA32(frag_tr(a_, a_ + TR2), ones, cs); } }
            WD(0); XD(0); WD(1); XD(1); WD(2); XD(2); WD(3);
            FST(1);
            f32x16 cs = {};
            const bf16x8 ones = __builtin_bit_cast(bf16x8, (u32x4){0x3f803f80u, 0x3f803f80u, 0x3f803f80u, 0x3f803f80u});
            asm volatile("s_waitcnt vmcnt(17)\n\ts_barrier" : "+v"(r0), "+v"(r1), "+v"(ep) :: "memory");
            c.tag = epoch_tag(ep); c0.tag = c.tag;
            FST(2);
            {
                float s0 = 0, q0 = 0, s1 = 0, q1 = 0;
#pragma unroll
                for (int j = 0; j < 4; ++j) {
                    float a = bflo(r0[j]), b_ = bfhi(r0[j]); s0 += a + b_; q0 += a * a + b_ * b_;
                    float d = bflo(r1[j]), e = bfhi(r1[j]); s1 += d + e; q1 += d * d + e * e;
                }
                s0 = wave_sum(s0); q0 = wave_sum(q0); s1 = wave_sum(s1); q1 = wave_sum(q1);
                const float s = lane ? s1 : s0, q = lane ? q1 : q0;
                const float mu = s * (1.0f / 512.0f), var = fmaxf(q * (1.0f / 512.0f) - mu * mu, 0.f);
                const float A = __builtin_amdgcn_rsqf(var + 1e-5f);
                if (lane < 2) stw(c.ws, TS + (tok + (u32)lane) * 16u, mk_unit(__float_as_uint(A), __float_as_uint(A * mu), c.tag));
            }
            CS(0);
            QWAIT(11); FST(3); XD(3); WD(4); CS(1);
            QWAIT(11); FST(4); XD(4); WD(5); CS(2);
            QWAIT(10); FST(5); XD(5); WD(6); CS(3);
            QWAIT(10); FST(6); XD(6); WD(7); CS(4);
            QWAIT(10); FST(7); XD(7); CS(5);
            QWAIT(7); FST(8); CS(6);
            QWAIT(0); FST(9); CS(7);
#undef WD
#undef XD
#undef QWAIT
#undef CS
            if (hw < 3 && r == 0) {
#pragma unroll
                for (int g = 0; g < 4; ++g) lds_st<f32x4>(P_CS + (u32)(hw * 32 + 8 * g + 4 * h) * 4u, (f32x4){cs[4 * g], cs[4 * g + 1], cs[4 * g + 2], cs[4 * g + 3]});
            }
            STAMP(1);
            BAR();
        }
        DRAIN(); BAR();
        STAMP(L ? 16 : 3);
        if (L == 0) keep_wq(c, pq);
        else {
            wait_sel(0);
            const int sel = __builtin_amdgcn_readfirstlane((int)lds_ld<u32>(P_SEL)) & 2047;
            if ((sel >> 7) == rb) { lds_wait_ge(P_WQ, 8u); qkv_patch(c, hsync); }
        }
    } else {
        const int T = rb * 4 + w;
        f32x16 aq = {}, ak = {}, av = {};
        float A, B;
        if (L == 0) {
            const u32 ep = __builtin_amdgcn_raw_buffer_load_b32(c.ws, OFF_EPOCH, 0, 16);
            u32 xa[4];
#pragma unroll
            for (int s_ = 0; s_ < 4; ++s_) xa[s_] = L_XR + (u32)w * 4096u + (u32)(r * 128 + (((2 * s_ + h) ^ ((r >> 1) & 7)) * 16));
            bf16x8 xr[2][4];
            WFrag f0, f1;
            asm volatile("s_barrier" ::: "memory");
            FST(2);
#pragma unroll
            for (int s_ = 0; s_ < 4; ++s_) xr[0][s_] = lds_ld<bf16x8>(xa[s_]);
            { const u32 a01 = trA + (u32)(rot & 7) * 4096u; wfrag_load<TR2>(f0, a01, a01 + 65536u); }
#pragma unroll
            for (int i = 0; i < 8; ++i) {
                const u32 a01 = trA + (u32)((i + rot) & 7) * 4096u, a2 = a01 + 65536u;
                const u32 n01 = trA + (u32)((i + 1 + rot) & 7) * 4096u, n2 = n01 + 65536u;
                qstep<TR2>(aq, ak, av, xr[i & 1][0], f0, f1, a01 + 1024, a2 + 1024, true);
                qstep<TR2>(aq, ak, av, xr[i & 1][1], f1, f0, a01 + 2048, a2 + 2048, true);
                qstep<TR2>(aq, ak, av, xr[i & 1][2], f0, f1, a01 + 3072, a2 + 3072, true);
                if (i < 7) {
                    asm volatile("s_barrier" ::: "memory");
                    FST(3 + i);
#pragma unroll
                    for (int s_ = 0; s_ < 4; ++s_) xr[(i + 1) & 1][s_] = lds_ld<bf16x8>(xa[s_] + (u32)((i + 1) % 3) * 16384u);
                }
                qstep<TR2>(aq, ak, av, xr[i & 1][3], f1, f0, n01, n2, i < 7);
            }
            FST(10);
            STAMP(1);
            c.tag = epoch_tag(ep); c0.tag = c.tag;
            if (w == 3) {
                const u32x4 v = *(const u32x4*)(p.wr + lane * 8); float s = 0;
#pragma unroll
                for (int j = 0; j < 4; ++j) s += bflo(v[j]) + bfhi(v[j]);
                s = wave_sum(s);
                if (lane == 0) lds_st<float>(P_WSUM, s);
            }
            u32 off[1] = {TS + (u32)(T * 32 + r) * 16u}; u32x4 v[1];
            wait_units<1>(c, off, v);
            A = __uint_as_float(v[0][0]); B = __uint_as_float(v[0][2]);
            BAR();
            STAMP(2);
        } else {
            wait_flags(c, OFF_FO + (u32)(rb * 16 + (lane & 15)) * 128u);
            STAMP(13);
            u32x4 xf[32];
            const u32 xo = OFF_X1 + (u32)T * 32768u + (u32)lane * 16u;
#pragma unroll
            for (int s = 0; s < 32; ++s) xf[s] = ldw(c.ws, xo + (u32)s * 1024u);
            lds_wait_ge(P_WQ, 8u);
            STAMP(14);
            {
                u32 a01 = trA, a2 = trA + 65536u;
                asm volatile("" : "+v"(a01), "+v"(a2));
                WFrag f0, f1;
                wfrag_load<TR2>(f0, a01, a2);
#pragma unroll
                for (int st = 0; st < 32; st += 2) {
                    qstep<TR2>(aq, ak, av, __builtin_bit_cast(bf16x8, xf[st]), f0, f1, a01 + (u32)(st + 1) * 1024u, a2 + (u32)(st + 1) * 1024u, true);
                    qstep<TR2>(aq, ak, av, __builtin_bit_cast(bf16x8, xf[st + 1]), f1, f0, a01 + (u32)(st + 2) * 1024u, a2 + (u32)(st + 2) * 1024u, st + 2 < 32);
                }
            }
            u32 off[1] = {TS + (u32)(T * 32 + r) * 16u}; u32x4 v[1];
            wait_units<1>(c, off, v);
            A = __uint_as_float(v[0][0]); B = __uint_as_float(v[0][2]);
        }
        if (L == 1) STAMP(15);
        int ts = -1;
        if (L == 1) {
            wait_sel(0);
            const int sel = __builtin_amdgcn_readfirstlane((int)lds_ld<u32>(P_SEL)) & 2047;
            if ((sel >> 5) == T) ts = sel & 31;
        }
        const u32x4 pois = {POISON, POISON, POISON, POISON};
        const u32 tokl = L_TOK + (u32)w * 256u;
        if (h == 0) lds_st<f32x2>(tokl + (u32)r * 8u, (f32x2){A, B});
        constexpr float QS = 0.25f * 1.4426950408889634f;
#pragma unroll
        for (int t = 0; t < 2; ++t) {
            const f32x16& acc = t ? ak : aq;
            const float sA = t ? A : A * QS, sB = t ? B : B * QS;
            float o[16];
#pragma unroll
            for (int g = 0; g < 4; ++g) {
                const f32x4 cs = lds_ld<f32x4>(P_CS + (u32)(t * 32 + 8 * g + 4 * h) * 4u);
#pragma unroll
                for (int e = 0; e < 4; ++e) o[4 * g + e] = sA * acc[4 * g + e] - sB * cs[e];
            }
#pragma unroll
            for (int a = 0; a < 2; ++a) {
                u32x4 v = {pk(o[8 * a], o[8 * a + 1]), pk(o[8 * a + 2], o[8 * a + 3]), pk(o[8 * a + 4], o[8 * a + 5]), pk(o[8 * a + 6], o[8 * a + 7])};
                if (L == 1 && r == ts) v = pois;
                stw(c.ws, (t ? OK_ : OQ) + (u32)(((hp * 2 + a) * 64 + T) * 64 + lane) * 16u, v);
            }
        }
        u32x4 vlate = pois;
        const u32 vbase = OV + (u32)(((hp * 2 + (r >> 4)) * 64 + T) * 1024 + h * 256 + (r & 15) * 16);
        {
            asm volatile("s_waitcnt lgkmcnt(0)" ::: "memory");
            const float cs = lds_ld<float>(P_CS + (u32)(64 + r) * 4u);
            float o[16];
#pragma unroll
            for (int g = 0; g < 4; ++g) {
                const f32x4 ab0 = lds_ld<f32x4>(tokl + (u32)(8 * g + 4 * h) * 8u), ab1 = lds_ld<f32x4>(tokl + (u32)(8 * g + 4 * h + 2) * 8u);
                o[4 * g] = ab0[0] * av[4 * g] - ab0[1] * cs; o[4 * g + 1] = ab0[2] * av[4 * g + 1] - ab0[3] * cs;
                o[4 * g + 2] = ab1[0] * av[4 * g + 2] - ab1[1] * cs; o[4 * g + 3] = ab1[2] * av[4 * g + 3] - ab1[3] * cs;
            }
#pragma unroll
            for (int s = 0; s < 2; ++s) {
                u32x4 v = {pk(o[8 * s], o[8 * s + 1]), pk(o[8 * s + 2], o[8 * s + 3]), pk(o[8 * s + 4], o[8 * s + 5]), pk(o[8 * s + 6], o[8 * s + 7])};
                if (L == 1 && ts >= 0 && s == (ts >> 4) && h == ((ts >> 2) & 1)) { vlate = v; v = pois; }
                stw(c.ws, vbase + (u32)s * 512u, v);
            }
        }
        DRAIN(); BAR();
        STAMP(L ? 16 : 3);
        if (w == 0 && lane == 0) set_flag(c, (L ? OFF_FQ1 : OFF_FQ0) + (u32)(hp * 16 + rb) * 128u);
        if (L == 1 && ts >= 0) {
            lds_wait_ge(P_PATCH, 1u);
            if (r == ts) {
#pragma unroll
                for (int t = 0; t < 2; ++t)
#pragma unroll
                    for (int a = 0; a < 2; ++a) {
                        const f32x4 p0 = lds_ld<f32x4>(FS0 + 4608 + (u32)(t * 32 + 16 * a + 4 * h) * 4u), p1 = lds_ld<f32x4>(FS0 + 4608 + (u32)(t * 32 + 16 * a + 8 + 4 * h) * 4u);
                        stw(c.ws, (t ? OK_ : OQ) + (u32)(((hp * 2 + a) * 64 + T) * 64 + lane) * 16u, not_poison((u32x4){pk(p0[0], p0[1]), pk(p0[2], p0[3]), pk(p1[0], p1[1]), pk(p1[2], p1[3])}));
                    }
            }
            if (h == ((ts >> 2) & 1)) {
                const u32 pv = pk(lds_ld<float>(FS0 + 4608 + (u32)(64 + r) * 4u), 0.f) & 0xFFFFu;
                const int jj = (ts & 3) + 4 * ((ts >> 3) & 1);
#pragma unroll
                for (int k = 0; k < 4; ++k) if (k == (jj >> 1)) vlate[k] = (jj & 1) ? ((vlate[k] & 0xFFFFu) | (pv << 16)) : ((vlate[k] & 0xFFFF0000u) | pv);
                stw(c.ws, vbase + (u32)(ts >> 4) * 512u, not_poison(vlate));
            }
        }
        if (L == 0) keep_wq(c, pq);
    }
}

struct AState { f32x16 Sa, Sb, Oa, Ob; bf16x8 kC, kN, v0, v1; u32x4 pb1; };
#define EXP2(x) __builtin_amdgcn_exp2f(x)
#define BC8(x) __builtin_bit_cast(bf16x8, x)
#define PVM(O, v, p) O = MFMA32(v, BC8(p), O)
template <bool USE_C, class HOOK>
DI void attn_step(AState& s, bf16x8 qa, bf16x8 qb, const f32x16& ca, const f32x16& cb, u32 kp, u32 vp0, u32 vp1, u32 knext, u32 voff, HOOK hook) {
    const f32x16 zero = {};
    u32x4 pa0, pa1, pb0;
    s.kN = lds_ld<bf16x8>(kp + knext); SBAR();
    s.v0 = lds_ld<bf16x8>(vp0 + voff); SBAR();
    PVM(s.Ob, s.v1, s.pb1); SBAR();
    s.v1 = lds_ld<bf16x8>(vp1 + voff); SBAR();
    float a0 = EXP2(s.Sa[0]); SBAR(); float a1 = EXP2(s.Sa[1]); SBAR(); float a2 = EXP2(s.Sa[2]); SBAR(); float a3 = EXP2(s.Sa[3]); SBAR();
    s.Sb = MFMA32(s.kC, qb, USE_C ? cb : zero); SBAR();
    float a4 = EXP2(s.Sa[4]); SBAR(); float a5 = EXP2(s.Sa[5]); SBAR(); float a6 = EXP2(s.Sa[6]); SBAR(); float a7 = EXP2(s.Sa[7]); SBAR();
    pa0[0] = pk(a0, a1); SBAR(); pa0[1] = pk(a2, a3); SBAR(); float a8 = EXP2(s.Sa[8]); SBAR(); pa0[2] = pk(a4, a5); SBAR(); pa0[3] = pk(a6, a7); SBAR();
    float a9 = EXP2(s.Sa[9]); SBAR();
    asm volatile("" : "+v"(s.v0), "+v"(s.v1), "+v"(s.kN)); SBAR();
    float a10 = EXP2(s.Sa[10]); SBAR();
    PVM(s.Oa, s.v0, pa0); SBAR();
    float a11 = EXP2(s.Sa[11]); SBAR(); float a12 = EXP2(s.Sa[12]); SBAR(); float a13 = EXP2(s.Sa[13]); SBAR(); float a14 = EXP2(s.Sa[14]); SBAR(); float a15 = EXP2(s.Sa[15]); SBAR();
    pa1[0] = pk(a8, a9); SBAR(); pa1[1] = pk(a10, a11); SBAR(); pa1[2] = pk(a12, a13); SBAR(); pa1[3] = pk(a14, a15); SBAR();
    float b0 = EXP2(s.Sb[0]); SBAR(); float b1 = EXP2(s.Sb[1]); SBAR();
    PVM(s.Oa, s.v1, pa1); SBAR();
    hook(); SBAR();
    float b2 = EXP2(s.Sb[2]); SBAR(); float b3 = EXP2(s.Sb[3]); SBAR(); float b4 = EXP2(s.Sb[4]); SBAR(); float b5 = EXP2(s.Sb[5]); SBAR();
    s.Sa = MFMA32(s.kN, qa, USE_C ? ca : zero); SBAR();
    float b6 = EXP2(s.Sb[6]); SBAR(); float b7 = EXP2(s.Sb[7]); SBAR();
    pb0[0] = pk(b0, b1); SBAR(); pb0[1] = pk(b2, b3); SBAR(); float b8 = EXP2(s.Sb[8]); SBAR(); pb0[2] = pk(b4, b5); SBAR(); pb0[3] = pk(b6, b7); SBAR();
    float b9 = EXP2(s.Sb[9]); SBAR(); float b10 = EXP2(s.Sb[10]); SBAR();
    PVM(s.Ob, s.v0, pb0); SBAR();
    float b11 = EXP2(s.Sb[11]); SBAR(); float b12 = EXP2(s.Sb[12]); SBAR(); float b13 = EXP2(s.Sb[13]); SBAR(); float b14 = EXP2(s.Sb[14]); SBAR(); float b15 = EXP2(s.Sb[15]); SBAR();
    s.pb1[0] = pk(b8, b9); SBAR(); s.pb1[1] = pk(b10, b11); SBAR(); s.pb1[2] = pk(b12, b13); SBAR(); s.pb1[3] = pk(b14, b15); SBAR();
    s.kC = s.kN;
}
struct NoHook { DI void operator()() const {} };
template <bool USE_C>
DI void attn_chunk(AState& s, bf16x8 qa, bf16x8 qb, const f32x16& ca, const f32x16& cb, u32 kp, u32 vp0, u32 vp1) {
    attn_step<USE_C>(s, qa, qb, ca, cb, kp, vp0, vp1, 1024, 0, NoHook());
    attn_step<USE_C>(s, qa, qb, ca, cb, kp, vp0, vp1, 2048, VSTR, NoHook());
    attn_step<USE_C>(s, qa, qb, ca, cb, kp, vp0, vp1, 3072, 2 * VSTR, NoHook());
    attn_step<USE_C>(s, qa, qb, ca, cb, kp, vp0, vp1, 8192, 3 * VSTR, NoHook());
}
DI void attn_begin(AState& s, bf16x8 qa, const f32x16& ca, u32 kp) {
    const f32x16 zero = {};
    s.Oa = zero; s.Ob = zero; s.pb1 = (u32x4){0u, 0u, 0u, 0u}; s.v1 = BC8(s.pb1);
    s.kC = lds_ld<bf16x8>(kp);
    s.Sa = MFMA32(s.kC, qa, ca);
}
DI void attn_combine(const Ctx& c, AState& s, float (&O)[9]) {
    const int kh = c.w >> 2;
    s.Ob = MFMA32(s.v1, BC8(s.pb1), s.Ob);
    const f32x16& snd = kh ? s.Oa : s.Ob; const f32x16& keep = kh ? s.Ob : s.Oa;
    const u32 xb = L_AX + (u32)c.w * 2304u;
    lds_st<f32x4>(xb + (u32)c.lane * 16u, (f32x4){snd[0], snd[1], snd[2], snd[3]});
    lds_st<f32x4>(xb + 1024u + (u32)c.lane * 16u, (f32x4){snd[4], snd[5], snd[6], snd[7]});
    lds_st<float>(xb + 2048u + (u32)c.lane * 4u, snd[8]);
    BAR();
    const u32 xp = L_AX + (u32)(c.w ^ 4) * 2304u;
    const f32x4 u0 = lds_ld<f32x4>(xp + (u32)c.lane * 16u), u1 = lds_ld<f32x4>(xp + 1024u + (u32)c.lane * 16u);
    const float l2 = lds_ld<float>(xp + 2048u + (u32)c.lane * 4u);
#pragma unroll
    for (int q = 0; q < 4; ++q) { O[q] = keep[q] + u0[q]; O[4 + q] = keep[4 + q] + u1[q]; }
    O[8] = keep[8] + l2;
}

DI void phaseA(const Ctx& c0, const Params& p, const int L) {
    Ctx c = c0; FRESH(c);
    u32x4 pf[7] = {}, po[8];
    const int lane = c.lane, w = c.w, tid = w * 64 + lane;
    const int head = 4 * c.xg + (c.gi >> 3), qb = c.gi & 7;
    const int j = w & 3, kh = w >> 2;
    const u32 OQ = L ? OFF_Q1 : OFF_Q0, OK_ = L ? OFF_K1 : OFF_K0, OV = L ? OFF_V1 : OFF_V0, OO = L ? OFF_O1 : OFF_O0;
    if (tid == 0) lds_st<u32>(L_AX + 8 * 2304u, 0u);
    if (w == 0) {
        wait_flags(c, (L ? OFF_FQ1 : OFF_FQ0) + (u32)((head >> 1) * 16 + (lane & 15)) * 128u);
        if (L == 1) {
            const int sel = __builtin_amdgcn_readfirstlane((int)lds_ld<u32>(P_SEL)) & 2047, T = sel >> 5, ts = sel & 31;
            const u32 tk = (u32)((head * 64 + T) * 64 + (lane & 1) * 32 + ts) * 16u;
            u32 off = OK_ + tk;
            if (lane >= 2 && lane < 18) off = OV + (u32)((head * 64 + T) * 1024 + (ts >> 4) * 512 + ((ts >> 2) & 1) * 256 + (lane - 2) * 16);
            if (lane >= 18 && lane < 20 && (T >> 3) == qb) off = OQ + tk;
            u32x4 v = ldw(c.ws, off);
            wait_piece(c, off, v);
        }
    }
    if (L == 0 && w == 1) {
        const int qo = (4 * c.xg + (c.gi >> 3)) >> 2, hd = lane & 31, xg = hd >> 2, gi = (hd & 3) * 8 + qo;
        wait_flags(c, OFF_FQ0 + (u32)((8 * (xg >> 2) + (gi >> 2)) * 16 + 4 * (xg & 3) + (gi & 3)) * 128u);
    }
    BAR();
    STAMP(L ? 17 : 4);
    CSTAMP(9);
    const u32 koff = OK_ + (u32)((head * 64 + w) * 64 + lane) * 16u, voff = OV + (u32)((head * 64 + w) * 64 + lane) * 16u;
    u32x4 rk[8], rv[8];
    rk[0] = ldw(c.ws, koff); rv[0] = ldw(c.ws, voff);
    const u32 qo = OQ + (u32)((head * 64 + qb * 8 + 2 * j) * 64 + lane) * 16u;
    const u32x4 qar = ldw(c.ws, qo), qbr = ldw(c.ws, qo + 1024u);
    SBAR(); __builtin_amdgcn_s_barrier(); SBAR();
    rk[1] = ldw(c.ws, koff + 8192u); rv[1] = ldw(c.ws, voff + 8192u); rk[2] = ldw(c.ws, koff + 16384u); rv[2] = ldw(c.ws, voff + 16384u);
    SBAR(); __builtin_amdgcn_s_barrier(); SBAR();
    rk[3] = ldw(c.ws, koff + 24576u); rv[3] = ldw(c.ws, voff + 24576u); rk[4] = ldw(c.ws, koff + 32768u); rv[4] = ldw(c.ws, voff + 32768u);
    SBAR();
    { const int t = tid >> 3, s = tid & 7; const u32 val = (s & 3) == 0 ? 0x3f803f80u : 0u;
      lds_st<u32x4>(L_V + (u32)t * VSTR + 1024u + (u32)s * 16u, (u32x4){val, val, val, val}); }
    const u32 kw = L_K + (u32)w * 1024u + (u32)lane * 16u, vw = L_V + (u32)w * VSTR + (u32)lane * 16u;
    lds_st<u32x4>(kw, rk[0]); lds_st<u32x4>(vw, rv[0]);
    lds_st<u32x4>(kw + 8192u, rk[1]); lds_st<u32x4>(vw + 8u * VSTR, rv[1]);
    CSTAMP(10);
    BAR();
    const int r = lane & 31, h = lane >> 5;
    const u32 kp0 = L_K + (u32)kh * 4096u + (u32)lane * 16u;
    const u32 onesoff = 1024u + ((r >= 20 && r < 28) ? 64u : 0u) + ((r == 16 || r == 20) ? 0u : 16u);
    const u32 vp00 = L_V + (u32)kh * (4u * VSTR) + (r < 16 ? (u32)(h * 256 + r * 16) : onesoff);
    const u32 vp10 = L_V + (u32)kh * (4u * VSTR) + (r < 16 ? (u32)(512 + h * 256 + r * 16) : onesoff);
    const bf16x8 qa = BC8(qar), qbf = BC8(qbr);
    const f32x16 cz = {};
    AState s;
    STAMP(L ? 18 : 5);
#define PRIO(n) __builtin_amdgcn_s_setprio(n)
    PRIO(3);
    CSTAMP(0);
    attn_begin(s, qa, cz, kp0);
    {
#define PUT(ch) [&]() { lds_st<u32x4>(kw + (u32)(ch) * 8192u, rk[ch]); lds_st<u32x4>(vw + (u32)(ch) * (8u * VSTR), rv[ch]); }
#define GET(ch) [&]() { rk[ch] = ldw(c.ws, koff + (u32)(ch) * 8192u); rv[ch] = ldw(c.ws, voff + (u32)(ch) * 8192u); }
#define GETPUT(g, ch) [&]() { GET(g)(); PUT(ch)(); }
#define FF(j_) [&]() { if (ff.on && ((j_) < 4 || !ff.gu)) pf[j_] = gld(ff.m, ff.vo + (u32)(j_) * ff.step); }
#define WO(j_) [&]() { po[j_] = gld(p.wo, wv + (u32)(j_) * 16384u); }
#define ST(ch, t, HOOK) attn_step<false>(s, qa, qbf, cz, cz, kp, vp0, vp1, ((t) == 3 ? 8192 : ((t) + 1) * 1024) + ((ch) & 1) * 8192, (((ch) & 1) * 8 + (t)) * VSTR, HOOK)
#define NEXT2() do { kp += 16384; vp0 += 16 * VSTR; vp1 += 16 * VSTR; asm volatile("" : "+v"(kp), "+v"(vp0), "+v"(vp1)); } while (0)
        u32 kp = kp0, vp0 = vp00, vp1 = vp10;
        ST(0, 0, GET(5)); ST(0, 1, GETPUT(6, 2)); ST(0, 2, GETPUT(7, 3)); ST(0, 3, PUT(4));
        CSTAMP(1);
        ST(1, 0, PUT(5)); ST(1, 1, PUT(6)); ST(1, 2, PUT(7));
        BAR();
        ST(1, 3, NoHook());
        const FfSrc ff = ff_src(c, p);
        NEXT2(); PRIO(2);
        CSTAMP(2);
        ST(2, 0, FF(0)); ST(2, 1, FF(1)); ST(2, 2, FF(2)); ST(2, 3, FF(3));
        CSTAMP(3);
        ST(3, 0, FF(4)); ST(3, 1, FF(5)); ST(3, 2, FF(6)); ST(3, 3, NoHook());
        const u32 wv = wo_lane_off(c);
        NEXT2(); PRIO(1);
        CSTAMP(4);
        ST(4, 0, WO(0)); ST(4, 1, WO(1)); ST(4, 2, WO(2)); ST(4, 3, WO(3));
        CSTAMP(5);
        ST(5, 0, WO(4)); ST(5, 1, WO(5)); ST(5, 2, WO(6)); ST(5, 3, WO(7));
        NEXT2(); PRIO(0);
        CSTAMP(6);
        for (int ch = 6; ch < 8; ++ch) {
            attn_chunk<false>(s, qa, qbf, cz, cz, kp, vp0, vp1);
            kp += 8192; vp0 += 8 * VSTR; vp1 += 8 * VSTR;
            asm volatile("" : "+v"(kp), "+v"(vp0), "+v"(vp1));
        }
#undef PUT
#undef GET
#undef GETPUT
#undef FF
#undef WO
#undef ST
#undef NEXT2
    }
    CSTAMP(8);
    STAMP(L ? 19 : 6);
    FRESH(c);
    float O[9];
    attn_combine(c, s, O);
    const u32 oo = OO + (u32)((head * 64 + qb * 8 + 2 * j + kh) * 64 + c.lane) * 16u;
    bool redo;
    {
        bool bad = !(O[8] >= 0x1p-60f && O[8] <= 0x1p60f);
#pragma unroll
        for (int q = 0; q < 8; ++q) bad = bad || !(__builtin_fabsf(O[q]) <= 0x1p126f);
        redo = __any(bad);
        if (redo) { if (c.lane == 0) lds_st<u32>(L_AX + 8 * 2304u, 1u); }
        else {
            const float rl = __builtin_amdgcn_rcpf(O[8]);
            u32x4 o;
#pragma unroll
            for (int q = 0; q < 4; ++q) o[q] = pk(O[2 * q] * rl, O[2 * q + 1] * rl);
            stw(c.ws, oo, not_poison(o));
        }
    }
    BAR();
    if (__builtin_amdgcn_readfirstlane((int)lds_ld<u32>(L_AX + 8 * 2304u)) != 0) {
        float ma = -INFINITY, mb = -INFINITY;
        for (int ch = 0; ch < 8; ++ch)
            for (int t = 0; t < 4; ++t) {
                const bf16x8 kf = lds_ld<bf16x8>(kp0 + (u32)(ch * 8192 + t * 1024));
                const f32x16 Sa = MFMA32(kf, qa, cz), Sb = MFMA32(kf, qbf, cz);
#pragma unroll
                for (int q = 0; q < 16; ++q) { ma = fmaxf(ma, Sa[q]); mb = fmaxf(mb, Sb[q]); }
            }
        ma = fmaxf(ma, __shfl_xor(ma, 32)); mb = fmaxf(mb, __shfl_xor(mb, 32));
        const u32 xb = L_AX + (u32)w * 2304u, xp = L_AX + (u32)(w ^ 4) * 2304u;
        lds_st<f32x2>(xb + (u32)c.lane * 8u, (f32x2){ma, mb});
        BAR();
        const f32x2 mo = lds_ld<f32x2>(xp + (u32)c.lane * 8u);
        ma = fmaxf(ma, mo[0]); mb = fmaxf(mb, mo[1]);
        BAR();
        f32x16 ca, cb;
#pragma unroll
        for (int q = 0; q < 16; ++q) { ca[q] = -ma; cb[q] = -mb; }
        attn_begin(s, qa, ca, kp0);
        u32 kp = kp0, vp0 = vp00, vp1 = vp10;
        for (int ch = 0; ch < 8; ++ch) {
            attn_chunk<true>(s, qa, qbf, ca, cb, kp, vp0, vp1);
            kp += 8192; vp0 += 8 * VSTR; vp1 += 8 * VSTR;
        }
        attn_combine(c, s, O);
        if (redo) {
            const float rl = 1.0f / O[8];
            u32x4 o;
#pragma unroll
            for (int q = 0; q < 4; ++q) o[q] = pk(O[2 * q] * rl, O[2 * q + 1] * rl);
            stw(c.ws, oo, not_poison(o));
        }
        {
            const FfSrc ff = ff_src(c, p); const u32 wv = wo_lane_off(c);
#pragma unroll
            for (int q = 0; q < 7; ++q) if (ff.on && (q < 4 || !ff.gu)) pf[q] = gld(ff.m, ff.vo + (u32)q * ff.step);
#pragma unroll
            for (int q = 0; q < 8; ++q) po[q] = gld(p.wo, wv + (u32)q * 16384u);
        }
        BAR();
    }
    STAMP(L ? 20 : 7);
    pre_store_wo(c, po);
    pre_store_ff(c, pf);
}

DI void phaseO(const Ctx& c0, const Params& p, u32x4 (&x2)[2], u32& hsync, const u32x4 (&pq)[12], const int L) {
    Ctx c = c0; FRESH(c);
    const int lane = c.lane, w = c.w;
    const int tb = 4 * c.xg + (c.gi >> 3), fb = c.gi & 7;
    const int r = lane & 31, h = lane >> 5;
    const u32 OO = L ? OFF_O1 : OFF_O0, GS = L ? OFF_GS1 : OFF_GS0, GC = L ? OFF_GC1 : OFF_GC0, GM = L ? OFF_GM1 : OFF_GM0, GX = L ? OFF_GX1 : OFF_GX0;
    constexpr u32 L_TS = L_SB, L_CAND = P_ST + 8;
    if (w >= 4) {
        const int hw = w - 4;
        BAR();
        STAMP(L ? 21 : 8);
        BAR();
        BAR();
        __builtin_amdgcn_s_setprio(3);
        FST(30 + 20 * L);
        if (hw == 0) {
            const u32 a = L_SB + (u32)h * 384u + (u32)r * 12u;
            const float t1 = lds_ld<float>(a) + lds_ld<float>(a + 768), t2 = lds_ld<float>(a + 4) + lds_ld<float>(a + 772), t3 = lds_ld<float>(a + 8) + lds_ld<float>(a + 776);
            stw(c.ws, GS + (u32)((tb * 8 + fb) * 1024 + lane * 16), (u32x4){__float_as_uint(t1), __float_as_uint(t2), __float_as_uint(t3), c.tag});
        }
        FST(31 + 20 * L);
        if (L == 0) {
            if (hw != 0) { pre_store_wq(c, pq); lds_signal_add(c, P_WQ); }
            BAR();
        }
        FST(32 + 20 * L);
        if (hw == 0) {
            u32x4 v[8]; u32 n = 0;
            for (;;) {
                bool ok = true;
#pragma unroll
                for (int k = 0; k < 8; ++k) v[k] = ldw(c.ws, GS + (u32)((tb * 8 + k) * 1024 + lane * 16));
                MEMFENCE();
#pragma unroll
                for (int k = 0; k < 8; ++k) ok = ok && v[k][3] == c.tag;
                if (__all(ok)) break;
                if (spin_fail(c, n)) break;
            }
            FST(33 + 20 * L);
            float t1 = 0, t2 = 0, t3 = 0;
#pragma unroll
            for (int k = 0; k < 8; ++k) { t1 += __uint_as_float(v[k][0]); t2 += __uint_as_float(v[k][1]); t3 += __uint_as_float(v[k][2]); }
            const float mu = t1 * (1.0f / 512.0f), var = fmaxf(t2 * (1.0f / 512.0f) - mu * mu, 0.f);
            const float rs = __builtin_amdgcn_rsqf(var + 1e-5f);
            const float rt = rbf(rs * (t3 - mu * lds_ld<float>(P_WSUM)));
            const u32 key = half_umax(score_key(__float_as_uint(rt) >> 16, (u32)r));
            const int bt = (int)key_idx(key);
            if (fb == 0) {
                const u32 k0 = (u32)__builtin_amdgcn_readlane((int)key, 0), k1 = (u32)__builtin_amdgcn_readlane((int)key, 32);
                const u32 kT = (lane & 1) ? k1 : k0;
                const u64 g = (cand_tag(c.tag) << 21) | (u64)(key_bf(kT) << 5) | (u64)key_idx(kT);
                if (lane < 16) __builtin_amdgcn_raw_buffer_store_b64((u32x2){(u32)g, (u32)(g >> 32)}, c.ws, GC + (u32)(lane >> 1) * 512u + (u32)(tb * 2 + (lane & 1)) * 8u, 0, 16);
                if (r == bt) stw(c.ws, GM + (u32)(tb * 2 + h) * 16u, mk_unit(__float_as_uint(mu), __float_as_uint(rs), c.tag));
                if (L == 0) {
                    const float g1 = 1.0f + rs, A = g1 * __builtin_amdgcn_rsqf(var * g1 * g1 + 1e-5f);
                    stw(c.ws, OFF_TS1 + (u32)(tb * 64 + lane) * 16u, mk_unit(__float_as_uint(A), __float_as_uint(A * mu), c.tag));
                }
            }
            FST(34 + 20 * L);
            lds_st<f32x2>(L_TS + (u32)lane * 8u, (f32x2){mu, rs});
            if (r == 0) lds_st<u32>(L_CAND + (u32)h * 4u, (u32)bt);
            lds_signal_add(c, P_ST + L * 4);
            {
                const int ct = (lane >> 4) & 1, u = lane & 15;
                const int cb = __builtin_amdgcn_readlane(bt, 0), cb1 = __builtin_amdgcn_readlane(bt, 32);
                const int row = ct ? cb1 : cb;
                const u32x2 d = lds_ld<u32x2>(L_X1T + (u32)(ct + 2 * (u >> 3)) * 2048u + (u32)((u >> 2) & 1) * 1024u + (u32)((u & 1) * 32 + row) * 16u + (u32)((u >> 1) & 1) * 8u);
                if (lane < 32) stw(c.ws, GX + (u32)(tb * 2 + ct) * 2048u + (u32)(fb * 16 + u) * 16u, mk_unit(d[0], d[1], c.tag));
            }
            if (L == 0) { pre_store_wq(c, pq); lds_signal_add(c, P_WQ); }
        }
        STAMP(L ? 23 : 12);
        ffn_chain(c, p, hsync, L);
        if (L == 0) STAMP(15);
        return;
    }
    const int tt = w & 1, kh = w >> 1;
    const int tile = tb * 2 + tt, tok = tile * 32 + r;
    const int f0 = fb * 64 + kh * 32;
    u32x2 res[4], wr[4];
#pragma unroll
    for (int g = 0; g < 4; ++g) {
        if (L == 0) res[g] = *(const u32x2*)(p.x0 + (size_t)tok * 512 + f0 + 8 * g + 4 * h);
        wr[g] = *(const u32x2*)(p.wr + f0 + 8 * g + 4 * h);
    }
    if (L == 1) {
        const int sel0 = __builtin_amdgcn_readfirstlane((int)lds_ld<u32>(P_SEL)) & 2047;
        if ((sel0 >> 5) == tile) {
            const u32 ub = OFF_GY + (u32)(f0 / 4 + h) * 16u;
            u32 off[4] = {ub, ub + 32u, ub + 64u, ub + 96u}; u32x4 v[4];
            wait_units<4>(c, off, v);
            if (r == (sel0 & 31)) { x2[0] = (u32x4){v[0][0], v[0][2], v[1][0], v[1][2]}; x2[1] = (u32x4){v[2][0], v[2][2], v[3][0], v[3][2]}; }
        }
#pragma unroll
        for (int g = 0; g < 4; ++g) res[g] = (u32x2){x2[g >> 1][(g & 1) * 2], x2[g >> 1][(g & 1) * 2 + 1]};
    }
    BAR();
    STAMP(L ? 21 : 8);
    u32x4 bf[16];
    const u32 oo = OO + (u32)(((kh * 16) * 64 + tile) * 64 + lane) * 16u;
    {
        u32 n = 0;
        for (;;) {
#pragma unroll
            for (int s = 0; s < 16; ++s) bf[s] = ldw(c.ws, oo + (u32)s * 65536u);
            MEMFENCE();
            u32 m = 0;
#pragma unroll
            for (int s = 0; s < 16; ++s) m = max(m, max(max(bf[s][0], bf[s][1]), max(bf[s][2], bf[s][3])));
            if (!__any(m == POISON)) break;
            if (spin_fail(c, n)) break;
        }
    }
    const int i16 = lane & 15, blk = (lane >> 4) & 1;
    const u32 trA = L_WO + (u32)((kh * 256 + 4 * h + (i16 >> 2)) * 64 + blk * 32 + (i16 & 3) * 8);
    f32x16 a0 = {}, a1 = {};
    {
        u32 ta = trA, tb_ = trA + WO_FT;
        asm volatile("" : "+v"(ta), "+v"(tb_));
        bf16x8 f0_[2], f1_[2];
        f0_[0] = frag_tr(ta, ta + 512); f0_[1] = frag_tr(tb_, tb_ + 512);
#pragma unroll
        for (int s = 0; s < 16; s += 2) {
            f1_[0] = frag_tr(ta + (u32)(s + 1) * 1024u, ta + (u32)(s + 1) * 1024u + 512); SBAR();
            a0 = MFMA32(f0_[0], BC8(bf[s]), a0); SBAR();
            f1_[1] = frag_tr(tb_ + (u32)(s + 1) * 1024u, tb_ + (u32)(s + 1) * 1024u + 512); SBAR();
            a1 = MFMA32(f0_[1], BC8(bf[s]), a1); SBAR();
            if (s + 2 < 16) f0_[0] = frag_tr(ta + (u32)(s + 2) * 1024u, ta + (u32)(s + 2) * 1024u + 512);
            SBAR(); a0 = MFMA32(f1_[0], BC8(bf[s + 1]), a0); SBAR();
            if (s + 2 < 16) f0_[1] = frag_tr(tb_ + (u32)(s + 2) * 1024u, tb_ + (u32)(s + 2) * 1024u + 512);
            SBAR(); a1 = MFMA32(f1_[1], BC8(bf[s + 1]), a1); SBAR();
        }
    }
    {
        const f32x16& snd = kh ? a0 : a1;
#pragma unroll
        for (int g = 0; g < 4; ++g) lds_st<f32x4>(L_XB + (u32)w * 4096u + (u32)g * 1024u + (u32)lane * 16u, (f32x4){snd[4 * g], snd[4 * g + 1], snd[4 * g + 2], snd[4 * g + 3]});
    }
    BAR();
    STAMP(L ? 22 : 9);
    u32x4 xp[2];
    {
        const f32x16& keep = kh ? a1 : a0;
#pragma unroll
        for (int g = 0; g < 4; ++g) {
            const f32x4 o = lds_ld<f32x4>(L_XB + (u32)(w ^ 2) * 4096u + (u32)g * 1024u + (u32)lane * 16u);
            const u32 t0 = pk(keep[4 * g] + o[0], keep[4 * g + 1] + o[1]), t1 = pk(keep[4 * g + 2] + o[2], keep[4 * g + 3] + o[3]);
            xp[g >> 1][(g & 1) * 2] = pk(bflo(res[g][0]) + bflo(t0), bfhi(res[g][0]) + bfhi(t0));
            xp[g >> 1][(g & 1) * 2 + 1] = pk(bflo(res[g][1]) + bflo(t1), bfhi(res[g][1]) + bfhi(t1));
        }
    }
    if (L == 0) {
#pragma unroll
        for (int s = 0; s < 2; ++s) stw(c.ws, OFF_X1 + (u32)((tile * 32 + f0 / 16 + s) * 64 + lane) * 16u, xp[s]);
    }
#pragma unroll
    for (int s = 0; s < 2; ++s) lds_st<u32x4>(L_X1T + (u32)w * 2048u + (u32)s * 1024u + (u32)lane * 16u, xp[s]);
    float x1[16];
    {
        float s1 = 0, s2 = 0, s3 = 0;
#pragma unroll
        for (int i = 0; i < 16; ++i) {
            const u32 wd = xp[i >> 3][(i & 7) >> 1];
            x1[i] = (i & 1) ? bfhi(wd) : bflo(wd);
            const u32 ww = wr[i >> 2][(i & 3) >> 1];
            const float wv = (i & 1) ? bfhi(ww) : bflo(ww);
            s1 += x1[i]; s2 += x1[i] * x1[i]; s3 += x1[i] * wv;
        }
        s1 = swap32_add(s1); s2 = swap32_add(s2); s3 = swap32_add(s3);
        if (h == 0) { const u32 a = L_SB + (u32)w * 384u + (u32)r * 12u; lds_st<float>(a, s1); lds_st<float>(a + 4, s2); lds_st<float>(a + 8, s3); }
    }
    BAR();
    if (L == 0) {
        DRAIN(); BAR();
        STAMP(10);
        if (w == 0 && lane == 0) set_flag(c, OFF_FO + (u32)(tb * 8 + fb) * 128u);
        pre_store_wq(c, pq); lds_signal_add(c, P_WQ);
    }
    lds_wait_ge(P_ST + L * 4, 1u);
    STAMP(L ? 23 : 11);
    const f32x2 st = lds_ld<f32x2>(L_TS + (u32)(tt * 32 + r) * 8u);
    const int bt = (int)lds_ld<u32>(L_CAND + (u32)tt * 4u) & 31;
#pragma unroll
    for (int s = 0; s < 2; ++s)
#pragma unroll
        for (int j = 0; j < 4; ++j) {
            const float a = x1[8 * s + 2 * j], b_ = x1[8 * s + 2 * j + 1];
            const u32 hh = pk((a - st[0]) * st[1], (b_ - st[0]) * st[1]);
            x2[s][j] = pk(a + bflo(hh), b_ + bfhi(hh));
        }
    if (L == 1) {
        const u32 tl = L_XB + (u32)w * 4096u;
#pragma unroll
        for (int g = 0; g < 4; ++g) lds_st<u32x2>(tl + (u32)(r * 80 + (8 * g + 4 * h) * 2), (u32x2){x2[g >> 1][(g & 1) * 2], x2[g >> 1][(g & 1) * 2 + 1]});
        asm volatile("s_waitcnt lgkmcnt(0)" ::: "memory");
        u32x4 rowv[2];
#pragma unroll
        for (int q = 0; q < 2; ++q) {
            const int row = q * 16 + (lane >> 2);
            rowv[q] = lds_ld<u32x4>(tl + (u32)(row * 80 + (lane & 3) * 16));
            if (row != bt) *(u32x4*)(p.out + (size_t)(tile * 32 + row) * 512 + f0 + (lane & 3) * 8) = rowv[q];
        }
        wait_sel(1);
        const int sel1 = __builtin_amdgcn_readfirstlane((int)lds_ld<u32>(P_SEL + 32)) & 2047;
        if (tile * 32 + bt != sel1) {
#pragma unroll
            for (int q = 0; q < 2; ++q) {
                const int row = q * 16 + (lane >> 2);
                if (row == bt) *(u32x4*)(p.out + (size_t)(tile * 32 + row) * 512 + f0 + (lane & 3) * 8) = rowv[q];
            }
        }
        }
}

#if defined(__HIP_DEVICE_COMPILE__)
__attribute__((target("no-packed-fp32-ops")))
#endif
__global__ __launch_bounds__(512, 2) void fused_layers_kernel(Params p) {
    extern __shared__ __attribute__((aligned(16))) char lds[];
    Ctx c;
    const int tid = threadIdx.x;
    c.lane = tid & 63; c.w = __builtin_amdgcn_readfirstlane(tid >> 6);
    c.xg = blockIdx.x & 7; c.gi = blockIdx.x >> 3;
    c.ws = __builtin_amdgcn_make_buffer_rsrc(p.ws, 0, WS_BYTES, 0x00020000);
    c.tag = 0u;
    {
        const u32 o = (u32)(((4 * c.xg + (c.gi >> 3)) * 64 + (c.gi & 7) * 8 + 2 * (c.w & 3) + (c.w >> 2)) * 64 + c.lane) * 16u;
        const u32x4 pois = {POISON, POISON, POISON, POISON};
        stw(c.ws, OFF_O0 + o, pois); stw(c.ws, OFF_O1 + o, pois);
    }
    if (c.w == 6) {
        const u32 o = (u32)(((4 * c.xg + (c.gi >> 3)) * 8 + (c.gi & 7)) * 1024 + c.lane * 16);
        stw(c.ws, OFF_GS0 + o, (u32x4){0u, 0u, 0u, 0u}); stw(c.ws, OFF_GS1 + o, (u32x4){0u, 0u, 0u, 0u});
    }
    {
        u64 pc; u32 len;
        asm volatile("s_getpc_b64 %0\nfl_code_here:\n\ts_mov_b32 %1, fl_code_end-fl_code_here" : "=s"(pc), "=s"(len));
        if (c.w == 7) {
            const u32 o0 = min((u32)c.gi * 3072u + (u32)c.lane * 16u, len - 16u) & ~15u, o1 = min(o0 + 1024u, len - 16u) & ~15u, o2 = min(o0 + 2048u, len - 16u) & ~15u;
            const u64 base = pc & ~15ull;
            glds((const void*)base, o0, 147456u); glds((const void*)base, o1, 148480u); glds((const void*)base, o2, 149504u);
        }
    }
    if (c.w == 5) {
        const int l = c.lane;
        const char* a = p.ws + (size_t)(l < 10 ? l : 0) * 0x200000;
        a = l == 10 ? (const char*)p.wo : l == 11 ? (const char*)p.wr : l == 12 ? (const char*)p.wg : l == 13 ? (const char*)p.wu : l == 14 ? (const char*)p.wd : l == 15 ? (const char*)p.out : a;
        a = l == 16 ? (const char*)p.wg + 1835004 : l == 17 ? (const char*)p.wu + 1835004 : l == 18 ? (const char*)p.wd + 1835004 : l == 19 ? (const char*)p.out + 2097148 : a;
        u32 sv;
        asm volatile("s_mov_b32 %0, m0\n\ts_mov_b32 m0, %2\n\ts_nop 0\n\tglobal_load_lds_dword %1, off\n\ts_mov_b32 m0, %0" : "=&s"(sv) : "v"(a), "s"(150528u) : "memory");
    }
    if (tid < 24 && tid != 3) lds_st<u32>(P_HCNT + (u32)tid * 4u, 0u);
    asm volatile("" :: "v"(lds));
    BAR();
    STAMP(0);
    FST(0);
    u32 hsync = 0;
    u32x4 x2[2];
    asm volatile("" : "=v"(x2[0]), "=v"(x2[1]));
    u32x4 pq[12];
#pragma nounroll
    for (int Li = 0; Li < 2; ++Li) {
        int Lv = Li; asm volatile("" : "+v"(Lv));
        const int L = __builtin_amdgcn_readfirstlane(Lv);
        if (L == 0) phaseQ<0>(c, p, hsync, pq);
        else {
            phaseQ<1>(c, p, hsync, pq);
#pragma unroll
            for (int j = 0; j < 12; ++j) asm volatile("" : "=v"(pq[j]));
        }
        if (STOP_AFTER < 3 * L + 1) break;
        phaseA(c, p, L);
        if (STOP_AFTER < 3 * L + 2) break;
        phaseO(c, p, x2, hsync, pq, L);
        if (STOP_AFTER < 3 * L + 3) break;
    }
    STAMP(24);
    if (blockIdx.x == 14 * 8 && c.w == 4 && c.lane == 0) __builtin_amdgcn_raw_buffer_store_b32(c.tag, c.ws, OFF_EPOCH, 0, 16);
    asm volatile("fl_code_end:" ::: "memory");
}

extern "C" void kernel_launch(void* const* d_in, const int* in_sizes, int n_in, void* d_out, int out_size, void* d_ws, size_t ws_size, hipStream_t stream) {
    Params p;
    p.x0 = (const uint16_t*)d_in[0]; p.wqkv = (const uint16_t*)d_in[1]; p.wo = (const uint16_t*)d_in[2];
    p.wr = (const uint16_t*)d_in[4]; p.wg = (const uint16_t*)d_in[5]; p.wu = (const uint16_t*)d_in[6]; p.wd = (const uint16_t*)d_in[7];
    p.out = (uint16_t*)d_out; p.ws = (char*)d_ws;
    (void)hipFuncSetAttribute((const void*)fused_layers_kernel, hipFuncAttributeMaxDynamicSharedMemorySize, LDS_BYTES);
    fused_layers_kernel<<<256, 512, LDS_BYTES, stream>>>(p);
}
```

```cpp
#include <hip/hip_runtime.h>
#include <stdint.h>

typedef unsigned u32;
typedef unsigned long long u64;
typedef __attribute__((ext_vector_type(2))) u32 u32x2;
typedef __attribute__((ext_vector_type(4))) u32 u32x4;
typedef __attribute__((ext_vector_type(2))) float f32x2;
typedef __attribute__((ext_vector_type(4))) float f32x4;
typedef __attribute__((ext_vector_type(16))) float f32x16;
typedef __attribute__((ext_vector_type(8))) __bf16 bf16x8;
typedef __attribute__((ext_vector_type(2))) __bf16 bf16x2;
typedef __attribute__((ext_vector_type(4))) short s16x4;
typedef __attribute__((ext_vector_type(8))) short s16x8;
#define LAS __attribute__((address_space(3)))
#define DI __device__ __forceinline__
#define MFMA32(a, b, c) __builtin_amdgcn_mfma_f32_32x32x16_bf16((a), (b), (c), 0, 0, 0)
#define MFMA16(a, b, c) __builtin_amdgcn_mfma_f32_16x16x32_bf16((a), (b), (c), 0, 0, 0)
#define SBAR() __builtin_amdgcn_sched_barrier(0)
#define BAR() asm volatile("s_waitcnt lgkmcnt(0)\n\ts_barrier" ::: "memory")
#define DRAIN() asm volatile("s_waitcnt vmcnt(0)" ::: "memory")
#define MEMFENCE() asm volatile("" ::: "memory")

#define STOP_AFTER 99

constexpr u32 OFF_EPOCH = 0, OFF_ABORT = 256;
constexpr u32 OFF_FQ0 = 0x10000, OFF_FO = 0x20000, OFF_FQ1 = 0x28000;
constexpr u32 OFF_TS0 = 0x40000, OFF_TS1 = 0x48000;
constexpr u32 OFF_GC0 = 0x50000, OFF_GC1 = 0x51000;
constexpr u32 OFF_GM0 = 0x52000, OFF_GM1 = 0x52400;
constexpr u32 OFF_GA0 = 0x58000, OFF_GA1 = 0x5A000;
constexpr u32 OFF_GY = 0x5C000;
constexpr u32 OFF_GS0 = 0x80000, OFF_GS1 = 0xC0000;
constexpr u32 OFF_GX0 = 0x180000, OFF_GX1 = 0x1A0000;
constexpr u32 OFF_Q0 = 0x200000, OFF_K0 = 0x400000, OFF_V0 = 0x600000, OFF_O0 = 0x800000, OFF_X1 = 0xA00000;
constexpr u32 OFF_Q1 = 0xC00000, OFF_K1 = 0xE00000, OFF_V1 = 0x1000000, OFF_O1 = 0x1200000;
constexpr u32 WS_BYTES = 0x1400000;

constexpr u32 LDS_BYTES = 163840;
constexpr u32 L_W = 0;
constexpr u32 L_XR = 98304;
constexpr u32 L_AX = 140288;
constexpr u32 L_TOK = 161792 + 512;
constexpr u32 L_K = 0, L_V = 65536, VSTR = 1152;
constexpr u32 L_WO = 0, WO_FT = 65536;
constexpr u32 L_XB = 32768;
constexpr u32 L_SB = 161792;
constexpr u32 L_P = 163328;
constexpr u32 P_CS = L_P, P_HCNT = L_P + 384, P_PATCH = L_P + 388, P_WQ = L_P + 392, P_WSUM = L_P + 396, P_SEL = L_P + 400, P_ST = L_P + 464;
constexpr u32 L_X1T = 49152;
constexpr u32 FW0 = 98304, FS0 = 155648;
constexpr u32 FW1 = FW0, FS1 = FS0;
constexpr u32 SPIN_CAP = 40000;

struct Params {
    const uint16_t* x0; const uint16_t* wqkv; const uint16_t* wo; const uint16_t* wr;
    const uint16_t* wg; const uint16_t* wu; const uint16_t* wd; uint16_t* out; char* ws;
};

typedef __amdgpu_buffer_rsrc_t rsrc_t;

DI u32 pk(float a, float b) { f32x2 v = {a, b}; return __builtin_bit_cast(u32, __builtin_convertvector(v, bf16x2)); }
DI float bflo(u32 v) { return __uint_as_float(v << 16); }
DI float bfhi(u32 v) { return __uint_as_float(v & 0xffff0000u); }
DI float rbf(float a) { return bflo(pk(a, 0.f)); }
DI u32x4 ldw(rsrc_t r, u32 off) { return __builtin_amdgcn_raw_buffer_load_b128(r, off, 0, 16); }
DI void stw(rsrc_t r, u32 off, u32x4 v) { __builtin_amdgcn_raw_buffer_store_b128(v, r, off, 0, 16); }
template <class T> DI T lds_ld(u32 a) { return *(const LAS T*)(uintptr_t)a; }
template <class T> DI void lds_st(u32 a, T v) { *(LAS T*)(uintptr_t)a = v; }
DI s16x4 lds_tr(u32 a) { return __builtin_amdgcn_ds_read_tr16_b64_v4i16((LAS s16x4*)(uintptr_t)a); }
DI bf16x8 frag_tr(u32 a0, u32 a1) { s16x4 lo = lds_tr(a0), hi = lds_tr(a1); return __builtin_bit_cast(bf16x8, __builtin_shufflevector(lo, hi, 0, 1, 2, 3, 4, 5, 6, 7)); }
DI float swap32_add(float v) { auto r = __builtin_amdgcn_permlane32_swap(__float_as_uint(v), __float_as_uint(v), false, false); return __uint_as_float(r[0]) + __uint_as_float(r[1]); }
template <int CTRL> DI u32 dpp(u32 v) { return (u32)__builtin_amdgcn_update_dpp((int)v, (int)v, CTRL, 0xf, 0xf, true); }
DI float wave_sum(float v) {
    v += __uint_as_float(dpp<0xB1>(__float_as_uint(v)));
    v += __uint_as_float(dpp<0x4E>(__float_as_uint(v)));
    v += __uint_as_float(dpp<0x141>(__float_as_uint(v)));
    v += __uint_as_float(dpp<0x140>(__float_as_uint(v)));
    { auto r = __builtin_amdgcn_permlane16_swap(__float_as_uint(v), __float_as_uint(v), false, false); v = __uint_as_float(r[0]) + __uint_as_float(r[1]); }
    return swap32_add(v);
}
DI u32 half_umax(u32 v) {
    v = max(v, dpp<0xB1>(v)); v = max(v, dpp<0x4E>(v)); v = max(v, dpp<0x141>(v)); v = max(v, dpp<0x140>(v));
    auto r = __builtin_amdgcn_permlane16_swap(v, v, false, false);
    return max(r[0], r[1]);
}
DI u32 wave_umax(u32 v) { v = half_umax(v); auto r = __builtin_amdgcn_permlane32_swap(v, v, false, false); return max(r[0], r[1]); }
DI u32 score_key(u32 bf, u32 idx) { return ((bf ^ ((bf & 0x8000u) ? 0xFFFFu : 0x8000u)) << 16) | (0xFFFFu - idx); }
DI u32 key_bf(u32 k) { const u32 b = k >> 16; return b ^ ((b & 0x8000u) ? 0x8000u : 0xFFFFu); }
DI u32 key_idx(u32 k) { return 0xFFFFu - (k & 0xFFFFu); }
DI void glds(const void* sbase, u32 voff, u32 ldsdst) {
    u32 sv;
    asm volatile("s_mov_b32 %0, m0\n\ts_mov_b32 m0, %3\n\ts_nop 0\n\tglobal_load_lds_dwordx4 %1, %2\n\ts_mov_b32 m0, %0"
                 : "=&s"(sv) : "v"(voff), "s"(sbase), "s"(ldsdst) : "memory");
}
struct Ctx {
    rsrc_t ws; u32 tag; int lane, w, xg, gi;
};
#define FRESH(c) asm volatile("" : "+v"((c).lane), "+s"((c).w), "+s"((c).xg), "+s"((c).gi))
DI u32 epoch_tag(u32 v) { const u32 t = (u32)__builtin_amdgcn_readfirstlane((int)v) + 1u; return t ? t : 1u; }
DI bool spin_fail(const Ctx& c, u32& n) {
    __builtin_amdgcn_s_sleep(1);
    ++n;
    if (n > SPIN_CAP) { if (c.lane == 0) __builtin_amdgcn_raw_buffer_store_b32(c.tag, c.ws, OFF_ABORT, 0, 16); return true; }
    if ((n & 127u) == 0u) { u32 a = __builtin_amdgcn_raw_buffer_load_b32(c.ws, OFF_ABORT, 0, 16); if (__builtin_amdgcn_readfirstlane(a) == c.tag) return true; }
    return false;
}
DI void wait_flags(const Ctx& c, u32 off) {
    u32 n = 0;
    for (;;) {
        u32x2 v = __builtin_amdgcn_raw_buffer_load_b64(c.ws, off, 0, 16);
        MEMFENCE();
        if (__all(v[0] == c.tag && v[1] == ~c.tag)) break;
        if (spin_fail(c, n)) break;
    }
    MEMFENCE();
}
DI void set_flag(const Ctx& c, u32 off) { u32x2 v = {c.tag, ~c.tag}; __builtin_amdgcn_raw_buffer_store_b64(v, c.ws, off, 0, 16); }
constexpr u32 POISON = 0xFFFFFFFFu;
DI bool poisoned(u32x4 v) { return max(max(v[0], v[1]), max(v[2], v[3])) == POISON; }
DI u32x4 not_poison(u32x4 v) { return (u32x4){min(v[0], POISON - 1u), min(v[1], POISON - 1u), min(v[2], POISON - 1u), min(v[3], POISON - 1u)}; }
DI void wait_piece(const Ctx& c, u32 off, u32x4& v) {
    u32 n = 0;
    while (__any(poisoned(v))) { if (spin_fail(c, n)) break; v = ldw(c.ws, off); MEMFENCE(); }
}
DI bool unit_ok(u32x4 v, u32 tag) { return v[1] == tag && v[3] == tag; }
DI u32x4 mk_unit(u32 a, u32 b, u32 tag) { return (u32x4){a, tag, b, tag}; }
template <int N> DI void wait_units(const Ctx& c, const u32 (&off)[N], u32x4 (&v)[N]) {
    u32 n = 0;
    for (;;) {
        bool ok = true;
#pragma unroll
        for (int k = 0; k < N; ++k) v[k] = ldw(c.ws, off[k]);
        MEMFENCE();
#pragma unroll
        for (int k = 0; k < N; ++k) ok = ok && unit_ok(v[k], c.tag);
        if (__all(ok)) break;
        if (spin_fail(c, n)) break;
    }
    MEMFENCE();
}
DI u32 lds_poll_ld(u32 a) { return __hip_atomic_load((LAS u32*)(uintptr_t)a, __ATOMIC_RELAXED, __HIP_MEMORY_SCOPE_WORKGROUP); }
DI void helper_sync(const Ctx& c, u32& target) {
    target += 4;
    asm volatile("s_waitcnt lgkmcnt(0)" ::: "memory");
    if (c.lane == 0) __hip_atomic_fetch_add((LAS u32*)(uintptr_t)P_HCNT, 1u, __ATOMIC_RELAXED, __HIP_MEMORY_SCOPE_WORKGROUP);
    u32 n = 0;
    while (lds_poll_ld(P_HCNT) < target) { if (++n > (1u << 24)) break; }
    MEMFENCE();
}
DI void lds_wait_ge(u32 a, u32 v) {
    u32 n = 0;
    while (lds_poll_ld(a) < v) { __builtin_amdgcn_s_sleep(1); if (++n > (1u << 22)) break; }
    MEMFENCE();
}
DI void lds_signal_add(const Ctx& c, u32 a) {
    asm volatile("s_waitcnt lgkmcnt(0)" ::: "memory");
    if (c.lane == 0) __hip_atomic_fetch_add((LAS u32*)(uintptr_t)a, 1u, __ATOMIC_RELAXED, __HIP_MEMORY_SCOPE_WORKGROUP);
}
DI void wait_sel(int L) {
    u32 n = 0;
    while (lds_poll_ld(P_SEL + L * 32 + 16) == 0u) { __builtin_amdgcn_s_sleep(1); if (++n > (1u << 22)) break; }
    MEMFENCE();
}

#define STAMP(i) do { } while (0)
#define CSTAMP(i) do { } while (0)
#define FST(i) do { } while (0)

DI u64 cand_tag(u32 tag) { return ((u64)tag << 11) | (u64)((tag * 0x9E3779B1u) >> 21); }
DI void ffn_chain(const Ctx& c, const Params& p, u32& hsync, const int L) {
    const int lane = c.lane, hw = c.w - 4;
    constexpr u32 FW = FW0, FS = FS0;
    const u32 GC = L ? OFF_GC1 : OFF_GC0, GM = L ? OFF_GM1 : OFF_GM0, GX = L ? OFF_GX1 : OFF_GX0, GA = L ? OFF_GA1 : OFF_GA0;
    FST(20 + 20 * L);
    __builtin_amdgcn_s_setprio(3);
    const bool is_gu = c.gi < 14, is_dn = c.gi >= 14 && c.gi < 18;
    const int g16 = lane >> 4, i16 = lane & 15;
    bf16x8 wf[14];
    {
        const u32 tro = FW + (u32)((8 * g16 + (i16 >> 2)) * 32 + (i16 & 3) * 8) + (u32)hw * (is_gu ? 4096u : 14336u);
#pragma unroll
        for (int s = 0; s < 14; ++s) {
            const u32 bo = tro + (is_gu ? (u32)(s & 3) * 1024u + (u32)((s >> 2) & 1) * 16384u : (u32)s * 1024u);
            if (is_gu || is_dn) wf[s] = frag_tr(bo, bo + 128);
        }
    }
    if (hw == 0) {
        const u64 want = cand_tag(c.tag);
        u64 v; u32 n = 0;
        for (;;) {
            const u32x2 t = __builtin_amdgcn_raw_buffer_load_b64(c.ws, GC + c.xg * 512u + lane * 8u, 0, 16);
            MEMFENCE();
            v = ((u64)t[1] << 32) | t[0];
            if (__all((v >> 21) == want)) break;
            if (spin_fail(c, n)) break;
        }
        FST(21 + 20 * L);
        const u32 key = wave_umax(score_key(((u32)v >> 5) & 0xFFFFu, (u32)lane * 32u + ((u32)v & 31u)));
        const u32 tok = key_idx(key); const float r = __uint_as_float(key_bf(key) << 16);
        if (lane == 0) {
            lds_st<u32x2>(P_SEL + L * 32, (u32x2){(u32)tok, __float_as_uint(r)});
            asm volatile("s_waitcnt lgkmcnt(0)" ::: "memory");
            __hip_atomic_store((LAS u32*)(uintptr_t)(P_SEL + L * 32 + 16), 1u, __ATOMIC_RELAXED, __HIP_MEMORY_SCOPE_WORKGROUP);
        }
    }
    FST(22 + 20 * L);
    STAMP(L ? 25 : 13);
    if (!is_gu && !is_dn) return;
    wait_sel(L);
    const u32x2 si = lds_ld<u32x2>(P_SEL + L * 32);
    const int sel = __builtin_amdgcn_readfirstlane((int)si[0]) & 2047;
    const float rsel = __uint_as_float(si[1]);
    const u32 gxrow = GX + (u32)(sel >> 5) * 2048u, gm = GM + (u32)(sel >> 5) * 16u;
    if (is_gu) {
        const int gidx = c.xg * 14 + c.gi;
        u32 off[2] = {gxrow + (u32)(hw * 32 + (lane & 31)) * 16u, gm};
        u32x4 v[2];
        FST(29 + 20 * L);
        wait_units<2>(c, off, v);
        FST(23 + 20 * L);
        const float mu = __uint_as_float(v[1][0]), rs = __uint_as_float(v[1][2]);
        if (lane < 32) {
            u32x2 h;
            h[0] = pk((bflo(v[0][0]) - mu) * rs, (bfhi(v[0][0]) - mu) * rs);
            h[1] = pk((bflo(v[0][2]) - mu) * rs, (bfhi(v[0][2]) - mu) * rs);
            lds_st<u32x2>(FS + (u32)(hw * 32 + lane) * 8u, h);
        }
        STAMP(L ? 26 : 14);
        FST(24 + 20 * L);
        f32x4 ag = {}, au = {};
#pragma unroll
        for (int s = 0; s < 4; ++s) {
            const bf16x8 a = lds_ld<bf16x8>(FS + (u32)((hw * 4 + s) * 64 + g16 * 16));
            ag = MFMA16(a, wf[s], ag);
            au = MFMA16(a, wf[4 + s], au);
        }
        if (lane < 16) { lds_st<float>(FS + 4608 + (u32)(hw * 32 + lane) * 4u, ag[0]); lds_st<float>(FS + 4608 + (u32)(hw * 32 + 16 + lane) * 4u, au[0]); }
        FST(25 + 20 * L);
        helper_sync(c, hsync);
        FST(26 + 20 * L);
        if (hw == 0) {
            float g = 0.f, u = 0.f;
#pragma unroll
            for (int k = 0; k < 4; ++k) { g += lds_ld<float>(FS + 4608 + (u32)(k * 32 + i16) * 4u); u += lds_ld<float>(FS + 4608 + (u32)(k * 32 + 16 + i16) * 4u); }
            g = rbf(g); u = rbf(u);
            const float sg = rbf(g * __builtin_amdgcn_rcpf(1.0f + __expf(-g)));
            const float a = rbf(sg * u);
            const float a0 = __shfl(a, (lane & 3) * 4), a1 = __shfl(a, (lane & 3) * 4 + 1), a2 = __shfl(a, (lane & 3) * 4 + 2), a3 = __shfl(a, (lane & 3) * 4 + 3);
            if (lane < 4) stw(c.ws, GA + (u32)(gidx * 4 + lane) * 16u, mk_unit(pk(a0, a1), pk(a2, a3), c.tag));
            FST(27 + 20 * L);
        }
    } else {
        const int didx = c.xg * 4 + (c.gi - 14);
        float mu, rs;
        {
            const int u0 = hw * 112 + lane, u1 = hw * 112 + 64 + (lane < 48 ? lane : 47);
            u32 off[4] = {gxrow + (u32)(didx * 4 + (lane & 3)) * 16u, gm, GA + (u32)u0 * 16u, GA + (u32)u1 * 16u};
            u32x4 v[4];
            wait_units<4>(c, off, v);
            FST(23 + 20 * L);
            mu = __uint_as_float(v[1][0]); rs = __uint_as_float(v[1][2]);
            lds_st<u32x2>(FS + 1024 + (u32)u0 * 8u, (u32x2){v[2][0], v[2][2]});
            if (lane < 48) lds_st<u32x2>(FS + 1024 + (u32)u1 * 8u, (u32x2){v[3][0], v[3][2]});
            if (hw == 0 && lane < 4) lds_st<u32x2>(FS + (u32)lane * 8u, (u32x2){v[0][0], v[0][2]});
        }
        STAMP(L ? 26 : 14);
        FST(24 + 20 * L);
        f32x4 ay = {}, az = {};
        bf16x8 af[14];
#pragma unroll
        for (int s = 0; s < 14; ++s) af[s] = lds_ld<bf16x8>(FS + 1024 + (u32)((hw * 14 + s) * 64 + g16 * 16));
#pragma unroll
        for (int s = 0; s < 14; s += 2) { ay = MFMA16(af[s], wf[s], ay); az = MFMA16(af[s + 1], wf[s + 1], az); }
        if (lane < 16) lds_st<float>(FS + 4608 + (u32)(hw * 16 + lane) * 4u, ay[0] + az[0]);
        FST(25 + 20 * L);
        helper_sync(c, hsync);
        FST(26 + 20 * L);
        if (hw == 0) {
            float y = 0.f;
#pragma unroll
            for (int k = 0; k < 4; ++k) y += lds_ld<float>(FS + 4608 + (u32)(k * 16 + i16) * 4u);
            const float x1 = __uint_as_float((u32)lds_ld<uint16_t>(FS + (u32)i16 * 2u) << 16);
            const float sig = rbf(__builtin_amdgcn_rcpf(1.0f + __expf(-rsel)));
            y = rbf(rbf(y) * sig);
            const float h = rbf((x1 - mu) * rs);
            const float x2 = rbf(x1 + rbf(h + y));
            const float a0 = __shfl(x2, (lane & 3) * 4), a1 = __shfl(x2, (lane & 3) * 4 + 1), a2 = __shfl(x2, (lane & 3) * 4 + 2), a3 = __shfl(x2, (lane & 3) * 4 + 3);
            if (L == 0) { if (lane < 4) stw(c.ws, OFF_GY + (u32)(didx * 4 + lane) * 16u, mk_unit(pk(a0, a1), pk(a2, a3), c.tag)); }
            else if (lane < 4) *(u32x2*)(p.out + (size_t)sel * 512 + didx * 16 + lane * 4) = (u32x2){pk(a0, a1), pk(a2, a3)};
            FST(27 + 20 * L);
        }
    }
}

DI void qkv_patch(const Ctx& c, u32& hsync) {
    const int lane = c.lane, hw = c.w - 4;
    constexpr u32 FS = FS0;
    const int g16 = lane >> 4, i16 = lane & 15;
    bf16x8 wf[24];
    {
        const u32 tro = L_W + (u32)((8 * g16 + (i16 >> 2)) * 64 + (i16 & 3) * 8) + (u32)hw * 8192u;
#pragma unroll
        for (int s_ = 0; s_ < 4; ++s_)
#pragma unroll
            for (int ct = 0; ct < 6; ++ct) {
                const u32 bo = tro + (u32)s_ * 2048u + (u32)(ct >> 1) * 32768u + (u32)(ct & 1) * 32u;
                wf[s_ * 6 + ct] = frag_tr(bo, bo + 256);
            }
    }
    u32 off[2] = {OFF_GY + (u32)lane * 32u, OFF_GY + (u32)lane * 32u + 16u}; u32x4 v[2];
    wait_units<2>(c, off, v);
    float x[8], s = 0, q = 0;
#pragma unroll
    for (int k = 0; k < 2; ++k) { x[4 * k] = bflo(v[k][0]); x[4 * k + 1] = bfhi(v[k][0]); x[4 * k + 2] = bflo(v[k][2]); x[4 * k + 3] = bfhi(v[k][2]); }
#pragma unroll
    for (int k = 0; k < 8; ++k) { s += x[k]; q += x[k] * x[k]; }
    s = wave_sum(s); q = wave_sum(q);
    const float mu = s * (1.0f / 512.0f), var = fmaxf(q * (1.0f / 512.0f) - mu * mu, 0.f);
    const float A = __builtin_amdgcn_rsqf(var + 1e-5f);
    lds_st<u32x4>(FS + (u32)lane * 16u, (u32x4){pk((x[0] - mu) * A, (x[1] - mu) * A), pk((x[2] - mu) * A, (x[3] - mu) * A), pk((x[4] - mu) * A, (x[5] - mu) * A), pk((x[6] - mu) * A, (x[7] - mu) * A)});
    f32x4 acc[6] = {};
#pragma unroll
    for (int s_ = 0; s_ < 4; ++s_) {
        const bf16x8 a = lds_ld<bf16x8>(FS + (u32)((hw * 4 + s_) * 64 + g16 * 16));
#pragma unroll
        for (int ct = 0; ct < 6; ++ct) acc[ct] = MFMA16(a, wf[s_ * 6 + ct], acc[ct]);
    }
    if (lane < 16) {
#pragma unroll
        for (int ct = 0; ct < 6; ++ct) lds_st<float>(FS + 1024 + (u32)(hw * 96 + ct * 16 + lane) * 4u, acc[ct][0]);
    }
    helper_sync(c, hsync);
    if (hw == 0) {
#pragma unroll
        for (int k = 0; k < 2; ++k) {
            const int n = k * 64 + lane;
            if (n < 96) {
                float t = 0.f;
#pragma unroll
                for (int j = 0; j < 4; ++j) t += lds_ld<float>(FS + 1024 + (u32)(j * 96 + n) * 4u);
                lds_st<float>(FS + 4608 + (u32)n * 4u, n < 32 ? t * (0.25f * 1.4426950408889634f) : t);
            }
        }
        asm volatile("s_waitcnt lgkmcnt(0)" ::: "memory");
        if (lane == 0) __hip_atomic_store((LAS u32*)(uintptr_t)P_PATCH, 1u, __ATOMIC_RELAXED, __HIP_MEMORY_SCOPE_WORKGROUP);
    }
}

DI u32x4 gld(const void* base, u32 off) { return *(const u32x4*)((const char*)base + off); }
struct FfSrc { const uint16_t* m; u32 vo, step; bool on, gu; };
DI FfSrc ff_src(const Ctx& c, const Params& p) {
    int lane = c.lane; asm volatile("" : "+v"(lane));
    FfSrc f; const int w = c.w;
    f.on = c.gi < 18; f.gu = c.gi < 14;
    f.m = f.gu ? (w < 4 ? p.wg : p.wu) : p.wd;
    const u32 rowb = f.gu ? 3584u : 1024u, col = f.gu ? (u32)(c.xg * 14 + c.gi) * 32u : (u32)(c.xg * 4 + (c.gi - 14)) * 32u;
    const u32 first = f.gu ? (u32)(w & 3) * 4u : (u32)w * 7u;
    f.step = 32u * rowb;
    f.vo = (u32)(lane >> 1) * rowb + col + (u32)(lane & 1) * 16u + first * f.step;
    return f;
}
DI void keep_wq(const Ctx& c, u32x4 (&pq)[12]) {
#pragma unroll
    for (int j = 0; j < 12; ++j) pq[j] = lds_ld<u32x4>(L_W + (u32)(c.w * 12 + j) * 1024u + (u32)c.lane * 16u);
}
DI void pre_store_wq(const Ctx& c, const u32x4 (&pq)[12]) {
#pragma unroll
    for (int j = 0; j < 12; ++j) lds_st<u32x4>(L_W + (u32)(c.w * 12 + j) * 1024u + (u32)c.lane * 16u, pq[j]);
}
DI void pre_store_ff(const Ctx& c, const u32x4 (&pf)[7]) {
    if (c.gi < 18) {
        const bool gu = c.gi < 14;
        const u32 a = FW0 + (u32)c.w * (gu ? 4096u : 7168u) + (u32)c.lane * 16u;
#pragma unroll
        for (int j = 0; j < 7; ++j) if (j < 4 || !gu) lds_st<u32x4>(a + (u32)j * 1024u, pf[j]);
    }
}
DI u32 wo_lane_off(const Ctx& c) {
    int lane = c.lane; asm volatile("" : "+v"(lane));
    return (u32)((lane >> 2) * 1024 + (c.gi & 7) * 128 + (lane & 3) * 16 + (c.w >> 2) * 64 + (c.w & 3) * 8 * 16384);
}
DI void pre_store_wo(const Ctx& c, const u32x4 (&po)[8]) {
#pragma unroll
    for (int j = 0; j < 8; ++j) lds_st<u32x4>(L_WO + (u32)(c.w >> 2) * WO_FT + (u32)((c.w & 3) * 8 + j) * 1024u + (u32)c.lane * 16u, po[j]);
}

struct WFrag { bf16x8 q, k, v; };
template <u32 TR2> DI void wfrag_load(WFrag& f, u32 a01, u32 a2) { f.q = frag_tr(a01, a01 + TR2); f.k = frag_tr(a01 + 32768, a01 + 32768 + TR2); f.v = frag_tr(a2, a2 + TR2); }
template <u32 TR2> DI void qstep(f32x16& aq, f32x16& ak, f32x16& av, bf16x8 x, WFrag& cur, WFrag& nxt, u32 a01, u32 a2, bool more) {
    if (more) nxt.q = frag_tr(a01, a01 + TR2);
    SBAR(); aq = MFMA32(cur.q, x, aq); SBAR();
    if (more) nxt.k = frag_tr(a01 + 32768, a01 + 32768 + TR2);
    SBAR(); ak = MFMA32(cur.k, x, ak); SBAR();
    if (more) nxt.v = frag_tr(a2, a2 + TR2);
    SBAR(); av = MFMA32(x, cur.v, av); SBAR();
}

template <int L>
DI void phaseQ(Ctx& c0, const Params& p, u32& hsync, u32x4 (&pq)[12]) {
    Ctx c = c0; FRESH(c);
    const int lane = c.lane, w = c.w;
    const int rb = 4 * (c.xg & 3) + (c.gi & 3), hp = 8 * (c.xg >> 2) + (c.gi >> 2);
    const int r = lane & 31, h = lane >> 5;
    const int i16 = lane & 15, blk = (lane >> 4) & 1;
    const u32 trA = L_W + (u32)(((L == 0 ? 8 : 4) * h + (i16 >> 2)) * 64 + blk * 32 + (i16 & 3) * 8);
    constexpr u32 TR2 = L == 0 ? 256 : 512;
    constexpr u32 OQ = L ? OFF_Q1 : OFF_Q0, OK_ = L ? OFF_K1 : OFF_K0, OV = L ? OFF_V1 : OFF_V0, TS = L ? OFF_TS1 : OFF_TS0;
    const int rot = ((c.gi >> 2) + 2 * (c.gi & 3)) & 7;

    if (w >= 4) {
        const int hw = w - 4;
        if (L == 0) {
            u32x4 r0, r1; u32 ep;
            asm volatile("global_load_dword %0, %1, %2 sc1" : "=v"(ep) : "v"(0u), "s"(p.ws));
            const u32 tok = (u32)(rb * 128 + hp * 8 + hw * 2);
            const u32 vo = tok * 1024u + (u32)lane * 16u;
            asm volatile("global_load_dwordx4 %0, %1, %2" : "=v"(r0) : "v"(vo), "s"(p.x0));
            asm volatile("global_load_dwordx4 %0, %1, %2 offset:1024" : "=v"(r1) : "v"(vo), "s"(p.x0));
            const u32 wv = (u32)((lane >> 2) * 3072 + hp * 64 + (lane & 3) * 16 + hw * 16 * 3072);
            u32 xv[4];
#pragma unroll
            for (int q = 0; q < 4; ++q) { const int row = 8 * q + (lane >> 3); xv[q] = (u32)(((rb * 4 + hw) * 32 + row) * 1024 + (((lane & 7) ^ ((row >> 1) & 7)) * 16)); }
#define WD(i) do { const u32 kc_ = (u32)(((i) + rot) & 7); _Pragma("unroll") for (int t = 0; t < 3; ++t) \
                glds(p.wqkv, wv + (u32)t * 1024u + kc_ * (64u * 3072u), L_W + (u32)t * 32768u + (kc_ * 64u + (u32)hw * 16u) * 64u); } while (0)
#define XD(i) do { const u32 kc_ = (u32)(((i) + rot) & 7); _Pragma("unroll") for (int q = 0; q < 4; ++q) \
                glds(p.x0, xv[q] + kc_ * 128u, L_XR + (u32)((i) % 3) * 16384u + (u32)hw * 4096u + (u32)q * 1024u); } while (0)
#define QWAIT(n) asm volatile("s_waitcnt vmcnt(" #n ")\n\ts_barrier" ::: "memory")
#define CS(i) if (hw < 3) { _Pragma("unroll") for (int s_ = 0; s_ < 4; ++s_) { \
                const u32 a_ = trA + (u32)hw * 32768u + (u32)((((i) + rot) & 7) * 4 + s_) * 1024u; cs = MFMA32(frag_tr(a_, a_ + TR2), ones, cs); } }
            WD(0); XD(0); WD(1); XD(1); WD(2); XD(2); WD(3);
            FST(1);
            f32x16 cs = {};
            const bf16x8 ones = __builtin_bit_cast(bf16x8, (u32x4){0x3f803f80u, 0x3f803f80u, 0x3f803f80u, 0x3f803f80u});
            asm volatile("s_waitcnt vmcnt(17)\n\ts_barrier" : "+v"(r0), "+v"(r1), "+v"(ep) :: "memory");
            c.tag = epoch_tag(ep); c0.tag = c.tag;
            FST(2);
            {
                float s0 = 0, q0 = 0, s1 = 0, q1 = 0;
#pragma unroll
                for (int j = 0; j < 4; ++j) {
                    float a = bflo(r0[j]), b_ = bfhi(r0[j]); s0 += a + b_; q0 += a * a + b_ * b_;
                    float d = bflo(r1[j]), e = bfhi(r1[j]); s1 += d + e; q1 += d * d + e * e;
                }
                s0 = wave_sum(s0); q0 = wave_sum(q0); s1 = wave_sum(s1); q1 = wave_sum(q1);
                const float s = lane ? s1 : s0, q = lane ? q1 : q0;
                const float mu = s * (1.0f / 512.0f), var = fmaxf(q * (1.0f / 512.0f) - mu * mu, 0.f);
                const float A = __builtin_amdgcn_rsqf(var + 1e-5f);
                if (lane < 2) stw(c.ws, TS + (tok + (u32)lane) * 16u, mk_unit(__float_as_uint(A), __float_as_uint(A * mu), c.tag));
            }
            CS(0);
            QWAIT(11); FST(3); XD(3); WD(4); CS(1);
            QWAIT(11); FST(4); XD(4); WD(5); CS(2);
            QWAIT(10); FST(5); XD(5); WD(6); CS(3);
            QWAIT(10); FST(6); XD(6); WD(7); CS(4);
            QWAIT(10); FST(7); XD(7); CS(5);
            QWAIT(7); FST(8); CS(6);
            QWAIT(0); FST(9); CS(7);
#undef WD
#undef XD
#undef QWAIT
#undef CS
            if (hw < 3 && r == 0) {
#pragma unroll
                for (int g = 0; g < 4; ++g) lds_st<f32x4>(P_CS + (u32)(hw * 32 + 8 * g + 4 * h) * 4u, (f32x4){cs[4 * g], cs[4 * g + 1], cs[4 * g + 2], cs[4 * g + 3]});
            }
            STAMP(1);
            BAR();
        }
        DRAIN(); BAR();
        STAMP(L ? 16 : 3);
        if (L == 0) keep_wq(c, pq);
        else {
            wait_sel(0);
            const int sel = __builtin_amdgcn_readfirstlane((int)lds_ld<u32>(P_SEL)) & 2047;
            if ((sel >> 7) == rb) { lds_wait_ge(P_WQ, 8u); qkv_patch(c, hsync); }
        }
    } else {
        const int T = rb * 4 + w;
        f32x16 aq = {}, ak = {}, av = {};
        float A, B;
        if (L == 0) {
            const u32 ep = __builtin_amdgcn_raw_buffer_load_b32(c.ws, OFF_EPOCH, 0, 16);
            u32 xa[4];
#pragma unroll
            for (int s_ = 0; s_ < 4; ++s_) xa[s_] = L_XR + (u32)w * 4096u + (u32)(r * 128 + (((2 * s_ + h) ^ ((r >> 1) & 7)) * 16));
            bf16x8 xr[2][4];
            u32x4 tsv;
            WFrag f0, f1;
            asm volatile("s_barrier" ::: "memory");
            FST(2);
#pragma unroll
            for (int s_ = 0; s_ < 4; ++s_) xr[0][s_] = lds_ld<bf16x8>(xa[s_]);
            { const u32 a01 = trA + (u32)(rot & 7) * 4096u; wfrag_load<TR2>(f0, a01, a01 + 65536u); }
#pragma unroll
            for (int i = 0; i < 8; ++i) {
                const u32 a01 = trA + (u32)((i + rot) & 7) * 4096u, a2 = a01 + 65536u;
                const u32 n01 = trA + (u32)((i + 1 + rot) & 7) * 4096u, n2 = n01 + 65536u;
                qstep<TR2>(aq, ak, av, xr[i & 1][0], f0, f1, a01 + 1024, a2 + 1024, true);
                qstep<TR2>(aq, ak, av, xr[i & 1][1], f1, f0, a01 + 2048, a2 + 2048, true);
                qstep<TR2>(aq, ak, av, xr[i & 1][2], f0, f1, a01 + 3072, a2 + 3072, true);
                if (i == 6) tsv = ldw(c.ws, TS + (u32)(T * 32 + r) * 16u);
                if (i < 7) {
                    asm volatile("s_barrier" ::: "memory");
                    FST(3 + i);
#pragma unroll
                    for (int s_ = 0; s_ < 4; ++s_) xr[(i + 1) & 1][s_] = lds_ld<bf16x8>(xa[s_] + (u32)((i + 1) % 3) * 16384u);
                }
                qstep<TR2>(aq, ak, av, xr[i & 1][3], f1, f0, n01, n2, i < 7);
            }
            FST(10);
            STAMP(1);
            c.tag = epoch_tag(ep); c0.tag = c.tag;
            if (w == 3) {
                const u32x4 v = *(const u32x4*)(p.wr + lane * 8); float s = 0;
#pragma unroll
                for (int j = 0; j < 4; ++j) s += bflo(v[j]) + bfhi(v[j]);
                s = wave_sum(s);
                if (lane == 0) lds_st<float>(P_WSUM, s);
            }
            u32 off[1] = {TS + (u32)(T * 32 + r) * 16u}; u32x4 v[1] = {tsv};
            if (!__all(unit_ok(tsv, c.tag))) wait_units<1>(c, off, v);
            A = __uint_as_float(v[0][0]); B = __uint_as_float(v[0][2]);
            BAR();
            STAMP(2);
        } else {
            wait_flags(c, OFF_FO + (u32)(rb * 16 + (lane & 15)) * 128u);
            STAMP(13);
            u32x4 xf[32];
            const u32 xo = OFF_X1 + (u32)T * 32768u + (u32)lane * 16u;
#pragma unroll
            for (int s = 0; s < 32; ++s) xf[s] = ldw(c.ws, xo + (u32)s * 1024u);
            const u32x4 tsv = ldw(c.ws, TS + (u32)(T * 32 + r) * 16u);
            lds_wait_ge(P_WQ, 8u);
            STAMP(14);
            {
                u32 a01 = trA, a2 = trA + 65536u;
                asm volatile("" : "+v"(a01), "+v"(a2));
                WFrag f0, f1;
                wfrag_load<TR2>(f0, a01, a2);
#pragma unroll
                for (int st = 0; st < 32; st += 2) {
                    qstep<TR2>(aq, ak, av, __builtin_bit_cast(bf16x8, xf[st]), f0, f1, a01 + (u32)(st + 1) * 1024u, a2 + (u32)(st + 1) * 1024u, true);
                    qstep<TR2>(aq, ak, av, __builtin_bit_cast(bf16x8, xf[st + 1]), f1, f0, a01 + (u32)(st + 2) * 1024u, a2 + (u32)(st + 2) * 1024u, st + 2 < 32);
                }
            }
            u32 off[1] = {TS + (u32)(T * 32 + r) * 16u}; u32x4 v[1] = {tsv};
            if (!__all(unit_ok(tsv, c.tag))) wait_units<1>(c, off, v);
            A = __uint_as_float(v[0][0]); B = __uint_as_float(v[0][2]);
        }
        if (L == 1) STAMP(15);
        int ts = -1;
        if (L == 1) {
            wait_sel(0);
            const int sel = __builtin_amdgcn_readfirstlane((int)lds_ld<u32>(P_SEL)) & 2047;
            if ((sel >> 5) == T) ts = sel & 31;
        }
        const u32x4 pois = {POISON, POISON, POISON, POISON};
        const u32 tokl = L_TOK + (u32)w * 256u;
        if (h == 0) lds_st<f32x2>(tokl + (u32)r * 8u, (f32x2){A, B});
        constexpr float QS = 0.25f * 1.4426950408889634f;
#pragma unroll
        for (int t = 0; t < 2; ++t) {
            const f32x16& acc = t ? ak : aq;
            const float sA = t ? A : A * QS, sB = t ? B : B * QS;
            float o[16];
#pragma unroll
            for (int g = 0; g < 4; ++g) {
                const f32x4 cs = lds_ld<f32x4>(P_CS + (u32)(t * 32 + 8 * g + 4 * h) * 4u);
#pragma unroll
                for (int e = 0; e < 4; ++e) o[4 * g + e] = sA * acc[4 * g + e] - sB * cs[e];
            }
#pragma unroll
            for (int a = 0; a < 2; ++a) {
                u32x4 v = {pk(o[8 * a], o[8 * a + 1]), pk(o[8 * a + 2], o[8 * a + 3]), pk(o[8 * a + 4], o[8 * a + 5]), pk(o[8 * a + 6], o[8 * a + 7])};
                if (L == 1 && r == ts) v = pois;
                stw(c.ws, (t ? OK_ : OQ) + (u32)(((hp * 2 + a) * 64 + T) * 64 + lane) * 16u, v);
            }
        }
        u32x4 vlate = pois;
        const u32 vbase = OV + (u32)(((hp * 2 + (r >> 4)) * 64 + T) * 1024 + h * 256 + (r & 15) * 16);
        {
            asm volatile("s_waitcnt lgkmcnt(0)" ::: "memory");
            const float cs = lds_ld<float>(P_CS + (u32)(64 + r) * 4u);
            float o[16];
#pragma unroll
            for (int g = 0; g < 4; ++g) {
                const f32x4 ab0 = lds_ld<f32x4>(tokl + (u32)(8 * g + 4 * h) * 8u), ab1 = lds_ld<f32x4>(tokl + (u32)(8 * g + 4 * h + 2) * 8u);
                o[4 * g] = ab0[0] * av[4 * g] - ab0[1] * cs; o[4 * g + 1] = ab0[2] * av[4 * g + 1] - ab0[3] * cs;
                o[4 * g + 2] = ab1[0] * av[4 * g + 2] - ab1[1] * cs; o[4 * g + 3] = ab1[2] * av[4 * g + 3] - ab1[3] * cs;
            }
#pragma unroll
            for (int s = 0; s < 2; ++s) {
                u32x4 v = {pk(o[8 * s], o[8 * s + 1]), pk(o[8 * s + 2], o[8 * s + 3]), pk(o[8 * s + 4], o[8 * s + 5]), pk(o[8 * s + 6], o[8 * s + 7])};
                if (L == 1 && ts >= 0 && s == (ts >> 4) && h == ((ts >> 2) & 1)) { vlate = v; v = pois; }
                stw(c.ws, vbase + (u32)s * 512u, v);
            }
        }
        DRAIN(); BAR();
        STAMP(L ? 16 : 3);
        if (w == 0 && lane == 0) set_flag(c, (L ? OFF_FQ1 : OFF_FQ0) + (u32)(hp * 16 + rb) * 128u);
        if (L == 1 && ts >= 0) {
            lds_wait_ge(P_PATCH, 1u);
            if (r == ts) {
#pragma unroll
                for (int t = 0; t < 2; ++t)
#pragma unroll
                    for (int a = 0; a < 2; ++a) {
                        const f32x4 p0 = lds_ld<f32x4>(FS0 + 4608 + (u32)(t * 32 + 16 * a + 4 * h) * 4u), p1 = lds_ld<f32x4>(FS0 + 4608 + (u32)(t * 32 + 16 * a + 8 + 4 * h) * 4u);
                        stw(c.ws, (t ? OK_ : OQ) + (u32)(((hp * 2 + a) * 64 + T) * 64 + lane) * 16u, not_poison((u32x4){pk(p0[0], p0[1]), pk(p0[2], p0[3]), pk(p1[0], p1[1]), pk(p1[2], p1[3])}));
                    }
            }
            if (h == ((ts >> 2) & 1)) {
                const u32 pv = pk(lds_ld<float>(FS0 + 4608 + (u32)(64 + r) * 4u), 0.f) & 0xFFFFu;
                const int jj = (ts & 3) + 4 * ((ts >> 3) & 1);
#pragma unroll
                for (int k = 0; k < 4; ++k) if (k == (jj >> 1)) vlate[k] = (jj & 1) ? ((vlate[k] & 0xFFFFu) | (pv << 16)) : ((vlate[k] & 0xFFFF0000u) | pv);
                stw(c.ws, vbase + (u32)(ts >> 4) * 512u, not_poison(vlate));
            }
        }
        if (L == 0) keep_wq(c, pq);
    }
}

struct AState { f32x16 Sa, Sb, Oa, Ob; bf16x8 kC, kN, v0, v1; u32x4 pb1; };
#define EXP2(x) __builtin_amdgcn_exp2f(x)
#define BC8(x) __builtin_bit_cast(bf16x8, x)
#define PVM(O, v, p) O = MFMA32(v, BC8(p), O)
template <bool USE_C, class HOOK>
DI void attn_step(AState& s, bf16x8 qa, bf16x8 qb, const f32x16& ca, const f32x16& cb, u32 kp, u32 vp0, u32 vp1, u32 knext, u32 voff, HOOK hook) {
    const f32x16 zero = {};
    u32x4 pa0, pa1, pb0;
    s.kN = lds_ld<bf16x8>(kp + knext); SBAR();
    s.v0 = lds_ld<bf16x8>(vp0 + voff); SBAR();
    PVM(s.Ob, s.v1, s.pb1); SBAR();
    s.v1 = lds_ld<bf16x8>(vp1 + voff); SBAR();
    float a0 = EXP2(s.Sa[0]); SBAR(); float a1 = EXP2(s.Sa[1]); SBAR(); float a2 = EXP2(s.Sa[2]); SBAR(); float a3 = EXP2(s.Sa[3]); SBAR();
    s.Sb = MFMA32(s.kC, qb, USE_C ? cb : zero); SBAR();
    float a4 = EXP2(s.Sa[4]); SBAR(); float a5 = EXP2(s.Sa[5]); SBAR(); float a6 = EXP2(s.Sa[6]); SBAR(); float a7 = EXP2(s.Sa[7]); SBAR();
    pa0[0] = pk(a0, a1); SBAR(); pa0[1] = pk(a2, a3); SBAR(); float a8 = EXP2(s.Sa[8]); SBAR(); pa0[2] = pk(a4, a5); SBAR(); pa0[3] = pk(a6, a7); SBAR();
    float a9 = EXP2(s.Sa[9]); SBAR();
    asm volatile("" : "+v"(s.v0), "+v"(s.v1), "+v"(s.kN)); SBAR();
    float a10 = EXP2(s.Sa[10]); SBAR();
    PVM(s.Oa, s.v0, pa0); SBAR();
    float a11 = EXP2(s.Sa[11]); SBAR(); float a12 = EXP2(s.Sa[12]); SBAR(); float a13 = EXP2(s.Sa[13]); SBAR(); float a14 = EXP2(s.Sa[14]); SBAR(); float a15 = EXP2(s.Sa[15]); SBAR();
    pa1[0] = pk(a8, a9); SBAR(); pa1[1] = pk(a10, a11); SBAR(); pa1[2] = pk(a12, a13); SBAR(); pa1[3] = pk(a14, a15); SBAR();
    float b0 = EXP2(s.Sb[0]); SBAR(); float b1 = EXP2(s.Sb[1]); SBAR();
    PVM(s.Oa, s.v1, pa1); SBAR();
    hook(); SBAR();
    float b2 = EXP2(s.Sb[2]); SBAR(); float b3 = EXP2(s.Sb[3]); SBAR(); float b4 = EXP2(s.Sb[4]); SBAR(); float b5 = EXP2(s.Sb[5]); SBAR();
    s.Sa = MFMA32(s.kN, qa, USE_C ? ca : zero); SBAR();
    float b6 = EXP2(s.Sb[6]); SBAR(); float b7 = EXP2(s.Sb[7]); SBAR();
    pb0[0] = pk(b0, b1); SBAR(); pb0[1] = pk(b2, b3); SBAR(); float b8 = EXP2(s.Sb[8]); SBAR(); pb0[2] = pk(b4, b5); SBAR(); pb0[3] = pk(b6, b7); SBAR();
    float b9 = EXP2(s.Sb[9]); SBAR(); float b10 = EXP2(s.Sb[10]); SBAR();
    PVM(s.Ob, s.v0, pb0); SBAR();
    float b11 = EXP2(s.Sb[11]); SBAR(); float b12 = EXP2(s.Sb[12]); SBAR(); float b13 = EXP2(s.Sb[13]); SBAR(); float b14 = EXP2(s.Sb[14]); SBAR(); float b15 = EXP2(s.Sb[15]); SBAR();
    s.pb1[0] = pk(b8, b9); SBAR(); s.pb1[1] = pk(b10, b11); SBAR(); s.pb1[2] = pk(b12, b13); SBAR(); s.pb1[3] = pk(b14, b15); SBAR();
    s.kC = s.kN;
}
struct NoHook { DI void operator()() const {} };
template <bool USE_C>
DI void attn_chunk(AState& s, bf16x8 qa, bf16x8 qb, const f32x16& ca, const f32x16& cb, u32 kp, u32 vp0, u32 vp1) {
    attn_step<USE_C>(s, qa, qb, ca, cb, kp, vp0, vp1, 1024, 0, NoHook());
    attn_step<USE_C>(s, qa, qb, ca, cb, kp, vp0, vp1, 2048, VSTR, NoHook());
    attn_step<USE_C>(s, qa, qb, ca, cb, kp, vp0, vp1, 3072, 2 * VSTR, NoHook());
    attn_step<USE_C>(s, qa, qb, ca, cb, kp, vp0, vp1, 8192, 3 * VSTR, NoHook());
}
DI void attn_begin(AState& s, bf16x8 qa, const f32x16& ca, u32 kp) {
    const f32x16 zero = {};
    s.Oa = zero; s.Ob = zero; s.pb1 = (u32x4){0u, 0u, 0u, 0u}; s.v1 = BC8(s.pb1);
    s.kC = lds_ld<bf16x8>(kp);
    s.Sa = MFMA32(s.kC, qa, ca);
}
DI void attn_combine(const Ctx& c, AState& s, float (&O)[9]) {
    const int kh = c.w >> 2;
    s.Ob = MFMA32(s.v1, BC8(s.pb1), s.Ob);
    const f32x16& snd = kh ? s.Oa : s.Ob; const f32x16& keep = kh ? s.Ob : s.Oa;
    const u32 xb = L_AX + (u32)c.w * 2304u;
    lds_st<f32x4>(xb + (u32)c.lane * 16u, (f32x4){snd[0], snd[1], snd[2], snd[3]});
    lds_st<f32x4>(xb + 1024u + (u32)c.lane * 16u, (f32x4){snd[4], snd[5], snd[6], snd[7]});
    lds_st<float>(xb + 2048u + (u32)c.lane * 4u, snd[8]);
    BAR();
    const u32 xp = L_AX + (u32)(c.w ^ 4) * 2304u;
    const f32x4 u0 = lds_ld<f32x4>(xp + (u32)c.lane * 16u), u1 = lds_ld<f32x4>(xp + 1024u + (u32)c.lane * 16u);
    const float l2 = lds_ld<float>(xp + 2048u + (u32)c.lane * 4u);
#pragma unroll
    for (int q = 0; q < 4; ++q) { O[q] = keep[q] + u0[q]; O[4 + q] = keep[4 + q] + u1[q]; }
    O[8] = keep[8] + l2;
}

DI void phaseA(const Ctx& c0, const Params& p, const int L) {
    Ctx c = c0; FRESH(c);
    u32x4 pf[7] = {}, po[8];
    const int lane = c.lane, w = c.w, tid = w * 64 + lane;
    const int head = 4 * c.xg + (c.gi >> 3), qb = c.gi & 7;
    const int j = w & 3, kh = w >> 2;
    const u32 OQ = L ? OFF_Q1 : OFF_Q0, OK_ = L ? OFF_K1 : OFF_K0, OV = L ? OFF_V1 : OFF_V0, OO = L ? OFF_O1 : OFF_O0;
    if (tid == 0) lds_st<u32>(L_AX + 8 * 2304u, 0u);
    if (w == 0) {
        wait_flags(c, (L ? OFF_FQ1 : OFF_FQ0) + (u32)((head >> 1) * 16 + (lane & 15)) * 128u);
        if (L == 1) {
            const int sel = __builtin_amdgcn_readfirstlane((int)lds_ld<u32>(P_SEL)) & 2047, T = sel >> 5, ts = sel & 31;
            const u32 tk = (u32)((head * 64 + T) * 64 + (lane & 1) * 32 + ts) * 16u;
            u32 off = OK_ + tk;
            if (lane >= 2 && lane < 18) off = OV + (u32)((head * 64 + T) * 1024 + (ts >> 4) * 512 + ((ts >> 2) & 1) * 256 + (lane - 2) * 16);
            if (lane >= 18 && lane < 20 && (T >> 3) == qb) off = OQ + tk;
            u32x4 v = ldw(c.ws, off);
            wait_piece(c, off, v);
        }
    }
    if (L == 0 && w == 1) {
        const int qo = (4 * c.xg + (c.gi >> 3)) >> 2, hd = lane & 31, xg = hd >> 2, gi = (hd & 3) * 8 + qo;
        wait_flags(c, OFF_FQ0 + (u32)((8 * (xg >> 2) + (gi >> 2)) * 16 + 4 * (xg & 3) + (gi & 3)) * 128u);
    }
    BAR();
    STAMP(L ? 17 : 4);
    CSTAMP(9);
    const u32 koff = OK_ + (u32)((head * 64 + w) * 64 + lane) * 16u, voff = OV + (u32)((head * 64 + w) * 64 + lane) * 16u;
    u32x4 rk[8], rv[8];
    rk[0] = ldw(c.ws, koff); rv[0] = ldw(c.ws, voff);
    const u32 qo = OQ + (u32)((head * 64 + qb * 8 + 2 * j) * 64 + lane) * 16u;
    const u32x4 qar = ldw(c.ws, qo), qbr = ldw(c.ws, qo + 1024u);
    SBAR(); __builtin_amdgcn_s_barrier(); SBAR();
    rk[1] = ldw(c.ws, koff + 8192u); rv[1] = ldw(c.ws, voff + 8192u); rk[2] = ldw(c.ws, koff + 16384u); rv[2] = ldw(c.ws, voff + 16384u);
    SBAR(); __builtin_amdgcn_s_barrier(); SBAR();
    rk[3] = ldw(c.ws, koff + 24576u); rv[3] = ldw(c.ws, voff + 24576u); rk[4] = ldw(c.ws, koff + 32768u); rv[4] = ldw(c.ws, voff + 32768u);
    SBAR();
    { const int t = tid >> 3, s = tid & 7; const u32 val = (s & 3) == 0 ? 0x3f803f80u : 0u;
      lds_st<u32x4>(L_V + (u32)t * VSTR + 1024u + (u32)s * 16u, (u32x4){val, val, val, val}); }
    const u32 kw = L_K + (u32)w * 1024u + (u32)lane * 16u, vw = L_V + (u32)w * VSTR + (u32)lane * 16u;
    lds_st<u32x4>(kw, rk[0]); lds_st<u32x4>(vw, rv[0]);
    lds_st<u32x4>(kw + 8192u, rk[1]); lds_st<u32x4>(vw + 8u * VSTR, rv[1]);
    CSTAMP(10);
    BAR();
    const int r = lane & 31, h = lane >> 5;
    const u32 kp0 = L_K + (u32)kh * 4096u + (u32)lane * 16u;
    const u32 onesoff = 1024u + ((r >= 20 && r < 28) ? 64u : 0u) + ((r == 16 || r == 20) ? 0u : 16u);
    const u32 vp00 = L_V + (u32)kh * (4u * VSTR) + (r < 16 ? (u32)(h * 256 + r * 16) : onesoff);
    const u32 vp10 = L_V + (u32)kh * (4u * VSTR) + (r < 16 ? (u32)(512 + h * 256 + r * 16) : onesoff);
    const bf16x8 qa = BC8(qar), qbf = BC8(qbr);
    const f32x16 cz = {};
    AState s;
    STAMP(L ? 18 : 5);
#define PRIO(n) __builtin_amdgcn_s_setprio(n)
    PRIO(3);
    CSTAMP(0);
    attn_begin(s, qa, cz, kp0);
    {
#define PUT(ch) [&]() { lds_st<u32x4>(kw + (u32)(ch) * 8192u, rk[ch]); lds_st<u32x4>(vw + (u32)(ch) * (8u * VSTR), rv[ch]); }
#define GET(ch) [&]() { rk[ch] = ldw(c.ws, koff + (u32)(ch) * 8192u); rv[ch] = ldw(c.ws, voff + (u32)(ch) * 8192u); }
#define GETPUT(g, ch) [&]() { GET(g)(); PUT(ch)(); }
#define FF(j_) [&]() { if (ff.on && ((j_) < 4 || !ff.gu)) pf[j_] = gld(ff.m, ff.vo + (u32)(j_) * ff.step); }
#define WO(j_) [&]() { po[j_] = gld(p.wo, wv + (u32)(j_) * 16384u); }
#define ST(ch, t, HOOK) attn_step<false>(s, qa, qbf, cz, cz, kp, vp0, vp1, ((t) == 3 ? 8192 : ((t) + 1) * 1024) + ((ch) & 1) * 8192, (((ch) & 1) * 8 + (t)) * VSTR, HOOK)
#define NEXT2() do { kp += 16384; vp0 += 16 * VSTR; vp1 += 16 * VSTR; asm volatile("" : "+v"(kp), "+v"(vp0), "+v"(vp1)); } while (0)
        u32 kp = kp0, vp0 = vp00, vp1 = vp10;
        ST(0, 0, GET(5)); ST(0, 1, GETPUT(6, 2)); ST(0, 2, GETPUT(7, 3)); ST(0, 3, PUT(4));
        CSTAMP(1);
        ST(1, 0, PUT(5)); ST(1, 1, PUT(6)); ST(1, 2, PUT(7));
        BAR();
        ST(1, 3, NoHook());
        const FfSrc ff = ff_src(c, p);
        NEXT2(); PRIO(2);
        CSTAMP(2);
        ST(2, 0, FF(0)); ST(2, 1, FF(1)); ST(2, 2, FF(2)); ST(2, 3, FF(3));
        CSTAMP(3);
        ST(3, 0, FF(4)); ST(3, 1, FF(5)); ST(3, 2, FF(6)); ST(3, 3, NoHook());
        const u32 wv = wo_lane_off(c);
        NEXT2(); PRIO(1);
        CSTAMP(4);
        ST(4, 0, WO(0)); ST(4, 1, WO(1)); ST(4, 2, WO(2)); ST(4, 3, WO(3));
        CSTAMP(5);
        ST(5, 0, WO(4)); ST(5, 1, WO(5)); ST(5, 2, WO(6)); ST(5, 3, WO(7));
        NEXT2(); PRIO(0);
        CSTAMP(6);
        for (int ch = 6; ch < 8; ++ch) {
            attn_chunk<false>(s, qa, qbf, cz, cz, kp, vp0, vp1);
            kp += 8192; vp0 += 8 * VSTR; vp1 += 8 * VSTR;
            asm volatile("" : "+v"(kp), "+v"(vp0), "+v"(vp1));
        }
#undef PUT
#undef GET
#undef GETPUT
#undef FF
#undef WO
#undef ST
#undef NEXT2
    }
    CSTAMP(8);
    STAMP(L ? 19 : 6);
    FRESH(c);
    float O[9];
    attn_combine(c, s, O);
    const u32 oo = OO + (u32)((head * 64 + qb * 8 + 2 * j + kh) * 64 + c.lane) * 16u;
    bool redo;
    {
        bool bad = !(O[8] >= 0x1p-60f && O[8] <= 0x1p60f);
#pragma unroll
        for (int q = 0; q < 8; ++q) bad = bad || !(__builtin_fabsf(O[q]) <= 0x1p126f);
        redo = __any(bad);
        if (redo) { if (c.lane == 0) lds_st<u32>(L_AX + 8 * 2304u, 1u); }
        else {
            const float rl = __builtin_amdgcn_rcpf(O[8]);
            u32x4 o;
#pragma unroll
            for (int q = 0; q < 4; ++q) o[q] = pk(O[2 * q] * rl, O[2 * q + 1] * rl);
            stw(c.ws, oo, not_poison(o));
        }
    }
    pre_store_wo(c, po);
    BAR();
    if (__builtin_amdgcn_readfirstlane((int)lds_ld<u32>(L_AX + 8 * 2304u)) != 0) {
        {
            const u32 ko = OK_ + (u32)((head * 64 + w) * 64 + c.lane) * 16u, vo = OV + (u32)((head * 64 + w) * 64 + c.lane) * 16u;
            for (int ch = 0; ch < 4; ++ch) {
                const u32x4 a = ldw(c.ws, ko + (u32)ch * 8192u), b_ = ldw(c.ws, vo + (u32)ch * 8192u);
                lds_st<u32x4>(L_K + (u32)(ch * 8 + w) * 1024u + (u32)c.lane * 16u, a);
                lds_st<u32x4>(L_V + (u32)(ch * 8 + w) * VSTR + (u32)c.lane * 16u, b_);
            }
            { const int t = (w * 64 + c.lane) >> 3, s_ = c.lane & 7; const u32 val = (s_ & 3) == 0 ? 0x3f803f80u : 0u;
              lds_st<u32x4>(L_V + (u32)t * VSTR + 1024u + (u32)s_ * 16u, (u32x4){val, val, val, val}); }
        }
        BAR();
        float ma = -INFINITY, mb = -INFINITY;
        for (int ch = 0; ch < 8; ++ch)
            for (int t = 0; t < 4; ++t) {
                const bf16x8 kf = lds_ld<bf16x8>(kp0 + (u32)(ch * 8192 + t * 1024));
                const f32x16 Sa = MFMA32(kf, qa, cz), Sb = MFMA32(kf, qbf, cz);
#pragma unroll
                for (int q = 0; q < 16; ++q) { ma = fmaxf(ma, Sa[q]); mb = fmaxf(mb, Sb[q]); }
            }
        ma = fmaxf(ma, __shfl_xor(ma, 32)); mb = fmaxf(mb, __shfl_xor(mb, 32));
        const u32 xb = L_AX + (u32)w * 2304u, xp = L_AX + (u32)(w ^ 4) * 2304u;
        lds_st<f32x2>(xb + (u32)c.lane * 8u, (f32x2){ma, mb});
        BAR();
        const f32x2 mo = lds_ld<f32x2>(xp + (u32)c.lane * 8u);
        ma = fmaxf(ma, mo[0]); mb = fmaxf(mb, mo[1]);
        BAR();
        f32x16 ca, cb;
#pragma unroll
        for (int q = 0; q < 16; ++q) { ca[q] = -ma; cb[q] = -mb; }
        attn_begin(s, qa, ca, kp0);
        u32 kp = kp0, vp0 = vp00, vp1 = vp10;
        for (int ch = 0; ch < 8; ++ch) {
            attn_chunk<true>(s, qa, qbf, ca, cb, kp, vp0, vp1);
            kp += 8192; vp0 += 8 * VSTR; vp1 += 8 * VSTR;
        }
        attn_combine(c, s, O);
        if (redo) {
            const float rl = 1.0f / O[8];
            u32x4 o;
#pragma unroll
            for (int q = 0; q < 4; ++q) o[q] = pk(O[2 * q] * rl, O[2 * q + 1] * rl);
            stw(c.ws, oo, not_poison(o));
        }
        {
            const FfSrc ff = ff_src(c, p); const u32 wv = wo_lane_off(c);
#pragma unroll
            for (int q = 0; q < 7; ++q) if (ff.on && (q < 4 || !ff.gu)) pf[q] = gld(ff.m, ff.vo + (u32)q * ff.step);
#pragma unroll
            for (int q = 0; q < 8; ++q) po[q] = gld(p.wo, wv + (u32)q * 16384u);
        }
        BAR();
        pre_store_wo(c, po);
        BAR();
    }
    STAMP(L ? 20 : 7);
    pre_store_ff(c, pf);
}

DI void phaseO(const Ctx& c0, const Params& p, u32x4 (&x2)[2], u32& hsync, const u32x4 (&pq)[12], const int L) {
    Ctx c = c0; FRESH(c);
    const int lane = c.lane, w = c.w;
    const int tb = 4 * c.xg + (c.gi >> 3), fb = c.gi & 7;
    const int r = lane & 31, h = lane >> 5;
    const u32 OO = L ? OFF_O1 : OFF_O0, GS = L ? OFF_GS1 : OFF_GS0, GC = L ? OFF_GC1 : OFF_GC0, GM = L ? OFF_GM1 : OFF_GM0, GX = L ? OFF_GX1 : OFF_GX0;
    constexpr u32 L_TS = L_SB, L_CAND = P_ST + 8;
    if (w >= 4) {
        const int hw = w - 4;
        STAMP(L ? 21 : 8);
        BAR();
        BAR();
        __builtin_amdgcn_s_setprio(3);
        FST(30 + 20 * L);
        if (hw == 0) {
            const u32 a = L_SB + (u32)h * 384u + (u32)r * 12u;
            const float t1 = lds_ld<float>(a) + lds_ld<float>(a + 768), t2 = lds_ld<float>(a + 4) + lds_ld<float>(a + 772), t3 = lds_ld<float>(a + 8) + lds_ld<float>(a + 776);
            stw(c.ws, GS + (u32)((tb * 8 + fb) * 1024 + lane * 16), (u32x4){__float_as_uint(t1), __float_as_uint(t2), __float_as_uint(t3), c.tag});
        }
        FST(31 + 20 * L);
        if (L == 0) {
            if (hw != 0) { pre_store_wq(c, pq); lds_signal_add(c, P_WQ); }
            BAR();
        }
        FST(32 + 20 * L);
        if (hw == 0) {
            u32x4 v[8]; u32 n = 0;
            for (;;) {
                bool ok = true;
#pragma unroll
                for (int k = 0; k < 8; ++k) v[k] = ldw(c.ws, GS + (u32)((tb * 8 + k) * 1024 + lane * 16));
                MEMFENCE();
#pragma unroll
                for (int k = 0; k < 8; ++k) ok = ok && v[k][3] == c.tag;
                if (__all(ok)) break;
                if (spin_fail(c, n)) break;
            }
            FST(33 + 20 * L);
            float t1 = 0, t2 = 0, t3 = 0;
#pragma unroll
            for (int k = 0; k < 8; ++k) { t1 += __uint_as_float(v[k][0]); t2 += __uint_as_float(v[k][1]); t3 += __uint_as_float(v[k][2]); }
            const float mu = t1 * (1.0f / 512.0f), var = fmaxf(t2 * (1.0f / 512.0f) - mu * mu, 0.f);
            const float rs = __builtin_amdgcn_rsqf(var + 1e-5f);
            const float rt = rbf(rs * (t3 - mu * lds_ld<float>(P_WSUM)));
            const u32 key = half_umax(score_key(__float_as_uint(rt) >> 16, (u32)r));
            const int bt = (int)key_idx(key);
            if (fb == 0) {
                const u32 k0 = (u32)__builtin_amdgcn_readlane((int)key, 0), k1 = (u32)__builtin_amdgcn_readlane((int)key, 32);
                const u32 kT = (lane & 1) ? k1 : k0;
                const u64 g = (cand_tag(c.tag) << 21) | (u64)(key_bf(kT) << 5) | (u64)key_idx(kT);
                if (lane < 16) __builtin_amdgcn_raw_buffer_store_b64((u32x2){(u32)g, (u32)(g >> 32)}, c.ws, GC + (u32)(lane >> 1) * 512u + (u32)(tb * 2 + (lane & 1)) * 8u, 0, 16);
                if (r == bt) stw(c.ws, GM + (u32)(tb * 2 + h) * 16u, mk_unit(__float_as_uint(mu), __float_as_uint(rs), c.tag));
                if (L == 0) {
                    const float g1 = 1.0f + rs, A = g1 * __builtin_amdgcn_rsqf(var * g1 * g1 + 1e-5f);
                    stw(c.ws, OFF_TS1 + (u32)(tb * 64 + lane) * 16u, mk_unit(__float_as_uint(A), __float_as_uint(A * mu), c.tag));
                }
            }
            FST(34 + 20 * L);
            lds_st<f32x2>(L_TS + (u32)lane * 8u, (f32x2){mu, rs});
            if (r == 0) lds_st<u32>(L_CAND + (u32)h * 4u, (u32)bt);
            lds_signal_add(c, P_ST + L * 4);
            {
                const int ct = (lane >> 4) & 1, u = lane & 15;
                const int cb = __builtin_amdgcn_readlane(bt, 0), cb1 = __builtin_amdgcn_readlane(bt, 32);
                const int row = ct ? cb1 : cb;
                const u32x2 d = lds_ld<u32x2>(L_X1T + (u32)(ct + 2 * (u >> 3)) * 2048u + (u32)((u >> 2) & 1) * 1024u + (u32)((u & 1) * 32 + row) * 16u + (u32)((u >> 1) & 1) * 8u);
                if (lane < 32) stw(c.ws, GX + (u32)(tb * 2 + ct) * 2048u + (u32)(fb * 16 + u) * 16u, mk_unit(d[0], d[1], c.tag));
            }
            if (L == 0) { pre_store_wq(c, pq); lds_signal_add(c, P_WQ); }
        }
        STAMP(L ? 23 : 12);
        ffn_chain(c, p, hsync, L);
        if (L == 0) STAMP(15);
        return;
    }
    const int tt = w & 1, kh = w >> 1;
    const int tile = tb * 2 + tt, tok = tile * 32 + r;
    const int f0 = fb * 64 + kh * 32;
    u32x2 res[4], wr[4];
#pragma unroll
    for (int g = 0; g < 4; ++g) {
        if (L == 0) res[g] = *(const u32x2*)(p.x0 + (size_t)tok * 512 + f0 + 8 * g + 4 * h);
        wr[g] = *(const u32x2*)(p.wr + f0 + 8 * g + 4 * h);
    }
    if (L == 1) {
        const int sel0 = __builtin_amdgcn_readfirstlane((int)lds_ld<u32>(P_SEL)) & 2047;
        if ((sel0 >> 5) == tile) {
            const u32 ub = OFF_GY + (u32)(f0 / 4 + h) * 16u;
            u32 off[4] = {ub, ub + 32u, ub + 64u, ub + 96u}; u32x4 v[4];
            wait_units<4>(c, off, v);
            if (r == (sel0 & 31)) { x2[0] = (u32x4){v[0][0], v[0][2], v[1][0], v[1][2]}; x2[1] = (u32x4){v[2][0], v[2][2], v[3][0], v[3][2]}; }
        }
#pragma unroll
        for (int g = 0; g < 4; ++g) res[g] = (u32x2){x2[g >> 1][(g & 1) * 2], x2[g >> 1][(g & 1) * 2 + 1]};
    }
    STAMP(L ? 21 : 8);
    u32x4 bf[16];
    const u32 oo = OO + (u32)(((kh * 16) * 64 + tile) * 64 + lane) * 16u;
    {
        u32 n = 0;
        for (;;) {
#pragma unroll
            for (int s = 0; s < 16; ++s) bf[s] = ldw(c.ws, oo + (u32)s * 65536u);
            MEMFENCE();
            u32 m = 0;
#pragma unroll
            for (int s = 0; s < 16; ++s) m = max(m, max(max(bf[s][0], bf[s][1]), max(bf[s][2], bf[s][3])));
            if (!__any(m == POISON)) break;
            if (spin_fail(c, n)) break;
        }
    }
    const int i16 = lane & 15, blk = (lane >> 4) & 1;
    const u32 trA = L_WO + (u32)((kh * 256 + 4 * h + (i16 >> 2)) * 64 + blk * 32 + (i16 & 3) * 8);
    f32x16 a0 = {}, a1 = {};
    {
        u32 ta = trA, tb_ = trA + WO_FT;
        asm volatile("" : "+v"(ta), "+v"(tb_));
        bf16x8 f0_[2], f1_[2];
        f0_[0] = frag_tr(ta, ta + 512); f0_[1] = frag_tr(tb_, tb_ + 512);
#pragma unroll
        for (int s = 0; s < 16; s += 2) {
            f1_[0] = frag_tr(ta + (u32)(s + 1) * 1024u, ta + (u32)(s + 1) * 1024u + 512); SBAR();
            a0 = MFMA32(f0_[0], BC8(bf[s]), a0); SBAR();
            f1_[1] = frag_tr(tb_ + (u32)(s + 1) * 1024u, tb_ + (u32)(s + 1) * 1024u + 512); SBAR();
            a1 = MFMA32(f0_[1], BC8(bf[s]), a1); SBAR();
            if (s + 2 < 16) f0_[0] = frag_tr(ta + (u32)(s + 2) * 1024u, ta + (u32)(s + 2) * 1024u + 512);
            SBAR(); a0 = MFMA32(f1_[0], BC8(bf[s + 1]), a0); SBAR();
            if (s + 2 < 16) f0_[1] = frag_tr(tb_ + (u32)(s + 2) * 1024u, tb_ + (u32)(s + 2) * 1024u + 512);
            SBAR(); a1 = MFMA32(f1_[1], BC8(bf[s + 1]), a1); SBAR();
        }
    }
    {
        const f32x16& snd = kh ? a0 : a1;
#pragma unroll
        for (int g = 0; g < 4; ++g) lds_st<f32x4>(L_XB + (u32)w * 4096u + (u32)g * 1024u + (u32)lane * 16u, (f32x4){snd[4 * g], snd[4 * g + 1], snd[4 * g + 2], snd[4 * g + 3]});
    }
    BAR();
    STAMP(L ? 22 : 9);
    u32x4 xp[2];
    {
        const f32x16& keep = kh ? a1 : a0;
#pragma unroll
        for (int g = 0; g < 4; ++g) {
            const f32x4 o = lds_ld<f32x4>(L_XB + (u32)(w ^ 2) * 4096u + (u32)g * 1024u + (u32)lane * 16u);
            const u32 t0 = pk(keep[4 * g] + o[0], keep[4 * g + 1] + o[1]), t1 = pk(keep[4 * g + 2] + o[2], keep[4 * g + 3] + o[3]);
            xp[g >> 1][(g & 1) * 2] = pk(bflo(res[g][0]) + bflo(t0), bfhi(res[g][0]) + bfhi(t0));
            xp[g >> 1][(g & 1) * 2 + 1] = pk(bflo(res[g][1]) + bflo(t1), bfhi(res[g][1]) + bfhi(t1));
        }
    }
    if (L == 0) {
#pragma unroll
        for (int s = 0; s < 2; ++s) stw(c.ws, OFF_X1 + (u32)((tile * 32 + f0 / 16 + s) * 64 + lane) * 16u, xp[s]);
    }
#pragma unroll
    for (int s = 0; s < 2; ++s) lds_st<u32x4>(L_X1T + (u32)w * 2048u + (u32)s * 1024u + (u32)lane * 16u, xp[s]);
    float x1[16];
    {
        float s1 = 0, s2 = 0, s3 = 0;
#pragma unroll
        for (int i = 0; i < 16; ++i) {
            const u32 wd = xp[i >> 3][(i & 7) >> 1];
            x1[i] = (i & 1) ? bfhi(wd) : bflo(wd);
            const u32 ww = wr[i >> 2][(i & 3) >> 1];
            const float wv = (i & 1) ? bfhi(ww) : bflo(ww);
            s1 += x1[i]; s2 += x1[i] * x1[i]; s3 += x1[i] * wv;
        }
        s1 = swap32_add(s1); s2 = swap32_add(s2); s3 = swap32_add(s3);
        if (h == 0) { const u32 a = L_SB + (u32)w * 384u + (u32)r * 12u; lds_st<float>(a, s1); lds_st<float>(a + 4, s2); lds_st<float>(a + 8, s3); }
    }
    BAR();
    if (L == 0) {
        DRAIN(); BAR();
        STAMP(10);
        if (w == 0 && lane == 0) set_flag(c, OFF_FO + (u32)(tb * 8 + fb) * 128u);
        pre_store_wq(c, pq); lds_signal_add(c, P_WQ);
    }
    lds_wait_ge(P_ST + L * 4, 1u);
    STAMP(L ? 23 : 11);
    const f32x2 st = lds_ld<f32x2>(L_TS + (u32)(tt * 32 + r) * 8u);
    const int bt = (int)lds_ld<u32>(L_CAND + (u32)tt * 4u) & 31;
#pragma unroll
    for (int s = 0; s < 2; ++s)
#pragma unroll
        for (int j = 0; j < 4; ++j) {
            const float a = x1[8 * s + 2 * j], b_ = x1[8 * s + 2 * j + 1];
            const u32 hh = pk((a - st[0]) * st[1], (b_ - st[0]) * st[1]);
            x2[s][j] = pk(a + bflo(hh), b_ + bfhi(hh));
        }
    if (L == 1) {
        const u32 tl = L_XB + (u32)w * 4096u;
#pragma unroll
        for (int g = 0; g < 4; ++g) lds_st<u32x2>(tl + (u32)(r * 80 + (8 * g + 4 * h) * 2), (u32x2){x2[g >> 1][(g & 1) * 2], x2[g >> 1][(g & 1) * 2 + 1]});
        asm volatile("s_waitcnt lgkmcnt(0)" ::: "memory");
        u32x4 rowv[2];
#pragma unroll
        for (int q = 0; q < 2; ++q) {
            const int row = q * 16 + (lane >> 2);
            rowv[q] = lds_ld<u32x4>(tl + (u32)(row * 80 + (lane & 3) * 16));
            if (row != bt) *(u32x4*)(p.out + (size_t)(tile * 32 + row) * 512 + f0 + (lane & 3) * 8) = rowv[q];
        }
        wait_sel(1);
        const int sel1 = __builtin_amdgcn_readfirstlane((int)lds_ld<u32>(P_SEL + 32)) & 2047;
        if (tile * 32 + bt != sel1) {
#pragma unroll
            for (int q = 0; q < 2; ++q) {
                const int row = q * 16 + (lane >> 2);
                if (row == bt) *(u32x4*)(p.out + (size_t)(tile * 32 + row) * 512 + f0 + (lane & 3) * 8) = rowv[q];
            }
        }
        }
}

#if defined(__HIP_DEVICE_COMPILE__)
__attribute__((target("no-packed-fp32-ops")))
#endif
__global__ __launch_bounds__(512, 2) void fused_layers_kernel(Params p) {
    extern __shared__ __attribute__((aligned(16))) char lds[];
    Ctx c;
    const int tid = threadIdx.x;
    c.lane = tid & 63; c.w = __builtin_amdgcn_readfirstlane(tid >> 6);
    c.xg = blockIdx.x & 7; c.gi = blockIdx.x >> 3;
    c.ws = __builtin_amdgcn_make_buffer_rsrc(p.ws, 0, WS_BYTES, 0x00020000);
    c.tag = 0u;
    {
        const u32 o = (u32)(((4 * c.xg + (c.gi >> 3)) * 64 + (c.gi & 7) * 8 + 2 * (c.w & 3) + (c.w >> 2)) * 64 + c.lane) * 16u;
        const u32x4 pois = {POISON, POISON, POISON, POISON};
        stw(c.ws, OFF_O0 + o, pois); stw(c.ws, OFF_O1 + o, pois);
    }
    if (c.w == 6) {
        const u32 o = (u32)(((4 * c.xg + (c.gi >> 3)) * 8 + (c.gi & 7)) * 1024 + c.lane * 16);
        stw(c.ws, OFF_GS0 + o, (u32x4){0u, 0u, 0u, 0u}); stw(c.ws, OFF_GS1 + o, (u32x4){0u, 0u, 0u, 0u});
    }
    {
        u64 pc; u32 len;
        asm volatile("s_getpc_b64 %0\nfl_code_here:\n\ts_mov_b32 %1, fl_code_end-fl_code_here" : "=s"(pc), "=s"(len));
        if (c.w == 7) {
            const u32 o0 = min((u32)c.gi * 3072u + (u32)c.lane * 16u, len - 16u) & ~15u, o1 = min(o0 + 1024u, len - 16u) & ~15u, o2 = min(o0 + 2048u, len - 16u) & ~15u;
            const u64 base = pc & ~15ull;
            glds((const void*)base, o0, 147456u); glds((const void*)base, o1, 148480u); glds((const void*)base, o2, 149504u);
        }
    }
    if (c.w == 5) {
        const int l = c.lane, k = (l - 22) % 14;
        const char* a = p.ws + (size_t)(l < 10 ? l : 0) * 0x200000;
        a = l >= 10 && l < 14 ? (const char*)p.wo + (l - 10) * 131072 : l == 14 ? (const char*)p.wr : a;
        a = l >= 15 && l < 22 ? (const char*)p.out + (l - 15) * 299008 : a;
        a = l >= 22 ? (const char*)(l < 36 ? p.wg : l < 50 ? p.wu : p.wd) + k * 131072 : a;
        u32 sv;
        asm volatile("s_mov_b32 %0, m0\n\ts_mov_b32 m0, %2\n\ts_nop 0\n\tglobal_load_lds_dword %1, off\n\ts_mov_b32 m0, %0" : "=&s"(sv) : "v"(a), "s"(150528u) : "memory");
    }
    if (tid < 24 && tid != 3) lds_st<u32>(P_HCNT + (u32)tid * 4u, 0u);
    asm volatile("" :: "v"(lds));
    BAR();
    STAMP(0);
    FST(0);
    u32 hsync = 0;
    u32x4 x2[2];
    asm volatile("" : "=v"(x2[0]), "=v"(x2[1]));
    u32x4 pq[12];
#pragma nounroll
    for (int Li = 0; Li < 2; ++Li) {
        int Lv = Li; asm volatile("" : "+v"(Lv));
        const int L = __builtin_amdgcn_readfirstlane(Lv);
        if (L == 0) phaseQ<0>(c, p, hsync, pq);
        else {
            phaseQ<1>(c, p, hsync, pq);
#pragma unroll
            for (int j = 0; j < 12; ++j) asm volatile("" : "=v"(pq[j]));
        }
        if (STOP_AFTER < 3 * L + 1) break;
        phaseA(c, p, L);
        if (STOP_AFTER < 3 * L + 2) break;
        phaseO(c, p, x2, hsync, pq, L);
        if (STOP_AFTER < 3 * L + 3) break;
    }
    STAMP(24);
    if (blockIdx.x == 14 * 8 && c.w == 4 && c.lane == 0) __builtin_amdgcn_raw_buffer_store_b32(c.tag, c.ws, OFF_EPOCH, 0, 16);
    asm volatile("fl_code_end:" ::: "memory");
}

extern "C" void kernel_launch(void* const* d_in, const int* in_sizes, int n_in, void* d_out, int out_size, void* d_ws, size_t ws_size, hipStream_t stream) {
    Params p;
    p.x0 = (const uint16_t*)d_in[0]; p.wqkv = (const uint16_t*)d_in[1]; p.wo = (const uint16_t*)d_in[2];
    p.wr = (const uint16_t*)d_in[4]; p.wg = (const uint16_t*)d_in[5]; p.wu = (const uint16_t*)d_in[6]; p.wd = (const uint16_t*)d_in[7];
    p.out = (uint16_t*)d_out; p.ws = (char*)d_ws;
    (void)hipFuncSetAttribute((const void*)fused_layers_kernel, hipFuncAttributeMaxDynamicSharedMemorySize, LDS_BYTES);
    fused_layers_kernel<<<256, 512, LDS_BYTES, stream>>>(p);
}
```

```cpp
#define OUTAUX 16
#include <hip/hip_runtime.h>
#include <stdint.h>

typedef unsigned u32;
typedef unsigned long long u64;
typedef __attribute__((ext_vector_type(2))) u32 u32x2;
typedef __attribute__((ext_vector_type(4))) u32 u32x4;
typedef __attribute__((ext_vector_type(2))) float f32x2;
typedef __attribute__((ext_vector_type(4))) float f32x4;
typedef __attribute__((ext_vector_type(16))) float f32x16;
typedef __attribute__((ext_vector_type(8))) __bf16 bf16x8;
typedef __attribute__((ext_vector_type(2))) __bf16 bf16x2;
typedef __attribute__((ext_vector_type(4))) short s16x4;
typedef __attribute__((ext_vector_type(8))) short s16x8;
#define LAS __attribute__((address_space(3)))
#define DI __device__ __forceinline__
#define MFMA32(a, b, c) __builtin_amdgcn_mfma_f32_32x32x16_bf16((a), (b), (c), 0, 0, 0)
#define MFMA16(a, b, c) __builtin_amdgcn_mfma_f32_16x16x32_bf16((a), (b), (c), 0, 0, 0)
#define SBAR() __builtin_amdgcn_sched_barrier(0)
#define BAR() asm volatile("s_waitcnt lgkmcnt(0)\n\ts_barrier" ::: "memory")
#define DRAIN() asm volatile("s_waitcnt vmcnt(0)" ::: "memory")
#define MEMFENCE() asm volatile("" ::: "memory")

#define STOP_AFTER 99

constexpr u32 OFF_EPOCH = 0, OFF_ABORT = 256;
constexpr u32 OFF_FQ0 = 0x10000, OFF_FO = 0x20000, OFF_FQ1 = 0x28000;
constexpr u32 OFF_TS0 = 0x40000, OFF_TS1 = 0x48000;
constexpr u32 OFF_GC0 = 0x50000, OFF_GC1 = 0x51000;
constexpr u32 OFF_GM0 = 0x52000, OFF_GM1 = 0x52400;
constexpr u32 OFF_GA0 = 0x58000, OFF_GA1 = 0x5A000;
constexpr u32 OFF_GY = 0x5C000;
constexpr u32 OFF_GS0 = 0x80000, OFF_GS1 = 0xC0000;
constexpr u32 OFF_GX0 = 0x180000, OFF_GX1 = 0x1A0000;
constexpr u32 OFF_Q0 = 0x200000, OFF_K0 = 0x400000, OFF_V0 = 0x600000, OFF_O0 = 0x800000, OFF_X1 = 0xA00000;
constexpr u32 OFF_Q1 = 0xC00000, OFF_K1 = 0xE00000, OFF_V1 = 0x1000000, OFF_O1 = 0x1200000;
constexpr u32 WS_BYTES = 0x1400000;

constexpr u32 LDS_BYTES = 163840;
constexpr u32 L_W = 0;
constexpr u32 L_XR = 98304;
constexpr u32 L_AX = 140288;
constexpr u32 L_TOK = 161792 + 512;
constexpr u32 L_K = 0, L_V = 65536, VSTR = 1152;
constexpr u32 L_WO = 0, WO_FT = 65536;
constexpr u32 L_XB = 32768;
constexpr u32 L_SB = 161792;
constexpr u32 L_P = 163328;
constexpr u32 P_CS = L_P, P_HCNT = L_P + 384, P_PATCH = L_P + 388, P_WQ = L_P + 392, P_WSUM = L_P + 396, P_SEL = L_P + 400, P_ST = L_P + 464;
constexpr u32 L_X1T = 49152;
constexpr u32 FW0 = 98304, FS0 = 155648;
constexpr u32 FW1 = FW0, FS1 = FS0;
constexpr u32 SPIN_CAP = 40000;

struct Params {
    const uint16_t* x0; const uint16_t* wqkv; const uint16_t* wo; const uint16_t* wr;
    const uint16_t* wg; const uint16_t* wu; const uint16_t* wd; uint16_t* out; char* ws;
};

typedef __amdgpu_buffer_rsrc_t rsrc_t;

DI u32 pk(float a, float b) { f32x2 v = {a, b}; return __builtin_bit_cast(u32, __builtin_convertvector(v, bf16x2)); }
DI float bflo(u32 v) { return __uint_as_float(v << 16); }
DI float bfhi(u32 v) { return __uint_as_float(v & 0xffff0000u); }
DI float rbf(float a) { return bflo(pk(a, 0.f)); }
DI u32x4 ldw(rsrc_t r, u32 off) { return __builtin_amdgcn_raw_buffer_load_b128(r, off, 0, 16); }
DI void stw(rsrc_t r, u32 off, u32x4 v) { __builtin_amdgcn_raw_buffer_store_b128(v, r, off, 0, 16); }
template <class T> DI T lds_ld(u32 a) { return *(const LAS T*)(uintptr_t)a; }
template <class T> DI void lds_st(u32 a, T v) { *(LAS T*)(uintptr_t)a = v; }
DI s16x4 lds_tr(u32 a) { return __builtin_amdgcn_ds_read_tr16_b64_v4i16((LAS s16x4*)(uintptr_t)a); }
DI bf16x8 frag_tr(u32 a0, u32 a1) { s16x4 lo = lds_tr(a0), hi = lds_tr(a1); return __builtin_bit_cast(bf16x8, __builtin_shufflevector(lo, hi, 0, 1, 2, 3, 4, 5, 6, 7)); }
DI float swap32_add(float v) { auto r = __builtin_amdgcn_permlane32_swap(__float_as_uint(v), __float_as_uint(v), false, false); return __uint_as_float(r[0]) + __uint_as_float(r[1]); }
template <int CTRL> DI u32 dpp(u32 v) { return (u32)__builtin_amdgcn_update_dpp((int)v, (int)v, CTRL, 0xf, 0xf, true); }
DI float wave_sum(float v) {
    v += __uint_as_float(dpp<0xB1>(__float_as_uint(v)));
    v += __uint_as_float(dpp<0x4E>(__float_as_uint(v)));
    v += __uint_as_float(dpp<0x141>(__float_as_uint(v)));
    v += __uint_as_float(dpp<0x140>(__float_as_uint(v)));
    { auto r = __builtin_amdgcn_permlane16_swap(__float_as_uint(v), __float_as_uint(v), false, false); v = __uint_as_float(r[0]) + __uint_as_float(r[1]); }
    return swap32_add(v);
}
DI u32 half_umax(u32 v) {
    v = max(v, dpp<0xB1>(v)); v = max(v, dpp<0x4E>(v)); v = max(v, dpp<0x141>(v)); v = max(v, dpp<0x140>(v));
    auto r = __builtin_amdgcn_permlane16_swap(v, v, false, false);
    return max(r[0], r[1]);
}
DI u32 wave_umax(u32 v) { v = half_umax(v); auto r = __builtin_amdgcn_permlane32_swap(v, v, false, false); return max(r[0], r[1]); }
DI u32 score_key(u32 bf, u32 idx) { return ((bf ^ ((bf & 0x8000u) ? 0xFFFFu : 0x8000u)) << 16) | (0xFFFFu - idx); }
DI u32 key_bf(u32 k) { const u32 b = k >> 16; return b ^ ((b & 0x8000u) ? 0x8000u : 0xFFFFu); }
DI u32 key_idx(u32 k) { return 0xFFFFu - (k & 0xFFFFu); }
DI void glds(const void* sbase, u32 voff, u32 ldsdst) {
    u32 sv;
    asm volatile("s_mov_b32 %0, m0\n\ts_mov_b32 m0, %3\n\ts_nop 0\n\tglobal_load_lds_dwordx4 %1, %2\n\ts_mov_b32 m0, %0"
                 : "=&s"(sv) : "v"(voff), "s"(sbase), "s"(ldsdst) : "memory");
}
struct Ctx {
    rsrc_t ws; u32 tag; int lane, w, xg, gi;
};
#define FRESH(c) asm volatile("" : "+v"((c).lane), "+s"((c).w), "+s"((c).xg), "+s"((c).gi))
DI u32 epoch_tag(u32 v) { const u32 t = (u32)__builtin_amdgcn_readfirstlane((int)v) + 1u; return t ? t : 1u; }
DI bool spin_fail(const Ctx& c, u32& n) {
    __builtin_amdgcn_s_sleep(1);
    ++n;
    if (n > SPIN_CAP) { if (c.lane == 0) __builtin_amdgcn_raw_buffer_store_b32(c.tag, c.ws, OFF_ABORT, 0, 16); return true; }
    if ((n & 127u) == 0u) { u32 a = __builtin_amdgcn_raw_buffer_load_b32(c.ws, OFF_ABORT, 0, 16); if (__builtin_amdgcn_readfirstlane(a) == c.tag) return true; }
    return false;
}
DI void wait_flags(const Ctx& c, u32 off) {
    u32 n = 0;
    for (;;) {
        u32x2 v = __builtin_amdgcn_raw_buffer_load_b64(c.ws, off, 0, 16);
        MEMFENCE();
        if (__all(v[0] == c.tag && v[1] == ~c.tag)) break;
        if (spin_fail(c, n)) break;
    }
    MEMFENCE();
}
DI void set_flag(const Ctx& c, u32 off) { u32x2 v = {c.tag, ~c.tag}; __builtin_amdgcn_raw_buffer_store_b64(v, c.ws, off, 0, 16); }
constexpr u32 POISON = 0xFFFFFFFFu;
DI bool poisoned(u32x4 v) { return max(max(v[0], v[1]), max(v[2], v[3])) == POISON; }
DI u32x4 not_poison(u32x4 v) { return (u32x4){min(v[0], POISON - 1u), min(v[1], POISON - 1u), min(v[2], POISON - 1u), min(v[3], POISON - 1u)}; }
DI void wait_piece(const Ctx& c, u32 off, u32x4& v) {
    u32 n = 0;
    while (__any(poisoned(v))) { if (spin_fail(c, n)) break; v = ldw(c.ws, off); MEMFENCE(); }
}
DI bool unit_ok(u32x4 v, u32 tag) { return v[1] == tag && v[3] == tag; }
DI u32x4 mk_unit(u32 a, u32 b, u32 tag) { return (u32x4){a, tag, b, tag}; }
template <int N> DI void wait_units(const Ctx& c, const u32 (&off)[N], u32x4 (&v)[N]) {
    u32 n = 0;
    for (;;) {
        bool ok = true;
#pragma unroll
        for (int k = 0; k < N; ++k) v[k] = ldw(c.ws, off[k]);
        MEMFENCE();
#pragma unroll
        for (int k = 0; k < N; ++k) ok = ok && unit_ok(v[k], c.tag);
        if (__all(ok)) break;
        if (spin_fail(c, n)) break;
    }
    MEMFENCE();
}
DI u32 lds_poll_ld(u32 a) { return __hip_atomic_load((LAS u32*)(uintptr_t)a, __ATOMIC_RELAXED, __HIP_MEMORY_SCOPE_WORKGROUP); }
DI void helper_sync(const Ctx& c, u32& target) {
    target += 4;
    asm volatile("s_waitcnt lgkmcnt(0)" ::: "memory");
    if (c.lane == 0) __hip_atomic_fetch_add((LAS u32*)(uintptr_t)P_HCNT, 1u, __ATOMIC_RELAXED, __HIP_MEMORY_SCOPE_WORKGROUP);
    u32 n = 0;
    while (lds_poll_ld(P_HCNT) < target) { if (++n > (1u << 24)) break; }
    MEMFENCE();
}
DI void lds_wait_ge(u32 a, u32 v) {
    u32 n = 0;
    while (lds_poll_ld(a) < v) { __builtin_amdgcn_s_sleep(1); if (++n > (1u << 22)) break; }
    MEMFENCE();
}
DI void lds_signal_add(const Ctx& c, u32 a) {
    asm volatile("s_waitcnt lgkmcnt(0)" ::: "memory");
    if (c.lane == 0) __hip_atomic_fetch_add((LAS u32*)(uintptr_t)a, 1u, __ATOMIC_RELAXED, __HIP_MEMORY_SCOPE_WORKGROUP);
}
DI void wait_sel(int L) {
    u32 n = 0;
    while (lds_poll_ld(P_SEL + L * 32 + 16) == 0u) { __builtin_amdgcn_s_sleep(1); if (++n > (1u << 22)) break; }
    MEMFENCE();
}

#define STAMP(i) do { } while (0)
#define CSTAMP(i) do { } while (0)
#define FST(i) do { } while (0)

DI u64 cand_tag(u32 tag) { return ((u64)tag << 11) | (u64)((tag * 0x9E3779B1u) >> 21); }
DI void ffn_chain(const Ctx& c, const Params& p, u32& hsync, const int L) {
    const int lane = c.lane, hw = c.w - 4;
    constexpr u32 FW = FW0, FS = FS0;
    const u32 GC = L ? OFF_GC1 : OFF_GC0, GM = L ? OFF_GM1 : OFF_GM0, GX = L ? OFF_GX1 : OFF_GX0, GA = L ? OFF_GA1 : OFF_GA0;
    FST(20 + 20 * L);
    __builtin_amdgcn_s_setprio(3);
    const bool is_gu = c.gi < 14, is_dn = c.gi >= 14 && c.gi < 18;
    const int g16 = lane >> 4, i16 = lane & 15;
    bf16x8 wf[14];
    {
        const u32 tro = FW + (u32)((8 * g16 + (i16 >> 2)) * 32 + (i16 & 3) * 8) + (u32)hw * (is_gu ? 4096u : 14336u);
#pragma unroll
        for (int s = 0; s < 14; ++s) {
            const u32 bo = tro + (is_gu ? (u32)(s & 3) * 1024u + (u32)((s >> 2) & 1) * 16384u : (u32)s * 1024u);
            if (is_gu || is_dn) wf[s] = frag_tr(bo, bo + 128);
        }
    }
    if (hw == 0) {
        const u64 want = cand_tag(c.tag);
        u64 v; u32 n = 0;
        for (;;) {
            const u32x2 t = __builtin_amdgcn_raw_buffer_load_b64(c.ws, GC + c.xg * 512u + lane * 8u, 0, 16);
            MEMFENCE();
            v = ((u64)t[1] << 32) | t[0];
            if (__all((v >> 21) == want)) break;
            if (spin_fail(c, n)) break;
        }
        FST(21 + 20 * L);
        const u32 key = wave_umax(score_key(((u32)v >> 5) & 0xFFFFu, (u32)lane * 32u + ((u32)v & 31u)));
        const u32 tok = key_idx(key); const float r = __uint_as_float(key_bf(key) << 16);
        if (lane == 0) {
            lds_st<u32x2>(P_SEL + L * 32, (u32x2){(u32)tok, __float_as_uint(r)});
            asm volatile("s_waitcnt lgkmcnt(0)" ::: "memory");
            __hip_atomic_store((LAS u32*)(uintptr_t)(P_SEL + L * 32 + 16), 1u, __ATOMIC_RELAXED, __HIP_MEMORY_SCOPE_WORKGROUP);
        }
    }
    FST(22 + 20 * L);
    STAMP(L ? 25 : 13);
    if (!is_gu && !is_dn) return;
    wait_sel(L);
    const u32x2 si = lds_ld<u32x2>(P_SEL + L * 32);
    const int sel = __builtin_amdgcn_readfirstlane((int)si[0]) & 2047;
    const float rsel = __uint_as_float(si[1]);
    const u32 gxrow = GX + (u32)(sel >> 5) * 2048u, gm = GM + (u32)(sel >> 5) * 16u;
    if (is_gu) {
        const int gidx = c.xg * 14 + c.gi;
        u32 off[2] = {gxrow + (u32)(hw * 32 + (lane & 31)) * 16u, gm};
        u32x4 v[2];
        FST(29 + 20 * L);
        wait_units<2>(c, off, v);
        FST(23 + 20 * L);
        const float mu = __uint_as_float(v[1][0]), rs = __uint_as_float(v[1][2]);
        if (lane < 32) {
            u32x2 h;
            h[0] = pk((bflo(v[0][0]) - mu) * rs, (bfhi(v[0][0]) - mu) * rs);
            h[1] = pk((bflo(v[0][2]) - mu) * rs, (bfhi(v[0][2]) - mu) * rs);
            lds_st<u32x2>(FS + (u32)(hw * 32 + lane) * 8u, h);
        }
        STAMP(L ? 26 : 14);
        FST(24 + 20 * L);
        f32x4 ag = {}, au = {};
#pragma unroll
        for (int s = 0; s < 4; ++s) {
            const bf16x8 a = lds_ld<bf16x8>(FS + (u32)((hw * 4 + s) * 64 + g16 * 16));
            ag = MFMA16(a, wf[s], ag);
            au = MFMA16(a, wf[4 + s], au);
        }
        if (lane < 16) { lds_st<float>(FS + 4608 + (u32)(hw * 32 + lane) * 4u, ag[0]); lds_st<float>(FS + 4608 + (u32)(hw * 32 + 16 + lane) * 4u, au[0]); }
        FST(25 + 20 * L);
        helper_sync(c, hsync);
        FST(26 + 20 * L);
        if (hw == 0) {
            float g = 0.f, u = 0.f;
#pragma unroll
            for (int k = 0; k < 4; ++k) { g += lds_ld<float>(FS + 4608 + (u32)(k * 32 + i16) * 4u); u += lds_ld<float>(FS + 4608 + (u32)(k * 32 + 16 + i16) * 4u); }
            g = rbf(g); u = rbf(u);
            const float sg = rbf(g * __builtin_amdgcn_rcpf(1.0f + __expf(-g)));
            const float a = rbf(sg * u);
            const float a0 = __shfl(a, (lane & 3) * 4), a1 = __shfl(a, (lane & 3) * 4 + 1), a2 = __shfl(a, (lane & 3) * 4 + 2), a3 = __shfl(a, (lane & 3) * 4 + 3);
            if (lane < 4) stw(c.ws, GA + (u32)(gidx * 4 + lane) * 16u, mk_unit(pk(a0, a1), pk(a2, a3), c.tag));
            FST(27 + 20 * L);
        }
    } else {
        const int didx = c.xg * 4 + (c.gi - 14);
        float mu, rs;
        {
            const int u0 = hw * 112 + lane, u1 = hw * 112 + 64 + (lane < 48 ? lane : 47);
            u32 off[4] = {gxrow + (u32)(didx * 4 + (lane & 3)) * 16u, gm, GA + (u32)u0 * 16u, GA + (u32)u1 * 16u};
            u32x4 v[4];
            wait_units<4>(c, off, v);
            FST(23 + 20 * L);
            mu = __uint_as_float(v[1][0]); rs = __uint_as_float(v[1][2]);
            lds_st<u32x2>(FS + 1024 + (u32)u0 * 8u, (u32x2){v[2][0], v[2][2]});
            if (lane < 48) lds_st<u32x2>(FS + 1024 + (u32)u1 * 8u, (u32x2){v[3][0], v[3][2]});
            if (hw == 0 && lane < 4) lds_st<u32x2>(FS + (u32)lane * 8u, (u32x2){v[0][0], v[0][2]});
        }
        STAMP(L ? 26 : 14);
        FST(24 + 20 * L);
        f32x4 ay = {}, az = {};
        bf16x8 af[14];
#pragma unroll
        for (int s = 0; s < 14; ++s) af[s] = lds_ld<bf16x8>(FS + 1024 + (u32)((hw * 14 + s) * 64 + g16 * 16));
#pragma unroll
        for (int s = 0; s < 14; s += 2) { ay = MFMA16(af[s], wf[s], ay); az = MFMA16(af[s + 1], wf[s + 1], az); }
        if (lane < 16) lds_st<float>(FS + 4608 + (u32)(hw * 16 + lane) * 4u, ay[0] + az[0]);
        FST(25 + 20 * L);
        helper_sync(c, hsync);
        FST(26 + 20 * L);
        if (hw == 0) {
            float y = 0.f;
#pragma unroll
            for (int k = 0; k < 4; ++k) y += lds_ld<float>(FS + 4608 + (u32)(k * 16 + i16) * 4u);
            const float x1 = __uint_as_float((u32)lds_ld<uint16_t>(FS + (u32)i16 * 2u) << 16);
            const float sig = rbf(__builtin_amdgcn_rcpf(1.0f + __expf(-rsel)));
            y = rbf(rbf(y) * sig);
            const float h = rbf((x1 - mu) * rs);
            const float x2 = rbf(x1 + rbf(h + y));
            const float a0 = __shfl(x2, (lane & 3) * 4), a1 = __shfl(x2, (lane & 3) * 4 + 1), a2 = __shfl(x2, (lane & 3) * 4 + 2), a3 = __shfl(x2, (lane & 3) * 4 + 3);
            if (L == 0) { if (lane < 4) stw(c.ws, OFF_GY + (u32)(didx * 4 + lane) * 16u, mk_unit(pk(a0, a1), pk(a2, a3), c.tag)); }
            else if (lane < 4) *(u32x2*)(p.out + (size_t)sel * 512 + didx * 16 + lane * 4) = (u32x2){pk(a0, a1), pk(a2, a3)};
            FST(27 + 20 * L);
        }
    }
}

DI void qkv_patch(const Ctx& c, u32& hsync) {
    const int lane = c.lane, hw = c.w - 4;
    constexpr u32 FS = FS0;
    const int g16 = lane >> 4, i16 = lane & 15;
    bf16x8 wf[24];
    {
        const u32 tro = L_W + (u32)((8 * g16 + (i16 >> 2)) * 64 + (i16 & 3) * 8) + (u32)hw * 8192u;
#pragma unroll
        for (int s_ = 0; s_ < 4; ++s_)
#pragma unroll
            for (int ct = 0; ct < 6; ++ct) {
                const u32 bo = tro + (u32)s_ * 2048u + (u32)(ct >> 1) * 32768u + (u32)(ct & 1) * 32u;
                wf[s_ * 6 + ct] = frag_tr(bo, bo + 256);
            }
    }
    u32 off[2] = {OFF_GY + (u32)lane * 32u, OFF_GY + (u32)lane * 32u + 16u}; u32x4 v[2];
    wait_units<2>(c, off, v);
    float x[8], s = 0, q = 0;
#pragma unroll
    for (int k = 0; k < 2; ++k) { x[4 * k] = bflo(v[k][0]); x[4 * k + 1] = bfhi(v[k][0]); x[4 * k + 2] = bflo(v[k][2]); x[4 * k + 3] = bfhi(v[k][2]); }
#pragma unroll
    for (int k = 0; k < 8; ++k) { s += x[k]; q += x[k] * x[k]; }
    s = wave_sum(s); q = wave_sum(q);
    const float mu = s * (1.0f / 512.0f), var = fmaxf(q * (1.0f / 512.0f) - mu * mu, 0.f);
    const float A = __builtin_amdgcn_rsqf(var + 1e-5f);
    lds_st<u32x4>(FS + (u32)lane * 16u, (u32x4){pk((x[0] - mu) * A, (x[1] - mu) * A), pk((x[2] - mu) * A, (x[3] - mu) * A), pk((x[4] - mu) * A, (x[5] - mu) * A), pk((x[6] - mu) * A, (x[7] - mu) * A)});
    f32x4 acc[6] = {};
#pragma unroll
    for (int s_ = 0; s_ < 4; ++s_) {
        const bf16x8 a = lds_ld<bf16x8>(FS + (u32)((hw * 4 + s_) * 64 + g16 * 16));
#pragma unroll
        for (int ct = 0; ct < 6; ++ct) acc[ct] = MFMA16(a, wf[s_ * 6 + ct], acc[ct]);
    }
    if (lane < 16) {
#pragma unroll
        for (int ct = 0; ct < 6; ++ct) lds_st<float>(FS + 1024 + (u32)(hw * 96 + ct * 16 + lane) * 4u, acc[ct][0]);
    }
    helper_sync(c, hsync);
    if (hw == 0) {
#pragma unroll
        for (int k = 0; k < 2; ++k) {
            const int n = k * 64 + lane;
            if (n < 96) {
                float t = 0.f;
#pragma unroll
                for (int j = 0; j < 4; ++j) t += lds_ld<float>(FS + 1024 + (u32)(j * 96 + n) * 4u);
                lds_st<float>(FS + 4608 + (u32)n * 4u, n < 32 ? t * (0.25f * 1.4426950408889634f) : t);
            }
        }
        asm volatile("s_waitcnt lgkmcnt(0)" ::: "memory");
        if (lane == 0) __hip_atomic_store((LAS u32*)(uintptr_t)P_PATCH, 1u, __ATOMIC_RELAXED, __HIP_MEMORY_SCOPE_WORKGROUP);
    }
}

DI u32x4 gld(const void* base, u32 off) { return *(const u32x4*)((const char*)base + off); }
struct FfSrc { const uint16_t* m; u32 vo, step; bool on, gu; };
DI FfSrc ff_src(const Ctx& c, const Params& p) {
    int lane = c.lane; asm volatile("" : "+v"(lane));
    FfSrc f; const int w = c.w;
    f.on = c.gi < 18; f.gu = c.gi < 14;
    f.m = f.gu ? (w < 4 ? p.wg : p.wu) : p.wd;
    const u32 rowb = f.gu ? 3584u : 1024u, col = f.gu ? (u32)(c.xg * 14 + c.gi) * 32u : (u32)(c.xg * 4 + (c.gi - 14)) * 32u;
    const u32 first = f.gu ? (u32)(w & 3) * 4u : (u32)w * 7u;
    f.step = 32u * rowb;
    f.vo = (u32)(lane >> 1) * rowb + col + (u32)(lane & 1) * 16u + first * f.step;
    return f;
}
DI void keep_wq(const Ctx& c, u32x4 (&pq)[12]) {
#pragma unroll
    for (int j = 0; j < 12; ++j) pq[j] = lds_ld<u32x4>(L_W + (u32)(c.w * 12 + j) * 1024u + (u32)c.lane * 16u);
}
DI void pre_store_wq(const Ctx& c, const u32x4 (&pq)[12]) {
#pragma unroll
    for (int j = 0; j < 12; ++j) lds_st<u32x4>(L_W + (u32)(c.w * 12 + j) * 1024u + (u32)c.lane * 16u, pq[j]);
}
DI void pre_store_ff(const Ctx& c, const u32x4 (&pf)[7]) {
    if (c.gi < 18) {
        const bool gu = c.gi < 14;
        const u32 a = FW0 + (u32)c.w * (gu ? 4096u : 7168u) + (u32)c.lane * 16u;
#pragma unroll
        for (int j = 0; j < 7; ++j) if (j < 4 || !gu) lds_st<u32x4>(a + (u32)j * 1024u, pf[j]);
    }
}
DI u32 wo_lane_off(const Ctx& c) {
    int lane = c.lane; asm volatile("" : "+v"(lane));
    return (u32)((lane >> 2) * 1024 + (c.gi & 7) * 128 + (lane & 3) * 16 + (c.w >> 2) * 64 + (c.w & 3) * 8 * 16384);
}
DI void pre_store_wo(const Ctx& c, const u32x4 (&po)[8]) {
#pragma unroll
    for (int j = 0; j < 8; ++j) lds_st<u32x4>(L_WO + (u32)(c.w >> 2) * WO_FT + (u32)((c.w & 3) * 8 + j) * 1024u + (u32)c.lane * 16u, po[j]);
}

struct WFrag { bf16x8 q, k, v; };
template <u32 TR2> DI void wfrag_load(WFrag& f, u32 a01, u32 a2) { f.q = frag_tr(a01, a01 + TR2); f.k = frag_tr(a01 + 32768, a01 + 32768 + TR2); f.v = frag_tr(a2, a2 + TR2); }
template <u32 TR2> DI void qstep(f32x16& aq, f32x16& ak, f32x16& av, bf16x8 x, WFrag& cur, WFrag& nxt, u32 a01, u32 a2, bool more) {
    if (more) nxt.q = frag_tr(a01, a01 + TR2);
    SBAR(); aq = MFMA32(cur.q, x, aq); SBAR();
    if (more) nxt.k = frag_tr(a01 + 32768, a01 + 32768 + TR2);
    SBAR(); ak = MFMA32(cur.k, x, ak); SBAR();
    if (more) nxt.v = frag_tr(a2, a2 + TR2);
    SBAR(); av = MFMA32(x, cur.v, av); SBAR();
}

template <int L>
DI void phaseQ(Ctx& c0, const Params& p, u32& hsync, u32x4 (&pq)[12]) {
    Ctx c = c0; FRESH(c);
    const int lane = c.lane, w = c.w;
    const int rb = 4 * (c.xg & 3) + (c.gi & 3), hp = 8 * (c.xg >> 2) + (c.gi >> 2);
    const int r = lane & 31, h = lane >> 5;
    const int i16 = lane & 15, blk = (lane >> 4) & 1;
    const u32 trA = L_W + (u32)(((L == 0 ? 8 : 4) * h + (i16 >> 2)) * 64 + blk * 32 + (i16 & 3) * 8);
    constexpr u32 TR2 = L == 0 ? 256 : 512;
    constexpr u32 OQ = L ? OFF_Q1 : OFF_Q0, OK_ = L ? OFF_K1 : OFF_K0, OV = L ? OFF_V1 : OFF_V0, TS = L ? OFF_TS1 : OFF_TS0;
    const int rot = ((c.gi >> 2) + 2 * (c.gi & 3)) & 7;

    if (w >= 4) {
        const int hw = w - 4;
        if (L == 0) {
            u32x4 r0, r1; u32 ep;
            asm volatile("global_load_dword %0, %1, %2 sc1" : "=v"(ep) : "v"(0u), "s"(p.ws));
            const u32 tok = (u32)(rb * 128 + hp * 8 + hw * 2);
            const u32 vo = tok * 1024u + (u32)lane * 16u;
            asm volatile("global_load_dwordx4 %0, %1, %2" : "=v"(r0) : "v"(vo), "s"(p.x0));
            asm volatile("global_load_dwordx4 %0, %1, %2 offset:1024" : "=v"(r1) : "v"(vo), "s"(p.x0));
            const u32 wv = (u32)((lane >> 2) * 3072 + hp * 64 + (lane & 3) * 16 + hw * 16 * 3072);
            u32 xv[4];
#pragma unroll
            for (int q = 0; q < 4; ++q) { const int row = 8 * q + (lane >> 3); xv[q] = (u32)(((rb * 4 + hw) * 32 + row) * 1024 + (((lane & 7) ^ ((row >> 1) & 7)) * 16)); }
#define WD(i) do { const u32 kc_ = (u32)(((i) + rot) & 7); _Pragma("unroll") for (int t = 0; t < 3; ++t) \
                glds(p.wqkv, wv + (u32)t * 1024u + kc_ * (64u * 3072u), L_W + (u32)t * 32768u + (kc_ * 64u + (u32)hw * 16u) * 64u); } while (0)
#define XD(i) do { const u32 kc_ = (u32)(((i) + rot) & 7); _Pragma("unroll") for (int q = 0; q < 4; ++q) \
                glds(p.x0, xv[q] + kc_ * 128u, L_XR + (u32)((i) % 3) * 16384u + (u32)hw * 4096u + (u32)q * 1024u); } while (0)
#define QWAIT(n) asm volatile("s_waitcnt vmcnt(" #n ")\n\ts_barrier" ::: "memory")
#define CS(i) if (hw < 3) { _Pragma("unroll") for (int s_ = 0; s_ < 4; ++s_) { \
                const u32 a_ = trA + (u32)hw * 32768u + (u32)((((i) + rot) & 7) * 4 + s_) * 1024u; cs = MFMA32(frag_tr(a_, a_ + TR2), ones, cs); } }
            WD(0); XD(0); WD(1); XD(1); WD(2); XD(2); WD(3);
            FST(1);
            f32x16 cs = {};
            const bf16x8 ones = __builtin_bit_cast(bf16x8, (u32x4){0x3f803f80u, 0x3f803f80u, 0x3f803f80u, 0x3f803f80u});
            asm volatile("s_waitcnt vmcnt(17)\n\ts_barrier" : "+v"(r0), "+v"(r1), "+v"(ep) :: "memory");
            c.tag = epoch_tag(ep); c0.tag = c.tag;
            FST(2);
            {
                float s0 = 0, q0 = 0, s1 = 0, q1 = 0;
#pragma unroll
                for (int j = 0; j < 4; ++j) {
                    float a = bflo(r0[j]), b_ = bfhi(r0[j]); s0 += a + b_; q0 += a * a + b_ * b_;
                    float d = bflo(r1[j]), e = bfhi(r1[j]); s1 += d + e; q1 += d * d + e * e;
                }
                s0 = wave_sum(s0); q0 = wave_sum(q0); s1 = wave_sum(s1); q1 = wave_sum(q1);
                const float s = lane ? s1 : s0, q = lane ? q1 : q0;
                const float mu = s * (1.0f / 512.0f), var = fmaxf(q * (1.0f / 512.0f) - mu * mu, 0.f);
                const float A = __builtin_amdgcn_rsqf(var + 1e-5f);
                if (lane < 2) stw(c.ws, TS + (tok + (u32)lane) * 16u, mk_unit(__float_as_uint(A), __float_as_uint(A * mu), c.tag));
            }
            CS(0);
            QWAIT(11); FST(3); XD(3); WD(4); CS(1);
            QWAIT(11); FST(4); XD(4); WD(5); CS(2);
            QWAIT(10); FST(5); XD(5); WD(6); CS(3);
            QWAIT(10); FST(6); XD(6); WD(7); CS(4);
            QWAIT(10); FST(7); XD(7); CS(5);
            QWAIT(7); FST(8); CS(6);
            QWAIT(0); FST(9); CS(7);
#undef WD
#undef XD
#undef QWAIT
#undef CS
            if (hw < 3 && r == 0) {
#pragma unroll
                for (int g = 0; g < 4; ++g) lds_st<f32x4>(P_CS + (u32)(hw * 32 + 8 * g + 4 * h) * 4u, (f32x4){cs[4 * g], cs[4 * g + 1], cs[4 * g + 2], cs[4 * g + 3]});
            }
            STAMP(1);
            BAR();
        }
        DRAIN(); BAR();
        STAMP(L ? 16 : 3);
        if (L == 0) keep_wq(c, pq);
        else {
            wait_sel(0);
            const int sel = __builtin_amdgcn_readfirstlane((int)lds_ld<u32>(P_SEL)) & 2047;
            if ((sel >> 7) == rb) { lds_wait_ge(P_WQ, 8u); qkv_patch(c, hsync); }
        }
    } else {
        const int T = rb * 4 + w;
        f32x16 aq = {}, ak = {}, av = {};
        float A, B;
        if (L == 0) {
            const u32 ep = __builtin_amdgcn_raw_buffer_load_b32(c.ws, OFF_EPOCH, 0, 16);
            u32 xa[4];
#pragma unroll
            for (int s_ = 0; s_ < 4; ++s_) xa[s_] = L_XR + (u32)w * 4096u + (u32)(r * 128 + (((2 * s_ + h) ^ ((r >> 1) & 7)) * 16));
            bf16x8 xr[2][4];
            u32x4 tsv;
            WFrag f0, f1;
            BAR();
            FST(2);
#pragma unroll
            for (int s_ = 0; s_ < 4; ++s_) xr[0][s_] = lds_ld<bf16x8>(xa[s_]);
            { const u32 a01 = trA + (u32)(rot & 7) * 4096u; wfrag_load<TR2>(f0, a01, a01 + 65536u); }
#pragma unroll
            for (int i = 0; i < 8; ++i) {
                const u32 a01 = trA + (u32)((i + rot) & 7) * 4096u, a2 = a01 + 65536u;
                const u32 n01 = trA + (u32)((i + 1 + rot) & 7) * 4096u, n2 = n01 + 65536u;
                qstep<TR2>(aq, ak, av, xr[i & 1][0], f0, f1, a01 + 1024, a2 + 1024, true);
                qstep<TR2>(aq, ak, av, xr[i & 1][1], f1, f0, a01 + 2048, a2 + 2048, true);
                qstep<TR2>(aq, ak, av, xr[i & 1][2], f0, f1, a01 + 3072, a2 + 3072, true);
                if (i == 6) tsv = ldw(c.ws, TS + (u32)(T * 32 + r) * 16u);
                if (i < 7) {
                    asm volatile("s_barrier" ::: "memory");
                    FST(3 + i);
#pragma unroll
                    for (int s_ = 0; s_ < 4; ++s_) xr[(i + 1) & 1][s_] = lds_ld<bf16x8>(xa[s_] + (u32)((i + 1) % 3) * 16384u);
                }
                qstep<TR2>(aq, ak, av, xr[i & 1][3], f1, f0, n01, n2, i < 7);
            }
            FST(10);
            STAMP(1);
            c.tag = epoch_tag(ep); c0.tag = c.tag;
            if (w == 3) {
                const u32x4 v = *(const u32x4*)(p.wr + lane * 8); float s = 0;
#pragma unroll
                for (int j = 0; j < 4; ++j) s += bflo(v[j]) + bfhi(v[j]);
                s = wave_sum(s);
                if (lane == 0) lds_st<float>(P_WSUM, s);
            }
            u32 off[1] = {TS + (u32)(T * 32 + r) * 16u}; u32x4 v[1] = {tsv};
            if (!__all(unit_ok(tsv, c.tag))) wait_units<1>(c, off, v);
            A = __uint_as_float(v[0][0]); B = __uint_as_float(v[0][2]);
            BAR();
            STAMP(2);
        } else {
            wait_flags(c, OFF_FO + (u32)(rb * 16 + (lane & 15)) * 128u);
            STAMP(13);
            u32x4 xf[32];
            const u32 xo = OFF_X1 + (u32)T * 32768u + (u32)lane * 16u;
#pragma unroll
            for (int s = 0; s < 32; ++s) xf[s] = ldw(c.ws, xo + (u32)s * 1024u);
            const u32x4 tsv = ldw(c.ws, TS + (u32)(T * 32 + r) * 16u);
            lds_wait_ge(P_WQ, 8u);
            STAMP(14);
            {
                u32 a01 = trA, a2 = trA + 65536u;
                asm volatile("" : "+v"(a01), "+v"(a2));
                WFrag f0, f1;
                wfrag_load<TR2>(f0, a01, a2);
#pragma unroll
                for (int st = 0; st < 32; st += 2) {
                    qstep<TR2>(aq, ak, av, __builtin_bit_cast(bf16x8, xf[st]), f0, f1, a01 + (u32)(st + 1) * 1024u, a2 + (u32)(st + 1) * 1024u, true);
                    qstep<TR2>(aq, ak, av, __builtin_bit_cast(bf16x8, xf[st + 1]), f1, f0, a01 + (u32)(st + 2) * 1024u, a2 + (u32)(st + 2) * 1024u, st + 2 < 32);
                }
            }
            u32 off[1] = {TS + (u32)(T * 32 + r) * 16u}; u32x4 v[1] = {tsv};
            if (!__all(unit_ok(tsv, c.tag))) wait_units<1>(c, off, v);
            A = __uint_as_float(v[0][0]); B = __uint_as_float(v[0][2]);
        }
        if (L == 1) STAMP(15);
        int ts = -1;
        if (L == 1) {
            wait_sel(0);
            const int sel = __builtin_amdgcn_readfirstlane((int)lds_ld<u32>(P_SEL)) & 2047;
            if ((sel >> 5) == T) ts = sel & 31;
        }
        const u32x4 pois = {POISON, POISON, POISON, POISON};
        const u32 tokl = L_TOK + (u32)w * 256u;
        if (h == 0) lds_st<f32x2>(tokl + (u32)r * 8u, (f32x2){A, B});
        constexpr float QS = 0.25f * 1.4426950408889634f;
#pragma unroll
        for (int t = 0; t < 2; ++t) {
            const f32x16& acc = t ? ak : aq;
            const float sA = t ? A : A * QS, sB = t ? B : B * QS;
            float o[16];
#pragma unroll
            for (int g = 0; g < 4; ++g) {
                const f32x4 cs = lds_ld<f32x4>(P_CS + (u32)(t * 32 + 8 * g + 4 * h) * 4u);
#pragma unroll
                for (int e = 0; e < 4; ++e) o[4 * g + e] = sA * acc[4 * g + e] - sB * cs[e];
            }
#pragma unroll
            for (int a = 0; a < 2; ++a) {
                u32x4 v = {pk(o[8 * a], o[8 * a + 1]), pk(o[8 * a + 2], o[8 * a + 3]), pk(o[8 * a + 4], o[8 * a + 5]), pk(o[8 * a + 6], o[8 * a + 7])};
                if (L == 1 && r == ts) v = pois;
                stw(c.ws, (t ? OK_ : OQ) + (u32)(((hp * 2 + a) * 64 + T) * 64 + lane) * 16u, v);
            }
        }
        u32x4 vlate = pois;
        const u32 vbase = OV + (u32)(((hp * 2 + (r >> 4)) * 64 + T) * 1024 + h * 256 + (r & 15) * 16);
        {
            asm volatile("s_waitcnt lgkmcnt(0)" ::: "memory");
            const float cs = lds_ld<float>(P_CS + (u32)(64 + r) * 4u);
            float o[16];
#pragma unroll
            for (int g = 0; g < 4; ++g) {
                const f32x4 ab0 = lds_ld<f32x4>(tokl + (u32)(8 * g + 4 * h) * 8u), ab1 = lds_ld<f32x4>(tokl + (u32)(8 * g + 4 * h + 2) * 8u);
                o[4 * g] = ab0[0] * av[4 * g] - ab0[1] * cs; o[4 * g + 1] = ab0[2] * av[4 * g + 1] - ab0[3] * cs;
                o[4 * g + 2] = ab1[0] * av[4 * g + 2] - ab1[1] * cs; o[4 * g + 3] = ab1[2] * av[4 * g + 3] - ab1[3] * cs;
            }
#pragma unroll
            for (int s = 0; s < 2; ++s) {
                u32x4 v = {pk(o[8 * s], o[8 * s + 1]), pk(o[8 * s + 2], o[8 * s + 3]), pk(o[8 * s + 4], o[8 * s + 5]), pk(o[8 * s + 6], o[8 * s + 7])};
                if (L == 1 && ts >= 0 && s == (ts >> 4) && h == ((ts >> 2) & 1)) { vlate = v; v = pois; }
                stw(c.ws, vbase + (u32)s * 512u, v);
            }
        }
        DRAIN(); BAR();
        STAMP(L ? 16 : 3);
        if (w == 0 && lane == 0) set_flag(c, (L ? OFF_FQ1 : OFF_FQ0) + (u32)(hp * 16 + rb) * 128u);
        if (L == 1 && ts >= 0) {
            lds_wait_ge(P_PATCH, 1u);
            if (r == ts) {
#pragma unroll
                for (int t = 0; t < 2; ++t)
#pragma unroll
                    for (int a = 0; a < 2; ++a) {
                        const f32x4 p0 = lds_ld<f32x4>(FS0 + 4608 + (u32)(t * 32 + 16 * a + 4 * h) * 4u), p1 = lds_ld<f32x4>(FS0 + 4608 + (u32)(t * 32 + 16 * a + 8 + 4 * h) * 4u);
                        stw(c.ws, (t ? OK_ : OQ) + (u32)(((hp * 2 + a) * 64 + T) * 64 + lane) * 16u, not_poison((u32x4){pk(p0[0], p0[1]), pk(p0[2], p0[3]), pk(p1[0], p1[1]), pk(p1[2], p1[3])}));
                    }
            }
            if (h == ((ts >> 2) & 1)) {
                const u32 pv = pk(lds_ld<float>(FS0 + 4608 + (u32)(64 + r) * 4u), 0.f) & 0xFFFFu;
                const int jj = (ts & 3) + 4 * ((ts >> 3) & 1);
#pragma unroll
                for (int k = 0; k < 4; ++k) if (k == (jj >> 1)) vlate[k] = (jj & 1) ? ((vlate[k] & 0xFFFFu) | (pv << 16)) : ((vlate[k] & 0xFFFF0000u) | pv);
                stw(c.ws, vbase + (u32)(ts >> 4) * 512u, not_poison(vlate));
            }
        }
        if (L == 0) keep_wq(c, pq);
    }
}

struct AState { f32x16 Sa, Sb, Oa, Ob; bf16x8 kC, kN, v0, v1; u32x4 pb1; };
#define EXP2(x) __builtin_amdgcn_exp2f(x)
#define BC8(x) __builtin_bit_cast(bf16x8, x)
#define PVM(O, v, p) O = MFMA32(v, BC8(p), O)
template <bool USE_C, class HOOK>
DI void attn_step(AState& s, bf16x8 qa, bf16x8 qb, const f32x16& ca, const f32x16& cb, u32 kp, u32 vp0, u32 vp1, u32 knext, u32 voff, HOOK hook) {
    const f32x16 zero = {};
    u32x4 pa0, pa1, pb0;
    s.kN = lds_ld<bf16x8>(kp + knext); SBAR();
    s.v0 = lds_ld<bf16x8>(vp0 + voff); SBAR();
    PVM(s.Ob, s.v1, s.pb1); SBAR();
    s.v1 = lds_ld<bf16x8>(vp1 + voff); SBAR();
    float a0 = EXP2(s.Sa[0]); SBAR(); float a1 = EXP2(s.Sa[1]); SBAR(); float a2 = EXP2(s.Sa[2]); SBAR(); float a3 = EXP2(s.Sa[3]); SBAR();
    s.Sb = MFMA32(s.kC, qb, USE_C ? cb : zero); SBAR();
    float a4 = EXP2(s.Sa[4]); SBAR(); float a5 = EXP2(s.Sa[5]); SBAR(); float a6 = EXP2(s.Sa[6]); SBAR(); float a7 = EXP2(s.Sa[7]); SBAR();
    pa0[0] = pk(a0, a1); SBAR(); pa0[1] = pk(a2, a3); SBAR(); float a8 = EXP2(s.Sa[8]); SBAR(); pa0[2] = pk(a4, a5); SBAR(); pa0[3] = pk(a6, a7); SBAR();
    float a9 = EXP2(s.Sa[9]); SBAR();
    asm volatile("" : "+v"(s.v0), "+v"(s.v1), "+v"(s.kN)); SBAR();
    float a10 = EXP2(s.Sa[10]); SBAR();
    PVM(s.Oa, s.v0, pa0); SBAR();
    float a11 = EXP2(s.Sa[11]); SBAR(); float a12 = EXP2(s.Sa[12]); SBAR(); float a13 = EXP2(s.Sa[13]); SBAR(); float a14 = EXP2(s.Sa[14]); SBAR(); float a15 = EXP2(s.Sa[15]); SBAR();
    pa1[0] = pk(a8, a9); SBAR(); pa1[1] = pk(a10, a11); SBAR(); pa1[2] = pk(a12, a13); SBAR(); pa1[3] = pk(a14, a15); SBAR();
    float b0 = EXP2(s.Sb[0]); SBAR(); float b1 = EXP2(s.Sb[1]); SBAR();
    PVM(s.Oa, s.v1, pa1); SBAR();
    hook(); SBAR();
    float b2 = EXP2(s.Sb[2]); SBAR(); float b3 = EXP2(s.Sb[3]); SBAR(); float b4 = EXP2(s.Sb[4]); SBAR(); float b5 = EXP2(s.Sb[5]); SBAR();
    s.Sa = MFMA32(s.kN, qa, USE_C ? ca : zero); SBAR();
    float b6 = EXP2(s.Sb[6]); SBAR(); float b7 = EXP2(s.Sb[7]); SBAR();
    pb0[0] = pk(b0, b1); SBAR(); pb0[1] = pk(b2, b3); SBAR(); float b8 = EXP2(s.Sb[8]); SBAR(); pb0[2] = pk(b4, b5); SBAR(); pb0[3] = pk(b6, b7); SBAR();
    float b9 = EXP2(s.Sb[9]); SBAR(); float b10 = EXP2(s.Sb[10]); SBAR();
    PVM(s.Ob, s.v0, pb0); SBAR();
    float b11 = EXP2(s.Sb[11]); SBAR(); float b12 = EXP2(s.Sb[12]); SBAR(); float b13 = EXP2(s.Sb[13]); SBAR(); float b14 = EXP2(s.Sb[14]); SBAR(); float b15 = EXP2(s.Sb[15]); SBAR();
    s.pb1[0] = pk(b8, b9); SBAR(); s.pb1[1] = pk(b10, b11); SBAR(); s.pb1[2] = pk(b12, b13); SBAR(); s.pb1[3] = pk(b14, b15); SBAR();
    s.kC = s.kN;
}
struct NoHook { DI void operator()() const {} };
template <bool USE_C>
DI void attn_chunk(AState& s, bf16x8 qa, bf16x8 qb, const f32x16& ca, const f32x16& cb, u32 kp, u32 vp0, u32 vp1) {
    attn_step<USE_C>(s, qa, qb, ca, cb, kp, vp0, vp1, 1024, 0, NoHook());
    attn_step<USE_C>(s, qa, qb, ca, cb, kp, vp0, vp1, 2048, VSTR, NoHook());
    attn_step<USE_C>(s, qa, qb, ca, cb, kp, vp0, vp1, 3072, 2 * VSTR, NoHook());
    attn_step<USE_C>(s, qa, qb, ca, cb, kp, vp0, vp1, 8192, 3 * VSTR, NoHook());
}
DI void attn_begin(AState& s, bf16x8 qa, const f32x16& ca, u32 kp) {
    const f32x16 zero = {};
    s.Oa = zero; s.Ob = zero; s.pb1 = (u32x4){0u, 0u, 0u, 0u}; s.v1 = BC8(s.pb1);
    s.kC = lds_ld<bf16x8>(kp);
    s.Sa = MFMA32(s.kC, qa, ca);
}
DI void attn_combine(const Ctx& c, AState& s, float (&O)[9]) {
    const int kh = c.w >> 2;
    s.Ob = MFMA32(s.v1, BC8(s.pb1), s.Ob);
    const f32x16& snd = kh ? s.Oa : s.Ob; const f32x16& keep = kh ? s.Ob : s.Oa;
    const u32 xb = L_AX + (u32)c.w * 2304u;
    lds_st<f32x4>(xb + (u32)c.lane * 16u, (f32x4){snd[0], snd[1], snd[2], snd[3]});
    lds_st<f32x4>(xb + 1024u + (u32)c.lane * 16u, (f32x4){snd[4], snd[5], snd[6], snd[7]});
    lds_st<float>(xb + 2048u + (u32)c.lane * 4u, snd[8]);
    BAR();
    const u32 xp = L_AX + (u32)(c.w ^ 4) * 2304u;
    const f32x4 u0 = lds_ld<f32x4>(xp + (u32)c.lane * 16u), u1 = lds_ld<f32x4>(xp + 1024u + (u32)c.lane * 16u);
    const float l2 = lds_ld<float>(xp + 2048u + (u32)c.lane * 4u);
#pragma unroll
    for (int q = 0; q < 4; ++q) { O[q] = keep[q] + u0[q]; O[4 + q] = keep[4 + q] + u1[q]; }
    O[8] = keep[8] + l2;
}

DI void phaseA(const Ctx& c0, const Params& p, const int L) {
    Ctx c = c0; FRESH(c);
    u32x4 pf[7] = {}, po[8];
    const int lane = c.lane, w = c.w, tid = w * 64 + lane;
    const int head = 4 * c.xg + (c.gi >> 3), qb = c.gi & 7;
    const int j = w & 3, kh = w >> 2;
    const u32 OQ = L ? OFF_Q1 : OFF_Q0, OK_ = L ? OFF_K1 : OFF_K0, OV = L ? OFF_V1 : OFF_V0, OO = L ? OFF_O1 : OFF_O0;
    if (tid == 0) lds_st<u32>(L_AX + 8 * 2304u, 0u);
    if (w == 0) {
        wait_flags(c, (L ? OFF_FQ1 : OFF_FQ0) + (u32)((head >> 1) * 16 + (lane & 15)) * 128u);
        if (L == 1) {
            const int sel = __builtin_amdgcn_readfirstlane((int)lds_ld<u32>(P_SEL)) & 2047, T = sel >> 5, ts = sel & 31;
            const u32 tk = (u32)((head * 64 + T) * 64 + (lane & 1) * 32 + ts) * 16u;
            u32 off = OK_ + tk;
            if (lane >= 2 && lane < 18) off = OV + (u32)((head * 64 + T) * 1024 + (ts >> 4) * 512 + ((ts >> 2) & 1) * 256 + (lane - 2) * 16);
            if (lane >= 18 && lane < 20 && (T >> 3) == qb) off = OQ + tk;
            u32x4 v = ldw(c.ws, off);
            wait_piece(c, off, v);
        }
    }
    if (L == 0 && w == 1) {
        const int qo = (4 * c.xg + (c.gi >> 3)) >> 2, hd = lane & 31, xg = hd >> 2, gi = (hd & 3) * 8 + qo;
        wait_flags(c, OFF_FQ0 + (u32)((8 * (xg >> 2) + (gi >> 2)) * 16 + 4 * (xg & 3) + (gi & 3)) * 128u);
    }
    BAR();
    STAMP(L ? 17 : 4);
    CSTAMP(9);
    const u32 koff = OK_ + (u32)((head * 64 + w) * 64 + lane) * 16u, voff = OV + (u32)((head * 64 + w) * 64 + lane) * 16u;
    u32x4 rk[8], rv[8];
    rk[0] = ldw(c.ws, koff); rv[0] = ldw(c.ws, voff);
    const u32 qo = OQ + (u32)((head * 64 + qb * 8 + 2 * j) * 64 + lane) * 16u;
    const u32x4 qar = ldw(c.ws, qo), qbr = ldw(c.ws, qo + 1024u);
    SBAR(); __builtin_amdgcn_s_barrier(); SBAR();
    rk[1] = ldw(c.ws, koff + 8192u); rv[1] = ldw(c.ws, voff + 8192u); rk[2] = ldw(c.ws, koff + 16384u); rv[2] = ldw(c.ws, voff + 16384u);
    SBAR(); __builtin_amdgcn_s_barrier(); SBAR();
    rk[3] = ldw(c.ws, koff + 24576u); rv[3] = ldw(c.ws, voff + 24576u); rk[4] = ldw(c.ws, koff + 32768u); rv[4] = ldw(c.ws, voff + 32768u);
    SBAR();
    { const int t = tid >> 3, s = tid & 7; const u32 val = (s & 3) == 0 ? 0x3f803f80u : 0u;
      lds_st<u32x4>(L_V + (u32)t * VSTR + 1024u + (u32)s * 16u, (u32x4){val, val, val, val}); }
    const u32 kw = L_K + (u32)w * 1024u + (u32)lane * 16u, vw = L_V + (u32)w * VSTR + (u32)lane * 16u;
    lds_st<u32x4>(kw, rk[0]); lds_st<u32x4>(vw, rv[0]);
    lds_st<u32x4>(kw + 8192u, rk[1]); lds_st<u32x4>(vw + 8u * VSTR, rv[1]);
    CSTAMP(10);
    BAR();
    const int r = lane & 31, h = lane >> 5;
    const u32 kp0 = L_K + (u32)kh * 4096u + (u32)lane * 16u;
    const u32 onesoff = 1024u + ((r >= 20 && r < 28) ? 64u : 0u) + ((r == 16 || r == 20) ? 0u : 16u);
    const u32 vp00 = L_V + (u32)kh * (4u * VSTR) + (r < 16 ? (u32)(h * 256 + r * 16) : onesoff);
    const u32 vp10 = L_V + (u32)kh * (4u * VSTR) + (r < 16 ? (u32)(512 + h * 256 + r * 16) : onesoff);
    const bf16x8 qa = BC8(qar), qbf = BC8(qbr);
    const f32x16 cz = {};
    AState s;
    STAMP(L ? 18 : 5);
#define PRIO(n) __builtin_amdgcn_s_setprio(n)
    PRIO(3);
    CSTAMP(0);
    attn_begin(s, qa, cz, kp0);
    {
#define PUT(ch) [&]() { lds_st<u32x4>(kw + (u32)(ch) * 8192u, rk[ch]); lds_st<u32x4>(vw + (u32)(ch) * (8u * VSTR), rv[ch]); }
#define GET(ch) [&]() { rk[ch] = ldw(c.ws, koff + (u32)(ch) * 8192u); rv[ch] = ldw(c.ws, voff + (u32)(ch) * 8192u); }
#define GETPUT(g, ch) [&]() { GET(g)(); PUT(ch)(); }
#define FF(j_) [&]() { if (ff.on && ((j_) < 4 || !ff.gu)) pf[j_] = gld(ff.m, ff.vo + (u32)(j_) * ff.step); }
#define WO(j_) [&]() { po[j_] = gld(p.wo, wv + (u32)(j_) * 16384u); }
#define ST(ch, t, HOOK) attn_step<false>(s, qa, qbf, cz, cz, kp, vp0, vp1, ((t) == 3 ? 8192 : ((t) + 1) * 1024) + ((ch) & 1) * 8192, (((ch) & 1) * 8 + (t)) * VSTR, HOOK)
#define NEXT2() do { kp += 16384; vp0 += 16 * VSTR; vp1 += 16 * VSTR; asm volatile("" : "+v"(kp), "+v"(vp0), "+v"(vp1)); } while (0)
        u32 kp = kp0, vp0 = vp00, vp1 = vp10;
        ST(0, 0, GET(5)); ST(0, 1, GETPUT(6, 2)); ST(0, 2, GETPUT(7, 3)); ST(0, 3, PUT(4));
        CSTAMP(1);
        ST(1, 0, PUT(5)); ST(1, 1, PUT(6)); ST(1, 2, PUT(7));
        BAR();
        ST(1, 3, NoHook());
        const FfSrc ff = ff_src(c, p);
        NEXT2(); PRIO(2);
        CSTAMP(2);
        ST(2, 0, FF(0)); ST(2, 1, FF(1)); ST(2, 2, FF(2)); ST(2, 3, FF(3));
        CSTAMP(3);
        ST(3, 0, FF(4)); ST(3, 1, FF(5)); ST(3, 2, FF(6)); ST(3, 3, NoHook());
        const u32 wv = wo_lane_off(c);
        NEXT2(); PRIO(1);
        CSTAMP(4);
        ST(4, 0, WO(0)); ST(4, 1, WO(1)); ST(4, 2, WO(2)); ST(4, 3, WO(3));
        CSTAMP(5);
        ST(5, 0, WO(4)); ST(5, 1, WO(5)); ST(5, 2, WO(6)); ST(5, 3, WO(7));
        NEXT2(); PRIO(0);
        CSTAMP(6);
        for (int ch = 6; ch < 8; ++ch) {
            attn_chunk<false>(s, qa, qbf, cz, cz, kp, vp0, vp1);
            kp += 8192; vp0 += 8 * VSTR; vp1 += 8 * VSTR;
            asm volatile("" : "+v"(kp), "+v"(vp0), "+v"(vp1));
        }
#undef PUT
#undef GET
#undef GETPUT
#undef FF
#undef WO
#undef ST
#undef NEXT2
    }
    CSTAMP(8);
    STAMP(L ? 19 : 6);
    FRESH(c);
    float O[9];
    attn_combine(c, s, O);
    const u32 oo = OO + (u32)((head * 64 + qb * 8 + 2 * j + kh) * 64 + c.lane) * 16u;
    bool redo;
    {
        bool bad = !(O[8] >= 0x1p-60f && O[8] <= 0x1p60f);
#pragma unroll
        for (int q = 0; q < 8; ++q) bad = bad || !(__builtin_fabsf(O[q]) <= 0x1p126f);
        redo = __any(bad);
        if (redo) { if (c.lane == 0) lds_st<u32>(L_AX + 8 * 2304u, 1u); }
        else {
            const float rl = __builtin_amdgcn_rcpf(O[8]);
            u32x4 o;
#pragma unroll
            for (int q = 0; q < 4; ++q) o[q] = pk(O[2 * q] * rl, O[2 * q + 1] * rl);
            stw(c.ws, oo, not_poison(o));
        }
    }
    pre_store_wo(c, po);
    BAR();
    if (__builtin_amdgcn_readfirstlane((int)lds_ld<u32>(L_AX + 8 * 2304u)) != 0) {
        {
            const u32 ko = OK_ + (u32)((head * 64 + w) * 64 + c.lane) * 16u, vo = OV + (u32)((head * 64 + w) * 64 + c.lane) * 16u;
            for (int ch = 0; ch < 4; ++ch) {
                const u32x4 a = ldw(c.ws, ko + (u32)ch * 8192u), b_ = ldw(c.ws, vo + (u32)ch * 8192u);
                lds_st<u32x4>(L_K + (u32)(ch * 8 + w) * 1024u + (u32)c.lane * 16u, a);
                lds_st<u32x4>(L_V + (u32)(ch * 8 + w) * VSTR + (u32)c.lane * 16u, b_);
            }
            { const int t = (w * 64 + c.lane) >> 3, s_ = c.lane & 7; const u32 val = (s_ & 3) == 0 ? 0x3f803f80u : 0u;
              lds_st<u32x4>(L_V + (u32)t * VSTR + 1024u + (u32)s_ * 16u, (u32x4){val, val, val, val}); }
        }
        BAR();
        float ma = -INFINITY, mb = -INFINITY;
        for (int ch = 0; ch < 8; ++ch)
            for (int t = 0; t < 4; ++t) {
                const bf16x8 kf = lds_ld<bf16x8>(kp0 + (u32)(ch * 8192 + t * 1024));
                const f32x16 Sa = MFMA32(kf, qa, cz), Sb = MFMA32(kf, qbf, cz);
#pragma unroll
                for (int q = 0; q < 16; ++q) { ma = fmaxf(ma, Sa[q]); mb = fmaxf(mb, Sb[q]); }
            }
        ma = fmaxf(ma, __shfl_xor(ma, 32)); mb = fmaxf(mb, __shfl_xor(mb, 32));
        const u32 xb = L_AX + (u32)w * 2304u, xp = L_AX + (u32)(w ^ 4) * 2304u;
        lds_st<f32x2>(xb + (u32)c.lane * 8u, (f32x2){ma, mb});
        BAR();
        const f32x2 mo = lds_ld<f32x2>(xp + (u32)c.lane * 8u);
        ma = fmaxf(ma, mo[0]); mb = fmaxf(mb, mo[1]);
        BAR();
        f32x16 ca, cb;
#pragma unroll
        for (int q = 0; q < 16; ++q) { ca[q] = -ma; cb[q] = -mb; }
        attn_begin(s, qa, ca, kp0);
        u32 kp = kp0, vp0 = vp00, vp1 = vp10;
        for (int ch = 0; ch < 8; ++ch) {
            attn_chunk<true>(s, qa, qbf, ca, cb, kp, vp0, vp1);
            kp += 8192; vp0 += 8 * VSTR; vp1 += 8 * VSTR;
        }
        attn_combine(c, s, O);
        if (redo) {
            const float rl = 1.0f / O[8];
            u32x4 o;
#pragma unroll
            for (int q = 0; q < 4; ++q) o[q] = pk(O[2 * q] * rl, O[2 * q + 1] * rl);
            stw(c.ws, oo, not_poison(o));
        }
        {
            const FfSrc ff = ff_src(c, p); const u32 wv = wo_lane_off(c);
#pragma unroll
            for (int q = 0; q < 7; ++q) if (ff.on && (q < 4 || !ff.gu)) pf[q] = gld(ff.m, ff.vo + (u32)q * ff.step);
#pragma unroll
            for (int q = 0; q < 8; ++q) po[q] = gld(p.wo, wv + (u32)q * 16384u);
        }
        BAR();
        pre_store_wo(c, po);
        BAR();
    }
    STAMP(L ? 20 : 7);
    pre_store_ff(c, pf);
}

DI void phaseO(const Ctx& c0, const Params& p, u32x4 (&x2)[2], u32& hsync, const u32x4 (&pq)[12], const int L) {
    Ctx c = c0; FRESH(c);
    const int lane = c.lane, w = c.w;
    const int tb = 4 * c.xg + (c.gi >> 3), fb = c.gi & 7;
    const int r = lane & 31, h = lane >> 5;
    const u32 OO = L ? OFF_O1 : OFF_O0, GS = L ? OFF_GS1 : OFF_GS0, GC = L ? OFF_GC1 : OFF_GC0, GM = L ? OFF_GM1 : OFF_GM0, GX = L ? OFF_GX1 : OFF_GX0;
    constexpr u32 L_TS = L_SB, L_CAND = P_ST + 8;
    if (w >= 4) {
        const int hw = w - 4;
        STAMP(L ? 21 : 8);
        BAR();
        BAR();
        __builtin_amdgcn_s_setprio(3);
        FST(30 + 20 * L);
        if (hw == 0) {
            const u32 a = L_SB + (u32)h * 384u + (u32)r * 12u;
            const float t1 = lds_ld<float>(a) + lds_ld<float>(a + 768), t2 = lds_ld<float>(a + 4) + lds_ld<float>(a + 772), t3 = lds_ld<float>(a + 8) + lds_ld<float>(a + 776);
            stw(c.ws, GS + (u32)((tb * 8 + fb) * 1024 + lane * 16), (u32x4){__float_as_uint(t1), __float_as_uint(t2), __float_as_uint(t3), c.tag});
        }
        FST(31 + 20 * L);
        if (L == 0) {
            if (hw != 0) { pre_store_wq(c, pq); lds_signal_add(c, P_WQ); }
            BAR();
        }
        FST(32 + 20 * L);
        if (hw == 0) {
            u32x4 v[8]; u32 n = 0;
            for (;;) {
                bool ok = true;
#pragma unroll
                for (int k = 0; k < 8; ++k) v[k] = ldw(c.ws, GS + (u32)((tb * 8 + k) * 1024 + lane * 16));
                MEMFENCE();
#pragma unroll
                for (int k = 0; k < 8; ++k) ok = ok && v[k][3] == c.tag;
                if (__all(ok)) break;
                if (spin_fail(c, n)) break;
            }
            FST(33 + 20 * L);
            float t1 = 0, t2 = 0, t3 = 0;
#pragma unroll
            for (int k = 0; k < 8; ++k) { t1 += __uint_as_float(v[k][0]); t2 += __uint_as_float(v[k][1]); t3 += __uint_as_float(v[k][2]); }
            const float mu = t1 * (1.0f / 512.0f), var = fmaxf(t2 * (1.0f / 512.0f) - mu * mu, 0.f);
            const float rs = __builtin_amdgcn_rsqf(var + 1e-5f);
            const float rt = rbf(rs * (t3 - mu * lds_ld<float>(P_WSUM)));
            const u32 key = half_umax(score_key(__float_as_uint(rt) >> 16, (u32)r));
            const int bt = (int)key_idx(key);
            if (fb == 0) {
                const u32 k0 = (u32)__builtin_amdgcn_readlane((int)key, 0), k1 = (u32)__builtin_amdgcn_readlane((int)key, 32);
                const u32 kT = (lane & 1) ? k1 : k0;
                const u64 g = (cand_tag(c.tag) << 21) | (u64)(key_bf(kT) << 5) | (u64)key_idx(kT);
                if (lane < 16) __builtin_amdgcn_raw_buffer_store_b64((u32x2){(u32)g, (u32)(g >> 32)}, c.ws, GC + (u32)(lane >> 1) * 512u + (u32)(tb * 2 + (lane & 1)) * 8u, 0, 16);
                if (r == bt) stw(c.ws, GM + (u32)(tb * 2 + h) * 16u, mk_unit(__float_as_uint(mu), __float_as_uint(rs), c.tag));
                if (L == 0) {
                    const float g1 = 1.0f + rs, A = g1 * __builtin_amdgcn_rsqf(var * g1 * g1 + 1e-5f);
                    stw(c.ws, OFF_TS1 + (u32)(tb * 64 + lane) * 16u, mk_unit(__float_as_uint(A), __float_as_uint(A * mu), c.tag));
                }
            }
            FST(34 + 20 * L);
            lds_st<f32x2>(L_TS + (u32)lane * 8u, (f32x2){mu, rs});
            if (r == 0) lds_st<u32>(L_CAND + (u32)h * 4u, (u32)bt);
            lds_signal_add(c, P_ST + L * 4);
            {
                const int ct = (lane >> 4) & 1, u = lane & 15;
                const int cb = __builtin_amdgcn_readlane(bt, 0), cb1 = __builtin_amdgcn_readlane(bt, 32);
                const int row = ct ? cb1 : cb;
                const u32x2 d = lds_ld<u32x2>(L_X1T + (u32)(ct + 2 * (u >> 3)) * 2048u + (u32)((u >> 2) & 1) * 1024u + (u32)((u & 1) * 32 + row) * 16u + (u32)((u >> 1) & 1) * 8u);
                if (lane < 32) stw(c.ws, GX + (u32)(tb * 2 + ct) * 2048u + (u32)(fb * 16 + u) * 16u, mk_unit(d[0], d[1], c.tag));
            }
            if (L == 0) { pre_store_wq(c, pq); lds_signal_add(c, P_WQ); }
        }
        STAMP(L ? 23 : 12);
        ffn_chain(c, p, hsync, L);
        if (L == 0) STAMP(15);
        return;
    }
    const int tt = w & 1, kh = w >> 1;
    const int tile = tb * 2 + tt, tok = tile * 32 + r;
    const int f0 = fb * 64 + kh * 32;
    u32x2 res[4], wr[4];
#pragma unroll
    for (int g = 0; g < 4; ++g) {
        if (L == 0) res[g] = *(const u32x2*)(p.x0 + (size_t)tok * 512 + f0 + 8 * g + 4 * h);
        wr[g] = *(const u32x2*)(p.wr + f0 + 8 * g + 4 * h);
    }
    if (L == 1) {
        const int sel0 = __builtin_amdgcn_readfirstlane((int)lds_ld<u32>(P_SEL)) & 2047;
        if ((sel0 >> 5) == tile) {
            const u32 ub = OFF_GY + (u32)(f0 / 4 + h) * 16u;
            u32 off[4] = {ub, ub + 32u, ub + 64u, ub + 96u}; u32x4 v[4];
            wait_units<4>(c, off, v);
            if (r == (sel0 & 31)) { x2[0] = (u32x4){v[0][0], v[0][2], v[1][0], v[1][2]}; x2[1] = (u32x4){v[2][0], v[2][2], v[3][0], v[3][2]}; }
        }
#pragma unroll
        for (int g = 0; g < 4; ++g) res[g] = (u32x2){x2[g >> 1][(g & 1) * 2], x2[g >> 1][(g & 1) * 2 + 1]};
    }
    STAMP(L ? 21 : 8);
    u32x4 bf[16];
    const u32 oo = OO + (u32)(((kh * 16) * 64 + tile) * 64 + lane) * 16u;
    {
        u32 n = 0;
        for (;;) {
#pragma unroll
            for (int s = 0; s < 16; ++s) bf[s] = ldw(c.ws, oo + (u32)s * 65536u);
            MEMFENCE();
            u32 m = 0;
#pragma unroll
            for (int s = 0; s < 16; ++s) m = max(m, max(max(bf[s][0], bf[s][1]), max(bf[s][2], bf[s][3])));
            if (!__any(m == POISON)) break;
            if (spin_fail(c, n)) break;
        }
    }
    const int i16 = lane & 15, blk = (lane >> 4) & 1;
    const u32 trA = L_WO + (u32)((kh * 256 + 4 * h + (i16 >> 2)) * 64 + blk * 32 + (i16 & 3) * 8);
    f32x16 a0 = {}, a1 = {};
    {
        u32 ta = trA, tb_ = trA + WO_FT;
        asm volatile("" : "+v"(ta), "+v"(tb_));
        bf16x8 f0_[2], f1_[2];
        f0_[0] = frag_tr(ta, ta + 512); f0_[1] = frag_tr(tb_, tb_ + 512);
#pragma unroll
        for (int s = 0; s < 16; s += 2) {
            f1_[0] = frag_tr(ta + (u32)(s + 1) * 1024u, ta + (u32)(s + 1) * 1024u + 512); SBAR();
            a0 = MFMA32(f0_[0], BC8(bf[s]), a0); SBAR();
            f1_[1] = frag_tr(tb_ + (u32)(s + 1) * 1024u, tb_ + (u32)(s + 1) * 1024u + 512); SBAR();
            a1 = MFMA32(f0_[1], BC8(bf[s]), a1); SBAR();
            if (s + 2 < 16) f0_[0] = frag_tr(ta + (u32)(s + 2) * 1024u, ta + (u32)(s + 2) * 1024u + 512);
            SBAR(); a0 = MFMA32(f1_[0], BC8(bf[s + 1]), a0); SBAR();
            if (s + 2 < 16) f0_[1] = frag_tr(tb_ + (u32)(s + 2) * 1024u, tb_ + (u32)(s + 2) * 1024u + 512);
            SBAR(); a1 = MFMA32(f1_[1], BC8(bf[s + 1]), a1); SBAR();
        }
    }
    {
        const f32x16& snd = kh ? a0 : a1;
#pragma unroll
        for (int g = 0; g < 4; ++g) lds_st<f32x4>(L_XB + (u32)w * 4096u + (u32)g * 1024u + (u32)lane * 16u, (f32x4){snd[4 * g], snd[4 * g + 1], snd[4 * g + 2], snd[4 * g + 3]});
    }
    BAR();
    STAMP(L ? 22 : 9);
    u32x4 xp[2];
    {
        const f32x16& keep = kh ? a1 : a0;
#pragma unroll
        for (int g = 0; g < 4; ++g) {
            const f32x4 o = lds_ld<f32x4>(L_XB + (u32)(w ^ 2) * 4096u + (u32)g * 1024u + (u32)lane * 16u);
            const u32 t0 = pk(keep[4 * g] + o[0], keep[4 * g + 1] + o[1]), t1 = pk(keep[4 * g + 2] + o[2], keep[4 * g + 3] + o[3]);
            xp[g >> 1][(g & 1) * 2] = pk(bflo(res[g][0]) + bflo(t0), bfhi(res[g][0]) + bfhi(t0));
            xp[g >> 1][(g & 1) * 2 + 1] = pk(bflo(res[g][1]) + bflo(t1), bfhi(res[g][1]) + bfhi(t1));
        }
    }
    if (L == 0) {
#pragma unroll
        for (int s = 0; s < 2; ++s) stw(c.ws, OFF_X1 + (u32)((tile * 32 + f0 / 16 + s) * 64 + lane) * 16u, xp[s]);
    }
#pragma unroll
    for (int s = 0; s < 2; ++s) lds_st<u32x4>(L_X1T + (u32)w * 2048u + (u32)s * 1024u + (u32)lane * 16u, xp[s]);
    float x1[16];
    {
        float s1 = 0, s2 = 0, s3 = 0;
#pragma unroll
        for (int i = 0; i < 16; ++i) {
            const u32 wd = xp[i >> 3][(i & 7) >> 1];
            x1[i] = (i & 1) ? bfhi(wd) : bflo(wd);
            const u32 ww = wr[i >> 2][(i & 3) >> 1];
            const float wv = (i & 1) ? bfhi(ww) : bflo(ww);
            s1 += x1[i]; s2 += x1[i] * x1[i]; s3 += x1[i] * wv;
        }
        s1 = swap32_add(s1); s2 = swap32_add(s2); s3 = swap32_add(s3);
        if (h == 0) { const u32 a = L_SB + (u32)w * 384u + (u32)r * 12u; lds_st<float>(a, s1); lds_st<float>(a + 4, s2); lds_st<float>(a + 8, s3); }
    }
    BAR();
    if (L == 0) {
        DRAIN(); BAR();
        STAMP(10);
        if (w == 0 && lane == 0) set_flag(c, OFF_FO + (u32)(tb * 8 + fb) * 128u);
        pre_store_wq(c, pq); lds_signal_add(c, P_WQ);
    }
    lds_wait_ge(P_ST + L * 4, 1u);
    STAMP(L ? 23 : 11);
    const f32x2 st = lds_ld<f32x2>(L_TS + (u32)(tt * 32 + r) * 8u);
    const int bt = (int)lds_ld<u32>(L_CAND + (u32)tt * 4u) & 31;
#pragma unroll
    for (int s = 0; s < 2; ++s)
#pragma unroll
        for (int j = 0; j < 4; ++j) {
            const float a = x1[8 * s + 2 * j], b_ = x1[8 * s + 2 * j + 1];
            const u32 hh = pk((a - st[0]) * st[1], (b_ - st[0]) * st[1]);
            x2[s][j] = pk(a + bflo(hh), b_ + bfhi(hh));
        }
    if (L == 1) {
        const rsrc_t outr = __builtin_amdgcn_make_buffer_rsrc(p.out, 0, 2097152, 0x00020000);
        const u32 tl = L_XB + (u32)w * 4096u;
#pragma unroll
        for (int g = 0; g < 4; ++g) lds_st<u32x2>(tl + (u32)(r * 80 + (8 * g + 4 * h) * 2), (u32x2){x2[g >> 1][(g & 1) * 2], x2[g >> 1][(g & 1) * 2 + 1]});
        asm volatile("s_waitcnt lgkmcnt(0)" ::: "memory");
        u32x4 rowv[2];
#pragma unroll
        for (int q = 0; q < 2; ++q) {
            const int row = q * 16 + (lane >> 2);
            rowv[q] = lds_ld<u32x4>(tl + (u32)(row * 80 + (lane & 3) * 16));
            if (row != bt) __builtin_amdgcn_raw_buffer_store_b128(rowv[q], outr, (u32)(((tile * 32 + row) * 512 + f0 + (lane & 3) * 8) * 2), 0, OUTAUX);
        }
        wait_sel(1);
        const int sel1 = __builtin_amdgcn_readfirstlane((int)lds_ld<u32>(P_SEL + 32)) & 2047;
        if (tile * 32 + bt != sel1) {
#pragma unroll
            for (int q = 0; q < 2; ++q) {
                const int row = q * 16 + (lane >> 2);
                if (row == bt) __builtin_amdgcn_raw_buffer_store_b128(rowv[q], outr, (u32)(((tile * 32 + row) * 512 + f0 + (lane & 3) * 8) * 2), 0, OUTAUX);
            }
        }
        }
}

#if defined(__HIP_DEVICE_COMPILE__)
__attribute__((target("no-packed-fp32-ops")))
#endif
__global__ __launch_bounds__(512, 2) void fused_layers_kernel(Params p) {
    extern __shared__ __attribute__((aligned(16))) char lds[];
    Ctx c;
    const int tid = threadIdx.x;
    c.lane = tid & 63; c.w = __builtin_amdgcn_readfirstlane(tid >> 6);
    c.xg = blockIdx.x & 7; c.gi = blockIdx.x >> 3;
    c.ws = __builtin_amdgcn_make_buffer_rsrc(p.ws, 0, WS_BYTES, 0x00020000);
    c.tag = 0u;
    if (c.w < 4) {
        const u32x4 pois = {POISON, POISON, POISON, POISON};
#pragma unroll
        for (int k = 0; k < 2; ++k) {
            const u32 o = (u32)(((4 * c.xg + (c.gi >> 3)) * 64 + (c.gi & 7) * 8 + 2 * c.w + k) * 64 + c.lane) * 16u;
            stw(c.ws, OFF_O0 + o, pois); stw(c.ws, OFF_O1 + o, pois);
        }
        if (c.w == 0) {
            const u32 o = (u32)(((4 * c.xg + (c.gi >> 3)) * 8 + (c.gi & 7)) * 1024 + c.lane * 16);
            stw(c.ws, OFF_GS0 + o, (u32x4){0u, 0u, 0u, 0u}); stw(c.ws, OFF_GS1 + o, (u32x4){0u, 0u, 0u, 0u});
        }
        u64 pc; u32 len;
        asm volatile("s_getpc_b64 %0\nfl_code_here:\n\ts_mov_b32 %1, fl_code_end-fl_code_here" : "=s"(pc), "=s"(len));
        if (c.w == 1) {
            const u32 o0 = min((u32)c.gi * 3072u + (u32)c.lane * 16u, len - 16u) & ~15u, o1 = min(o0 + 1024u, len - 16u) & ~15u, o2 = min(o0 + 2048u, len - 16u) & ~15u;
            const u64 base = pc & ~15ull;
            glds((const void*)base, o0, 147456u); glds((const void*)base, o1, 148480u); glds((const void*)base, o2, 149504u);
        }
        if (c.w == 2) {
            const int l = c.lane, k = (l - 22) % 14;
            const char* a = p.ws + (size_t)(l < 10 ? l : 0) * 0x200000;
            a = l >= 10 && l < 14 ? (const char*)p.wo + (l - 10) * 131072 : l == 14 ? (const char*)p.wr : a;
            a = l >= 15 && l < 22 ? (const char*)p.out + (l - 15) * 299008 : a;
            a = l >= 22 ? (const char*)(l < 36 ? p.wg : l < 50 ? p.wu : p.wd) + k * 131072 : a;
            u32 sv;
            asm volatile("s_mov_b32 %0, m0\n\ts_mov_b32 m0, %2\n\ts_nop 0\n\tglobal_load_lds_dword %1, off\n\ts_mov_b32 m0, %0" : "=&s"(sv) : "v"(a), "s"(150528u) : "memory");
        }
        if (c.w == 3 && c.lane < 24 && c.lane != 3) lds_st<u32>(P_HCNT + (u32)c.lane * 4u, 0u);
    }
    asm volatile("" :: "v"(lds));
    STAMP(0);
    FST(0);
    u32 hsync = 0;
    u32x4 x2[2];
    asm volatile("" : "=v"(x2[0]), "=v"(x2[1]));
    u32x4 pq[12];
#pragma nounroll
    for (int Li = 0; Li < 2; ++Li) {
        int Lv = Li; asm volatile("" : "+v"(Lv));
        const int L = __builtin_amdgcn_readfirstlane(Lv);
        if (L == 0) phaseQ<0>(c, p, hsync, pq);
        else {
            phaseQ<1>(c, p, hsync, pq);
#pragma unroll
            for (int j = 0; j < 12; ++j) asm volatile("" : "=v"(pq[j]));
        }
        if (STOP_AFTER < 3 * L + 1) break;
        phaseA(c, p, L);
        if (STOP_AFTER < 3 * L + 2) break;
        phaseO(c, p, x2, hsync, pq, L);
        if (STOP_AFTER < 3 * L + 3) break;
    }
    STAMP(24);
    if (blockIdx.x == 14 * 8 && c.w == 4 && c.lane == 0) __builtin_amdgcn_raw_buffer_store_b32(c.tag, c.ws, OFF_EPOCH, 0, 16);
    asm volatile("fl_code_end:" ::: "memory");
}

extern "C" void kernel_launch(void* const* d_in, const int* in_sizes, int n_in, void* d_out, int out_size, void* d_ws, size_t ws_size, hipStream_t stream) {
    Params p;
    p.x0 = (const uint16_t*)d_in[0]; p.wqkv = (const uint16_t*)d_in[1]; p.wo = (const uint16_t*)d_in[2];
    p.wr = (const uint16_t*)d_in[4]; p.wg = (const uint16_t*)d_in[5]; p.wu = (const uint16_t*)d_in[6]; p.wd = (const uint16_t*)d_in[7];
    p.out = (uint16_t*)d_out; p.ws = (char*)d_ws;
    (void)hipFuncSetAttribute((const void*)fused_layers_kernel, hipFuncAttributeMaxDynamicSharedMemorySize, LDS_BYTES);
    fused_layers_kernel<<<256, 512, LDS_BYTES, stream>>>(p);
}
```

```cpp
#include <hip/hip_runtime.h>
#include <stdint.h>

typedef unsigned u32;
typedef unsigned long long u64;
typedef __attribute__((ext_vector_type(2))) u32 u32x2;
typedef __attribute__((ext_vector_type(4))) u32 u32x4;
typedef __attribute__((ext_vector_type(2))) float f32x2;
typedef __attribute__((ext_vector_type(4))) float f32x4;
typedef __attribute__((ext_vector_type(16))) float f32x16;
typedef __attribute__((ext_vector_type(8))) __bf16 bf16x8;
typedef __attribute__((ext_vector_type(2))) __bf16 bf16x2;
typedef __attribute__((ext_vector_type(4))) short s16x4;
typedef __attribute__((ext_vector_type(8))) short s16x8;
#define LAS __attribute__((address_space(3)))
#define DI __device__ __forceinline__
#define MFMA32(a, b, c) __builtin_amdgcn_mfma_f32_32x32x16_bf16((a), (b), (c), 0, 0, 0)
#define MFMA16(a, b, c) __builtin_amdgcn_mfma_f32_16x16x32_bf16((a), (b), (c), 0, 0, 0)
#define SBAR() __builtin_amdgcn_sched_barrier(0)
#define BAR() asm volatile("s_waitcnt lgkmcnt(0)\n\ts_barrier" ::: "memory")
#define DRAIN() asm volatile("s_waitcnt vmcnt(0)" ::: "memory")
#define MEMFENCE() asm volatile("" ::: "memory")

#define STOP_AFTER 99

constexpr u32 OFF_EPOCH = 0, OFF_ABORT = 256;
constexpr u32 OFF_FQ0 = 0x10000, OFF_FP = 0x18000, OFF_FO = 0x20000, OFF_FQ1 = 0x28000;
constexpr u32 OFF_TS0 = 0x40000, OFF_TS1 = 0x48000;
constexpr u32 OFF_GC0 = 0x50000, OFF_GC1 = 0x51000;
constexpr u32 OFF_GM0 = 0x52000, OFF_GM1 = 0x52400;
constexpr u32 OFF_GA0 = 0x58000, OFF_GA1 = 0x5A000;
constexpr u32 OFF_GY = 0x5C000;
constexpr u32 OFF_GS0 = 0x80000, OFF_GS1 = 0xC0000;
constexpr u32 OFF_GX0 = 0x180000, OFF_GX1 = 0x1A0000;
constexpr u32 OFF_Q0 = 0x200000, OFF_K0 = 0x400000, OFF_V0 = 0x600000, OFF_O0 = 0x800000, OFF_X1 = 0xA00000;
constexpr u32 OFF_Q1 = 0xC00000, OFF_K1 = 0xE00000, OFF_V1 = 0x1000000, OFF_O1 = 0x1200000;
constexpr u32 WS_BYTES = 0x1400000;

constexpr u32 LDS_BYTES = 163840;
constexpr u32 L_W = 0;
constexpr u32 L_XR = 98304;
constexpr u32 L_AX = 140288;
constexpr u32 L_TOK = 161792 + 512;
constexpr u32 L_K = 0, L_V = 65536, VSTR = 1152;
constexpr u32 L_WO = 0, WO_FT = 65536;
constexpr u32 L_XB = 32768;
constexpr u32 L_SB = 161792;
constexpr u32 L_P = 163328;
constexpr u32 P_CS = L_P, P_HCNT = L_P + 384, P_PATCH = L_P + 388, P_WQ = L_P + 392, P_WSUM = L_P + 396, P_SEL = L_P + 400, P_ST = L_P + 464, P_PD = L_P + 480, P_FQ = L_P + 484;
constexpr u32 L_X1T = 49152;
constexpr u32 FW0 = 98304, FS0 = 155648;
constexpr u32 FW1 = FW0, FS1 = FS0;
constexpr u32 SPIN_CAP = 40000;

struct Params {
    const uint16_t* x0; const uint16_t* wqkv; const uint16_t* wo; const uint16_t* wr;
    const uint16_t* wg; const uint16_t* wu; const uint16_t* wd; uint16_t* out; char* ws;
};

typedef __amdgpu_buffer_rsrc_t rsrc_t;

DI u32 pk(float a, float b) { f32x2 v = {a, b}; return __builtin_bit_cast(u32, __builtin_convertvector(v, bf16x2)); }
DI float bflo(u32 v) { return __uint_as_float(v << 16); }
DI float bfhi(u32 v) { return __uint_as_float(v & 0xffff0000u); }
DI float rbf(float a) { return bflo(pk(a, 0.f)); }
DI u32x4 ldw(rsrc_t r, u32 off) { return __builtin_amdgcn_raw_buffer_load_b128(r, off, 0, 16); }
DI void stw(rsrc_t r, u32 off, u32x4 v) { __builtin_amdgcn_raw_buffer_store_b128(v, r, off, 0, 16); }
template <class T> DI T lds_ld(u32 a) { return *(const LAS T*)(uintptr_t)a; }
template <class T> DI void lds_st(u32 a, T v) { *(LAS T*)(uintptr_t)a = v; }
DI s16x4 lds_tr(u32 a) { return __builtin_amdgcn_ds_read_tr16_b64_v4i16((LAS s16x4*)(uintptr_t)a); }
DI bf16x8 frag_tr(u32 a0, u32 a1) { s16x4 lo = lds_tr(a0), hi = lds_tr(a1); return __builtin_bit_cast(bf16x8, __builtin_shufflevector(lo, hi, 0, 1, 2, 3, 4, 5, 6, 7)); }
DI float swap32_add(float v) { auto r = __builtin_amdgcn_permlane32_swap(__float_as_uint(v), __float_as_uint(v), false, false); return __uint_as_float(r[0]) + __uint_as_float(r[1]); }
template <int CTRL> DI u32 dpp(u32 v) { return (u32)__builtin_amdgcn_update_dpp((int)v, (int)v, CTRL, 0xf, 0xf, true); }
DI float wave_sum(float v) {
    v += __uint_as_float(dpp<0xB1>(__float_as_uint(v)));
    v += __uint_as_float(dpp<0x4E>(__float_as_uint(v)));
    v += __uint_as_float(dpp<0x141>(__float_as_uint(v)));
    v += __uint_as_float(dpp<0x140>(__float_as_uint(v)));
    { auto r = __builtin_amdgcn_permlane16_swap(__float_as_uint(v), __float_as_uint(v), false, false); v = __uint_as_float(r[0]) + __uint_as_float(r[1]); }
    return swap32_add(v);
}
DI u32 half_umax(u32 v) {
    v = max(v, dpp<0xB1>(v)); v = max(v, dpp<0x4E>(v)); v = max(v, dpp<0x141>(v)); v = max(v, dpp<0x140>(v));
    auto r = __builtin_amdgcn_permlane16_swap(v, v, false, false);
    return max(r[0], r[1]);
}
DI u32 wave_umax(u32 v) { v = half_umax(v); auto r = __builtin_amdgcn_permlane32_swap(v, v, false, false); return max(r[0], r[1]); }
DI u32 score_key(u32 bf, u32 idx) { return ((bf ^ ((bf & 0x8000u) ? 0xFFFFu : 0x8000u)) << 16) | (0xFFFFu - idx); }
DI u32 key_bf(u32 k) { const u32 b = k >> 16; return b ^ ((b & 0x8000u) ? 0x8000u : 0xFFFFu); }
DI u32 key_idx(u32 k) { return 0xFFFFu - (k & 0xFFFFu); }
DI void glds(const void* sbase, u32 voff, u32 ldsdst) {
    u32 sv;
    asm volatile("s_mov_b32 %0, m0\n\ts_mov_b32 m0, %3\n\ts_nop 0\n\tglobal_load_lds_dwordx4 %1, %2\n\ts_mov_b32 m0, %0"
                 : "=&s"(sv) : "v"(voff), "s"(sbase), "s"(ldsdst) : "memory");
}
struct Ctx {
    rsrc_t ws; u32 tag; int lane, w, xg, gi;
};
#define FRESH(c) asm volatile("" : "+v"((c).lane), "+s"((c).w), "+s"((c).xg), "+s"((c).gi))
DI u32 epoch_tag(u32 v) { const u32 t = (u32)__builtin_amdgcn_readfirstlane((int)v) + 1u; return t ? t : 1u; }
DI bool spin_fail(const Ctx& c, u32& n) {
    __builtin_amdgcn_s_sleep(1);
    ++n;
    if (n > SPIN_CAP) { if (c.lane == 0) __builtin_amdgcn_raw_buffer_store_b32(c.tag, c.ws, OFF_ABORT, 0, 16); return true; }
    if ((n & 127u) == 0u) { u32 a = __builtin_amdgcn_raw_buffer_load_b32(c.ws, OFF_ABORT, 0, 16); if (__builtin_amdgcn_readfirstlane(a) == c.tag) return true; }
    return false;
}
DI void wait_flags(const Ctx& c, u32 off) {
    u32 n = 0;
    for (;;) {
        u32x2 v = __builtin_amdgcn_raw_buffer_load_b64(c.ws, off, 0, 16);
        MEMFENCE();
        if (__all(v[0] == c.tag && v[1] == ~c.tag)) break;
        if (spin_fail(c, n)) break;
    }
    MEMFENCE();
}
DI void set_flag(const Ctx& c, u32 off) { u32x2 v = {c.tag, ~c.tag}; __builtin_amdgcn_raw_buffer_store_b64(v, c.ws, off, 0, 16); }
constexpr u32 POISON = 0xFFFFFFFFu;
DI bool poisoned(u32x4 v) { return max(max(v[0], v[1]), max(v[2], v[3])) == POISON; }
DI u32x4 not_poison(u32x4 v) { return (u32x4){min(v[0], POISON - 1u), min(v[1], POISON - 1u), min(v[2], POISON - 1u), min(v[3], POISON - 1u)}; }
DI void wait_piece(const Ctx& c, u32 off, u32x4& v) {
    u32 n = 0;
    while (__any(poisoned(v))) { if (spin_fail(c, n)) break; v = ldw(c.ws, off); MEMFENCE(); }
}
DI bool unit_ok(u32x4 v, u32 tag) { return v[1] == tag && v[3] == tag; }
DI u32x4 mk_unit(u32 a, u32 b, u32 tag) { return (u32x4){a, tag, b, tag}; }
template <int N> DI void wait_units(const Ctx& c, const u32 (&off)[N], u32x4 (&v)[N]) {
    u32 n = 0;
    for (;;) {
        bool ok = true;
#pragma unroll
        for (int k = 0; k < N; ++k) v[k] = ldw(c.ws, off[k]);
        MEMFENCE();
#pragma unroll
        for (int k = 0; k < N; ++k) ok = ok && unit_ok(v[k], c.tag);
        if (__all(ok)) break;
        if (spin_fail(c, n)) break;
    }
    MEMFENCE();
}
DI u32 lds_poll_ld(u32 a) { return __hip_atomic_load((LAS u32*)(uintptr_t)a, __ATOMIC_RELAXED, __HIP_MEMORY_SCOPE_WORKGROUP); }
DI void helper_sync(const Ctx& c, u32& target) {
    target += 4;
    asm volatile("s_waitcnt lgkmcnt(0)" ::: "memory");
    if (c.lane == 0) __hip_atomic_fetch_add((LAS u32*)(uintptr_t)P_HCNT, 1u, __ATOMIC_RELAXED, __HIP_MEMORY_SCOPE_WORKGROUP);
    u32 n = 0;
    while (lds_poll_ld(P_HCNT) < target) { if (++n > (1u << 24)) break; }
    MEMFENCE();
}
DI void lds_wait_ge(u32 a, u32 v) {
    u32 n = 0;
    while (lds_poll_ld(a) < v) { __builtin_amdgcn_s_sleep(1); if (++n > (1u << 22)) break; }
    MEMFENCE();
}
DI void lds_signal_add(const Ctx& c, u32 a) {
    asm volatile("s_waitcnt lgkmcnt(0)" ::: "memory");
    if (c.lane == 0) __hip_atomic_fetch_add((LAS u32*)(uintptr_t)a, 1u, __ATOMIC_RELAXED, __HIP_MEMORY_SCOPE_WORKGROUP);
}
DI void wait_sel(int L) {
    u32 n = 0;
    while (lds_poll_ld(P_SEL + L * 32 + 16) == 0u) { __builtin_amdgcn_s_sleep(1); if (++n > (1u << 22)) break; }
    MEMFENCE();
}

#define STAMP(i) do { } while (0)
#define CSTAMP(i) do { } while (0)
#define FST(i) do { } while (0)

DI u64 cand_tag(u32 tag) { return ((u64)tag << 11) | (u64)((tag * 0x9E3779B1u) >> 21); }
DI void ffn_chain(const Ctx& c, const Params& p, u32& hsync, const int L) {
    const int lane = c.lane, hw = c.w - 4;
    constexpr u32 FW = FW0, FS = FS0;
    const u32 GC = L ? OFF_GC1 : OFF_GC0, GM = L ? OFF_GM1 : OFF_GM0, GX = L ? OFF_GX1 : OFF_GX0, GA = L ? OFF_GA1 : OFF_GA0;
    FST(20 + 20 * L);
    __builtin_amdgcn_s_setprio(3);
    const bool is_gu = c.gi < 14, is_dn = c.gi >= 14 && c.gi < 18;
    const int g16 = lane >> 4, i16 = lane & 15;
    bf16x8 wf[14];
    {
        const u32 tro = FW + (u32)((8 * g16 + (i16 >> 2)) * 32 + (i16 & 3) * 8) + (u32)hw * (is_gu ? 4096u : 14336u);
#pragma unroll
        for (int s = 0; s < 14; ++s) {
            const u32 bo = tro + (is_gu ? (u32)(s & 3) * 1024u + (u32)((s >> 2) & 1) * 16384u : (u32)s * 1024u);
            if (is_gu || is_dn) wf[s] = frag_tr(bo, bo + 128);
        }
    }
    if (hw == 0) {
        const u64 want = cand_tag(c.tag);
        u64 v; u32 n = 0;
        for (;;) {
            const u32x2 t = __builtin_amdgcn_raw_buffer_load_b64(c.ws, GC + c.xg * 512u + lane * 8u, 0, 16);
            MEMFENCE();
            v = ((u64)t[1] << 32) | t[0];
            if (__all((v >> 21) == want)) break;
            if (spin_fail(c, n)) break;
        }
        FST(21 + 20 * L);
        const u32 key = wave_umax(score_key(((u32)v >> 5) & 0xFFFFu, (u32)lane * 32u + ((u32)v & 31u)));
        const u32 tok = key_idx(key); const float r = __uint_as_float(key_bf(key) << 16);
        if (lane == 0) {
            lds_st<u32x2>(P_SEL + L * 32, (u32x2){(u32)tok, __float_as_uint(r)});
            asm volatile("s_waitcnt lgkmcnt(0)" ::: "memory");
            __hip_atomic_store((LAS u32*)(uintptr_t)(P_SEL + L * 32 + 16), 1u, __ATOMIC_RELAXED, __HIP_MEMORY_SCOPE_WORKGROUP);
        }
    }
    FST(22 + 20 * L);
    STAMP(L ? 25 : 13);
    if (!is_gu && !is_dn) return;
    wait_sel(L);
    const u32x2 si = lds_ld<u32x2>(P_SEL + L * 32);
    const int sel = __builtin_amdgcn_readfirstlane((int)si[0]) & 2047;
    const float rsel = __uint_as_float(si[1]);
    const u32 gxrow = GX + (u32)(sel >> 5) * 2048u, gm = GM + (u32)(sel >> 5) * 16u;
    if (is_gu) {
        const int gidx = c.xg * 14 + c.gi;
        u32 off[2] = {gxrow + (u32)(hw * 32 + (lane & 31)) * 16u, gm};
        u32x4 v[2];
        FST(29 + 20 * L);
        wait_units<2>(c, off, v);
        FST(23 + 20 * L);
        const float mu = __uint_as_float(v[1][0]), rs = __uint_as_float(v[1][2]);
        if (lane < 32) {
            u32x2 h;
            h[0] = pk((bflo(v[0][0]) - mu) * rs, (bfhi(v[0][0]) - mu) * rs);
            h[1] = pk((bflo(v[0][2]) - mu) * rs, (bfhi(v[0][2]) - mu) * rs);
            lds_st<u32x2>(FS + (u32)(hw * 32 + lane) * 8u, h);
        }
        STAMP(L ? 26 : 14);
        FST(24 + 20 * L);
        f32x4 ag = {}, au = {};
#pragma unroll
        for (int s = 0; s < 4; ++s) {
            const bf16x8 a = lds_ld<bf16x8>(FS + (u32)((hw * 4 + s) * 64 + g16 * 16));
            ag = MFMA16(a, wf[s], ag);
            au = MFMA16(a, wf[4 + s], au);
        }
        if (lane < 16) { lds_st<float>(FS + 4608 + (u32)(hw * 32 + lane) * 4u, ag[0]); lds_st<float>(FS + 4608 + (u32)(hw * 32 + 16 + lane) * 4u, au[0]); }
        FST(25 + 20 * L);
        helper_sync(c, hsync);
        FST(26 + 20 * L);
        if (hw == 0) {
            float g = 0.f, u = 0.f;
#pragma unroll
            for (int k = 0; k < 4; ++k) { g += lds_ld<float>(FS + 4608 + (u32)(k * 32 + i16) * 4u); u += lds_ld<float>(FS + 4608 + (u32)(k * 32 + 16 + i16) * 4u); }
            g = rbf(g); u = rbf(u);
            const float sg = rbf(g * __builtin_amdgcn_rcpf(1.0f + __expf(-g)));
            const float a = rbf(sg * u);
            const float a0 = __shfl(a, (lane & 3) * 4), a1 = __shfl(a, (lane & 3) * 4 + 1), a2 = __shfl(a, (lane & 3) * 4 + 2), a3 = __shfl(a, (lane & 3) * 4 + 3);
            if (lane < 4) stw(c.ws, GA + (u32)(gidx * 4 + lane) * 16u, mk_unit(pk(a0, a1), pk(a2, a3), c.tag));
            FST(27 + 20 * L);
        }
    } else {
        const int didx = c.xg * 4 + (c.gi - 14);
        float mu, rs;
        {
            const int u0 = hw * 112 + lane, u1 = hw * 112 + 64 + (lane < 48 ? lane : 47);
            u32 off[4] = {gxrow + (u32)(didx * 4 + (lane & 3)) * 16u, gm, GA + (u32)u0 * 16u, GA + (u32)u1 * 16u};
            u32x4 v[4];
            wait_units<4>(c, off, v);
            FST(23 + 20 * L);
            mu = __uint_as_float(v[1][0]); rs = __uint_as_float(v[1][2]);
            lds_st<u32x2>(FS + 1024 + (u32)u0 * 8u, (u32x2){v[2][0], v[2][2]});
            if (lane < 48) lds_st<u32x2>(FS + 1024 + (u32)u1 * 8u, (u32x2){v[3][0], v[3][2]});
            if (hw == 0 && lane < 4) lds_st<u32x2>(FS + (u32)lane * 8u, (u32x2){v[0][0], v[0][2]});
        }
        STAMP(L ? 26 : 14);
        FST(24 + 20 * L);
        f32x4 ay = {}, az = {};
        bf16x8 af[14];
#pragma unroll
        for (int s = 0; s < 14; ++s) af[s] = lds_ld<bf16x8>(FS + 1024 + (u32)((hw * 14 + s) * 64 + g16 * 16));
#pragma unroll
        for (int s = 0; s < 14; s += 2) { ay = MFMA16(af[s], wf[s], ay); az = MFMA16(af[s + 1], wf[s + 1], az); }
        if (lane < 16) lds_st<float>(FS + 4608 + (u32)(hw * 16 + lane) * 4u, ay[0] + az[0]);
        FST(25 + 20 * L);
        helper_sync(c, hsync);
        FST(26 + 20 * L);
        if (hw == 0) {
            float y = 0.f;
#pragma unroll
            for (int k = 0; k < 4; ++k) y += lds_ld<float>(FS + 4608 + (u32)(k * 16 + i16) * 4u);
            const float x1 = __uint_as_float((u32)lds_ld<uint16_t>(FS + (u32)i16 * 2u) << 16);
            const float sig = rbf(__builtin_amdgcn_rcpf(1.0f + __expf(-rsel)));
            y = rbf(rbf(y) * sig);
            const float h = rbf((x1 - mu) * rs);
            const float x2 = rbf(x1 + rbf(h + y));
            const float a0 = __shfl(x2, (lane & 3) * 4), a1 = __shfl(x2, (lane & 3) * 4 + 1), a2 = __shfl(x2, (lane & 3) * 4 + 2), a3 = __shfl(x2, (lane & 3) * 4 + 3);
            if (L == 0) { if (lane < 4) stw(c.ws, OFF_GY + (u32)(didx * 4 + lane) * 16u, mk_unit(pk(a0, a1), pk(a2, a3), c.tag)); }
            else if (lane < 4) *(u32x2*)(p.out + (size_t)sel * 512 + didx * 16 + lane * 4) = (u32x2){pk(a0, a1), pk(a2, a3)};
            FST(27 + 20 * L);
        }
    }
}

DI void qkv_patch(const Ctx& c, u32& hsync) {
    const int lane = c.lane, hw = c.w - 4;
    constexpr u32 FS = FS0;
    const int g16 = lane >> 4, i16 = lane & 15;
    bf16x8 wf[24];
    {
        const u32 tro = L_W + (u32)((8 * g16 + (i16 >> 2)) * 64 + (i16 & 3) * 8) + (u32)hw * 8192u;
#pragma unroll
        for (int s_ = 0; s_ < 4; ++s_)
#pragma unroll
            for (int ct = 0; ct < 6; ++ct) {
                const u32 bo = tro + (u32)s_ * 2048u + (u32)(ct >> 1) * 32768u + (u32)(ct & 1) * 32u;
                wf[s_ * 6 + ct] = frag_tr(bo, bo + 256);
            }
    }
    u32 off[2] = {OFF_GY + (u32)lane * 32u, OFF_GY + (u32)lane * 32u + 16u}; u32x4 v[2];
    wait_units<2>(c, off, v);
    float x[8], s = 0, q = 0;
#pragma unroll
    for (int k = 0; k < 2; ++k) { x[4 * k] = bflo(v[k][0]); x[4 * k + 1] = bfhi(v[k][0]); x[4 * k + 2] = bflo(v[k][2]); x[4 * k + 3] = bfhi(v[k][2]); }
#pragma unroll
    for (int k = 0; k < 8; ++k) { s += x[k]; q += x[k] * x[k]; }
    s = wave_sum(s); q = wave_sum(q);
    const float mu = s * (1.0f / 512.0f), var = fmaxf(q * (1.0f / 512.0f) - mu * mu, 0.f);
    const float A = __builtin_amdgcn_rsqf(var + 1e-5f);
    lds_st<u32x4>(FS + (u32)lane * 16u, (u32x4){pk((x[0] - mu) * A, (x[1] - mu) * A), pk((x[2] - mu) * A, (x[3] - mu) * A), pk((x[4] - mu) * A, (x[5] - mu) * A), pk((x[6] - mu) * A, (x[7] - mu) * A)});
    f32x4 acc[6] = {};
#pragma unroll
    for (int s_ = 0; s_ < 4; ++s_) {
        const bf16x8 a = lds_ld<bf16x8>(FS + (u32)((hw * 4 + s_) * 64 + g16 * 16));
#pragma unroll
        for (int ct = 0; ct < 6; ++ct) acc[ct] = MFMA16(a, wf[s_ * 6 + ct], acc[ct]);
    }
    if (lane < 16) {
#pragma unroll
        for (int ct = 0; ct < 6; ++ct) lds_st<float>(FS + 1024 + (u32)(hw * 96 + ct * 16 + lane) * 4u, acc[ct][0]);
    }
    helper_sync(c, hsync);
    if (hw == 0) {
#pragma unroll
        for (int k = 0; k < 2; ++k) {
            const int n = k * 64 + lane;
            if (n < 96) {
                float t = 0.f;
#pragma unroll
                for (int j = 0; j < 4; ++j) t += lds_ld<float>(FS + 1024 + (u32)(j * 96 + n) * 4u);
                lds_st<float>(FS + 4608 + (u32)n * 4u, n < 32 ? t * (0.25f * 1.4426950408889634f) : t);
            }
        }
        asm volatile("s_waitcnt lgkmcnt(0)" ::: "memory");
        if (lane == 0) __hip_atomic_store((LAS u32*)(uintptr_t)P_PATCH, 1u, __ATOMIC_RELAXED, __HIP_MEMORY_SCOPE_WORKGROUP);
    }
}

DI u32x4 gld(const void* base, u32 off) { return *(const u32x4*)((const char*)base + off); }
struct FfSrc { const uint16_t* m; u32 vo, step; bool on, gu; };
DI FfSrc ff_src(const Ctx& c, const Params& p) {
    int lane = c.lane; asm volatile("" : "+v"(lane));
    FfSrc f; const int w = c.w;
    f.on = c.gi < 18; f.gu = c.gi < 14;
    f.m = f.gu ? (w < 4 ? p.wg : p.wu) : p.wd;
    const u32 rowb = f.gu ? 3584u : 1024u, col = f.gu ? (u32)(c.xg * 14 + c.gi) * 32u : (u32)(c.xg * 4 + (c.gi - 14)) * 32u;
    const u32 first = f.gu ? (u32)(w & 3) * 4u : (u32)w * 7u;
    f.step = 32u * rowb;
    f.vo = (u32)(lane >> 1) * rowb + col + (u32)(lane & 1) * 16u + first * f.step;
    return f;
}
DI void keep_wq(const Ctx& c, u32x4 (&pq)[12]) {
#pragma unroll
    for (int j = 0; j < 12; ++j) pq[j] = lds_ld<u32x4>(L_W + (u32)(c.w * 12 + j) * 1024u + (u32)c.lane * 16u);
}
DI void pre_store_wq(const Ctx& c, const u32x4 (&pq)[12]) {
#pragma unroll
    for (int j = 0; j < 12; ++j) lds_st<u32x4>(L_W + (u32)(c.w * 12 + j) * 1024u + (u32)c.lane * 16u, pq[j]);
}
DI void pre_store_ff(const Ctx& c, const u32x4 (&pf)[7]) {
    if (c.gi < 18) {
        const bool gu = c.gi < 14;
        const u32 a = FW0 + (u32)c.w * (gu ? 4096u : 7168u) + (u32)c.lane * 16u;
#pragma unroll
        for (int j = 0; j < 7; ++j) if (j < 4 || !gu) lds_st<u32x4>(a + (u32)j * 1024u, pf[j]);
    }
}
DI u32 wo_lane_off(const Ctx& c) {
    int lane = c.lane; asm volatile("" : "+v"(lane));
    return (u32)((lane >> 2) * 1024 + (c.gi & 7) * 128 + (lane & 3) * 16 + (c.w >> 2) * 64 + (c.w & 3) * 8 * 16384);
}
DI void pre_store_wo(const Ctx& c, const u32x4 (&po)[8]) {
#pragma unroll
    for (int j = 0; j < 8; ++j) lds_st<u32x4>(L_WO + (u32)(c.w >> 2) * WO_FT + (u32)((c.w & 3) * 8 + j) * 1024u + (u32)c.lane * 16u, po[j]);
}

struct WFrag { bf16x8 q, k, v; };
template <u32 TR2> DI void wfrag_load(WFrag& f, u32 a01, u32 a2) { f.q = frag_tr(a01, a01 + TR2); f.k = frag_tr(a01 + 32768, a01 + 32768 + TR2); f.v = frag_tr(a2, a2 + TR2); }
template <u32 TR2> DI void qstep(f32x16& aq, f32x16& ak, f32x16& av, bf16x8 x, WFrag& cur, WFrag& nxt, u32 a01, u32 a2, bool more) {
    if (more) nxt.q = frag_tr(a01, a01 + TR2);
    SBAR(); aq = MFMA32(cur.q, x, aq); SBAR();
    if (more) nxt.k = frag_tr(a01 + 32768, a01 + 32768 + TR2);
    SBAR(); ak = MFMA32(cur.k, x, ak); SBAR();
    if (more) nxt.v = frag_tr(a2, a2 + TR2);
    SBAR(); av = MFMA32(x, cur.v, av); SBAR();
}

template <int L>
DI void phaseQ(Ctx& c0, const Params& p, u32& hsync, u32x4 (&pq)[12]) {
    Ctx c = c0; FRESH(c);
    const int lane = c.lane, w = c.w;
    const int rb = 4 * (c.xg & 3) + (c.gi & 3), hp = 8 * (c.xg >> 2) + (c.gi >> 2);
    const int r = lane & 31, h = lane >> 5;
    const int i16 = lane & 15, blk = (lane >> 4) & 1;
    const u32 trA = L_W + (u32)(((L == 0 ? 8 : 4) * h + (i16 >> 2)) * 64 + blk * 32 + (i16 & 3) * 8);
    constexpr u32 TR2 = L == 0 ? 256 : 512;
    constexpr u32 OQ = L ? OFF_Q1 : OFF_Q0, OK_ = L ? OFF_K1 : OFF_K0, OV = L ? OFF_V1 : OFF_V0, TS = L ? OFF_TS1 : OFF_TS0;
    const int rot = ((c.gi >> 2) + 2 * (c.gi & 3)) & 7;

    if (w >= 4) {
        const int hw = w - 4;
        if (L == 0) {
            u32x4 r0, r1; u32 ep;
            asm volatile("global_load_dword %0, %1, %2 sc1" : "=v"(ep) : "v"(0u), "s"(p.ws));
            const u32 tok = (u32)(rb * 128 + hp * 8 + hw * 2);
            const u32 vo = tok * 1024u + (u32)lane * 16u;
            asm volatile("global_load_dwordx4 %0, %1, %2" : "=v"(r0) : "v"(vo), "s"(p.x0));
            asm volatile("global_load_dwordx4 %0, %1, %2 offset:1024" : "=v"(r1) : "v"(vo), "s"(p.x0));
            const u32 wv = (u32)((lane >> 2) * 3072 + hp * 64 + (lane & 3) * 16 + hw * 16 * 3072);
            u32 xv[4];
#pragma unroll
            for (int q = 0; q < 4; ++q) { const int row = 8 * q + (lane >> 3); xv[q] = (u32)(((rb * 4 + hw) * 32 + row) * 1024 + (((lane & 7) ^ ((row >> 1) & 7)) * 16)); }
#define WD(i) do { const u32 kc_ = (u32)(((i) + rot) & 7); _Pragma("unroll") for (int t = 0; t < 3; ++t) \
                glds(p.wqkv, wv + (u32)t * 1024u + kc_ * (64u * 3072u), L_W + (u32)t * 32768u + (kc_ * 64u + (u32)hw * 16u) * 64u); } while (0)
#define XD(i) do { const u32 kc_ = (u32)(((i) + rot) & 7); _Pragma("unroll") for (int q = 0; q < 4; ++q) \
                glds(p.x0, xv[q] + kc_ * 128u, L_XR + (u32)((i) % 3) * 16384u + (u32)hw * 4096u + (u32)q * 1024u); } while (0)
#define QWAIT(n) asm volatile("s_waitcnt vmcnt(" #n ")\n\ts_barrier" ::: "memory")
#define CS(i) if (hw < 3) { _Pragma("unroll") for (int s_ = 0; s_ < 4; ++s_) { \
                const u32 a_ = trA + (u32)hw * 32768u + (u32)((((i) + rot) & 7) * 4 + s_) * 1024u; cs = MFMA32(frag_tr(a_, a_ + TR2), ones, cs); } }
            WD(0); XD(0); WD(1); XD(1); WD(2); XD(2); WD(3);
            FST(1);
            f32x16 cs = {};
            const bf16x8 ones = __builtin_bit_cast(bf16x8, (u32x4){0x3f803f80u, 0x3f803f80u, 0x3f803f80u, 0x3f803f80u});
            asm volatile("s_waitcnt vmcnt(17)\n\ts_barrier" : "+v"(r0), "+v"(r1), "+v"(ep) :: "memory");
            c.tag = epoch_tag(ep); c0.tag = c.tag;
            FST(2);
            {
                float s0 = 0, q0 = 0, s1 = 0, q1 = 0;
#pragma unroll
                for (int j = 0; j < 4; ++j) {
                    float a = bflo(r0[j]), b_ = bfhi(r0[j]); s0 += a + b_; q0 += a * a + b_ * b_;
                    float d = bflo(r1[j]), e = bfhi(r1[j]); s1 += d + e; q1 += d * d + e * e;
                }
                s0 = wave_sum(s0); q0 = wave_sum(q0); s1 = wave_sum(s1); q1 = wave_sum(q1);
                const float s = lane ? s1 : s0, q = lane ? q1 : q0;
                const float mu = s * (1.0f / 512.0f), var = fmaxf(q * (1.0f / 512.0f) - mu * mu, 0.f);
                const float A = __builtin_amdgcn_rsqf(var + 1e-5f);
                if (lane < 2) stw(c.ws, TS + (tok + (u32)lane) * 16u, mk_unit(__float_as_uint(A), __float_as_uint(A * mu), c.tag));
            }
            CS(0);
            QWAIT(11); FST(3); XD(3); WD(4); CS(1);
            QWAIT(11); FST(4); XD(4); WD(5); CS(2);
            QWAIT(10); FST(5); XD(5); WD(6); CS(3);
            QWAIT(10); FST(6); XD(6); WD(7); CS(4);
            QWAIT(10); FST(7); XD(7); CS(5);
            QWAIT(4); FST(8); CS(6); CS(7);
            if (hw < 3 && r == 0) {
#pragma unroll
                for (int g = 0; g < 4; ++g) lds_st<f32x4>(P_CS + (u32)(hw * 32 + 8 * g + 4 * h) * 4u, (f32x4){cs[4 * g], cs[4 * g + 1], cs[4 * g + 2], cs[4 * g + 3]});
            }
            asm volatile("s_waitcnt vmcnt(0) lgkmcnt(0)\n\ts_barrier" ::: "memory");
            STAMP(1);
#undef WD
#undef XD
#undef QWAIT
#undef CS
        }
        DRAIN(); BAR();
        STAMP(L ? 16 : 3);
        if (L == 0) keep_wq(c, pq);
        else {
            wait_sel(0);
            const int sel = __builtin_amdgcn_readfirstlane((int)lds_ld<u32>(P_SEL)) & 2047;
            if ((sel >> 7) == rb) { lds_wait_ge(P_WQ, 8u); qkv_patch(c, hsync); }
        }
    } else {
        const int T = rb * 4 + w;
        f32x16 aq = {}, ak = {}, av = {};
        float A, B;
        if (L == 0) {
            const u32 ep = __builtin_amdgcn_raw_buffer_load_b32(c.ws, OFF_EPOCH, 0, 16);
            u32 xa[4];
#pragma unroll
            for (int s_ = 0; s_ < 4; ++s_) xa[s_] = L_XR + (u32)w * 4096u + (u32)(r * 128 + (((2 * s_ + h) ^ ((r >> 1) & 7)) * 16));
            bf16x8 xr[2][4];
            u32x4 tsv;
            WFrag f0, f1;
            BAR();
            FST(2);
#pragma unroll
            for (int s_ = 0; s_ < 4; ++s_) xr[0][s_] = lds_ld<bf16x8>(xa[s_]);
            { const u32 a01 = trA + (u32)(rot & 7) * 4096u; wfrag_load<TR2>(f0, a01, a01 + 65536u); }
#pragma unroll
            for (int i = 0; i < 8; ++i) {
                const u32 a01 = trA + (u32)((i + rot) & 7) * 4096u, a2 = a01 + 65536u;
                const u32 n01 = trA + (u32)((i + 1 + rot) & 7) * 4096u, n2 = n01 + 65536u;
                qstep<TR2>(aq, ak, av, xr[i & 1][0], f0, f1, a01 + 1024, a2 + 1024, true);
                qstep<TR2>(aq, ak, av, xr[i & 1][1], f1, f0, a01 + 2048, a2 + 2048, true);
                qstep<TR2>(aq, ak, av, xr[i & 1][2], f0, f1, a01 + 3072, a2 + 3072, true);
                {
                    const u32x4 pois = {POISON, POISON, POISON, POISON};
                    const u32 oo = (u32)(((4 * c.xg + (c.gi >> 3)) * 64 + (c.gi & 7) * 8 + 2 * w) * 64 + lane) * 16u;
                    const u32 oq = (u32)((hp * 2 * 64 + T) * 64 + lane) * 16u;
                    if (i == 0) { stw(c.ws, OFF_O0 + oo, pois); stw(c.ws, OFF_O0 + oo + 1024u, pois); }
                    if (i == 1) { stw(c.ws, OFF_Q0 + oq, pois); stw(c.ws, OFF_Q0 + oq + 65536u, pois); }
                    if (i == 2 && rb < 6) { stw(c.ws, OFF_K0 + oq, pois); stw(c.ws, OFF_K0 + oq + 65536u, pois); }
                    if (i == 3 && rb < 6) { stw(c.ws, OFF_V0 + oq, pois); stw(c.ws, OFF_V0 + oq + 65536u, pois); }
                    if (i == 4) { stw(c.ws, OFF_O1 + oo, pois); stw(c.ws, OFF_O1 + oo + 1024u, pois); }
                    if (i == 5 && w == 0) {
                        const u32 o = (u32)(((4 * c.xg + (c.gi >> 3)) * 8 + (c.gi & 7)) * 1024 + lane * 16);
                        stw(c.ws, OFF_GS0 + o, (u32x4){0u, 0u, 0u, 0u}); stw(c.ws, OFF_GS1 + o, (u32x4){0u, 0u, 0u, 0u});
                    }
                    if (i == 7) { DRAIN(); lds_signal_add(c, P_PD); }
                }
                if (i < 7) {
                    asm volatile("s_barrier" ::: "memory");
                    FST(3 + i);
#pragma unroll
                    for (int s_ = 0; s_ < 4; ++s_) xr[(i + 1) & 1][s_] = lds_ld<bf16x8>(xa[s_] + (u32)((i + 1) % 3) * 16384u);
                }
                if (i == 6) tsv = ldw(c.ws, TS + (u32)(T * 32 + r) * 16u);
                qstep<TR2>(aq, ak, av, xr[i & 1][3], f1, f0, n01, n2, i < 7);
            }
            FST(10);
            STAMP(1);
            c.tag = epoch_tag(ep); c0.tag = c.tag;
            if (w == 0) { lds_wait_ge(P_PD, 4u); if (lane == 0) set_flag(c, OFF_FP + (u32)(hp * 16 + rb) * 128u); }
            if (w == 3) {
                const u32x4 v = *(const u32x4*)(p.wr + lane * 8); float s = 0;
#pragma unroll
                for (int j = 0; j < 4; ++j) s += bflo(v[j]) + bfhi(v[j]);
                s = wave_sum(s);
                if (lane == 0) lds_st<float>(P_WSUM, s);
            }
            u32 off[1] = {TS + (u32)(T * 32 + r) * 16u}; u32x4 v[1] = {tsv};
            if (!__all(unit_ok(tsv, c.tag))) wait_units<1>(c, off, v);
            A = __uint_as_float(v[0][0]); B = __uint_as_float(v[0][2]);
            STAMP(2);
        } else {
            wait_flags(c, OFF_FO + (u32)(rb * 16 + (lane & 15)) * 128u);
            STAMP(13);
            u32x4 xf[32];
            const u32 xo = OFF_X1 + (u32)T * 32768u + (u32)lane * 16u;
#pragma unroll
            for (int s = 0; s < 32; ++s) xf[s] = ldw(c.ws, xo + (u32)s * 1024u);
            const u32x4 tsv = ldw(c.ws, TS + (u32)(T * 32 + r) * 16u);
            lds_wait_ge(P_WQ, 8u);
            STAMP(14);
            {
                u32 a01 = trA, a2 = trA + 65536u;
                asm volatile("" : "+v"(a01), "+v"(a2));
                WFrag f0, f1;
                wfrag_load<TR2>(f0, a01, a2);
#pragma unroll
                for (int st = 0; st < 32; st += 2) {
                    qstep<TR2>(aq, ak, av, __builtin_bit_cast(bf16x8, xf[st]), f0, f1, a01 + (u32)(st + 1) * 1024u, a2 + (u32)(st + 1) * 1024u, true);
                    qstep<TR2>(aq, ak, av, __builtin_bit_cast(bf16x8, xf[st + 1]), f1, f0, a01 + (u32)(st + 2) * 1024u, a2 + (u32)(st + 2) * 1024u, st + 2 < 32);
                }
            }
            u32 off[1] = {TS + (u32)(T * 32 + r) * 16u}; u32x4 v[1] = {tsv};
            if (!__all(unit_ok(tsv, c.tag))) wait_units<1>(c, off, v);
            A = __uint_as_float(v[0][0]); B = __uint_as_float(v[0][2]);
        }
        if (L == 1) STAMP(15);
        int ts = -1;
        if (L == 1) {
            wait_sel(0);
            const int sel = __builtin_amdgcn_readfirstlane((int)lds_ld<u32>(P_SEL)) & 2047;
            if ((sel >> 5) == T) ts = sel & 31;
        }
        const u32x4 pois = {POISON, POISON, POISON, POISON};
        const u32 tokl = L_TOK + (u32)w * 256u;
        if (h == 0) lds_st<f32x2>(tokl + (u32)r * 8u, (f32x2){A, B});
        constexpr float QS = 0.25f * 1.4426950408889634f;
#pragma unroll
        for (int t = 0; t < 2; ++t) {
            const f32x16& acc = t ? ak : aq;
            const float sA = t ? A : A * QS, sB = t ? B : B * QS;
            float o[16];
#pragma unroll
            for (int g = 0; g < 4; ++g) {
                const f32x4 cs = lds_ld<f32x4>(P_CS + (u32)(t * 32 + 8 * g + 4 * h) * 4u);
#pragma unroll
                for (int e = 0; e < 4; ++e) o[4 * g + e] = sA * acc[4 * g + e] - sB * cs[e];
            }
#pragma unroll
            for (int a = 0; a < 2; ++a) {
                u32x4 v = not_poison((u32x4){pk(o[8 * a], o[8 * a + 1]), pk(o[8 * a + 2], o[8 * a + 3]), pk(o[8 * a + 4], o[8 * a + 5]), pk(o[8 * a + 6], o[8 * a + 7])});
                if (L == 1 && r == ts) v = pois;
                stw(c.ws, (t ? OK_ : OQ) + (u32)(((hp * 2 + a) * 64 + T) * 64 + lane) * 16u, v);
            }
        }
        u32x4 vlate = pois;
        const u32 vbase = OV + (u32)(((hp * 2 + (r >> 4)) * 64 + T) * 1024 + h * 256 + (r & 15) * 16);
        {
            asm volatile("s_waitcnt lgkmcnt(0)" ::: "memory");
            const float cs = lds_ld<float>(P_CS + (u32)(64 + r) * 4u);
            float o[16];
#pragma unroll
            for (int g = 0; g < 4; ++g) {
                const f32x4 ab0 = lds_ld<f32x4>(tokl + (u32)(8 * g + 4 * h) * 8u), ab1 = lds_ld<f32x4>(tokl + (u32)(8 * g + 4 * h + 2) * 8u);
                o[4 * g] = ab0[0] * av[4 * g] - ab0[1] * cs; o[4 * g + 1] = ab0[2] * av[4 * g + 1] - ab0[3] * cs;
                o[4 * g + 2] = ab1[0] * av[4 * g + 2] - ab1[1] * cs; o[4 * g + 3] = ab1[2] * av[4 * g + 3] - ab1[3] * cs;
            }
#pragma unroll
            for (int s = 0; s < 2; ++s) {
                u32x4 v = not_poison((u32x4){pk(o[8 * s], o[8 * s + 1]), pk(o[8 * s + 2], o[8 * s + 3]), pk(o[8 * s + 4], o[8 * s + 5]), pk(o[8 * s + 6], o[8 * s + 7])});
                if (L == 1 && ts >= 0 && s == (ts >> 4) && h == ((ts >> 2) & 1)) { vlate = v; v = pois; }
                stw(c.ws, vbase + (u32)s * 512u, v);
            }
        }
        DRAIN(); BAR();
        STAMP(L ? 16 : 3);
        if (w == 0 && lane == 0) set_flag(c, (L ? OFF_FQ1 : OFF_FQ0) + (u32)(hp * 16 + rb) * 128u);
        if (L == 1 && ts >= 0) {
            lds_wait_ge(P_PATCH, 1u);
            if (r == ts) {
#pragma unroll
                for (int t = 0; t < 2; ++t)
#pragma unroll
                    for (int a = 0; a < 2; ++a) {
                        const f32x4 p0 = lds_ld<f32x4>(FS0 + 4608 + (u32)(t * 32 + 16 * a + 4 * h) * 4u), p1 = lds_ld<f32x4>(FS0 + 4608 + (u32)(t * 32 + 16 * a + 8 + 4 * h) * 4u);
                        stw(c.ws, (t ? OK_ : OQ) + (u32)(((hp * 2 + a) * 64 + T) * 64 + lane) * 16u, not_poison((u32x4){pk(p0[0], p0[1]), pk(p0[2], p0[3]), pk(p1[0], p1[1]), pk(p1[2], p1[3])}));
                    }
            }
            if (h == ((ts >> 2) & 1)) {
                const u32 pv = pk(lds_ld<float>(FS0 + 4608 + (u32)(64 + r) * 4u), 0.f) & 0xFFFFu;
                const int jj = (ts & 3) + 4 * ((ts >> 3) & 1);
#pragma unroll
                for (int k = 0; k < 4; ++k) if (k == (jj >> 1)) vlate[k] = (jj & 1) ? ((vlate[k] & 0xFFFFu) | (pv << 16)) : ((vlate[k] & 0xFFFF0000u) | pv);
                stw(c.ws, vbase + (u32)(ts >> 4) * 512u, not_poison(vlate));
            }
        }
        if (L == 0) keep_wq(c, pq);
    }
}

struct AState { f32x16 Sa, Sb, Oa, Ob; bf16x8 kC, kN, v0, v1; u32x4 pb1; };
#define EXP2(x) __builtin_amdgcn_exp2f(x)
#define BC8(x) __builtin_bit_cast(bf16x8, x)
#define PVM(O, v, p) O = MFMA32(v, BC8(p), O)
template <bool USE_C, class HOOK>
DI void attn_step(AState& s, bf16x8 qa, bf16x8 qb, const f32x16& ca, const f32x16& cb, u32 kp, u32 vp0, u32 vp1, u32 knext, u32 voff, HOOK hook) {
    const f32x16 zero = {};
    u32x4 pa0, pa1, pb0;
    s.kN = lds_ld<bf16x8>(kp + knext); SBAR();
    s.v0 = lds_ld<bf16x8>(vp0 + voff); SBAR();
    PVM(s.Ob, s.v1, s.pb1); SBAR();
    s.v1 = lds_ld<bf16x8>(vp1 + voff); SBAR();
    float a0 = EXP2(s.Sa[0]); SBAR(); float a1 = EXP2(s.Sa[1]); SBAR(); float a2 = EXP2(s.Sa[2]); SBAR(); float a3 = EXP2(s.Sa[3]); SBAR();
    s.Sb = MFMA32(s.kC, qb, USE_C ? cb : zero); SBAR();
    float a4 = EXP2(s.Sa[4]); SBAR(); float a5 = EXP2(s.Sa[5]); SBAR(); float a6 = EXP2(s.Sa[6]); SBAR(); float a7 = EXP2(s.Sa[7]); SBAR();
    pa0[0] = pk(a0, a1); SBAR(); pa0[1] = pk(a2, a3); SBAR(); float a8 = EXP2(s.Sa[8]); SBAR(); pa0[2] = pk(a4, a5); SBAR(); pa0[3] = pk(a6, a7); SBAR();
    float a9 = EXP2(s.Sa[9]); SBAR();
    asm volatile("" : "+v"(s.v0), "+v"(s.v1), "+v"(s.kN)); SBAR();
    float a10 = EXP2(s.Sa[10]); SBAR();
    PVM(s.Oa, s.v0, pa0); SBAR();
    float a11 = EXP2(s.Sa[11]); SBAR(); float a12 = EXP2(s.Sa[12]); SBAR(); float a13 = EXP2(s.Sa[13]); SBAR(); float a14 = EXP2(s.Sa[14]); SBAR(); float a15 = EXP2(s.Sa[15]); SBAR();
    pa1[0] = pk(a8, a9); SBAR(); pa1[1] = pk(a10, a11); SBAR(); pa1[2] = pk(a12, a13); SBAR(); pa1[3] = pk(a14, a15); SBAR();
    float b0 = EXP2(s.Sb[0]); SBAR(); float b1 = EXP2(s.Sb[1]); SBAR();
    PVM(s.Oa, s.v1, pa1); SBAR();
    hook(); SBAR();
    float b2 = EXP2(s.Sb[2]); SBAR(); float b3 = EXP2(s.Sb[3]); SBAR(); float b4 = EXP2(s.Sb[4]); SBAR(); float b5 = EXP2(s.Sb[5]); SBAR();
    s.Sa = MFMA32(s.kN, qa, USE_C ? ca : zero); SBAR();
    float b6 = EXP2(s.Sb[6]); SBAR(); float b7 = EXP2(s.Sb[7]); SBAR();
    pb0[0] = pk(b0, b1); SBAR(); pb0[1] = pk(b2, b3); SBAR(); float b8 = EXP2(s.Sb[8]); SBAR(); pb0[2] = pk(b4, b5); SBAR(); pb0[3] = pk(b6, b7); SBAR();
    float b9 = EXP2(s.Sb[9]); SBAR(); float b10 = EXP2(s.Sb[10]); SBAR();
    PVM(s.Ob, s.v0, pb0); SBAR();
    float b11 = EXP2(s.Sb[11]); SBAR(); float b12 = EXP2(s.Sb[12]); SBAR(); float b13 = EXP2(s.Sb[13]); SBAR(); float b14 = EXP2(s.Sb[14]); SBAR(); float b15 = EXP2(s.Sb[15]); SBAR();
    s.pb1[0] = pk(b8, b9); SBAR(); s.pb1[1] = pk(b10, b11); SBAR(); s.pb1[2] = pk(b12, b13); SBAR(); s.pb1[3] = pk(b14, b15); SBAR();
    s.kC = s.kN;
}
struct NoHook { DI void operator()() const {} };
template <bool USE_C>
DI void attn_chunk(AState& s, bf16x8 qa, bf16x8 qb, const f32x16& ca, const f32x16& cb, u32 kp, u32 vp0, u32 vp1) {
    attn_step<USE_C>(s, qa, qb, ca, cb, kp, vp0, vp1, 1024, 0, NoHook());
    attn_step<USE_C>(s, qa, qb, ca, cb, kp, vp0, vp1, 2048, VSTR, NoHook());
    attn_step<USE_C>(s, qa, qb, ca, cb, kp, vp0, vp1, 3072, 2 * VSTR, NoHook());
    attn_step<USE_C>(s, qa, qb, ca, cb, kp, vp0, vp1, 8192, 3 * VSTR, NoHook());
}
DI void attn_begin(AState& s, bf16x8 qa, const f32x16& ca, u32 kp) {
    const f32x16 zero = {};
    s.Oa = zero; s.Ob = zero; s.pb1 = (u32x4){0u, 0u, 0u, 0u}; s.v1 = BC8(s.pb1);
    s.kC = lds_ld<bf16x8>(kp);
    s.Sa = MFMA32(s.kC, qa, ca);
}
DI void attn_combine(const Ctx& c, AState& s, float (&O)[9]) {
    s.Ob = MFMA32(s.v1, BC8(s.pb1), s.Ob);
    const f32x16& snd = s.Ob; const f32x16& keep = s.Oa;
    const u32 xb = L_AX + (u32)c.w * 2304u;
    lds_st<f32x4>(xb + (u32)c.lane * 16u, (f32x4){snd[0], snd[1], snd[2], snd[3]});
    lds_st<f32x4>(xb + 1024u + (u32)c.lane * 16u, (f32x4){snd[4], snd[5], snd[6], snd[7]});
    lds_st<float>(xb + 2048u + (u32)c.lane * 4u, snd[8]);
    BAR();
    const u32 xp = L_AX + (u32)(c.w ^ 4) * 2304u;
    const f32x4 u0 = lds_ld<f32x4>(xp + (u32)c.lane * 16u), u1 = lds_ld<f32x4>(xp + 1024u + (u32)c.lane * 16u);
    const float l2 = lds_ld<float>(xp + 2048u + (u32)c.lane * 4u);
#pragma unroll
    for (int q = 0; q < 4; ++q) { O[q] = keep[q] + u0[q]; O[4 + q] = keep[4 + q] + u1[q]; }
    O[8] = keep[8] + l2;
}

DI void phaseA(const Ctx& c0, const Params& p, const int L) {
    Ctx c = c0; FRESH(c);
    u32x4 pf[7] = {}, po[8];
    const int lane = c.lane, w = c.w, tid = w * 64 + lane;
    const int head = 4 * c.xg + (c.gi >> 3), qb = c.gi & 7;
    const int j = w & 3, kh = w >> 2;
    const u32 OQ = L ? OFF_Q1 : OFF_Q0, OK_ = L ? OFF_K1 : OFF_K0, OV = L ? OFF_V1 : OFF_V0, OO = L ? OFF_O1 : OFF_O0;
    if (L == 1 && w == 0) {
        wait_flags(c, OFF_FQ1 + (u32)((head >> 1) * 16 + (lane & 15)) * 128u);
        {
            const int sel = __builtin_amdgcn_readfirstlane((int)lds_ld<u32>(P_SEL)) & 2047, T = sel >> 5, ts = sel & 31;
            const u32 tk = (u32)((head * 64 + T) * 64 + (lane & 1) * 32 + ts) * 16u;
            u32 off = OK_ + tk;
            if (lane >= 2 && lane < 18) off = OV + (u32)((head * 64 + T) * 1024 + (ts >> 4) * 512 + ((ts >> 2) & 1) * 256 + (lane - 2) * 16);
            if (lane >= 18 && lane < 20 && (T >> 3) == qb) off = OQ + tk;
            u32x4 v = ldw(c.ws, off);
            wait_piece(c, off, v);
        }
        lds_signal_add(c, P_FQ);
    }
    if (L == 0 && w == 1) {
        const int qo_ = (4 * c.xg + (c.gi >> 3)) >> 2, hd = lane & 31, xg = hd >> 2, gi = (hd & 3) * 8 + qo_;
        u32 idx = (u32)((8 * (xg >> 2) + (gi >> 2)) * 16 + 4 * (xg & 3) + (gi & 3));
        if (lane >= 32) idx = (u32)((head >> 1) * 16 + (lane < 38 ? lane - 32 : lane < 40 ? 2 * qb + lane - 38 : 0));
        wait_flags(c, OFF_FP + idx * 128u);
    }
    BAR();
    if (tid == 0) lds_st<u32>(L_AX + 8 * 2304u, 0u);
    STAMP(L ? 17 : 4);
    CSTAMP(9);
    const u32 koff = OK_ + (u32)((head * 64 + w) * 64 + lane) * 16u, voff = OV + (u32)((head * 64 + w) * 64 + lane) * 16u;
    u32x4 rk[8], rv[8];
    const u32 qo = OQ + (u32)((head * 64 + qb * 8 + 2 * j) * 64 + lane) * 16u;
    u32x4 qar, qbr;
    {
        u32 n = 0;
        for (;;) {
            rk[0] = ldw(c.ws, koff); rv[0] = ldw(c.ws, voff);
            qar = ldw(c.ws, qo + (u32)kh * 1024u); qbr = ldw(c.ws, qo + (u32)(1 - kh) * 1024u);
            rk[1] = ldw(c.ws, koff + 8192u); rv[1] = ldw(c.ws, voff + 8192u); rk[2] = ldw(c.ws, koff + 16384u); rv[2] = ldw(c.ws, voff + 16384u);
            MEMFENCE();
            const bool bad = poisoned(rk[0]) || poisoned(rv[0]) || poisoned(qar) || poisoned(qbr) || poisoned(rk[1]) || poisoned(rv[1]) || poisoned(rk[2]) || poisoned(rv[2]);
            if (!__any(bad)) break;
            if (spin_fail(c, n)) break;
        }
    }
    { const int t = tid >> 3, s = tid & 7; const u32 val = (s & 3) == 0 ? 0x3f803f80u : 0u;
      lds_st<u32x4>(L_V + (u32)t * VSTR + 1024u + (u32)s * 16u, (u32x4){val, val, val, val}); }
    const u32 kw = L_K + (u32)w * 1024u + (u32)lane * 16u, vw = L_V + (u32)w * VSTR + (u32)lane * 16u;
#pragma unroll
    for (int ch = 0; ch < 3; ++ch) { lds_st<u32x4>(kw + (u32)ch * 8192u, rk[ch]); lds_st<u32x4>(vw + (u32)ch * (8u * VSTR), rv[ch]); }
    CSTAMP(10);
    BAR();
    const int r = lane & 31, h = lane >> 5;
    const u32 kp0 = L_K + (u32)kh * 4096u + (u32)lane * 16u;
    const u32 onesoff = 1024u + ((r >= 20 && r < 28) ? 64u : 0u) + ((r == 16 || r == 20) ? 0u : 16u);
    const u32 vp00 = L_V + (u32)kh * (4u * VSTR) + (r < 16 ? (u32)(h * 256 + r * 16) : onesoff);
    const u32 vp10 = L_V + (u32)kh * (4u * VSTR) + (r < 16 ? (u32)(512 + h * 256 + r * 16) : onesoff);
    const bf16x8 qa = BC8(qar), qbf = BC8(qbr);
    const f32x16 cz = {};
    AState s;
    STAMP(L ? 18 : 5);
#define PRIO(n) __builtin_amdgcn_s_setprio(n)
    PRIO(3);
    CSTAMP(0);
    attn_begin(s, qa, cz, kp0);
    {
#define PUT(ch) [&]() { lds_st<u32x4>(kw + (u32)(ch) * 8192u, rk[ch]); lds_st<u32x4>(vw + (u32)(ch) * (8u * VSTR), rv[ch]); }
#define GET(ch) [&]() { rk[ch] = ldw(c.ws, koff + (u32)(ch) * 8192u); rv[ch] = ldw(c.ws, voff + (u32)(ch) * 8192u); }
#define GETPUT(g, ch) [&]() { GET(g)(); PUT(ch)(); }
#define FF(j_) [&]() { if (ff.on && ((j_) < 4 || !ff.gu)) pf[j_] = gld(ff.m, ff.vo + (u32)(j_) * ff.step); }
#define WO(j_) [&]() { po[j_] = gld(p.wo, wv + (u32)(j_) * 16384u); }
#define ST(ch, t, HOOK) attn_step<false>(s, qa, qbf, cz, cz, kp, vp0, vp1, ((t) == 3 ? 8192 : ((t) + 1) * 1024) + ((ch) & 1) * 8192, (((ch) & 1) * 8 + (t)) * VSTR, HOOK)
#define NEXT2() do { kp += 16384; vp0 += 16 * VSTR; vp1 += 16 * VSTR; asm volatile("" : "+v"(kp), "+v"(vp0), "+v"(vp1)); } while (0)
        u32 kp = kp0, vp0 = vp00, vp1 = vp10;
#define FLAGS() [&]() { if (L == 0 && w == 4) { wait_flags(c, OFF_FQ0 + (u32)((head >> 1) * 16 + (lane & 15)) * 128u); lds_signal_add(c, P_FQ); } }
#define GET2(a_, b_) [&]() { GET(a_)(); GET(b_)(); }
#define GO2(a_, b_) [&]() { lds_wait_ge(P_FQ, (u32)L + 1u); GET(a_)(); GET(b_)(); }
#define WO2(j_) [&]() { WO(j_)(); WO((j_) + 1)(); }
        ST(0, 0, FLAGS()); ST(0, 1, NoHook()); ST(0, 2, NoHook()); ST(0, 3, GO2(3, 4));
        CSTAMP(1);
        ST(1, 0, GET2(5, 6)); ST(1, 1, GET(7)); ST(1, 2, PUT(3)); ST(1, 3, PUT(4));
        NEXT2(); PRIO(2);
        CSTAMP(2);
        ST(2, 0, PUT(5)); ST(2, 1, PUT(6)); ST(2, 2, PUT(7));
        BAR();
        ST(2, 3, NoHook());
        const u32 wv = wo_lane_off(c);
        CSTAMP(3);
        ST(3, 0, WO2(0)); ST(3, 1, WO2(2)); ST(3, 2, WO2(4)); ST(3, 3, WO2(6));
        NEXT2(); PRIO(1);
        const FfSrc ff = ff_src(c, p);
        CSTAMP(4);
        ST(4, 0, FF(0)); ST(4, 1, FF(1)); ST(4, 2, FF(2)); ST(4, 3, FF(3));
        CSTAMP(5);
        ST(5, 0, FF(4)); ST(5, 1, FF(5)); ST(5, 2, FF(6)); ST(5, 3, NoHook());
#undef FLAGS
#undef GET2
#undef GO2
#undef WO2
        NEXT2(); PRIO(0);
        CSTAMP(6);
        for (int ch = 6; ch < 8; ++ch) {
            attn_chunk<false>(s, qa, qbf, cz, cz, kp, vp0, vp1);
            kp += 8192; vp0 += 8 * VSTR; vp1 += 8 * VSTR;
            asm volatile("" : "+v"(kp), "+v"(vp0), "+v"(vp1));
        }
#undef PUT
#undef GET
#undef GETPUT
#undef FF
#undef WO
#undef ST
#undef NEXT2
    }
    CSTAMP(8);
    STAMP(L ? 19 : 6);
    FRESH(c);
    float O[9];
    attn_combine(c, s, O);
    const u32 oo = OO + (u32)((head * 64 + qb * 8 + 2 * j + kh) * 64 + c.lane) * 16u;
    bool redo;
    {
        bool bad = !(O[8] >= 0x1p-60f && O[8] <= 0x1p60f);
#pragma unroll
        for (int q = 0; q < 8; ++q) bad = bad || !(__builtin_fabsf(O[q]) <= 0x1p126f);
        redo = __any(bad);
        if (redo) { if (c.lane == 0) lds_st<u32>(L_AX + 8 * 2304u, 1u); }
        else {
            const float rl = __builtin_amdgcn_rcpf(O[8]);
            u32x4 o;
#pragma unroll
            for (int q = 0; q < 4; ++q) o[q] = pk(O[2 * q] * rl, O[2 * q + 1] * rl);
            stw(c.ws, oo, not_poison(o));
        }
    }
    pre_store_wo(c, po);
    BAR();
    if (__builtin_amdgcn_readfirstlane((int)lds_ld<u32>(L_AX + 8 * 2304u)) != 0) {
        {
            const u32 ko = OK_ + (u32)((head * 64 + w) * 64 + c.lane) * 16u, vo = OV + (u32)((head * 64 + w) * 64 + c.lane) * 16u;
            for (int ch = 0; ch < 4; ++ch) {
                const u32x4 a = ldw(c.ws, ko + (u32)ch * 8192u), b_ = ldw(c.ws, vo + (u32)ch * 8192u);
                lds_st<u32x4>(L_K + (u32)(ch * 8 + w) * 1024u + (u32)c.lane * 16u, a);
                lds_st<u32x4>(L_V + (u32)(ch * 8 + w) * VSTR + (u32)c.lane * 16u, b_);
            }
            { const int t = (w * 64 + c.lane) >> 3, s_ = c.lane & 7; const u32 val = (s_ & 3) == 0 ? 0x3f803f80u : 0u;
              lds_st<u32x4>(L_V + (u32)t * VSTR + 1024u + (u32)s_ * 16u, (u32x4){val, val, val, val}); }
        }
        BAR();
        float ma = -INFINITY, mb = -INFINITY;
        for (int ch = 0; ch < 8; ++ch)
            for (int t = 0; t < 4; ++t) {
                const bf16x8 kf = lds_ld<bf16x8>(kp0 + (u32)(ch * 8192 + t * 1024));
                const f32x16 Sa = MFMA32(kf, qa, cz), Sb = MFMA32(kf, qbf, cz);
#pragma unroll
                for (int q = 0; q < 16; ++q) { ma = fmaxf(ma, Sa[q]); mb = fmaxf(mb, Sb[q]); }
            }
        ma = fmaxf(ma, __shfl_xor(ma, 32)); mb = fmaxf(mb, __shfl_xor(mb, 32));
        const u32 xb = L_AX + (u32)w * 2304u, xp = L_AX + (u32)(w ^ 4) * 2304u;
        lds_st<f32x2>(xb + (u32)c.lane * 8u, (f32x2){ma, mb});
        BAR();
        const f32x2 mo = lds_ld<f32x2>(xp + (u32)c.lane * 8u);
        ma = fmaxf(ma, mo[1]); mb = fmaxf(mb, mo[0]);
        BAR();
        f32x16 ca, cb;
#pragma unroll
        for (int q = 0; q < 16; ++q) { ca[q] = -ma; cb[q] = -mb; }
        attn_begin(s, qa, ca, kp0);
        u32 kp = kp0, vp0 = vp00, vp1 = vp10;
        for (int ch = 0; ch < 8; ++ch) {
            attn_chunk<true>(s, qa, qbf, ca, cb, kp, vp0, vp1);
            kp += 8192; vp0 += 8 * VSTR; vp1 += 8 * VSTR;
        }
        attn_combine(c, s, O);
        if (redo) {
            const float rl = 1.0f / O[8];
            u32x4 o;
#pragma unroll
            for (int q = 0; q < 4; ++q) o[q] = pk(O[2 * q] * rl, O[2 * q + 1] * rl);
            stw(c.ws, oo, not_poison(o));
        }
        {
            const FfSrc ff = ff_src(c, p); const u32 wv = wo_lane_off(c);
#pragma unroll
            for (int q = 0; q < 7; ++q) if (ff.on && (q < 4 || !ff.gu)) pf[q] = gld(ff.m, ff.vo + (u32)q * ff.step);
#pragma unroll
            for (int q = 0; q < 8; ++q) po[q] = gld(p.wo, wv + (u32)q * 16384u);
        }
        BAR();
        pre_store_wo(c, po);
        BAR();
    }
    STAMP(L ? 20 : 7);
    pre_store_ff(c, pf);
}

DI void phaseO(const Ctx& c0, const Params& p, u32x4 (&x2)[2], u32& hsync, const u32x4 (&pq)[12], const int L) {
    Ctx c = c0; FRESH(c);
    const int lane = c.lane, w = c.w;
    const int tb = 4 * c.xg + (c.gi >> 3), fb = c.gi & 7;
    const int r = lane & 31, h = lane >> 5;
    const u32 OO = L ? OFF_O1 : OFF_O0, GS = L ? OFF_GS1 : OFF_GS0, GC = L ? OFF_GC1 : OFF_GC0, GM = L ? OFF_GM1 : OFF_GM0, GX = L ? OFF_GX1 : OFF_GX0;
    constexpr u32 L_TS = L_SB, L_CAND = P_ST + 8;
    if (w >= 4) {
        const int hw = w - 4;
        STAMP(L ? 21 : 8);
        BAR();
        BAR();
        __builtin_amdgcn_s_setprio(3);
        FST(30 + 20 * L);
        if (hw == 0) {
            const u32 a = L_SB + (u32)h * 384u + (u32)r * 12u;
            const float t1 = lds_ld<float>(a) + lds_ld<float>(a + 768), t2 = lds_ld<float>(a + 4) + lds_ld<float>(a + 772), t3 = lds_ld<float>(a + 8) + lds_ld<float>(a + 776);
            stw(c.ws, GS + (u32)((tb * 8 + fb) * 1024 + lane * 16), (u32x4){__float_as_uint(t1), __float_as_uint(t2), __float_as_uint(t3), c.tag});
        }
        FST(31 + 20 * L);
        if (L == 0) {
            if (hw != 0) { pre_store_wq(c, pq); lds_signal_add(c, P_WQ); }
            BAR();
        }
        FST(32 + 20 * L);
        if (hw == 0) {
            u32x4 v[8]; u32 n = 0;
            for (;;) {
                bool ok = true;
#pragma unroll
                for (int k = 0; k < 8; ++k) v[k] = ldw(c.ws, GS + (u32)((tb * 8 + k) * 1024 + lane * 16));
                MEMFENCE();
#pragma unroll
                for (int k = 0; k < 8; ++k) ok = ok && v[k][3] == c.tag;
                if (__all(ok)) break;
                if (spin_fail(c, n)) break;
            }
            FST(33 + 20 * L);
            float t1 = 0, t2 = 0, t3 = 0;
#pragma unroll
            for (int k = 0; k < 8; ++k) { t1 += __uint_as_float(v[k][0]); t2 += __uint_as_float(v[k][1]); t3 += __uint_as_float(v[k][2]); }
            const float mu = t1 * (1.0f / 512.0f), var = fmaxf(t2 * (1.0f / 512.0f) - mu * mu, 0.f);
            const float rs = __builtin_amdgcn_rsqf(var + 1e-5f);
            const float rt = rbf(rs * (t3 - mu * lds_ld<float>(P_WSUM)));
            const u32 key = half_umax(score_key(__float_as_uint(rt) >> 16, (u32)r));
            const int bt = (int)key_idx(key);
            if (fb == 0) {
                const u32 k0 = (u32)__builtin_amdgcn_readlane((int)key, 0), k1 = (u32)__builtin_amdgcn_readlane((int)key, 32);
                const u32 kT = (lane & 1) ? k1 : k0;
                const u64 g = (cand_tag(c.tag) << 21) | (u64)(key_bf(kT) << 5) | (u64)key_idx(kT);
                if (lane < 16) __builtin_amdgcn_raw_buffer_store_b64((u32x2){(u32)g, (u32)(g >> 32)}, c.ws, GC + (u32)(lane >> 1) * 512u + (u32)(tb * 2 + (lane & 1)) * 8u, 0, 16);
                if (r == bt) stw(c.ws, GM + (u32)(tb * 2 + h) * 16u, mk_unit(__float_as_uint(mu), __float_as_uint(rs), c.tag));
                if (L == 0) {
                    const float g1 = 1.0f + rs, A = g1 * __builtin_amdgcn_rsqf(var * g1 * g1 + 1e-5f);
                    stw(c.ws, OFF_TS1 + (u32)(tb * 64 + lane) * 16u, mk_unit(__float_as_uint(A), __float_as_uint(A * mu), c.tag));
                }
            }
            FST(34 + 20 * L);
            lds_st<f32x2>(L_TS + (u32)lane * 8u, (f32x2){mu, rs});
            if (r == 0) lds_st<u32>(L_CAND + (u32)h * 4u, (u32)bt);
            lds_signal_add(c, P_ST + L * 4);
            {
                const int ct = (lane >> 4) & 1, u = lane & 15;
                const int cb = __builtin_amdgcn_readlane(bt, 0), cb1 = __builtin_amdgcn_readlane(bt, 32);
                const int row = ct ? cb1 : cb;
                const u32x2 d = lds_ld<u32x2>(L_X1T + (u32)(ct + 2 * (u >> 3)) * 2048u + (u32)((u >> 2) & 1) * 1024u + (u32)((u & 1) * 32 + row) * 16u + (u32)((u >> 1) & 1) * 8u);
                if (lane < 32) stw(c.ws, GX + (u32)(tb * 2 + ct) * 2048u + (u32)(fb * 16 + u) * 16u, mk_unit(d[0], d[1], c.tag));
            }
            if (L == 0) { pre_store_wq(c, pq); lds_signal_add(c, P_WQ); }
        }
        STAMP(L ? 23 : 12);
        ffn_chain(c, p, hsync, L);
        if (L == 0) STAMP(15);
        return;
    }
    const int tt = w & 1, kh = w >> 1;
    const int tile = tb * 2 + tt, tok = tile * 32 + r;
    const int f0 = fb * 64 + kh * 32;
    u32x2 res[4], wr[4];
#pragma unroll
    for (int g = 0; g < 4; ++g) {
        if (L == 0) res[g] = *(const u32x2*)(p.x0 + (size_t)tok * 512 + f0 + 8 * g + 4 * h);
        wr[g] = *(const u32x2*)(p.wr + f0 + 8 * g + 4 * h);
    }
    if (L == 1) {
        const int sel0 = __builtin_amdgcn_readfirstlane((int)lds_ld<u32>(P_SEL)) & 2047;
        if ((sel0 >> 5) == tile) {
            const u32 ub = OFF_GY + (u32)(f0 / 4 + h) * 16u;
            u32 off[4] = {ub, ub + 32u, ub + 64u, ub + 96u}; u32x4 v[4];
            wait_units<4>(c, off, v);
            if (r == (sel0 & 31)) { x2[0] = (u32x4){v[0][0], v[0][2], v[1][0], v[1][2]}; x2[1] = (u32x4){v[2][0], v[2][2], v[3][0], v[3][2]}; }
        }
#pragma unroll
        for (int g = 0; g < 4; ++g) res[g] = (u32x2){x2[g >> 1][(g & 1) * 2], x2[g >> 1][(g & 1) * 2 + 1]};
    }
    STAMP(L ? 21 : 8);
    u32x4 bf[16];
    const u32 oo = OO + (u32)(((kh * 16) * 64 + tile) * 64 + lane) * 16u;
    {
        u32 n = 0;
        for (;;) {
#pragma unroll
            for (int s = 0; s < 16; ++s) bf[s] = ldw(c.ws, oo + (u32)s * 65536u);
            MEMFENCE();
            u32 m = 0;
#pragma unroll
            for (int s = 0; s < 16; ++s) m = max(m, max(max(bf[s][0], bf[s][1]), max(bf[s][2], bf[s][3])));
            if (!__any(m == POISON)) break;
            if (spin_fail(c, n)) break;
        }
    }
    const int i16 = lane & 15, blk = (lane >> 4) & 1;
    const u32 trA = L_WO + (u32)((kh * 256 + 4 * h + (i16 >> 2)) * 64 + blk * 32 + (i16 & 3) * 8);
    f32x16 a0 = {}, a1 = {};
    {
        u32 ta = trA + (u32)kh * WO_FT, tb_ = trA + (u32)(1 - kh) * WO_FT;
        asm volatile("" : "+v"(ta), "+v"(tb_));
        bf16x8 f0_[2], f1_[2];
        f0_[0] = frag_tr(ta, ta + 512); f0_[1] = frag_tr(tb_, tb_ + 512);
#pragma unroll
        for (int s = 0; s < 16; s += 2) {
            f1_[0] = frag_tr(ta + (u32)(s + 1) * 1024u, ta + (u32)(s + 1) * 1024u + 512); SBAR();
            a0 = MFMA32(f0_[0], BC8(bf[s]), a0); SBAR();
            f1_[1] = frag_tr(tb_ + (u32)(s + 1) * 1024u, tb_ + (u32)(s + 1) * 1024u + 512); SBAR();
            a1 = MFMA32(f0_[1], BC8(bf[s]), a1); SBAR();
            if (s + 2 < 16) f0_[0] = frag_tr(ta + (u32)(s + 2) * 1024u, ta + (u32)(s + 2) * 1024u + 512);
            SBAR(); a0 = MFMA32(f1_[0], BC8(bf[s + 1]), a0); SBAR();
            if (s + 2 < 16) f0_[1] = frag_tr(tb_ + (u32)(s + 2) * 1024u, tb_ + (u32)(s + 2) * 1024u + 512);
            SBAR(); a1 = MFMA32(f1_[1], BC8(bf[s + 1]), a1); SBAR();
        }
    }
    {
        const f32x16& snd = a1;
#pragma unroll
        for (int g = 0; g < 4; ++g) lds_st<f32x4>(L_XB + (u32)w * 4096u + (u32)g * 1024u + (u32)lane * 16u, (f32x4){snd[4 * g], snd[4 * g + 1], snd[4 * g + 2], snd[4 * g + 3]});
    }
    BAR();
    STAMP(L ? 22 : 9);
    u32x4 xp[2];
    {
        const f32x16& keep = a0;
#pragma unroll
        for (int g = 0; g < 4; ++g) {
            const f32x4 o = lds_ld<f32x4>(L_XB + (u32)(w ^ 2) * 4096u + (u32)g * 1024u + (u32)lane * 16u);
            const u32 t0 = pk(keep[4 * g] + o[0], keep[4 * g + 1] + o[1]), t1 = pk(keep[4 * g + 2] + o[2], keep[4 * g + 3] + o[3]);
            xp[g >> 1][(g & 1) * 2] = pk(bflo(res[g][0]) + bflo(t0), bfhi(res[g][0]) + bfhi(t0));
            xp[g >> 1][(g & 1) * 2 + 1] = pk(bflo(res[g][1]) + bflo(t1), bfhi(res[g][1]) + bfhi(t1));
        }
    }
    if (L == 0) {
#pragma unroll
        for (int s = 0; s < 2; ++s) stw(c.ws, OFF_X1 + (u32)((tile * 32 + f0 / 16 + s) * 64 + lane) * 16u, xp[s]);
    }
#pragma unroll
    for (int s = 0; s < 2; ++s) lds_st<u32x4>(L_X1T + (u32)w * 2048u + (u32)s * 1024u + (u32)lane * 16u, xp[s]);
    float x1[16];
    {
        float s1 = 0, s2 = 0, s3 = 0;
#pragma unroll
        for (int i = 0; i < 16; ++i) {
            const u32 wd = xp[i >> 3][(i & 7) >> 1];
            x1[i] = (i & 1) ? bfhi(wd) : bflo(wd);
            const u32 ww = wr[i >> 2][(i & 3) >> 1];
            const float wv = (i & 1) ? bfhi(ww) : bflo(ww);
            s1 += x1[i]; s2 += x1[i] * x1[i]; s3 += x1[i] * wv;
        }
        s1 = swap32_add(s1); s2 = swap32_add(s2); s3 = swap32_add(s3);
        if (h == 0) { const u32 a = L_SB + (u32)w * 384u + (u32)r * 12u; lds_st<float>(a, s1); lds_st<float>(a + 4, s2); lds_st<float>(a + 8, s3); }
    }
    BAR();
    if (L == 0) {
        DRAIN(); BAR();
        STAMP(10);
        if (w == 0 && lane == 0) set_flag(c, OFF_FO + (u32)(tb * 8 + fb) * 128u);
        pre_store_wq(c, pq); lds_signal_add(c, P_WQ);
    }
    lds_wait_ge(P_ST + L * 4, 1u);
    STAMP(L ? 23 : 11);
    const f32x2 st = lds_ld<f32x2>(L_TS + (u32)(tt * 32 + r) * 8u);
    const int bt = (int)lds_ld<u32>(L_CAND + (u32)tt * 4u) & 31;
#pragma unroll
    for (int s = 0; s < 2; ++s)
#pragma unroll
        for (int j = 0; j < 4; ++j) {
            const float a = x1[8 * s + 2 * j], b_ = x1[8 * s + 2 * j + 1];
            const u32 hh = pk((a - st[0]) * st[1], (b_ - st[0]) * st[1]);
            x2[s][j] = pk(a + bflo(hh), b_ + bfhi(hh));
        }
    if (L == 1) {
        const rsrc_t outr = __builtin_amdgcn_make_buffer_rsrc(p.out, 0, 2097152, 0x00020000);
        const u32 tl = L_XB + (u32)w * 4096u;
#pragma unroll
        for (int g = 0; g < 4; ++g) lds_st<u32x2>(tl + (u32)(r * 80 + (8 * g + 4 * h) * 2), (u32x2){x2[g >> 1][(g & 1) * 2], x2[g >> 1][(g & 1) * 2 + 1]});
        asm volatile("s_waitcnt lgkmcnt(0)" ::: "memory");
        u32x4 rowv[2];
#pragma unroll
        for (int q = 0; q < 2; ++q) {
            const int row = q * 16 + (lane >> 2);
            rowv[q] = lds_ld<u32x4>(tl + (u32)(row * 80 + (lane & 3) * 16));
            if (row != bt) __builtin_amdgcn_raw_buffer_store_b128(rowv[q], outr, (u32)(((tile * 32 + row) * 512 + f0 + (lane & 3) * 8) * 2), 0, 16);
        }
        wait_sel(1);
        const int sel1 = __builtin_amdgcn_readfirstlane((int)lds_ld<u32>(P_SEL + 32)) & 2047;
        if (tile * 32 + bt != sel1) {
#pragma unroll
            for (int q = 0; q < 2; ++q) {
                const int row = q * 16 + (lane >> 2);
                if (row == bt) __builtin_amdgcn_raw_buffer_store_b128(rowv[q], outr, (u32)(((tile * 32 + row) * 512 + f0 + (lane & 3) * 8) * 2), 0, 16);
            }
        }
        }
}

#if defined(__HIP_DEVICE_COMPILE__)
__attribute__((target("no-packed-fp32-ops")))
#endif
__global__ __launch_bounds__(512, 2) void fused_layers_kernel(Params p) {
    extern __shared__ __attribute__((aligned(16))) char lds[];
    Ctx c;
    const int tid = threadIdx.x;
    c.lane = tid & 63; c.w = __builtin_amdgcn_readfirstlane(tid >> 6);
    c.xg = blockIdx.x & 7; c.gi = blockIdx.x >> 3;
    c.ws = __builtin_amdgcn_make_buffer_rsrc(p.ws, 0, WS_BYTES, 0x00020000);
    c.tag = 0u;
    if (c.w < 4) {
        u64 pc; u32 len;
        asm volatile("s_getpc_b64 %0\nfl_code_here:\n\ts_mov_b32 %1, fl_code_end-fl_code_here" : "=s"(pc), "=s"(len));
        if (c.w == 1) {
            const u32 o0 = min((u32)c.gi * 3072u + (u32)c.lane * 16u, len - 16u) & ~15u, o1 = min(o0 + 1024u, len - 16u) & ~15u, o2 = min(o0 + 2048u, len - 16u) & ~15u;
            const u64 base = pc & ~15ull;
            glds((const void*)base, o0, 147456u); glds((const void*)base, o1, 148480u); glds((const void*)base, o2, 149504u);
        }
        if (c.w == 2) {
            const int l = c.lane, k = (l - 22) % 14;
            const char* a = p.ws + (size_t)(l < 10 ? l : 0) * 0x200000;
            a = l >= 10 && l < 14 ? (const char*)p.wo + (l - 10) * 131072 : l == 14 ? (const char*)p.wr : a;
            a = l >= 15 && l < 22 ? (const char*)p.out + (l - 15) * 299008 : a;
            a = l >= 22 ? (const char*)(l < 36 ? p.wg : l < 50 ? p.wu : p.wd) + k * 131072 : a;
            u32 sv;
            asm volatile("s_mov_b32 %0, m0\n\ts_mov_b32 m0, %2\n\ts_nop 0\n\tglobal_load_lds_dword %1, off\n\ts_mov_b32 m0, %0" : "=&s"(sv) : "v"(a), "s"(150528u) : "memory");
        }
        if (c.w == 3 && c.lane < 26 && c.lane != 3) lds_st<u32>(P_HCNT + (u32)c.lane * 4u, 0u);
    }
    asm volatile("" :: "v"(lds));
    STAMP(0);
    FST(0);
    u32 hsync = 0;
    u32x4 x2[2];
    asm volatile("" : "=v"(x2[0]), "=v"(x2[1]));
    u32x4 pq[12];
#pragma nounroll
    for (int Li = 0; Li < 2; ++Li) {
        int Lv = Li; asm volatile("" : "+v"(Lv));
        const int L = __builtin_amdgcn_readfirstlane(Lv);
        if (L == 0) phaseQ<0>(c, p, hsync, pq);
        else {
            phaseQ<1>(c, p, hsync, pq);
#pragma unroll
            for (int j = 0; j < 12; ++j) asm volatile("" : "=v"(pq[j]));
        }
        if (STOP_AFTER < 3 * L + 1) break;
        phaseA(c, p, L);
        if (STOP_AFTER < 3 * L + 2) break;
        phaseO(c, p, x2, hsync, pq, L);
        if (STOP_AFTER < 3 * L + 3) break;
    }
    STAMP(24);
    if (blockIdx.x == 14 * 8 && c.w == 4 && c.lane == 0) __builtin_amdgcn_raw_buffer_store_b32(c.tag, c.ws, OFF_EPOCH, 0, 16);
    asm volatile("fl_code_end:" ::: "memory");
}

extern "C" void kernel_launch(void* const* d_in, const int* in_sizes, int n_in, void* d_out, int out_size, void* d_ws, size_t ws_size, hipStream_t stream) {
    Params p;
    p.x0 = (const uint16_t*)d_in[0]; p.wqkv = (const uint16_t*)d_in[1]; p.wo = (const uint16_t*)d_in[2];
    p.wr = (const uint16_t*)d_in[4]; p.wg = (const uint16_t*)d_in[5]; p.wu = (const uint16_t*)d_in[6]; p.wd = (const uint16_t*)d_in[7];
    p.out = (uint16_t*)d_out; p.ws = (char*)d_ws;
    (void)hipFuncSetAttribute((const void*)fused_layers_kernel, hipFuncAttributeMaxDynamicSharedMemorySize, LDS_BYTES);
    fused_layers_kernel<<<256, 512, LDS_BYTES, stream>>>(p);
}
```

```cpp
#include <hip/hip_runtime.h>
#include <stdint.h>

typedef unsigned u32;
typedef unsigned long long u64;
typedef __attribute__((ext_vector_type(2))) u32 u32x2;
typedef __attribute__((ext_vector_type(4))) u32 u32x4;
typedef __attribute__((ext_vector_type(2))) float f32x2;
typedef __attribute__((ext_vector_type(4))) float f32x4;
typedef __attribute__((ext_vector_type(16))) float f32x16;
typedef __attribute__((ext_vector_type(8))) __bf16 bf16x8;
typedef __attribute__((ext_vector_type(2))) __bf16 bf16x2;
typedef __attribute__((ext_vector_type(4))) short s16x4;
typedef __attribute__((ext_vector_type(8))) short s16x8;
#define LAS __attribute__((address_space(3)))
#define DI __device__ __forceinline__
#define MFMA32(a, b, c) __builtin_amdgcn_mfma_f32_32x32x16_bf16((a), (b), (c), 0, 0, 0)
#define MFMA16(a, b, c) __builtin_amdgcn_mfma_f32_16x16x32_bf16((a), (b), (c), 0, 0, 0)
#define SBAR() __builtin_amdgcn_sched_barrier(0)
#define BAR() asm volatile("s_waitcnt lgkmcnt(0)\n\ts_barrier" ::: "memory")
#define DRAIN() asm volatile("s_waitcnt vmcnt(0)" ::: "memory")
#define MEMFENCE() asm volatile("" ::: "memory")

#define STOP_AFTER 99

constexpr u32 OFF_EPOCH = 0, OFF_ABORT = 256;
constexpr u32 OFF_FQ0 = 0x10000, OFF_FP = 0x18000, OFF_FO = 0x20000, OFF_FP2 = 0x28000;
constexpr u32 OFF_TS0 = 0x40000, OFF_TS1 = 0x48000;
constexpr u32 OFF_GC0 = 0x50000, OFF_GC1 = 0x51000;
constexpr u32 OFF_GM0 = 0x52000, OFF_GM1 = 0x52400;
constexpr u32 OFF_GA0 = 0x58000, OFF_GA1 = 0x5A000;
constexpr u32 OFF_GY = 0x5C000;
constexpr u32 OFF_GS0 = 0x80000, OFF_GS1 = 0xC0000;
constexpr u32 OFF_GX0 = 0x180000, OFF_GX1 = 0x1A0000;
constexpr u32 OFF_Q0 = 0x200000, OFF_K0 = 0x400000, OFF_V0 = 0x600000, OFF_O0 = 0x800000, OFF_X1 = 0xA00000;
constexpr u32 OFF_Q1 = 0xC00000, OFF_K1 = 0xE00000, OFF_V1 = 0x1000000, OFF_O1 = 0x1200000;
constexpr u32 WS_BYTES = 0x1400000;

constexpr u32 LDS_BYTES = 163840;
constexpr u32 L_W = 0;
constexpr u32 L_XR = 98304;
constexpr u32 L_AX = 140288;
constexpr u32 L_TOK = 161792 + 512;
constexpr u32 L_K = 0, L_V = 65536, VSTR = 1152;
constexpr u32 L_WO = 0, WO_FT = 65536;
constexpr u32 L_XB = 32768;
constexpr u32 L_SB = 161792;
constexpr u32 L_P = 163328;
constexpr u32 P_CS = L_P, P_HCNT = L_P + 384, P_PATCH = L_P + 388, P_WQ = L_P + 392, P_WSUM = L_P + 396, P_SEL = L_P + 400, P_ST = L_P + 464, P_PD = L_P + 480, P_FQ = L_P + 484, P_KV = L_P + 488, P_QD = L_P + 492, P_WF = L_P + 496;
constexpr u32 L_VL = 155648 + 5120;
constexpr u32 L_RP = 140288;
constexpr u32 L_X1T = 49152;
constexpr u32 FW0 = 98304, FS0 = 155648;
constexpr u32 FW1 = FW0, FS1 = FS0;
constexpr u32 SPIN_CAP = 40000;

struct Params {
    const uint16_t* x0; const uint16_t* wqkv; const uint16_t* wo; const uint16_t* wr;
    const uint16_t* wg; const uint16_t* wu; const uint16_t* wd; uint16_t* out; char* ws;
};

typedef __amdgpu_buffer_rsrc_t rsrc_t;

DI u32 pk(float a, float b) { f32x2 v = {a, b}; return __builtin_bit_cast(u32, __builtin_convertvector(v, bf16x2)); }
DI float bflo(u32 v) { return __uint_as_float(v << 16); }
DI float bfhi(u32 v) { return __uint_as_float(v & 0xffff0000u); }
DI float rbf(float a) { return bflo(pk(a, 0.f)); }
DI u32x4 ldw(rsrc_t r, u32 off) { return __builtin_amdgcn_raw_buffer_load_b128(r, off, 0, 16); }
DI void stw(rsrc_t r, u32 off, u32x4 v) { __builtin_amdgcn_raw_buffer_store_b128(v, r, off, 0, 16); }
template <class T> DI T lds_ld(u32 a) { return *(const LAS T*)(uintptr_t)a; }
template <class T> DI void lds_st(u32 a, T v) { *(LAS T*)(uintptr_t)a = v; }
DI s16x4 lds_tr(u32 a) { return __builtin_amdgcn_ds_read_tr16_b64_v4i16((LAS s16x4*)(uintptr_t)a); }
DI bf16x8 frag_tr(u32 a0, u32 a1) { s16x4 lo = lds_tr(a0), hi = lds_tr(a1); return __builtin_bit_cast(bf16x8, __builtin_shufflevector(lo, hi, 0, 1, 2, 3, 4, 5, 6, 7)); }
DI float swap32_add(float v) { auto r = __builtin_amdgcn_permlane32_swap(__float_as_uint(v), __float_as_uint(v), false, false); return __uint_as_float(r[0]) + __uint_as_float(r[1]); }
template <int CTRL> DI u32 dpp(u32 v) { return (u32)__builtin_amdgcn_update_dpp((int)v, (int)v, CTRL, 0xf, 0xf, true); }
DI float wave_sum(float v) {
    v += __uint_as_float(dpp<0xB1>(__float_as_uint(v)));
    v += __uint_as_float(dpp<0x4E>(__float_as_uint(v)));
    v += __uint_as_float(dpp<0x141>(__float_as_uint(v)));
    v += __uint_as_float(dpp<0x140>(__float_as_uint(v)));
    { auto r = __builtin_amdgcn_permlane16_swap(__float_as_uint(v), __float_as_uint(v), false, false); v = __uint_as_float(r[0]) + __uint_as_float(r[1]); }
    return swap32_add(v);
}
DI u32 half_umax(u32 v) {
    v = max(v, dpp<0xB1>(v)); v = max(v, dpp<0x4E>(v)); v = max(v, dpp<0x141>(v)); v = max(v, dpp<0x140>(v));
    auto r = __builtin_amdgcn_permlane16_swap(v, v, false, false);
    return max(r[0], r[1]);
}
DI u32 wave_umax(u32 v) { v = half_umax(v); auto r = __builtin_amdgcn_permlane32_swap(v, v, false, false); return max(r[0], r[1]); }
DI u32 score_key(u32 bf, u32 idx) { return ((bf ^ ((bf & 0x8000u) ? 0xFFFFu : 0x8000u)) << 16) | (0xFFFFu - idx); }
DI u32 key_bf(u32 k) { const u32 b = k >> 16; return b ^ ((b & 0x8000u) ? 0x8000u : 0xFFFFu); }
DI u32 key_idx(u32 k) { return 0xFFFFu - (k & 0xFFFFu); }
DI void glds(const void* sbase, u32 voff, u32 ldsdst) {
    u32 sv;
    asm volatile("s_mov_b32 %0, m0\n\ts_mov_b32 m0, %3\n\ts_nop 0\n\tglobal_load_lds_dwordx4 %1, %2\n\ts_mov_b32 m0, %0"
                 : "=&s"(sv) : "v"(voff), "s"(sbase), "s"(ldsdst) : "memory");
}
struct Ctx {
    rsrc_t ws; u32 tag; int lane, w, xg, gi;
};
#define FRESH(c) asm volatile("" : "+v"((c).lane), "+s"((c).w), "+s"((c).xg), "+s"((c).gi))
DI u32 epoch_tag(u32 v) { const u32 t = (u32)__builtin_amdgcn_readfirstlane((int)v) + 1u; return t ? t : 1u; }
DI bool spin_fail(const Ctx& c, u32& n) {
    __builtin_amdgcn_s_sleep(1);
    ++n;
    if (n > SPIN_CAP) { if (c.lane == 0) __builtin_amdgcn_raw_buffer_store_b32(c.tag, c.ws, OFF_ABORT, 0, 16); return true; }
    if ((n & 127u) == 0u) { u32 a = __builtin_amdgcn_raw_buffer_load_b32(c.ws, OFF_ABORT, 0, 16); if (__builtin_amdgcn_readfirstlane(a) == c.tag) return true; }
    return false;
}
DI void wait_flags(const Ctx& c, u32 off) {
    u32 n = 0;
    for (;;) {
        u32x2 v = __builtin_amdgcn_raw_buffer_load_b64(c.ws, off, 0, 16);
        MEMFENCE();
        if (__all(v[0] == c.tag && v[1] == ~c.tag)) break;
        if (spin_fail(c, n)) break;
    }
    MEMFENCE();
}
DI void set_flag(const Ctx& c, u32 off) { u32x2 v = {c.tag, ~c.tag}; __builtin_amdgcn_raw_buffer_store_b64(v, c.ws, off, 0, 16); }
constexpr u32 POISON = 0xFFFFFFFFu;
DI bool poisoned(u32x4 v) { return max(max(v[0], v[1]), max(v[2], v[3])) == POISON; }
DI u32x4 not_poison(u32x4 v) { return (u32x4){min(v[0], POISON - 1u), min(v[1], POISON - 1u), min(v[2], POISON - 1u), min(v[3], POISON - 1u)}; }
DI void wait_piece(const Ctx& c, u32 off, u32x4& v) {
    u32 n = 0;
    while (__any(poisoned(v))) { if (spin_fail(c, n)) break; v = ldw(c.ws, off); MEMFENCE(); }
}
DI bool unit_ok(u32x4 v, u32 tag) { return v[1] == tag && v[3] == tag; }
DI u32x4 mk_unit(u32 a, u32 b, u32 tag) { return (u32x4){a, tag, b, tag}; }
template <int N> DI void wait_units(const Ctx& c, const u32 (&off)[N], u32x4 (&v)[N]) {
    u32 n = 0;
    for (;;) {
        bool ok = true;
#pragma unroll
        for (int k = 0; k < N; ++k) v[k] = ldw(c.ws, off[k]);
        MEMFENCE();
#pragma unroll
        for (int k = 0; k < N; ++k) ok = ok && unit_ok(v[k], c.tag);
        if (__all(ok)) break;
        if (spin_fail(c, n)) break;
    }
    MEMFENCE();
}
DI u32 lds_poll_ld(u32 a) { return __hip_atomic_load((LAS u32*)(uintptr_t)a, __ATOMIC_RELAXED, __HIP_MEMORY_SCOPE_WORKGROUP); }
DI void helper_sync(const Ctx& c, u32& target) {
    target += 4;
    asm volatile("s_waitcnt lgkmcnt(0)" ::: "memory");
    if (c.lane == 0) __hip_atomic_fetch_add((LAS u32*)(uintptr_t)P_HCNT, 1u, __ATOMIC_RELAXED, __HIP_MEMORY_SCOPE_WORKGROUP);
    u32 n = 0;
    while (lds_poll_ld(P_HCNT) < target) { if (++n > (1u << 24)) break; }
    MEMFENCE();
}
DI void lds_wait_ge(u32 a, u32 v) {
    u32 n = 0;
    while (lds_poll_ld(a) < v) { __builtin_amdgcn_s_sleep(1); if (++n > (1u << 22)) break; }
    MEMFENCE();
}
DI void lds_signal_add(const Ctx& c, u32 a) {
    asm volatile("s_waitcnt lgkmcnt(0)" ::: "memory");
    if (c.lane == 0) __hip_atomic_fetch_add((LAS u32*)(uintptr_t)a, 1u, __ATOMIC_RELAXED, __HIP_MEMORY_SCOPE_WORKGROUP);
}
DI void wait_sel(int L) {
    u32 n = 0;
    while (lds_poll_ld(P_SEL + L * 32 + 16) == 0u) { __builtin_amdgcn_s_sleep(1); if (++n > (1u << 22)) break; }
    MEMFENCE();
}

#define STAMP(i) do { } while (0)
#define CSTAMP(i) do { } while (0)
#define FST(i) do { } while (0)

template <class T, int N> DI void fresh(T (&a)[N]) {
#pragma unroll
    for (int j = 0; j < N; ++j) asm volatile("" : "=v"(a[j]));
}
DI u64 cand_tag(u32 tag) { return ((u64)tag << 11) | (u64)((tag * 0x9E3779B1u) >> 21); }
DI void ffn_chain(const Ctx& c, const Params& p, u32& hsync, const int L, u32x4 (&wp)[24]) {
    const int lane = c.lane, hw = c.w - 4;
    constexpr u32 FW = FW0, FS = FS0;
    const u32 GC = L ? OFF_GC1 : OFF_GC0, GM = L ? OFF_GM1 : OFF_GM0, GX = L ? OFF_GX1 : OFF_GX0, GA = L ? OFF_GA1 : OFF_GA0;
    FST(20 + 20 * L);
    __builtin_amdgcn_s_setprio(3);
    fresh(wp);
    const bool is_gu = c.gi < 14, is_dn = c.gi >= 14 && c.gi < 18;
    const int g16 = lane >> 4, i16 = lane & 15;
    bf16x8 wf[14];
    {
        const u32 tro = FW + (u32)((8 * g16 + (i16 >> 2)) * 32 + (i16 & 3) * 8) + (u32)hw * (is_gu ? 4096u : 14336u);
#pragma unroll
        for (int s = 0; s < 14; ++s) {
            const u32 bo = tro + (is_gu ? (u32)(s & 3) * 1024u + (u32)((s >> 2) & 1) * 16384u : (u32)s * 1024u);
            if (is_gu || is_dn) wf[s] = frag_tr(bo, bo + 128);
        }
    }
    if (L == 0) lds_signal_add(c, P_WF);
    if (hw == 0) {
        const u64 want = cand_tag(c.tag);
        u64 v; u32 n = 0;
        for (;;) {
            const u32x2 t = __builtin_amdgcn_raw_buffer_load_b64(c.ws, GC + c.xg * 512u + lane * 8u, 0, 16);
            MEMFENCE();
            v = ((u64)t[1] << 32) | t[0];
            if (__all((v >> 21) == want)) break;
            if (spin_fail(c, n)) break;
        }
        FST(21 + 20 * L);
        const u32 key = wave_umax(score_key(((u32)v >> 5) & 0xFFFFu, (u32)lane * 32u + ((u32)v & 31u)));
        const u32 tok = key_idx(key); const float r = __uint_as_float(key_bf(key) << 16);
        if (lane == 0) {
            lds_st<u32x2>(P_SEL + L * 32, (u32x2){(u32)tok, __float_as_uint(r)});
            asm volatile("s_waitcnt lgkmcnt(0)" ::: "memory");
            __hip_atomic_store((LAS u32*)(uintptr_t)(P_SEL + L * 32 + 16), 1u, __ATOMIC_RELAXED, __HIP_MEMORY_SCOPE_WORKGROUP);
        }
    }
    FST(22 + 20 * L);
    STAMP(L ? 25 : 13);
    if (L == 0) {
        wait_sel(0);
        const int sel0 = __builtin_amdgcn_readfirstlane((int)lds_ld<u32>(P_SEL)) & 2047;
        if ((sel0 >> 7) == 4 * (c.xg & 3) + (c.gi & 3)) {
            lds_wait_ge(P_WQ, 8u);
            const u32 tro = L_W + (u32)((8 * g16 + (i16 >> 2)) * 64 + (i16 & 3) * 8) + (u32)hw * 8192u;
#pragma unroll
            for (int s_ = 0; s_ < 4; ++s_)
#pragma unroll
                for (int ct = 0; ct < 6; ++ct) {
                    const u32 bo = tro + (u32)s_ * 2048u + (u32)(ct >> 1) * 32768u + (u32)(ct & 1) * 32u;
                    wp[s_ * 6 + ct] = __builtin_bit_cast(u32x4, frag_tr(bo, bo + 256));
                }
            lds_signal_add(c, P_WF);
        }
    }
    if (!is_gu && !is_dn) return;
    wait_sel(L);
    const u32x2 si = lds_ld<u32x2>(P_SEL + L * 32);
    const int sel = __builtin_amdgcn_readfirstlane((int)si[0]) & 2047;
    const float rsel = __uint_as_float(si[1]);
    const u32 gxrow = GX + (u32)(sel >> 5) * 2048u, gm = GM + (u32)(sel >> 5) * 16u;
    if (is_gu) {
        const int gidx = c.xg * 14 + c.gi;
        u32 off[2] = {gxrow + (u32)(hw * 32 + (lane & 31)) * 16u, gm};
        u32x4 v[2];
        FST(29 + 20 * L);
        wait_units<2>(c, off, v);
        FST(23 + 20 * L);
        const float mu = __uint_as_float(v[1][0]), rs = __uint_as_float(v[1][2]);
        if (lane < 32) {
            u32x2 h;
            h[0] = pk((bflo(v[0][0]) - mu) * rs, (bfhi(v[0][0]) - mu) * rs);
            h[1] = pk((bflo(v[0][2]) - mu) * rs, (bfhi(v[0][2]) - mu) * rs);
            lds_st<u32x2>(FS + (u32)(hw * 32 + lane) * 8u, h);
        }
        STAMP(L ? 26 : 14);
        FST(24 + 20 * L);
        f32x4 ag = {}, au = {};
#pragma unroll
        for (int s = 0; s < 4; ++s) {
            const bf16x8 a = lds_ld<bf16x8>(FS + (u32)((hw * 4 + s) * 64 + g16 * 16));
            ag = MFMA16(a, wf[s], ag);
            au = MFMA16(a, wf[4 + s], au);
        }
        if (lane < 16) { lds_st<float>(FS + 4608 + (u32)(hw * 32 + lane) * 4u, ag[0]); lds_st<float>(FS + 4608 + (u32)(hw * 32 + 16 + lane) * 4u, au[0]); }
        FST(25 + 20 * L);
        helper_sync(c, hsync);
        FST(26 + 20 * L);
        if (hw == 0) {
            float g = 0.f, u = 0.f;
#pragma unroll
            for (int k = 0; k < 4; ++k) { g += lds_ld<float>(FS + 4608 + (u32)(k * 32 + i16) * 4u); u += lds_ld<float>(FS + 4608 + (u32)(k * 32 + 16 + i16) * 4u); }
            g = rbf(g); u = rbf(u);
            const float sg = rbf(g * __builtin_amdgcn_rcpf(1.0f + __expf(-g)));
            const float a = rbf(sg * u);
            const float a0 = __shfl(a, (lane & 3) * 4), a1 = __shfl(a, (lane & 3) * 4 + 1), a2 = __shfl(a, (lane & 3) * 4 + 2), a3 = __shfl(a, (lane & 3) * 4 + 3);
            if (lane < 4) stw(c.ws, GA + (u32)(gidx * 4 + lane) * 16u, mk_unit(pk(a0, a1), pk(a2, a3), c.tag));
            FST(27 + 20 * L);
        }
    } else {
        const int didx = c.xg * 4 + (c.gi - 14);
        float mu, rs;
        {
            const int u0 = hw * 112 + lane, u1 = hw * 112 + 64 + (lane < 48 ? lane : 47);
            u32 off[4] = {gxrow + (u32)(didx * 4 + (lane & 3)) * 16u, gm, GA + (u32)u0 * 16u, GA + (u32)u1 * 16u};
            u32x4 v[4];
            wait_units<4>(c, off, v);
            FST(23 + 20 * L);
            mu = __uint_as_float(v[1][0]); rs = __uint_as_float(v[1][2]);
            lds_st<u32x2>(FS + 1024 + (u32)u0 * 8u, (u32x2){v[2][0], v[2][2]});
            if (lane < 48) lds_st<u32x2>(FS + 1024 + (u32)u1 * 8u, (u32x2){v[3][0], v[3][2]});
            if (hw == 0 && lane < 4) lds_st<u32x2>(FS + (u32)lane * 8u, (u32x2){v[0][0], v[0][2]});
        }
        STAMP(L ? 26 : 14);
        FST(24 + 20 * L);
        f32x4 ay = {}, az = {};
#pragma unroll
        for (int s = 0; s < 14; s += 2) {
            const bf16x8 a0 = lds_ld<bf16x8>(FS + 1024 + (u32)((hw * 14 + s) * 64 + g16 * 16)), a1 = lds_ld<bf16x8>(FS + 1024 + (u32)((hw * 14 + s + 1) * 64 + g16 * 16));
            ay = MFMA16(a0, wf[s], ay); az = MFMA16(a1, wf[s + 1], az);
        }
        if (lane < 16) lds_st<float>(FS + 4608 + (u32)(hw * 16 + lane) * 4u, ay[0] + az[0]);
        FST(25 + 20 * L);
        helper_sync(c, hsync);
        FST(26 + 20 * L);
        if (hw == 0) {
            float y = 0.f;
#pragma unroll
            for (int k = 0; k < 4; ++k) y += lds_ld<float>(FS + 4608 + (u32)(k * 16 + i16) * 4u);
            const float x1 = __uint_as_float((u32)lds_ld<uint16_t>(FS + (u32)i16 * 2u) << 16);
            const float sig = rbf(__builtin_amdgcn_rcpf(1.0f + __expf(-rsel)));
            y = rbf(rbf(y) * sig);
            const float h = rbf((x1 - mu) * rs);
            const float x2 = rbf(x1 + rbf(h + y));
            const float a0 = __shfl(x2, (lane & 3) * 4), a1 = __shfl(x2, (lane & 3) * 4 + 1), a2 = __shfl(x2, (lane & 3) * 4 + 2), a3 = __shfl(x2, (lane & 3) * 4 + 3);
            if (L == 0) { if (lane < 4) stw(c.ws, OFF_GY + (u32)(didx * 4 + lane) * 16u, mk_unit(pk(a0, a1), pk(a2, a3), c.tag)); }
            else if (lane < 4) *(u32x2*)(p.out + (size_t)sel * 512 + didx * 16 + lane * 4) = (u32x2){pk(a0, a1), pk(a2, a3)};
            FST(27 + 20 * L);
        }
    }
}

DI void qkv_patch(const Ctx& c, u32& hsync, const u32x4 (&wf)[24], int sel) {
    const int lane = c.lane, hw = c.w - 4;
    constexpr u32 FS = FS0;
    const int g16 = lane >> 4, i16 = lane & 15;
    u32 off[2] = {OFF_GY + (u32)lane * 32u, OFF_GY + (u32)lane * 32u + 16u}; u32x4 v[2];
    wait_units<2>(c, off, v);
    float x[8], s = 0, q = 0;
#pragma unroll
    for (int k = 0; k < 2; ++k) { x[4 * k] = bflo(v[k][0]); x[4 * k + 1] = bfhi(v[k][0]); x[4 * k + 2] = bflo(v[k][2]); x[4 * k + 3] = bfhi(v[k][2]); }
#pragma unroll
    for (int k = 0; k < 8; ++k) { s += x[k]; q += x[k] * x[k]; }
    s = wave_sum(s); q = wave_sum(q);
    const float mu = s * (1.0f / 512.0f), var = fmaxf(q * (1.0f / 512.0f) - mu * mu, 0.f);
    const float A = __builtin_amdgcn_rsqf(var + 1e-5f);
    lds_st<u32x4>(FS + (u32)lane * 16u, (u32x4){pk((x[0] - mu) * A, (x[1] - mu) * A), pk((x[2] - mu) * A, (x[3] - mu) * A), pk((x[4] - mu) * A, (x[5] - mu) * A), pk((x[6] - mu) * A, (x[7] - mu) * A)});
    f32x4 acc[6] = {};
#pragma unroll
    for (int s_ = 0; s_ < 4; ++s_) {
        const bf16x8 a = lds_ld<bf16x8>(FS + (u32)((hw * 4 + s_) * 64 + g16 * 16));
#pragma unroll
        for (int ct = 0; ct < 6; ++ct) acc[ct] = MFMA16(a, __builtin_bit_cast(bf16x8, wf[s_ * 6 + ct]), acc[ct]);
    }
    if (lane < 16) {
#pragma unroll
        for (int ct = 0; ct < 6; ++ct) lds_st<float>(FS + 1024 + (u32)(hw * 96 + ct * 16 + lane) * 4u, acc[ct][0]);
    }
    helper_sync(c, hsync);
    if (hw == 0) {
#pragma unroll
        for (int k = 0; k < 2; ++k) {
            const int n = k * 64 + lane;
            if (n < 96) {
                float t = 0.f;
#pragma unroll
                for (int j = 0; j < 4; ++j) t += lds_ld<float>(FS + 1024 + (u32)(j * 96 + n) * 4u);
                lds_st<float>(FS + 4608 + (u32)n * 4u, n < 32 ? t * (0.25f * 1.4426950408889634f) : t);
            }
        }
        asm volatile("s_waitcnt lgkmcnt(0)" ::: "memory");
        const int hp = 8 * (c.xg >> 2) + (c.gi >> 2), T = sel >> 5, ts = sel & 31;
        if (lane < 8) {
            const int t = lane >> 2, a = (lane >> 1) & 1, h = lane & 1;
            const f32x4 p0 = lds_ld<f32x4>(FS + 4608 + (u32)(t * 32 + 16 * a + 4 * h) * 4u), p1 = lds_ld<f32x4>(FS + 4608 + (u32)(t * 32 + 16 * a + 8 + 4 * h) * 4u);
            stw(c.ws, (t ? OFF_K1 : OFF_Q1) + (u32)(((hp * 2 + a) * 64 + T) * 64 + h * 32 + ts) * 16u, not_poison((u32x4){pk(p0[0], p0[1]), pk(p0[2], p0[3]), pk(p1[0], p1[1]), pk(p1[2], p1[3])}));
        }
        if (lane < 32) {
            u32x4 vl = lds_ld<u32x4>(L_VL + (u32)lane * 16u);
            const u32 pv = pk(lds_ld<float>(FS + 4608 + (u32)(64 + lane) * 4u), 0.f) & 0xFFFFu;
            const int jj = (ts & 3) + 4 * ((ts >> 3) & 1);
#pragma unroll
            for (int k = 0; k < 4; ++k) if (k == (jj >> 1)) vl[k] = (jj & 1) ? ((vl[k] & 0xFFFFu) | (pv << 16)) : ((vl[k] & 0xFFFF0000u) | pv);
            stw(c.ws, OFF_V1 + (u32)(((hp * 2 + (lane >> 4)) * 64 + T) * 1024 + (ts >> 4) * 512 + ((ts >> 2) & 1) * 256 + (lane & 15) * 16), not_poison(vl));
        }
    }
}

DI void redo_row(const Ctx& c, u32& hsync, int head, int sel, u32 OQ, u32 OO) {
    const int lane = c.lane, s_ = c.w - 4;
    const int T = sel >> 5, ts = sel & 31, h = lane >> 5, key = lane & 31;
    const u32 qoff = OQ + (u32)((head * 64 + T) * 64 + h * 32 + ts) * 16u;
    u32x4 qv = ldw(c.ws, qoff);
    wait_piece(c, qoff, qv);
    const bf16x8 qrow = __builtin_bit_cast(bf16x8, qv);
    const f32x16 zero = {};
    float sc[8];
#pragma unroll
    for (int m = 0; m < 8; ++m) {
        const int t0 = s_ * 16 + 2 * m;
        const f32x16 d0 = MFMA32(qrow, lds_ld<bf16x8>(L_K + (u32)t0 * 1024u + (u32)lane * 16u), zero);
        const f32x16 d1 = MFMA32(qrow, lds_ld<bf16x8>(L_K + (u32)(t0 + 1) * 1024u + (u32)lane * 16u), zero);
        sc[m] = h ? d1[0] : d0[0];
    }
    float mx = sc[0];
#pragma unroll
    for (int m = 1; m < 8; ++m) mx = fmaxf(mx, sc[m]);
    {
        u32 k = __float_as_uint(mx); k = (k & 0x80000000u) ? ~k : (k | 0x80000000u);
        k = wave_umax(k);
        if (lane == 0) lds_st<u32>(L_RP + 4096u + (u32)s_ * 4u, k);
        helper_sync(c, hsync);
        const u32x4 all = lds_ld<u32x4>(L_RP + 4096u);
        k = max(max(all[0], all[1]), max(all[2], all[3]));
        mx = __uint_as_float((k & 0x80000000u) ? (k & 0x7FFFFFFFu) : ~k);
    }
    float l = 0.f;
    const u32 slot = (u32)((key >> 4) * 32 + ((key >> 2) & 1) * 16 + ((key & 3) + 4 * ((key >> 3) & 1)) * 2);
#pragma unroll
    for (int m = 0; m < 8; ++m) {
        const u32 pb = pk(__builtin_amdgcn_exp2f(sc[m] - mx), 0.f) & 0xFFFFu;
        l += bflo(pb);
        lds_st<uint16_t>(L_RP + (u32)(s_ * 16 + 2 * m + h) * 64u + slot, (uint16_t)pb);
    }
    l = wave_sum(l);
    asm volatile("s_waitcnt lgkmcnt(0)" ::: "memory");
    f32x4 acc = {};
#pragma unroll
    for (int t = 0; t < 16; ++t) {
        const int tile = s_ * 16 + t;
        acc = MFMA16(lds_ld<bf16x8>(L_V + (u32)tile * VSTR + (u32)lane * 16u), lds_ld<bf16x8>(L_RP + (u32)tile * 64u + (u32)(lane >> 4) * 16u), acc);
    }
    if ((lane & 15) == 0) lds_st<f32x4>(L_RP + 4160u + (u32)s_ * 80u + (u32)(lane >> 4) * 16u, acc);
    if (lane == 0) lds_st<float>(L_RP + 4160u + (u32)s_ * 80u + 64u, l);
    helper_sync(c, hsync);
    if (s_ == 0 && lane < 2) {
        float o[8], ls = 0.f;
#pragma unroll
        for (int j = 0; j < 8; ++j) o[j] = 0.f;
#pragma unroll
        for (int k = 0; k < 4; ++k) {
            const f32x4 a = lds_ld<f32x4>(L_RP + 4160u + (u32)k * 80u + (u32)lane * 16u), b_ = lds_ld<f32x4>(L_RP + 4160u + (u32)k * 80u + 32u + (u32)lane * 16u);
#pragma unroll
            for (int j = 0; j < 4; ++j) { o[j] += a[j]; o[4 + j] += b_[j]; }
            ls += lds_ld<float>(L_RP + 4160u + (u32)k * 80u + 64u);
        }
        const float rl = 1.0f / ls;
        stw(c.ws, OO + (u32)((head * 64 + T) * 64 + lane * 32 + ts) * 16u, not_poison((u32x4){pk(o[0] * rl, o[1] * rl), pk(o[2] * rl, o[3] * rl), pk(o[4] * rl, o[5] * rl), pk(o[6] * rl, o[7] * rl)}));
    }
}

DI u32x4 gld(const void* base, u32 off) { return *(const u32x4*)((const char*)base + off); }
struct FfSrc { const uint16_t* m; u32 vo, step; bool on, gu; };
DI FfSrc ff_src(const Ctx& c, const Params& p) {
    int lane = c.lane; asm volatile("" : "+v"(lane));
    FfSrc f; const int s = c.w - 4;
    f.on = c.gi < 18; f.gu = c.gi < 14;
    f.m = f.gu ? (s < 2 ? p.wg : p.wu) : p.wd;
    const u32 rowb = f.gu ? 3584u : 1024u, col = f.gu ? (u32)(c.xg * 14 + c.gi) * 32u : (u32)(c.xg * 4 + (c.gi - 14)) * 32u;
    const u32 first = f.gu ? (u32)(s & 1) * 8u : (u32)s * 14u;
    f.step = 32u * rowb;
    f.vo = (u32)(lane >> 1) * rowb + col + (u32)(lane & 1) * 16u + first * f.step;
    return f;
}
DI void keep_wq(const Ctx& c, u32x4 (&pq)[24]) {
#pragma unroll
    for (int j = 0; j < 12; ++j) pq[j] = lds_ld<u32x4>(L_W + (u32)(c.w * 12 + j) * 1024u + (u32)c.lane * 16u);
}
DI void pre_store_wq(const Ctx& c, const u32x4 (&pq)[24]) {
#pragma unroll
    for (int j = 0; j < 12; ++j) lds_st<u32x4>(L_W + (u32)(c.w * 12 + j) * 1024u + (u32)c.lane * 16u, pq[j]);
}
DI void pre_store_ff(const Ctx& c, const u32x4 (&pf)[14]) {
    if (c.gi < 18) {
        const bool gu = c.gi < 14;
        const u32 a = FW0 + (u32)(c.w - 4) * (gu ? 8192u : 14336u) + (u32)c.lane * 16u;
#pragma unroll
        for (int j = 0; j < 14; ++j) if (j < 8 || !gu) lds_st<u32x4>(a + (u32)j * 1024u, pf[j]);
    }
}
DI u32 wo_lane_off(const Ctx& c) {
    int lane = c.lane; asm volatile("" : "+v"(lane));
    const int s = c.w - 4;
    return (u32)((lane >> 2) * 1024 + (c.gi & 7) * 128 + (lane & 3) * 16 + (s >> 1) * 64 + (s & 1) * 16 * 16384);
}
DI void pre_store_wo(const Ctx& c, const u32x4 (&po)[16]) {
    const int s = c.w - 4;
#pragma unroll
    for (int j = 0; j < 16; ++j) lds_st<u32x4>(L_WO + (u32)(s >> 1) * WO_FT + (u32)((s & 1) * 16 + j) * 1024u + (u32)c.lane * 16u, po[j]);
}

struct WFrag { bf16x8 q, k, v; };
template <u32 TR2> DI void wfrag_load(WFrag& f, u32 a01, u32 a2) { f.q = frag_tr(a01, a01 + TR2); f.k = frag_tr(a01 + 32768, a01 + 32768 + TR2); f.v = frag_tr(a2, a2 + TR2); }
template <u32 TR2> DI void qstep(f32x16& aq, f32x16& ak, f32x16& av, bf16x8 x, WFrag& cur, WFrag& nxt, u32 a01, u32 a2, bool more) {
    if (more) nxt.q = frag_tr(a01, a01 + TR2);
    SBAR(); aq = MFMA32(cur.q, x, aq); SBAR();
    if (more) nxt.k = frag_tr(a01 + 32768, a01 + 32768 + TR2);
    SBAR(); ak = MFMA32(cur.k, x, ak); SBAR();
    if (more) nxt.v = frag_tr(a2, a2 + TR2);
    SBAR(); av = MFMA32(x, cur.v, av); SBAR();
}

template <int L>
DI void phaseQ(Ctx& c0, const Params& p, u32x4 (&pq)[24]) {
    Ctx c = c0; FRESH(c);
    const int lane = c.lane, w = c.w;
    const int rb = 4 * (c.xg & 3) + (c.gi & 3), hp = 8 * (c.xg >> 2) + (c.gi >> 2);
    const int r = lane & 31, h = lane >> 5;
    const int i16 = lane & 15, blk = (lane >> 4) & 1;
    const u32 trA = L_W + (u32)(((L == 0 ? 8 : 4) * h + (i16 >> 2)) * 64 + blk * 32 + (i16 & 3) * 8);
    constexpr u32 TR2 = L == 0 ? 256 : 512;
    constexpr u32 OQ = L ? OFF_Q1 : OFF_Q0, OK_ = L ? OFF_K1 : OFF_K0, OV = L ? OFF_V1 : OFF_V0, TS = L ? OFF_TS1 : OFF_TS0;
    const int rot = ((c.gi >> 2) + 2 * (c.gi & 3)) & 7;

    if (w >= 4) {
        const int hw = w - 4;
        if (L == 0) {
            u32x4 r0, r1; u32 ep;
            asm volatile("global_load_dword %0, %1, %2 sc1" : "=v"(ep) : "v"(0u), "s"(p.ws));
            const u32 tok = (u32)(rb * 128 + hp * 8 + hw * 2);
            const u32 vo = tok * 1024u + (u32)lane * 16u;
            asm volatile("global_load_dwordx4 %0, %1, %2" : "=v"(r0) : "v"(vo), "s"(p.x0));
            asm volatile("global_load_dwordx4 %0, %1, %2 offset:1024" : "=v"(r1) : "v"(vo), "s"(p.x0));
            const u32 wv = (u32)((lane >> 2) * 3072 + hp * 64 + (lane & 3) * 16 + hw * 16 * 3072);
            u32 xv[4];
#pragma unroll
            for (int q = 0; q < 4; ++q) { const int row = 8 * q + (lane >> 3); xv[q] = (u32)(((rb * 4 + hw) * 32 + row) * 1024 + (((lane & 7) ^ ((row >> 1) & 7)) * 16)); }
#define WD(i) do { const u32 kc_ = (u32)(((i) + rot) & 7); _Pragma("unroll") for (int t = 0; t < 3; ++t) \
                glds(p.wqkv, wv + (u32)t * 1024u + kc_ * (64u * 3072u), L_W + (u32)t * 32768u + (kc_ * 64u + (u32)hw * 16u) * 64u); } while (0)
#define XD(i) do { const u32 kc_ = (u32)(((i) + rot) & 7); _Pragma("unroll") for (int q = 0; q < 4; ++q) \
                glds(p.x0, xv[q] + kc_ * 128u, L_XR + (u32)((i) % 3) * 16384u + (u32)hw * 4096u + (u32)q * 1024u); } while (0)
#define QWAIT(n) asm volatile("s_waitcnt vmcnt(" #n ")\n\ts_barrier" ::: "memory")
#define CS(i) if (hw < 3) { _Pragma("unroll") for (int s_ = 0; s_ < 4; ++s_) { \
                const u32 a_ = trA + (u32)hw * 32768u + (u32)((((i) + rot) & 7) * 4 + s_) * 1024u; cs = MFMA32(frag_tr(a_, a_ + TR2), ones, cs); } }
            WD(0); XD(0); WD(1); XD(1); WD(2); XD(2); WD(3);
            FST(1);
            f32x16 cs = {};
            const bf16x8 ones = __builtin_bit_cast(bf16x8, (u32x4){0x3f803f80u, 0x3f803f80u, 0x3f803f80u, 0x3f803f80u});
            asm volatile("s_waitcnt vmcnt(17)\n\ts_barrier" : "+v"(r0), "+v"(r1), "+v"(ep) :: "memory");
            c.tag = epoch_tag(ep); c0.tag = c.tag;
            FST(2);
            {
                float s0 = 0, q0 = 0, s1 = 0, q1 = 0;
#pragma unroll
                for (int j = 0; j < 4; ++j) {
                    float a = bflo(r0[j]), b_ = bfhi(r0[j]); s0 += a + b_; q0 += a * a + b_ * b_;
                    float d = bflo(r1[j]), e = bfhi(r1[j]); s1 += d + e; q1 += d * d + e * e;
                }
                s0 = wave_sum(s0); q0 = wave_sum(q0); s1 = wave_sum(s1); q1 = wave_sum(q1);
                const float s = lane ? s1 : s0, q = lane ? q1 : q0;
                const float mu = s * (1.0f / 512.0f), var = fmaxf(q * (1.0f / 512.0f) - mu * mu, 0.f);
                const float A = __builtin_amdgcn_rsqf(var + 1e-5f);
                if (lane < 2) stw(c.ws, TS + (tok + (u32)lane) * 16u, mk_unit(__float_as_uint(A), __float_as_uint(A * mu), c.tag));
            }
            CS(0);
            QWAIT(11); FST(3); XD(3); WD(4); CS(1);
            QWAIT(11); FST(4); XD(4); WD(5); CS(2);
            QWAIT(10); FST(5); XD(5); WD(6); CS(3);
            QWAIT(10); FST(6); XD(6); WD(7); CS(4);
            QWAIT(10); FST(7); XD(7); CS(5);
            QWAIT(4); FST(8); CS(6); CS(7);
            if (hw < 3 && r == 0) {
#pragma unroll
                for (int g = 0; g < 4; ++g) lds_st<f32x4>(P_CS + (u32)(hw * 32 + 8 * g + 4 * h) * 4u, (f32x4){cs[4 * g], cs[4 * g + 1], cs[4 * g + 2], cs[4 * g + 3]});
            }
            asm volatile("s_waitcnt vmcnt(0) lgkmcnt(0)\n\ts_barrier" ::: "memory");
            STAMP(1);
#undef WD
#undef XD
#undef QWAIT
#undef CS
        }
        if (L == 0) { DRAIN(); BAR(); STAMP(3); fresh(pq); keep_wq(c, pq); }
    } else {
        const int T = rb * 4 + w;
        f32x16 aq = {}, ak = {}, av = {};
        float A, B;
        if (L == 0) {
            const u32 ep = __builtin_amdgcn_raw_buffer_load_b32(c.ws, OFF_EPOCH, 0, 16);
            u32 xa[4];
#pragma unroll
            for (int s_ = 0; s_ < 4; ++s_) xa[s_] = L_XR + (u32)w * 4096u + (u32)(r * 128 + (((2 * s_ + h) ^ ((r >> 1) & 7)) * 16));
            bf16x8 xr[2][4];
            u32x4 tsv;
            WFrag f0, f1;
            BAR();
            FST(2);
#pragma unroll
            for (int s_ = 0; s_ < 4; ++s_) xr[0][s_] = lds_ld<bf16x8>(xa[s_]);
            { const u32 a01 = trA + (u32)(rot & 7) * 4096u; wfrag_load<TR2>(f0, a01, a01 + 65536u); }
#pragma unroll
            for (int i = 0; i < 8; ++i) {
                const u32 a01 = trA + (u32)((i + rot) & 7) * 4096u, a2 = a01 + 65536u;
                const u32 n01 = trA + (u32)((i + 1 + rot) & 7) * 4096u, n2 = n01 + 65536u;
                qstep<TR2>(aq, ak, av, xr[i & 1][0], f0, f1, a01 + 1024, a2 + 1024, true);
                qstep<TR2>(aq, ak, av, xr[i & 1][1], f1, f0, a01 + 2048, a2 + 2048, true);
                qstep<TR2>(aq, ak, av, xr[i & 1][2], f0, f1, a01 + 3072, a2 + 3072, true);
                {
                    const u32x4 pois = {POISON, POISON, POISON, POISON};
                    const u32 oo = (u32)(((4 * c.xg + (c.gi >> 3)) * 64 + (c.gi & 7) * 8 + 2 * w) * 64 + lane) * 16u;
                    const u32 oq = (u32)((hp * 2 * 64 + T) * 64 + lane) * 16u;
                    if (i == 0) { stw(c.ws, OFF_O0 + oo, pois); stw(c.ws, OFF_O0 + oo + 1024u, pois); }
                    if (i == 1) { stw(c.ws, OFF_Q0 + oq, pois); stw(c.ws, OFF_Q0 + oq + 65536u, pois); }
                    if (i == 2 && rb < 6) { stw(c.ws, OFF_K0 + oq, pois); stw(c.ws, OFF_K0 + oq + 65536u, pois); }
                    if (i == 3 && rb < 6) { stw(c.ws, OFF_V0 + oq, pois); stw(c.ws, OFF_V0 + oq + 65536u, pois); }
                    if (i == 4) { stw(c.ws, OFF_O1 + oo, pois); stw(c.ws, OFF_O1 + oo + 1024u, pois); }
                    if (i == 5 && w == 0) {
                        const u32 o = (u32)(((4 * c.xg + (c.gi >> 3)) * 8 + (c.gi & 7)) * 1024 + lane * 16);
                        stw(c.ws, OFF_GS0 + o, (u32x4){0u, 0u, 0u, 0u}); stw(c.ws, OFF_GS1 + o, (u32x4){0u, 0u, 0u, 0u});
                    }
                    if (i == 7) { DRAIN(); lds_signal_add(c, P_PD); }
                }
                if (i < 7) {
                    asm volatile("s_barrier" ::: "memory");
                    FST(3 + i);
#pragma unroll
                    for (int s_ = 0; s_ < 4; ++s_) xr[(i + 1) & 1][s_] = lds_ld<bf16x8>(xa[s_] + (u32)((i + 1) % 3) * 16384u);
                }
                if (i == 6) tsv = ldw(c.ws, TS + (u32)(T * 32 + r) * 16u);
                qstep<TR2>(aq, ak, av, xr[i & 1][3], f1, f0, n01, n2, i < 7);
            }
            FST(10);
            STAMP(1);
            c.tag = epoch_tag(ep); c0.tag = c.tag;
            if (w == 0) { lds_wait_ge(P_PD, 4u); if (lane == 0) set_flag(c, OFF_FP + (u32)(hp * 16 + rb) * 128u); }
            if (w == 3) {
                const u32x4 v = *(const u32x4*)(p.wr + lane * 8); float s = 0;
#pragma unroll
                for (int j = 0; j < 4; ++j) s += bflo(v[j]) + bfhi(v[j]);
                s = wave_sum(s);
                if (lane == 0) lds_st<float>(P_WSUM, s);
            }
            u32 off[1] = {TS + (u32)(T * 32 + r) * 16u}; u32x4 v[1] = {tsv};
            if (!__all(unit_ok(tsv, c.tag))) wait_units<1>(c, off, v);
            A = __uint_as_float(v[0][0]); B = __uint_as_float(v[0][2]);
            STAMP(2);
        } else {
            wait_flags(c, OFF_FO + (u32)(rb * 16 + (lane & 15)) * 128u);
            STAMP(13);
            u32x4 xf[32];
            const u32 xo = OFF_X1 + (u32)T * 32768u + (u32)lane * 16u;
#pragma unroll
            for (int s = 0; s < 32; ++s) xf[s] = ldw(c.ws, xo + (u32)s * 1024u);
            const u32x4 tsv = ldw(c.ws, TS + (u32)(T * 32 + r) * 16u);
            lds_wait_ge(P_WQ, 8u);
            STAMP(14);
            {
                u32 a01 = trA, a2 = trA + 65536u;
                asm volatile("" : "+v"(a01), "+v"(a2));
                WFrag f0, f1;
                wfrag_load<TR2>(f0, a01, a2);
#pragma unroll
                for (int st = 0; st < 32; st += 2) {
                    qstep<TR2>(aq, ak, av, __builtin_bit_cast(bf16x8, xf[st]), f0, f1, a01 + (u32)(st + 1) * 1024u, a2 + (u32)(st + 1) * 1024u, true);
                    qstep<TR2>(aq, ak, av, __builtin_bit_cast(bf16x8, xf[st + 1]), f1, f0, a01 + (u32)(st + 2) * 1024u, a2 + (u32)(st + 2) * 1024u, st + 2 < 32);
                }
            }
            u32 off[1] = {TS + (u32)(T * 32 + r) * 16u}; u32x4 v[1] = {tsv};
            if (!__all(unit_ok(tsv, c.tag))) wait_units<1>(c, off, v);
            A = __uint_as_float(v[0][0]); B = __uint_as_float(v[0][2]);
        }
        if (L == 1) STAMP(15);
        int ts = -1;
        if (L == 1) {
            wait_sel(0);
            const int sel = __builtin_amdgcn_readfirstlane((int)lds_ld<u32>(P_SEL)) & 2047;
            if ((sel >> 5) == T) ts = sel & 31;
        }
        const u32x4 pois = {POISON, POISON, POISON, POISON};
        const u32 tokl = L_TOK + (u32)w * 256u;
        if (h == 0) lds_st<f32x2>(tokl + (u32)r * 8u, (f32x2){A, B});
        constexpr float QS = 0.25f * 1.4426950408889634f;
#pragma unroll
        for (int t = 0; t < 2; ++t) {
            const f32x16& acc = t ? ak : aq;
            const float sA = t ? A : A * QS, sB = t ? B : B * QS;
            float o[16];
#pragma unroll
            for (int g = 0; g < 4; ++g) {
                const f32x4 cs = lds_ld<f32x4>(P_CS + (u32)(t * 32 + 8 * g + 4 * h) * 4u);
#pragma unroll
                for (int e = 0; e < 4; ++e) o[4 * g + e] = sA * acc[4 * g + e] - sB * cs[e];
            }
#pragma unroll
            for (int a = 0; a < 2; ++a) {
                u32x4 v = not_poison((u32x4){pk(o[8 * a], o[8 * a + 1]), pk(o[8 * a + 2], o[8 * a + 3]), pk(o[8 * a + 4], o[8 * a + 5]), pk(o[8 * a + 6], o[8 * a + 7])});
                if (L == 1 && r == ts) v = pois;
                stw(c.ws, (t ? OK_ : OQ) + (u32)(((hp * 2 + a) * 64 + T) * 64 + lane) * 16u, v);
            }
        }
        u32x4 vlate = pois;
        const u32 vbase = OV + (u32)(((hp * 2 + (r >> 4)) * 64 + T) * 1024 + h * 256 + (r & 15) * 16);
        {
            asm volatile("s_waitcnt lgkmcnt(0)" ::: "memory");
            const float cs = lds_ld<float>(P_CS + (u32)(64 + r) * 4u);
            float o[16];
#pragma unroll
            for (int g = 0; g < 4; ++g) {
                const f32x4 ab0 = lds_ld<f32x4>(tokl + (u32)(8 * g + 4 * h) * 8u), ab1 = lds_ld<f32x4>(tokl + (u32)(8 * g + 4 * h + 2) * 8u);
                o[4 * g] = ab0[0] * av[4 * g] - ab0[1] * cs; o[4 * g + 1] = ab0[2] * av[4 * g + 1] - ab0[3] * cs;
                o[4 * g + 2] = ab1[0] * av[4 * g + 2] - ab1[1] * cs; o[4 * g + 3] = ab1[2] * av[4 * g + 3] - ab1[3] * cs;
            }
#pragma unroll
            for (int s = 0; s < 2; ++s) {
                u32x4 v = not_poison((u32x4){pk(o[8 * s], o[8 * s + 1]), pk(o[8 * s + 2], o[8 * s + 3]), pk(o[8 * s + 4], o[8 * s + 5]), pk(o[8 * s + 6], o[8 * s + 7])});
                if (L == 1 && ts >= 0 && s == (ts >> 4) && h == ((ts >> 2) & 1)) { vlate = v; v = pois; }
                stw(c.ws, vbase + (u32)s * 512u, v);
            }
        }
        if (L == 1 && ts >= 0 && h == ((ts >> 2) & 1)) lds_st<u32x4>(L_VL + (u32)r * 16u, vlate);
        if (L == 0) {
            DRAIN(); BAR();
            if (w == 0 && lane == 0) set_flag(c, OFF_FQ0 + (u32)(hp * 16 + rb) * 128u);
        } else {
            lds_signal_add(c, P_QD); lds_wait_ge(P_QD, 4u);
        }
        STAMP(L ? 16 : 3);
        fresh(pq);
        if (L == 0) keep_wq(c, pq);
    }
}

struct AState { f32x16 Sa, Sb, Oa, Ob; bf16x8 kC, kN, v0, v1; u32x4 pb1; float e0, e1; };
#define EXP2(x) __builtin_amdgcn_exp2f(x)
#define BC8(x) __builtin_bit_cast(bf16x8, x)
#define PVM(O, v, p) O = MFMA32(v, BC8(p), O)
#define X(stmt) stmt; SBAR()

template <bool USE_C, class HOOK>
DI void attn_step(AState& s, bf16x8 qa, bf16x8 qb, const f32x16& ca, const f32x16& cb, u32 kp, u32 vp0, u32 vp1, u32 knext, u32 voff, HOOK hook) {
    const f32x16 zero = {};
    u32x4 pa0, pa1, pb0;
    const float a0 = s.e0, a1 = s.e1;
    X(s.kN = lds_ld<bf16x8>(kp + knext));
    X(s.v0 = lds_ld<bf16x8>(vp0 + voff));
    X(PVM(s.Ob, s.v1, s.pb1));
    X(s.v1 = lds_ld<bf16x8>(vp1 + voff));
    X(float a2 = EXP2(s.Sa[2])); X(float a3 = EXP2(s.Sa[3])); X(float a4 = EXP2(s.Sa[4])); X(float a5 = EXP2(s.Sa[5]));
    X(s.Sb = MFMA32(s.kC, qb, USE_C ? cb : zero));
    X(pa0[0] = pk(a0, a1)); X(float a6 = EXP2(s.Sa[6])); X(pa0[1] = pk(a2, a3)); X(float a7 = EXP2(s.Sa[7])); X(float a8 = EXP2(s.Sa[8]));
    X(pa0[2] = pk(a4, a5)); X(float a9 = EXP2(s.Sa[9])); X(pa0[3] = pk(a6, a7));
    asm volatile("" : "+v"(s.v0), "+v"(s.v1), "+v"(s.kN)); SBAR();
    X(float a10 = EXP2(s.Sa[10]));
    X(PVM(s.Oa, s.v0, pa0));
    X(float a11 = EXP2(s.Sa[11])); X(float a12 = EXP2(s.Sa[12])); X(float a13 = EXP2(s.Sa[13])); X(float a14 = EXP2(s.Sa[14])); X(float a15 = EXP2(s.Sa[15]));
    X(float b0 = EXP2(s.Sb[0])); X(pa1[0] = pk(a8, a9)); X(float b1 = EXP2(s.Sb[1])); X(pa1[1] = pk(a10, a11));
    X(float b2 = EXP2(s.Sb[2])); X(pa1[2] = pk(a12, a13)); X(float b3 = EXP2(s.Sb[3])); X(pa1[3] = pk(a14, a15));
    X(float b4 = EXP2(s.Sb[4])); X(float b5 = EXP2(s.Sb[5]));
    X(PVM(s.Oa, s.v1, pa1));
    hook(); SBAR();
    X(float b6 = EXP2(s.Sb[6])); X(float b7 = EXP2(s.Sb[7])); X(float b8 = EXP2(s.Sb[8])); X(float b9 = EXP2(s.Sb[9]));
    X(s.Sa = MFMA32(s.kN, qa, USE_C ? ca : zero));
    X(pb0[0] = pk(b0, b1)); X(pb0[1] = pk(b2, b3)); X(pb0[2] = pk(b4, b5)); X(pb0[3] = pk(b6, b7));
    X(float b10 = EXP2(s.Sb[10])); X(float b11 = EXP2(s.Sb[11]));
    X(PVM(s.Ob, s.v0, pb0));
    X(float b12 = EXP2(s.Sb[12])); X(float b13 = EXP2(s.Sb[13])); X(float b14 = EXP2(s.Sb[14])); X(float b15 = EXP2(s.Sb[15]));
    X(s.pb1[0] = pk(b8, b9)); X(s.e0 = EXP2(s.Sa[0])); X(s.pb1[1] = pk(b10, b11)); X(s.e1 = EXP2(s.Sa[1]));
    X(s.pb1[2] = pk(b12, b13)); X(s.pb1[3] = pk(b14, b15));
    s.kC = s.kN;
}
struct NoHook { DI void operator()() const {} };
template <bool USE_C>
DI void attn_chunk(AState& s, bf16x8 qa, bf16x8 qb, const f32x16& ca, const f32x16& cb, u32 kp, u32 vp0, u32 vp1) {
    attn_step<USE_C>(s, qa, qb, ca, cb, kp, vp0, vp1, 1024, 0, NoHook());
    attn_step<USE_C>(s, qa, qb, ca, cb, kp, vp0, vp1, 2048, VSTR, NoHook());
    attn_step<USE_C>(s, qa, qb, ca, cb, kp, vp0, vp1, 3072, 2 * VSTR, NoHook());
    attn_step<USE_C>(s, qa, qb, ca, cb, kp, vp0, vp1, 8192, 3 * VSTR, NoHook());
}
DI void attn_begin(AState& s, bf16x8 qa, const f32x16& ca, u32 kp) {
    const f32x16 zero = {};
    s.Oa = zero; s.Ob = zero; s.pb1 = (u32x4){0u, 0u, 0u, 0u}; s.v1 = BC8(s.pb1);
    s.kC = lds_ld<bf16x8>(kp);
    s.Sa = MFMA32(s.kC, qa, ca);
    s.e0 = EXP2(s.Sa[0]); s.e1 = EXP2(s.Sa[1]);
}
template <bool USE_C>
DI void attn_chunk8(AState& s, bf16x8 qa, bf16x8 qb, const f32x16& ca, const f32x16& cb, u32 kp, u32 vp0, u32 vp1, bool bar) {
    attn_step<USE_C>(s, qa, qb, ca, cb, kp, vp0, vp1, 1024, 0, NoHook());
    attn_step<USE_C>(s, qa, qb, ca, cb, kp, vp0, vp1, 2048, VSTR, NoHook());
    attn_step<USE_C>(s, qa, qb, ca, cb, kp, vp0, vp1, 3072, 2 * VSTR, NoHook());
    attn_step<USE_C>(s, qa, qb, ca, cb, kp, vp0, vp1, 4096, 3 * VSTR, NoHook());
    attn_step<USE_C>(s, qa, qb, ca, cb, kp, vp0, vp1, 5120, 4 * VSTR, NoHook());
    attn_step<USE_C>(s, qa, qb, ca, cb, kp, vp0, vp1, 6144, 5 * VSTR, NoHook());
    attn_step<USE_C>(s, qa, qb, ca, cb, kp, vp0, vp1, 7168, 6 * VSTR, NoHook());
    if (bar) BAR();
    attn_step<USE_C>(s, qa, qb, ca, cb, kp, vp0, vp1, 8192, 7 * VSTR, NoHook());
}

DI void phaseA(const Ctx& c0, const Params& p, u32& hsync, const u32x4 (&wp)[24], const int L) {
    Ctx c = c0; FRESH(c);
    const int lane = c.lane, w = c.w;
    const int head = 4 * c.xg + (c.gi >> 3), qb = c.gi & 7;
    const u32 OQ = L ? OFF_Q1 : OFF_Q0, OK_ = L ? OFF_K1 : OFF_K0, OV = L ? OFF_V1 : OFF_V0, OO = L ? OFF_O1 : OFF_O0;
    int sel = 0; u32 rot8 = 0;
    if (L == 1) {
        sel = __builtin_amdgcn_readfirstlane((int)lds_ld<u32>(P_SEL)) & 2047;
        rot8 = (u32)(sel >> 8) + 1u;
        lds_wait_ge(P_FQ, 2u);
        if (w < 4) lds_wait_ge(P_WF, (sel >> 7) == 4 * (c.xg & 3) + (c.gi & 3) ? 8u : 4u);
    }
    if (L == 0 && w == 1) {
        const int qo_ = (4 * c.xg + (c.gi >> 3)) >> 2, hd = lane & 31, xg = hd >> 2, gi = (hd & 3) * 8 + qo_;
        u32 idx = (u32)((8 * (xg >> 2) + (gi >> 2)) * 16 + 4 * (xg & 3) + (gi & 3));
        if (lane >= 32) idx = (u32)((head >> 1) * 16 + (lane < 38 ? lane - 32 : lane < 40 ? 2 * qb + lane - 38 : 0));
        wait_flags(c, OFF_FP + idx * 128u);
    }
    if (L == 0) BAR();
    STAMP(L ? 17 : 4);
#define GCH(pos) ((((u32)(pos) + rot8) & 7u) * 8192u)
    const u32 koff = OK_ + (u32)((head * 64 + 2 * (w & 3)) * 64 + lane) * 16u, voff = OV + (u32)((head * 64 + 2 * (w & 3)) * 64 + lane) * 16u;
    const u32 kw = L_K + (u32)(2 * (w & 3)) * 1024u + (u32)lane * 16u, vw = L_V + (u32)(2 * (w & 3)) * VSTR + (u32)lane * 16u;
    if (w >= 4) {
        __builtin_amdgcn_s_setprio(2);
#define PACE() __builtin_amdgcn_s_sleep(2)
        lds_wait_ge(P_KV, 4u * (u32)(L + 1));
        {
            u32x4 rk[4], rv[4]; u32 n = 0;
            for (;;) {
#pragma unroll
                for (int q = 0; q < 4; ++q) { const u32 o = GCH(1 + (q >> 1)) + (u32)(q & 1) * 1024u; rk[q] = ldw(c.ws, koff + o); rv[q] = ldw(c.ws, voff + o); }
                MEMFENCE();
                bool bad = false;
#pragma unroll
                for (int q = 0; q < 4; ++q) bad = bad || poisoned(rk[q]) || poisoned(rv[q]);
                if (!__any(bad)) break;
                if (spin_fail(c, n)) break;
            }
#pragma unroll
            for (int q = 0; q < 4; ++q) {
                lds_st<u32x4>(kw + (u32)(1 + (q >> 1)) * 8192u + (u32)(q & 1) * 1024u, rk[q]);
                lds_st<u32x4>(vw + (u32)((1 + (q >> 1)) * 8 + (q & 1)) * VSTR, rv[q]);
            }
            BAR();
        }
        if (L == 0) {
            if (w == 4) { wait_flags(c, OFF_FQ0 + (u32)((head >> 1) * 16 + (lane & 15)) * 128u); lds_signal_add(c, P_FQ); }
            else lds_wait_ge(P_FQ, 1u);
        }
        {
            u32x4 rk[8], rv[8]; u32 n = 0;
            for (;;) {
#pragma unroll
                for (int q = 0; q < 8; ++q) { const u32 o = GCH(3 + (q >> 1)) + (u32)(q & 1) * 1024u; rk[q] = ldw(c.ws, koff + o); rv[q] = ldw(c.ws, voff + o); }
                MEMFENCE();
                bool bad = false;
#pragma unroll
                for (int q = 0; q < 8; ++q) bad = bad || poisoned(rk[q]) || poisoned(rv[q]);
                if (!__any(bad)) break;
                if (spin_fail(c, n)) break;
            }
#pragma unroll
            for (int q = 0; q < 8; ++q) {
                lds_st<u32x4>(kw + (u32)(3 + (q >> 1)) * 8192u + (u32)(q & 1) * 1024u, rk[q]); PACE();
                lds_st<u32x4>(vw + (u32)((3 + (q >> 1)) * 8 + (q & 1)) * VSTR, rv[q]); PACE();
            }
            BAR();
        }
        if (L == 0) {
            const int rb = 4 * (c.xg & 3) + (c.gi & 3), hp = 8 * (c.xg >> 2) + (c.gi >> 2);
            const u32x4 pois = {POISON, POISON, POISON, POISON};
            const u32 oq = (u32)((hp * 2 * 64 + rb * 4 + (w - 4)) * 64 + lane) * 16u;
#pragma unroll
            for (int a = 0; a < 2; ++a) { stw(c.ws, OFF_Q1 + oq + (u32)a * 65536u, pois); stw(c.ws, OFF_K1 + oq + (u32)a * 65536u, pois); stw(c.ws, OFF_V1 + oq + (u32)a * 65536u, pois); }
            DRAIN();
            helper_sync(c, hsync);
            if (w == 4 && lane == 0) set_flag(c, OFF_FP2 + (u32)(c.xg + 8 * c.gi) * 128u);
        }
        if (L == 1 && (sel >> 7) == 4 * (c.xg & 3) + (c.gi & 3)) qkv_patch(c, hsync, wp, sel);
        {
            u32x4 rk[2], rv[2]; u32 n = 0;
            for (;;) {
#pragma unroll
                for (int q = 0; q < 2; ++q) { rk[q] = ldw(c.ws, koff + GCH(7) + (u32)q * 1024u); rv[q] = ldw(c.ws, voff + GCH(7) + (u32)q * 1024u); }
                MEMFENCE();
                if (!__any(poisoned(rk[0]) || poisoned(rv[0]) || poisoned(rk[1]) || poisoned(rv[1]))) break;
                if (spin_fail(c, n)) break;
            }
#pragma unroll
            for (int q = 0; q < 2; ++q) { lds_st<u32x4>(kw + 7u * 8192u + (u32)q * 1024u, rk[q]); PACE(); lds_st<u32x4>(vw + (u32)(56 + q) * VSTR, rv[q]); PACE(); }
        }
        if (L == 1 && (sel >> 8) == qb) { helper_sync(c, hsync); redo_row(c, hsync, head, sel, OQ, OO); }
        u32x4 po[16], pf[14] = {};
        { const u32 wv = wo_lane_off(c);
#pragma unroll
          for (int q = 0; q < 16; ++q) po[q] = gld(p.wo, wv + (u32)q * 16384u); }
        { const FfSrc ff = ff_src(c, p);
#pragma unroll
          for (int q = 0; q < 14; ++q) if (ff.on && (q < 8 || !ff.gu)) pf[q] = gld(ff.m, ff.vo + (u32)q * ff.step); }
        BAR();
        {
            const int s_ = c.w - 4;
#pragma unroll
            for (int q = 0; q < 16; ++q) { lds_st<u32x4>(L_WO + (u32)(s_ >> 1) * WO_FT + (u32)((s_ & 1) * 16 + q) * 1024u + (u32)c.lane * 16u, po[q]); PACE(); }
        }
        BAR();
        pre_store_ff(c, pf);
#undef PACE
        STAMP(L ? 20 : 7);
        return;
    }
    const u32 qo = OQ + (u32)((head * 64 + qb * 8 + 2 * w) * 64 + lane) * 16u;
    u32x4 qar, qbr;
    {
        u32x4 rk[2], rv[2];
        u32 n = 0;
        for (;;) {
#pragma unroll
            for (int q = 0; q < 2; ++q) { rk[q] = ldw(c.ws, koff + GCH(0) + (u32)q * 1024u); rv[q] = ldw(c.ws, voff + GCH(0) + (u32)q * 1024u); }
            qar = ldw(c.ws, qo); qbr = ldw(c.ws, qo + 1024u);
            MEMFENCE();
            if (L == 1 && (lane & 31) == (sel & 31)) {
                if (qb * 8 + 2 * w == (sel >> 5)) qar = (u32x4){0u, 0u, 0u, 0u};
                if (qb * 8 + 2 * w + 1 == (sel >> 5)) qbr = (u32x4){0u, 0u, 0u, 0u};
            }
            const bool bad = poisoned(qar) || poisoned(qbr) || poisoned(rk[0]) || poisoned(rv[0]) || poisoned(rk[1]) || poisoned(rv[1]);
            if (!__any(bad)) break;
            if (spin_fail(c, n)) break;
        }
#pragma unroll
        for (int k = 0; k < 2; ++k) { const int id = (w * 2 + k) * 64 + lane, t = id >> 3, s_ = id & 7; const u32 val = (s_ & 3) == 0 ? 0x3f803f80u : 0u;
          lds_st<u32x4>(L_V + (u32)t * VSTR + 1024u + (u32)s_ * 16u, (u32x4){val, val, val, val}); }
#pragma unroll
        for (int q = 0; q < 2; ++q) { lds_st<u32x4>(kw + (u32)q * 1024u, rk[q]); lds_st<u32x4>(vw + (u32)q * VSTR, rv[q]); }
        lds_signal_add(c, P_KV);
        lds_wait_ge(P_KV, 4u * (u32)(L + 1));
    }
    const int r = lane & 31, h = lane >> 5;
    const u32 kp0 = L_K + (u32)lane * 16u;
    const u32 onesoff = 1024u + ((r >= 20 && r < 28) ? 64u : 0u) + ((r == 16 || r == 20) ? 0u : 16u);
    const u32 vp00 = L_V + (r < 16 ? (u32)(h * 256 + r * 16) : onesoff);
    const u32 vp10 = L_V + (r < 16 ? (u32)(512 + h * 256 + r * 16) : onesoff);
    const bf16x8 qa = BC8(qar), qbf = BC8(qbr);
    const f32x16 cz = {};
    AState s;
    STAMP(L ? 18 : 5);
    __builtin_amdgcn_s_setprio(1);
    attn_begin(s, qa, cz, kp0);
    {
        u32 kp = kp0, vp0 = vp00, vp1 = vp10;
#pragma nounroll
        for (int ch = 0; ch < 8; ++ch) {
            CSTAMP(ch);
            attn_chunk8<false>(s, qa, qbf, cz, cz, kp, vp0, vp1, ch == 0 || ch == 2 || ch == 6);
            kp += 8192; vp0 += 8 * VSTR; vp1 += 8 * VSTR;
            asm volatile("" : "+v"(kp), "+v"(vp0), "+v"(vp1));
        }
    }
    s.Ob = MFMA32(s.v1, BC8(s.pb1), s.Ob);
    CSTAMP(8);
    STAMP(L ? 19 : 6);
    FRESH(c);
    {
        bool bad = !(s.Oa[8] >= 0x1p-60f && s.Oa[8] <= 0x1p60f) || !(s.Ob[8] >= 0x1p-60f && s.Ob[8] <= 0x1p60f);
#pragma unroll
        for (int q = 0; q < 8; ++q) bad = bad || !(__builtin_fabsf(s.Oa[q]) <= 0x1p126f) || !(__builtin_fabsf(s.Ob[q]) <= 0x1p126f);
        if (__any(bad)) {
            const int ln = c.lane, rr = ln & 31, hh = ln >> 5;
            const u32 ko = OK_ + (u32)(head * 64 * 64 + ln) * 16u;
            const u32 vo = OV + (u32)(head * 65536 + hh * 256 + (rr & 15) * 16);
            const u32x4 cst = (rr == 16 || rr == 20) ? (u32x4){0x3f803f80u, 0x3f803f80u, 0x3f803f80u, 0x3f803f80u} : (u32x4){0u, 0u, 0u, 0u};
            float ma = -INFINITY, mb = -INFINITY;
            for (int t = 0; t < 64; ++t) {
                const bf16x8 kf = BC8(ldw(c.ws, ko + (u32)t * 1024u));
                const f32x16 Sa = MFMA32(kf, qa, cz), Sb = MFMA32(kf, qbf, cz);
#pragma unroll
                for (int q = 0; q < 16; ++q) { ma = fmaxf(ma, Sa[q]); mb = fmaxf(mb, Sb[q]); }
            }
            ma = fmaxf(ma, __shfl_xor(ma, 32)); mb = fmaxf(mb, __shfl_xor(mb, 32));
            f32x16 ca, cb;
#pragma unroll
            for (int q = 0; q < 16; ++q) { ca[q] = -ma; cb[q] = -mb; }
            s.Oa = cz; s.Ob = cz;
            for (int t = 0; t < 64; ++t) {
                const bf16x8 kf = BC8(ldw(c.ws, ko + (u32)t * 1024u));
                u32x4 v0 = ldw(c.ws, vo + (u32)t * 1024u), v1 = ldw(c.ws, vo + (u32)t * 1024u + 512u);
                if (rr >= 16) { v0 = cst; v1 = cst; }
                const f32x16 Sa = MFMA32(kf, qa, ca), Sb = MFMA32(kf, qbf, cb);
                u32x4 p0, p1;
#pragma unroll
                for (int q = 0; q < 4; ++q) { p0[q] = pk(EXP2(Sa[2 * q]), EXP2(Sa[2 * q + 1])); p1[q] = pk(EXP2(Sa[8 + 2 * q]), EXP2(Sa[9 + 2 * q])); }
                s.Oa = MFMA32(BC8(v0), BC8(p0), s.Oa); s.Oa = MFMA32(BC8(v1), BC8(p1), s.Oa);
#pragma unroll
                for (int q = 0; q < 4; ++q) { p0[q] = pk(EXP2(Sb[2 * q]), EXP2(Sb[2 * q + 1])); p1[q] = pk(EXP2(Sb[8 + 2 * q]), EXP2(Sb[9 + 2 * q])); }
                s.Ob = MFMA32(BC8(v0), BC8(p0), s.Ob); s.Ob = MFMA32(BC8(v1), BC8(p1), s.Ob);
            }
        }
        const u32 oo = OO + (u32)((head * 64 + qb * 8 + 2 * w) * 64 + c.lane) * 16u;
        const float ra = __builtin_amdgcn_rcpf(s.Oa[8]), rb_ = __builtin_amdgcn_rcpf(s.Ob[8]);
        u32x4 o;
#pragma unroll
        for (int q = 0; q < 4; ++q) o[q] = pk(s.Oa[2 * q] * ra, s.Oa[2 * q + 1] * ra);
        const int tq = qb * 8 + 2 * w, skip = (L == 1 && (c.lane & 31) == (sel & 31)) ? (sel >> 5) : -1;
        if (skip != tq) stw(c.ws, oo, not_poison(o));
#pragma unroll
        for (int q = 0; q < 4; ++q) o[q] = pk(s.Ob[2 * q] * rb_, s.Ob[2 * q + 1] * rb_);
        if (skip != tq + 1) stw(c.ws, oo + 1024u, not_poison(o));
    }
    __builtin_amdgcn_s_setprio(0);
    BAR();
    STAMP(L ? 20 : 7);
#undef GCH
}

DI void phaseO(const Ctx& c0, const Params& p, u32x4 (&x2)[2], u32& hsync, u32x4 (&pq)[24], const int L) {
    Ctx c = c0; FRESH(c);
    const int lane = c.lane, w = c.w;
    const int tb = 4 * c.xg + (c.gi >> 3), fb = c.gi & 7;
    const int r = lane & 31, h = lane >> 5;
    const u32 OO = L ? OFF_O1 : OFF_O0, GS = L ? OFF_GS1 : OFF_GS0, GC = L ? OFF_GC1 : OFF_GC0, GM = L ? OFF_GM1 : OFF_GM0, GX = L ? OFF_GX1 : OFF_GX0;
    constexpr u32 L_TS = L_SB, L_CAND = P_ST + 8;
    if (w >= 4) {
        const int hw = w - 4;
        STAMP(L ? 21 : 8);
        BAR();
        BAR();
        __builtin_amdgcn_s_setprio(3);
        FST(30 + 20 * L);
        if (hw == 0) {
            const u32 a = L_SB + (u32)h * 384u + (u32)r * 12u;
            const float t1 = lds_ld<float>(a) + lds_ld<float>(a + 768), t2 = lds_ld<float>(a + 4) + lds_ld<float>(a + 772), t3 = lds_ld<float>(a + 8) + lds_ld<float>(a + 776);
            stw(c.ws, GS + (u32)((tb * 8 + fb) * 1024 + lane * 16), (u32x4){__float_as_uint(t1), __float_as_uint(t2), __float_as_uint(t3), c.tag});
        }
        FST(31 + 20 * L);
        if (L == 0) {
            if (hw != 0) { pre_store_wq(c, pq); lds_signal_add(c, P_WQ); }
            BAR();
        }
        FST(32 + 20 * L);
        if (hw == 0) {
            u32x4 v[8]; u32 n = 0;
            for (;;) {
                bool ok = true;
#pragma unroll
                for (int k = 0; k < 8; ++k) v[k] = ldw(c.ws, GS + (u32)((tb * 8 + k) * 1024 + lane * 16));
                MEMFENCE();
#pragma unroll
                for (int k = 0; k < 8; ++k) ok = ok && v[k][3] == c.tag;
                if (__all(ok)) break;
                if (spin_fail(c, n)) break;
            }
            FST(33 + 20 * L);
            float t1 = 0, t2 = 0, t3 = 0;
#pragma unroll
            for (int k = 0; k < 8; ++k) { t1 += __uint_as_float(v[k][0]); t2 += __uint_as_float(v[k][1]); t3 += __uint_as_float(v[k][2]); }
            const float mu = t1 * (1.0f / 512.0f), var = fmaxf(t2 * (1.0f / 512.0f) - mu * mu, 0.f);
            const float rs = __builtin_amdgcn_rsqf(var + 1e-5f);
            const float rt = rbf(rs * (t3 - mu * lds_ld<float>(P_WSUM)));
            const u32 key = half_umax(score_key(__float_as_uint(rt) >> 16, (u32)r));
            const int bt = (int)key_idx(key);
            if (fb == 0) {
                const u32 k0 = (u32)__builtin_amdgcn_readlane((int)key, 0), k1 = (u32)__builtin_amdgcn_readlane((int)key, 32);
                const u32 kT = (lane & 1) ? k1 : k0;
                const u64 g = (cand_tag(c.tag) << 21) | (u64)(key_bf(kT) << 5) | (u64)key_idx(kT);
                if (lane < 16) __builtin_amdgcn_raw_buffer_store_b64((u32x2){(u32)g, (u32)(g >> 32)}, c.ws, GC + (u32)(lane >> 1) * 512u + (u32)(tb * 2 + (lane & 1)) * 8u, 0, 16);
                if (r == bt) stw(c.ws, GM + (u32)(tb * 2 + h) * 16u, mk_unit(__float_as_uint(mu), __float_as_uint(rs), c.tag));
                if (L == 0) {
                    const float g1 = 1.0f + rs, A = g1 * __builtin_amdgcn_rsqf(var * g1 * g1 + 1e-5f);
                    stw(c.ws, OFF_TS1 + (u32)(tb * 64 + lane) * 16u, mk_unit(__float_as_uint(A), __float_as_uint(A * mu), c.tag));
                }
            }
            FST(34 + 20 * L);
            lds_st<f32x2>(L_TS + (u32)lane * 8u, (f32x2){mu, rs});
            if (r == 0) lds_st<u32>(L_CAND + (u32)h * 4u, (u32)bt);
            lds_signal_add(c, P_ST + L * 4);
            {
                const int ct = (lane >> 4) & 1, u = lane & 15;
                const int cb = __builtin_amdgcn_readlane(bt, 0), cb1 = __builtin_amdgcn_readlane(bt, 32);
                const int row = ct ? cb1 : cb;
                const u32x2 d = lds_ld<u32x2>(L_X1T + (u32)(ct + 2 * (u >> 3)) * 2048u + (u32)((u >> 2) & 1) * 1024u + (u32)((u & 1) * 32 + row) * 16u + (u32)((u >> 1) & 1) * 8u);
                if (lane < 32) stw(c.ws, GX + (u32)(tb * 2 + ct) * 2048u + (u32)(fb * 16 + u) * 16u, mk_unit(d[0], d[1], c.tag));
            }
            if (L == 0) { pre_store_wq(c, pq); lds_signal_add(c, P_WQ); }
        }
        STAMP(L ? 23 : 12);
        ffn_chain(c, p, hsync, L, pq);
        if (L == 0) STAMP(15);
        return;
    }
    const int tt = w & 1, kh = w >> 1;
    const int tile = tb * 2 + tt, tok = tile * 32 + r;
    const int f0 = fb * 64 + kh * 32;
    u32x2 res[4], wr[4];
#pragma unroll
    for (int g = 0; g < 4; ++g) {
        if (L == 0) res[g] = *(const u32x2*)(p.x0 + (size_t)tok * 512 + f0 + 8 * g + 4 * h);
        wr[g] = *(const u32x2*)(p.wr + f0 + 8 * g + 4 * h);
    }
    if (L == 1) {
        const int sel0 = __builtin_amdgcn_readfirstlane((int)lds_ld<u32>(P_SEL)) & 2047;
        if ((sel0 >> 5) == tile) {
            const u32 ub = OFF_GY + (u32)(f0 / 4 + h) * 16u;
            u32 off[4] = {ub, ub + 32u, ub + 64u, ub + 96u}; u32x4 v[4];
            wait_units<4>(c, off, v);
            if (r == (sel0 & 31)) { x2[0] = (u32x4){v[0][0], v[0][2], v[1][0], v[1][2]}; x2[1] = (u32x4){v[2][0], v[2][2], v[3][0], v[3][2]}; }
        }
#pragma unroll
        for (int g = 0; g < 4; ++g) res[g] = (u32x2){x2[g >> 1][(g & 1) * 2], x2[g >> 1][(g & 1) * 2 + 1]};
    }
    STAMP(L ? 21 : 8);
    u32x4 bf[16];
    const u32 oo = OO + (u32)(((kh * 16) * 64 + tile) * 64 + lane) * 16u;
    {
        u32 n = 0;
        for (;;) {
#pragma unroll
            for (int s = 0; s < 16; ++s) bf[s] = ldw(c.ws, oo + (u32)s * 65536u);
            MEMFENCE();
            u32 m = 0;
#pragma unroll
            for (int s = 0; s < 16; ++s) m = max(m, max(max(bf[s][0], bf[s][1]), max(bf[s][2], bf[s][3])));
            if (!__any(m == POISON)) break;
            if (spin_fail(c, n)) break;
        }
    }
    const int i16 = lane & 15, blk = (lane >> 4) & 1;
    const u32 trA = L_WO + (u32)((kh * 256 + 4 * h + (i16 >> 2)) * 64 + blk * 32 + (i16 & 3) * 8);
    f32x16 a0 = {}, a1 = {};
    {
        u32 ta = trA + (u32)kh * WO_FT, tb_ = trA + (u32)(1 - kh) * WO_FT;
        asm volatile("" : "+v"(ta), "+v"(tb_));
        bf16x8 f0_[2], f1_[2];
        f0_[0] = frag_tr(ta, ta + 512); f0_[1] = frag_tr(tb_, tb_ + 512);
#pragma unroll
        for (int s = 0; s < 16; s += 2) {
            f1_[0] = frag_tr(ta + (u32)(s + 1) * 1024u, ta + (u32)(s + 1) * 1024u + 512); SBAR();
            a0 = MFMA32(f0_[0], BC8(bf[s]), a0); SBAR();
            f1_[1] = frag_tr(tb_ + (u32)(s + 1) * 1024u, tb_ + (u32)(s + 1) * 1024u + 512); SBAR();
            a1 = MFMA32(f0_[1], BC8(bf[s]), a1); SBAR();
            if (s + 2 < 16) f0_[0] = frag_tr(ta + (u32)(s + 2) * 1024u, ta + (u32)(s + 2) * 1024u + 512);
            SBAR(); a0 = MFMA32(f1_[0], BC8(bf[s + 1]), a0); SBAR();
            if (s + 2 < 16) f0_[1] = frag_tr(tb_ + (u32)(s + 2) * 1024u, tb_ + (u32)(s + 2) * 1024u + 512);
            SBAR(); a1 = MFMA32(f1_[1], BC8(bf[s + 1]), a1); SBAR();
        }
    }
    {
        const f32x16& snd = a1;
#pragma unroll
        for (int g = 0; g < 4; ++g) lds_st<f32x4>(L_XB + (u32)w * 4096u + (u32)g * 1024u + (u32)lane * 16u, (f32x4){snd[4 * g], snd[4 * g + 1], snd[4 * g + 2], snd[4 * g + 3]});
    }
    BAR();
    STAMP(L ? 22 : 9);
    u32x4 xp[2];
    {
        const f32x16& keep = a0;
#pragma unroll
        for (int g = 0; g < 4; ++g) {
            const f32x4 o = lds_ld<f32x4>(L_XB + (u32)(w ^ 2) * 4096u + (u32)g * 1024u + (u32)lane * 16u);
            const u32 t0 = pk(keep[4 * g] + o[0], keep[4 * g + 1] + o[1]), t1 = pk(keep[4 * g + 2] + o[2], keep[4 * g + 3] + o[3]);
            xp[g >> 1][(g & 1) * 2] = pk(bflo(res[g][0]) + bflo(t0), bfhi(res[g][0]) + bfhi(t0));
            xp[g >> 1][(g & 1) * 2 + 1] = pk(bflo(res[g][1]) + bflo(t1), bfhi(res[g][1]) + bfhi(t1));
        }
    }
    if (L == 0) {
#pragma unroll
        for (int s = 0; s < 2; ++s) stw(c.ws, OFF_X1 + (u32)((tile * 32 + f0 / 16 + s) * 64 + lane) * 16u, xp[s]);
    }
#pragma unroll
    for (int s = 0; s < 2; ++s) lds_st<u32x4>(L_X1T + (u32)w * 2048u + (u32)s * 1024u + (u32)lane * 16u, xp[s]);
    float x1[16];
    {
        float s1 = 0, s2 = 0, s3 = 0;
#pragma unroll
        for (int i = 0; i < 16; ++i) {
            const u32 wd = xp[i >> 3][(i & 7) >> 1];
            x1[i] = (i & 1) ? bfhi(wd) : bflo(wd);
            const u32 ww = wr[i >> 2][(i & 3) >> 1];
            const float wv = (i & 1) ? bfhi(ww) : bflo(ww);
            s1 += x1[i]; s2 += x1[i] * x1[i]; s3 += x1[i] * wv;
        }
        s1 = swap32_add(s1); s2 = swap32_add(s2); s3 = swap32_add(s3);
        if (h == 0) { const u32 a = L_SB + (u32)w * 384u + (u32)r * 12u; lds_st<float>(a, s1); lds_st<float>(a + 4, s2); lds_st<float>(a + 8, s3); }
    }
    BAR();
    if (L == 0) {
        DRAIN(); BAR();
        STAMP(10);
        if (w == 0 && lane == 0) set_flag(c, OFF_FO + (u32)(tb * 8 + fb) * 128u);
        pre_store_wq(c, pq); lds_signal_add(c, P_WQ);
        if (w == 3) {
            const int hpa = (4 * c.xg + (c.gi >> 3)) >> 1, rbp = lane & 15;
            wait_flags(c, OFF_FP2 + (u32)((rbp >> 2) + 4 * (hpa >> 3) + 8 * ((rbp & 3) + 4 * (hpa & 7))) * 128u);
            lds_signal_add(c, P_FQ);
        }
    }
    lds_wait_ge(P_ST + L * 4, 1u);
    STAMP(L ? 23 : 11);
    const f32x2 st = lds_ld<f32x2>(L_TS + (u32)(tt * 32 + r) * 8u);
    const int bt = (int)lds_ld<u32>(L_CAND + (u32)tt * 4u) & 31;
#pragma unroll
    for (int s = 0; s < 2; ++s)
#pragma unroll
        for (int j = 0; j < 4; ++j) {
            const float a = x1[8 * s + 2 * j], b_ = x1[8 * s + 2 * j + 1];
            const u32 hh = pk((a - st[0]) * st[1], (b_ - st[0]) * st[1]);
            x2[s][j] = pk(a + bflo(hh), b_ + bfhi(hh));
        }
    if (L == 1) {
        const rsrc_t outr = __builtin_amdgcn_make_buffer_rsrc(p.out, 0, 2097152, 0x00020000);
        const u32 tl = L_XB + (u32)w * 4096u;
#pragma unroll
        for (int g = 0; g < 4; ++g) lds_st<u32x2>(tl + (u32)(r * 80 + (8 * g + 4 * h) * 2), (u32x2){x2[g >> 1][(g & 1) * 2], x2[g >> 1][(g & 1) * 2 + 1]});
        asm volatile("s_waitcnt lgkmcnt(0)" ::: "memory");
        u32x4 rowv[2];
#pragma unroll
        for (int q = 0; q < 2; ++q) {
            const int row = q * 16 + (lane >> 2);
            rowv[q] = lds_ld<u32x4>(tl + (u32)(row * 80 + (lane & 3) * 16));
            if (row != bt) __builtin_amdgcn_raw_buffer_store_b128(rowv[q], outr, (u32)(((tile * 32 + row) * 512 + f0 + (lane & 3) * 8) * 2), 0, 16);
        }
        wait_sel(1);
        const int sel1 = __builtin_amdgcn_readfirstlane((int)lds_ld<u32>(P_SEL + 32)) & 2047;
        if (tile * 32 + bt != sel1) {
#pragma unroll
            for (int q = 0; q < 2; ++q) {
                const int row = q * 16 + (lane >> 2);
                if (row == bt) __builtin_amdgcn_raw_buffer_store_b128(rowv[q], outr, (u32)(((tile * 32 + row) * 512 + f0 + (lane & 3) * 8) * 2), 0, 16);
            }
        }
        }
    fresh(pq);
}

#if defined(__HIP_DEVICE_COMPILE__)
__attribute__((target("no-packed-fp32-ops")))
#endif
__global__ __launch_bounds__(512, 2) void fused_layers_kernel(Params p) {
    extern __shared__ __attribute__((aligned(16))) char lds[];
    Ctx c;
    const int tid = threadIdx.x;
    c.lane = tid & 63; c.w = __builtin_amdgcn_readfirstlane(tid >> 6);
    c.xg = blockIdx.x & 7; c.gi = blockIdx.x >> 3;
    c.ws = __builtin_amdgcn_make_buffer_rsrc(p.ws, 0, WS_BYTES, 0x00020000);
    c.tag = 0u;
    if (c.w < 4) {
        u64 pc; u32 len;
        asm volatile("s_getpc_b64 %0\nfl_code_here:\n\ts_mov_b32 %1, fl_code_end-fl_code_here" : "=s"(pc), "=s"(len));
        if (c.w == 1) {
            const u32 o0 = min((u32)c.gi * 3072u + (u32)c.lane * 16u, len - 16u) & ~15u, o1 = min(o0 + 1024u, len - 16u) & ~15u, o2 = min(o0 + 2048u, len - 16u) & ~15u;
            const u64 base = pc & ~15ull;
            glds((const void*)base, o0, 147456u); glds((const void*)base, o1, 148480u); glds((const void*)base, o2, 149504u);
        }
        if (c.w == 2) {
            const int l = c.lane, k = (l - 22) % 14;
            const char* a = p.ws + (size_t)(l < 10 ? l : 0) * 0x200000;
            a = l >= 10 && l < 14 ? (const char*)p.wo + (l - 10) * 131072 : l == 14 ? (const char*)p.wr : a;
            a = l >= 15 && l < 22 ? (const char*)p.out + (l - 15) * 299008 : a;
            a = l >= 22 ? (const char*)(l < 36 ? p.wg : l < 50 ? p.wu : p.wd) + k * 131072 : a;
            u32 sv;
            asm volatile("s_mov_b32 %0, m0\n\ts_mov_b32 m0, %2\n\ts_nop 0\n\tglobal_load_lds_dword %1, off\n\ts_mov_b32 m0, %0" : "=&s"(sv) : "v"(a), "s"(150528u) : "memory");
        }
        if (c.w == 3 && c.lane < 29 && c.lane != 3) lds_st<u32>(P_HCNT + (u32)c.lane * 4u, 0u);
    }
    asm volatile("" :: "v"(lds));
    STAMP(0);
    FST(0);
    u32 hsync = 0;
    u32x4 x2[2];
    asm volatile("" : "=v"(x2[0]), "=v"(x2[1]));
    u32x4 pq[24];
    fresh(pq);
#pragma nounroll
    for (int Li = 0; Li < 2; ++Li) {
        int Lv = Li; asm volatile("" : "+v"(Lv));
        const int L = __builtin_amdgcn_readfirstlane(Lv);
        if (L == 0) phaseQ<0>(c, p, pq);
        else {
            phaseQ<1>(c, p, pq);
        }
        if (STOP_AFTER < 3 * L + 1) break;
        phaseA(c, p, hsync, pq, L);
        if (STOP_AFTER < 3 * L + 2) break;
        phaseO(c, p, x2, hsync, pq, L);
        if (STOP_AFTER < 3 * L + 3) break;
    }
    STAMP(24);
    if (blockIdx.x == 14 * 8 && c.w == 4 && c.lane == 0) __builtin_amdgcn_raw_buffer_store_b32(c.tag, c.ws, OFF_EPOCH, 0, 16);
    asm volatile("fl_code_end:" ::: "memory");
}

extern "C" void kernel_launch(void* const* d_in, const int* in_sizes, int n_in, void* d_out, int out_size, void* d_ws, size_t ws_size, hipStream_t stream) {
    Params p;
    p.x0 = (const uint16_t*)d_in[0]; p.wqkv = (const uint16_t*)d_in[1]; p.wo = (const uint16_t*)d_in[2];
    p.wr = (const uint16_t*)d_in[4]; p.wg = (const uint16_t*)d_in[5]; p.wu = (const uint16_t*)d_in[6]; p.wd = (const uint16_t*)d_in[7];
    p.out = (uint16_t*)d_out; p.ws = (char*)d_ws;
    (void)hipFuncSetAttribute((const void*)fused_layers_kernel, hipFuncAttributeMaxDynamicSharedMemorySize, LDS_BYTES);
    fused_layers_kernel<<<256, 512, LDS_BYTES, stream>>>(p);
}
```

```cpp
#include <hip/hip_runtime.h>
#include <stdint.h>

typedef unsigned u32;
typedef unsigned long long u64;
typedef __attribute__((ext_vector_type(2))) u32 u32x2;
typedef __attribute__((ext_vector_type(4))) u32 u32x4;
typedef __attribute__((ext_vector_type(2))) float f32x2;
typedef __attribute__((ext_vector_type(4))) float f32x4;
typedef __attribute__((ext_vector_type(16))) float f32x16;
typedef __attribute__((ext_vector_type(8))) __bf16 bf16x8;
typedef __attribute__((ext_vector_type(2))) __bf16 bf16x2;
typedef __attribute__((ext_vector_type(4))) short s16x4;
#define LAS __attribute__((address_space(3)))
#define DI __device__ __forceinline__
#define MFMA32(a, b, c) __builtin_amdgcn_mfma_f32_32x32x16_bf16((a), (b), (c), 0, 0, 0)
#define MFMA16(a, b, c) __builtin_amdgcn_mfma_f32_16x16x32_bf16((a), (b), (c), 0, 0, 0)
#define SBAR() __builtin_amdgcn_sched_barrier(0)
#define BAR() asm volatile("s_waitcnt lgkmcnt(0)\n\ts_barrier" ::: "memory")
#define DRAIN() asm volatile("s_waitcnt vmcnt(0)" ::: "memory")
#define MEMFENCE() asm volatile("" ::: "memory")

constexpr u32 OFF_EPOCH = 0, OFF_ABORT = 256;
constexpr u32 OFF_FQ0 = 0x10000, OFF_FP = 0x18000, OFF_FO = 0x20000, OFF_FQ1 = 0x28000;
constexpr u32 OFF_TS0 = 0x40000, OFF_TS1 = 0x48000;
constexpr u32 OFF_GC0 = 0x50000, OFF_GC1 = 0x51000;
constexpr u32 OFF_GM0 = 0x52000, OFF_GM1 = 0x52400;
constexpr u32 OFF_GA0 = 0x58000, OFF_GA1 = 0x5A000;
constexpr u32 OFF_GY = 0x5C000;
constexpr u32 OFF_GS0 = 0x80000, OFF_GS1 = 0xC0000;
constexpr u32 OFF_GX0 = 0x180000, OFF_GX1 = 0x1A0000;
constexpr u32 OFF_Q0 = 0x200000, OFF_K0 = 0x400000, OFF_V0 = 0x600000, OFF_O0 = 0x800000, OFF_X1 = 0xA00000;
constexpr u32 OFF_Q1 = 0xC00000, OFF_K1 = 0xE00000, OFF_V1 = 0x1000000, OFF_O1 = 0x1200000;
constexpr u32 WS_BYTES = 0x1400000;

constexpr u32 LDS_BYTES = 163840;
constexpr u32 L_W = 0;
constexpr u32 L_XR = 98304;
constexpr u32 L_AX = 140288;
constexpr u32 L_TOK = 161792 + 512;
constexpr u32 L_K = 0, L_V = 65536, VSTR = 1152;
constexpr u32 L_WO = 0, WO_FT = 65536;
constexpr u32 L_XB = 32768;
constexpr u32 L_SB = 161792;
constexpr u32 L_P = 163328;
constexpr u32 P_CS = L_P, P_HCNT = L_P + 384, P_PATCH = L_P + 388, P_WQ = L_P + 392, P_WSUM = L_P + 396, P_SEL = L_P + 400, P_ST = L_P + 464, P_PD = L_P + 480, P_FQ = L_P + 484;
constexpr u32 L_X1T = 49152;
constexpr u32 FW0 = 98304, FS0 = 155648;
constexpr u32 SPIN_CAP = 40000;

struct Params {
    const uint16_t* x0; const uint16_t* wqkv; const uint16_t* wo; const uint16_t* wr;
    const uint16_t* wg; const uint16_t* wu; const uint16_t* wd; uint16_t* out; char* ws;
};

typedef __amdgpu_buffer_rsrc_t rsrc_t;

DI u32 pk(float a, float b) { f32x2 v = {a, b}; return __builtin_bit_cast(u32, __builtin_convertvector(v, bf16x2)); }
DI float bflo(u32 v) { return __uint_as_float(v << 16); }
DI float bfhi(u32 v) { return __uint_as_float(v & 0xffff0000u); }
DI float rbf(float a) { return bflo(pk(a, 0.f)); }
DI u32x4 ldw(rsrc_t r, u32 off) { return __builtin_amdgcn_raw_buffer_load_b128(r, off, 0, 16); }
DI void stw(rsrc_t r, u32 off, u32x4 v) { __builtin_amdgcn_raw_buffer_store_b128(v, r, off, 0, 16); }
template <class T> DI T lds_ld(u32 a) { return *(const LAS T*)(uintptr_t)a; }
template <class T> DI void lds_st(u32 a, T v) { *(LAS T*)(uintptr_t)a = v; }
DI s16x4 lds_tr(u32 a) { return __builtin_amdgcn_ds_read_tr16_b64_v4i16((LAS s16x4*)(uintptr_t)a); }
DI bf16x8 frag_tr(u32 a0, u32 a1) { s16x4 lo = lds_tr(a0), hi = lds_tr(a1); return __builtin_bit_cast(bf16x8, __builtin_shufflevector(lo, hi, 0, 1, 2, 3, 4, 5, 6, 7)); }
DI float swap32_add(float v) { auto r = __builtin_amdgcn_permlane32_swap(__float_as_uint(v), __float_as_uint(v), false, false); return __uint_as_float(r[0]) + __uint_as_float(r[1]); }
template <int CTRL> DI u32 dpp(u32 v) { return (u32)__builtin_amdgcn_update_dpp((int)v, (int)v, CTRL, 0xf, 0xf, true); }
DI float wave_sum(float v) {
    v += __uint_as_float(dpp<0xB1>(__float_as_uint(v)));
    v += __uint_as_float(dpp<0x4E>(__float_as_uint(v)));
    v += __uint_as_float(dpp<0x141>(__float_as_uint(v)));
    v += __uint_as_float(dpp<0x140>(__float_as_uint(v)));
    { auto r = __builtin_amdgcn_permlane16_swap(__float_as_uint(v), __float_as_uint(v), false, false); v = __uint_as_float(r[0]) + __uint_as_float(r[1]); }
    return swap32_add(v);
}
DI u32 half_umax(u32 v) {
    v = max(v, dpp<0xB1>(v)); v = max(v, dpp<0x4E>(v)); v = max(v, dpp<0x141>(v)); v = max(v, dpp<0x140>(v));
    auto r = __builtin_amdgcn_permlane16_swap(v, v, false, false);
    return max(r[0], r[1]);
}
DI u32 wave_umax(u32 v) { v = half_umax(v); auto r = __builtin_amdgcn_permlane32_swap(v, v, false, false); return max(r[0], r[1]); }
DI u32 score_key(u32 bf, u32 idx) { return ((bf ^ ((bf & 0x8000u) ? 0xFFFFu : 0x8000u)) << 16) | (0xFFFFu - idx); }
DI u32 key_bf(u32 k) { const u32 b = k >> 16; return b ^ ((b & 0x8000u) ? 0x8000u : 0xFFFFu); }
DI u32 key_idx(u32 k) { return 0xFFFFu - (k & 0xFFFFu); }
DI void glds(const void* sbase, u32 voff, u32 ldsdst) {
    u32 sv;
    asm volatile("s_mov_b32 %0, m0\n\ts_mov_b32 m0, %3\n\ts_nop 0\n\tglobal_load_lds_dwordx4 %1, %2\n\ts_mov_b32 m0, %0"
                 : "=&s"(sv) : "v"(voff), "s"(sbase), "s"(ldsdst) : "memory");
}
struct Ctx {
    rsrc_t ws; u32 tag; int lane, w, xg, gi;
};
#define FRESH(c) asm volatile("" : "+v"((c).lane), "+s"((c).w), "+s"((c).xg), "+s"((c).gi))
DI u32 epoch_tag(u32 v) { const u32 t = (u32)__builtin_amdgcn_readfirstlane((int)v) + 1u; return t ? t : 1u; }
DI bool spin_fail(const Ctx& c, u32& n) {
    __builtin_amdgcn_s_sleep(1);
    ++n;
    if (n > SPIN_CAP) { if (c.lane == 0) __builtin_amdgcn_raw_buffer_store_b32(c.tag, c.ws, OFF_ABORT, 0, 16); return true; }
    if ((n & 127u) == 0u) { u32 a = __builtin_amdgcn_raw_buffer_load_b32(c.ws, OFF_ABORT, 0, 16); if (__builtin_amdgcn_readfirstlane(a) == c.tag) return true; }
    return false;
}
DI void wait_flags(const Ctx& c, u32 off) {
    u32 n = 0;
    for (;;) {
        u32x2 v = __builtin_amdgcn_raw_buffer_load_b64(c.ws, off, 0, 16);
        MEMFENCE();
        if (__all(v[0] == c.tag && v[1] == ~c.tag)) break;
        if (spin_fail(c, n)) break;
    }
    MEMFENCE();
}
DI void set_flag(const Ctx& c, u32 off) { u32x2 v = {c.tag, ~c.tag}; __builtin_amdgcn_raw_buffer_store_b64(v, c.ws, off, 0, 16); }
constexpr u32 POISON = 0xFFFFFFFFu;
DI bool poisoned(u32x4 v) { return max(max(v[0], v[1]), max(v[2], v[3])) == POISON; }
DI u32x4 not_poison(u32x4 v) { return (u32x4){min(v[0], POISON - 1u), min(v[1], POISON - 1u), min(v[2], POISON - 1u), min(v[3], POISON - 1u)}; }
DI void wait_piece(const Ctx& c, u32 off, u32x4& v) {
    u32 n = 0;
    while (__any(poisoned(v))) { if (spin_fail(c, n)) break; v = ldw(c.ws, off); MEMFENCE(); }
}
DI bool unit_ok(u32x4 v, u32 tag) { return v[1] == tag && v[3] == tag; }
DI u32x4 mk_unit(u32 a, u32 b, u32 tag) { return (u32x4){a, tag, b, tag}; }
template <int N> DI void wait_units(const Ctx& c, const u32 (&off)[N], u32x4 (&v)[N]) {
    u32 n = 0;
    for (;;) {
        bool ok = true;
#pragma unroll
        for (int k = 0; k < N; ++k) v[k] = ldw(c.ws, off[k]);
        MEMFENCE();
#pragma unroll
        for (int k = 0; k < N; ++k) ok = ok && unit_ok(v[k], c.tag);
        if (__all(ok)) break;
        if (spin_fail(c, n)) break;
    }
    MEMFENCE();
}
DI u32 lds_poll_ld(u32 a) { return __hip_atomic_load((LAS u32*)(uintptr_t)a, __ATOMIC_RELAXED, __HIP_MEMORY_SCOPE_WORKGROUP); }
DI void helper_sync(const Ctx& c, u32& target) {
    target += 4;
    asm volatile("s_waitcnt lgkmcnt(0)" ::: "memory");
    if (c.lane == 0) __hip_atomic_fetch_add((LAS u32*)(uintptr_t)P_HCNT, 1u, __ATOMIC_RELAXED, __HIP_MEMORY_SCOPE_WORKGROUP);
    u32 n = 0;
    while (lds_poll_ld(P_HCNT) < target) { if (++n > (1u << 24)) break; }
    MEMFENCE();
}
DI void lds_wait_ge(u32 a, u32 v) {
    u32 n = 0;
    while (lds_poll_ld(a) < v) { __builtin_amdgcn_s_sleep(1); if (++n > (1u << 22)) break; }
    MEMFENCE();
}
DI void lds_signal_add(const Ctx& c, u32 a) {
    asm volatile("s_waitcnt lgkmcnt(0)" ::: "memory");
    if (c.lane == 0) __hip_atomic_fetch_add((LAS u32*)(uintptr_t)a, 1u, __ATOMIC_RELAXED, __HIP_MEMORY_SCOPE_WORKGROUP);
}
DI void wait_sel(int L) {
    u32 n = 0;
    while (lds_poll_ld(P_SEL + L * 32 + 16) == 0u) { __builtin_amdgcn_s_sleep(1); if (++n > (1u << 22)) break; }
    MEMFENCE();
}

DI u64 cand_tag(u32 tag) { return ((u64)tag << 11) | (u64)((tag * 0x9E3779B1u) >> 21); }
DI void ffn_chain(const Ctx& c, const Params& p, u32& hsync, const int L) {
    const int lane = c.lane, hw = c.w - 4;
    constexpr u32 FW = FW0, FS = FS0;
    const u32 GC = L ? OFF_GC1 : OFF_GC0, GM = L ? OFF_GM1 : OFF_GM0, GX = L ? OFF_GX1 : OFF_GX0, GA = L ? OFF_GA1 : OFF_GA0;
    __builtin_amdgcn_s_setprio(3);
    const bool is_gu = c.gi < 14, is_dn = c.gi >= 14 && c.gi < 18;
    const int g16 = lane >> 4, i16 = lane & 15;
    bf16x8 wf[14];
    {
        const u32 tro = FW + (u32)((8 * g16 + (i16 >> 2)) * 32 + (i16 & 3) * 8) + (u32)hw * (is_gu ? 4096u : 14336u);
#pragma unroll
        for (int s = 0; s < 14; ++s) {
            const u32 bo = tro + (is_gu ? (u32)(s & 3) * 1024u + (u32)((s >> 2) & 1) * 16384u : (u32)s * 1024u);
            if (is_gu || is_dn) wf[s] = frag_tr(bo, bo + 128);
        }
    }
    if (hw == 0) {
        const u64 want = cand_tag(c.tag);
        u64 v; u32 n = 0;
        for (;;) {
            const u32x2 t = __builtin_amdgcn_raw_buffer_load_b64(c.ws, GC + c.xg * 512u + lane * 8u, 0, 16);
            MEMFENCE();
            v = ((u64)t[1] << 32) | t[0];
            if (__all((v >> 21) == want)) break;
            if (spin_fail(c, n)) break;
        }
        const u32 key = wave_umax(score_key(((u32)v >> 5) & 0xFFFFu, (u32)lane * 32u + ((u32)v & 31u)));
        const u32 tok = key_idx(key); const float r = __uint_as_float(key_bf(key) << 16);
        if (lane == 0) {
            lds_st<u32x2>(P_SEL + L * 32, (u32x2){(u32)tok, __float_as_uint(r)});
            asm volatile("s_waitcnt lgkmcnt(0)" ::: "memory");
            __hip_atomic_store((LAS u32*)(uintptr_t)(P_SEL + L * 32 + 16), 1u, __ATOMIC_RELAXED, __HIP_MEMORY_SCOPE_WORKGROUP);
        }
    }
    if (!is_gu && !is_dn) return;
    wait_sel(L);
    const u32x2 si = lds_ld<u32x2>(P_SEL + L * 32);
    const int sel = __builtin_amdgcn_readfirstlane((int)si[0]) & 2047;
    const float rsel = __uint_as_float(si[1]);
    const u32 gxrow = GX + (u32)(sel >> 5) * 2048u, gm = GM + (u32)(sel >> 5) * 16u;
    if (is_gu) {
        const int gidx = c.xg * 14 + c.gi;
        u32 off[2] = {gxrow + (u32)(hw * 32 + (lane & 31)) * 16u, gm};
        u32x4 v[2];
        wait_units<2>(c, off, v);
        const float mu = __uint_as_float(v[1][0]), rs = __uint_as_float(v[1][2]);
        if (lane < 32) {
            u32x2 h;
            h[0] = pk((bflo(v[0][0]) - mu) * rs, (bfhi(v[0][0]) - mu) * rs);
            h[1] = pk((bflo(v[0][2]) - mu) * rs, (bfhi(v[0][2]) - mu) * rs);
            lds_st<u32x2>(FS + (u32)(hw * 32 + lane) * 8u, h);
        }
        f32x4 ag = {}, au = {};
#pragma unroll
        for (int s = 0; s < 4; ++s) {
            const bf16x8 a = lds_ld<bf16x8>(FS + (u32)((hw * 4 + s) * 64 + g16 * 16));
            ag = MFMA16(a, wf[s], ag);
            au = MFMA16(a, wf[4 + s], au);
        }
        if (lane < 16) { lds_st<float>(FS + 4608 + (u32)(hw * 32 + lane) * 4u, ag[0]); lds_st<float>(FS + 4608 + (u32)(hw * 32 + 16 + lane) * 4u, au[0]); }
        helper_sync(c, hsync);
        if (hw == 0) {
            float g = 0.f, u = 0.f;
#pragma unroll
            for (int k = 0; k < 4; ++k) { g += lds_ld<float>(FS + 4608 + (u32)(k * 32 + i16) * 4u); u += lds_ld<float>(FS + 4608 + (u32)(k * 32 + 16 + i16) * 4u); }
            g = rbf(g); u = rbf(u);
            const float sg = rbf(g * __builtin_amdgcn_rcpf(1.0f + __expf(-g)));
            const float a = rbf(sg * u);
            const float a0 = __shfl(a, (lane & 3) * 4), a1 = __shfl(a, (lane & 3) * 4 + 1), a2 = __shfl(a, (lane & 3) * 4 + 2), a3 = __shfl(a, (lane & 3) * 4 + 3);
            if (lane < 4) stw(c.ws, GA + (u32)(gidx * 4 + lane) * 16u, mk_unit(pk(a0, a1), pk(a2, a3), c.tag));
        }
    } else {
        const int didx = c.xg * 4 + (c.gi - 14);
        float mu, rs;
        {
            const int u0 = hw * 112 + lane, u1 = hw * 112 + 64 + (lane < 48 ? lane : 47);
            u32 off[4] = {gxrow + (u32)(didx * 4 + (lane & 3)) * 16u, gm, GA + (u32)u0 * 16u, GA + (u32)u1 * 16u};
            u32x4 v[4];
            wait_units<4>(c, off, v);
            mu = __uint_as_float(v[1][0]); rs = __uint_as_float(v[1][2]);
            lds_st<u32x2>(FS + 1024 + (u32)u0 * 8u, (u32x2){v[2][0], v[2][2]});
            if (lane < 48) lds_st<u32x2>(FS + 1024 + (u32)u1 * 8u, (u32x2){v[3][0], v[3][2]});
            if (hw == 0 && lane < 4) lds_st<u32x2>(FS + (u32)lane * 8u, (u32x2){v[0][0], v[0][2]});
        }
        f32x4 ay = {}, az = {};
        bf16x8 af[14];
#pragma unroll
        for (int s = 0; s < 14; ++s) af[s] = lds_ld<bf16x8>(FS + 1024 + (u32)((hw * 14 + s) * 64 + g16 * 16));
#pragma unroll
        for (int s = 0; s < 14; s += 2) { ay = MFMA16(af[s], wf[s], ay); az = MFMA16(af[s + 1], wf[s + 1], az); }
        if (lane < 16) lds_st<float>(FS + 4608 + (u32)(hw * 16 + lane) * 4u, ay[0] + az[0]);
        helper_sync(c, hsync);
        if (hw == 0) {
            float y = 0.f;
#pragma unroll
            for (int k = 0; k < 4; ++k) y += lds_ld<float>(FS + 4608 + (u32)(k * 16 + i16) * 4u);
            const float x1 = __uint_as_float((u32)lds_ld<uint16_t>(FS + (u32)i16 * 2u) << 16);
            const float sig = rbf(__builtin_amdgcn_rcpf(1.0f + __expf(-rsel)));
            y = rbf(rbf(y) * sig);
            const float h = rbf((x1 - mu) * rs);
            const float x2 = rbf(x1 + rbf(h + y));
            const float a0 = __shfl(x2, (lane & 3) * 4), a1 = __shfl(x2, (lane & 3) * 4 + 1), a2 = __shfl(x2, (lane & 3) * 4 + 2), a3 = __shfl(x2, (lane & 3) * 4 + 3);
            if (L == 0) { if (lane < 4) stw(c.ws, OFF_GY + (u32)(didx * 4 + lane) * 16u, mk_unit(pk(a0, a1), pk(a2, a3), c.tag)); }
            else if (lane < 4) *(u32x2*)(p.out + (size_t)sel * 512 + didx * 16 + lane * 4) = (u32x2){pk(a0, a1), pk(a2, a3)};
        }
    }
}

DI void qkv_patch(const Ctx& c, u32& hsync) {
    const int lane = c.lane, hw = c.w - 4;
    constexpr u32 FS = FS0;
    const int g16 = lane >> 4, i16 = lane & 15;
    bf16x8 wf[24];
    {
        const u32 tro = L_W + (u32)((8 * g16 + (i16 >> 2)) * 64 + (i16 & 3) * 8) + (u32)hw * 8192u;
#pragma unroll
        for (int s_ = 0; s_ < 4; ++s_)
#pragma unroll
            for (int ct = 0; ct < 6; ++ct) {
                const u32 bo = tro + (u32)s_ * 2048u + (u32)(ct >> 1) * 32768u + (u32)(ct & 1) * 32u;
                wf[s_ * 6 + ct] = frag_tr(bo, bo + 256);
            }
    }
    u32 off[2] = {OFF_GY + (u32)lane * 32u, OFF_GY + (u32)lane * 32u + 16u}; u32x4 v[2];
    wait_units<2>(c, off, v);
    float x[8], s = 0, q = 0;
#pragma unroll
    for (int k = 0; k < 2; ++k) { x[4 * k] = bflo(v[k][0]); x[4 * k + 1] = bfhi(v[k][0]); x[4 * k + 2] = bflo(v[k][2]); x[4 * k + 3] = bfhi(v[k][2]); }
#pragma unroll
    for (int k = 0; k < 8; ++k) { s += x[k]; q += x[k] * x[k]; }
    s = wave_sum(s); q = wave_sum(q);
    const float mu = s * (1.0f / 512.0f), var = fmaxf(q * (1.0f / 512.0f) - mu * mu, 0.f);
    const float A = __builtin_amdgcn_rsqf(var + 1e-5f);
    lds_st<u32x4>(FS + (u32)lane * 16u, (u32x4){pk((x[0] - mu) * A, (x[1] - mu) * A), pk((x[2] - mu) * A, (x[3] - mu) * A), pk((x[4] - mu) * A, (x[5] - mu) * A), pk((x[6] - mu) * A, (x[7] - mu) * A)});
    f32x4 acc[6] = {};
#pragma unroll
    for (int s_ = 0; s_ < 4; ++s_) {
        const bf16x8 a = lds_ld<bf16x8>(FS + (u32)((hw * 4 + s_) * 64 + g16 * 16));
#pragma unroll
        for (int ct = 0; ct < 6; ++ct) acc[ct] = MFMA16(a, wf[s_ * 6 + ct], acc[ct]);
    }
    if (lane < 16) {
#pragma unroll
        for (int ct = 0; ct < 6; ++ct) lds_st<float>(FS + 1024 + (u32)(hw * 96 + ct * 16 + lane) * 4u, acc[ct][0]);
    }
    helper_sync(c, hsync);
    if (hw == 0) {
#pragma unroll
        for (int k = 0; k < 2; ++k) {
            const int n = k * 64 + lane;
            if (n < 96) {
                float t = 0.f;
#pragma unroll
                for (int j = 0; j < 4; ++j) t += lds_ld<float>(FS + 1024 + (u32)(j * 96 + n) * 4u);
                lds_st<float>(FS + 4608 + (u32)n * 4u, n < 32 ? t * (0.25f * 1.4426950408889634f) : t);
            }
        }
        asm volatile("s_waitcnt lgkmcnt(0)" ::: "memory");
        if (lane == 0) __hip_atomic_store((LAS u32*)(uintptr_t)P_PATCH, 1u, __ATOMIC_RELAXED, __HIP_MEMORY_SCOPE_WORKGROUP);
    }
}

DI u32x4 gld(const void* base, u32 off) { return *(const u32x4*)((const char*)base + off); }
struct FfSrc { const uint16_t* m; u32 vo, step; bool on, gu; };
DI FfSrc ff_src(const Ctx& c, const Params& p) {
    int lane = c.lane; asm volatile("" : "+v"(lane));
    FfSrc f; const int w = c.w;
    f.on = c.gi < 18; f.gu = c.gi < 14;
    f.m = f.gu ? (w < 4 ? p.wg : p.wu) : p.wd;
    const u32 rowb = f.gu ? 3584u : 1024u, col = f.gu ? (u32)(c.xg * 14 + c.gi) * 32u : (u32)(c.xg * 4 + (c.gi - 14)) * 32u;
    const u32 first = f.gu ? (u32)(w & 3) * 4u : (u32)w * 7u;
    f.step = 32u * rowb;
    f.vo = (u32)(lane >> 1) * rowb + col + (u32)(lane & 1) * 16u + first * f.step;
    return f;
}
DI void keep_wq(const Ctx& c, u32x4 (&pq)[12]) {
#pragma unroll
    for (int j = 0; j < 12; ++j) pq[j] = lds_ld<u32x4>(L_W + (u32)(c.w * 12 + j) * 1024u + (u32)c.lane * 16u);
}
DI void pre_store_wq(const Ctx& c, const u32x4 (&pq)[12]) {
#pragma unroll
    for (int j = 0; j < 12; ++j) lds_st<u32x4>(L_W + (u32)(c.w * 12 + j) * 1024u + (u32)c.lane * 16u, pq[j]);
}
DI void pre_store_ff(const Ctx& c, const u32x4 (&pf)[7]) {
    if (c.gi < 18) {
        const bool gu = c.gi < 14;
        const u32 a = FW0 + (u32)c.w * (gu ? 4096u : 7168u) + (u32)c.lane * 16u;
#pragma unroll
        for (int j = 0; j < 7; ++j) if (j < 4 || !gu) lds_st<u32x4>(a + (u32)j * 1024u, pf[j]);
    }
}
DI u32 wo_lane_off(const Ctx& c) {
    int lane = c.lane; asm volatile("" : "+v"(lane));
    return (u32)((lane >> 2) * 1024 + (c.gi & 7) * 128 + (lane & 3) * 16 + (c.w >> 2) * 64 + (c.w & 3) * 8 * 16384);
}
DI void pre_store_wo(const Ctx& c, const u32x4 (&po)[8]) {
#pragma unroll
    for (int j = 0; j < 8; ++j) lds_st<u32x4>(L_WO + (u32)(c.w >> 2) * WO_FT + (u32)((c.w & 3) * 8 + j) * 1024u + (u32)c.lane * 16u, po[j]);
}

struct WFrag { bf16x8 q, k, v; };
template <u32 TR2> DI void wfrag_load(WFrag& f, u32 a01, u32 a2) { f.q = frag_tr(a01, a01 + TR2); f.k = frag_tr(a01 + 32768, a01 + 32768 + TR2); f.v = frag_tr(a2, a2 + TR2); }
template <u32 TR2> DI void qstep(f32x16& aq, f32x16& ak, f32x16& av, bf16x8 x, WFrag& cur, WFrag& nxt, u32 a01, u32 a2, bool more) {
    if (more) nxt.q = frag_tr(a01, a01 + TR2);
    SBAR(); aq = MFMA32(cur.q, x, aq); SBAR();
    if (more) nxt.k = frag_tr(a01 + 32768, a01 + 32768 + TR2);
    SBAR(); ak = MFMA32(cur.k, x, ak); SBAR();
    if (more) nxt.v = frag_tr(a2, a2 + TR2);
    SBAR(); av = MFMA32(x, cur.v, av); SBAR();
}

template <int L>
DI void phaseQ(Ctx& c0, const Params& p, u32& hsync, u32x4 (&pq)[12]) {
    Ctx c = c0; FRESH(c);
    const int lane = c.lane, w = c.w;
    const int rb = 4 * (c.xg & 3) + (c.gi & 3), hp = 8 * (c.xg >> 2) + (c.gi >> 2);
    const int r = lane & 31, h = lane >> 5;
    const int i16 = lane & 15, blk = (lane >> 4) & 1;
    const u32 trA = L_W + (u32)(((L == 0 ? 8 : 4) * h + (i16 >> 2)) * 64 + blk * 32 + (i16 & 3) * 8);
    constexpr u32 TR2 = L == 0 ? 256 : 512;
    constexpr u32 OQ = L ? OFF_Q1 : OFF_Q0, OK_ = L ? OFF_K1 : OFF_K0, OV = L ? OFF_V1 : OFF_V0, TS = L ? OFF_TS1 : OFF_TS0;
    const int rot = ((c.gi >> 2) + 2 * (c.gi & 3)) & 7;

    if (w >= 4) {
        const int hw = w - 4;
        if (L == 0) {
            u32x4 r0, r1; u32 ep;
            asm volatile("global_load_dword %0, %1, %2 sc1" : "=v"(ep) : "v"(0u), "s"(p.ws));
            const u32 tok = (u32)(rb * 128 + hp * 8 + hw * 2);
            const u32 vo = tok * 1024u + (u32)lane * 16u;
            asm volatile("global_load_dwordx4 %0, %1, %2" : "=v"(r0) : "v"(vo), "s"(p.x0));
            asm volatile("global_load_dwordx4 %0, %1, %2 offset:1024" : "=v"(r1) : "v"(vo), "s"(p.x0));
            const u32 wv = (u32)((lane >> 2) * 3072 + hp * 64 + (lane & 3) * 16 + hw * 16 * 3072);
            u32 xv[4];
#pragma unroll
            for (int q = 0; q < 4; ++q) { const int row = 8 * q + (lane >> 3); xv[q] = (u32)(((rb * 4 + hw) * 32 + row) * 1024 + (((lane & 7) ^ ((row >> 1) & 7)) * 16)); }
#define WD(i) do { const u32 kc_ = (u32)(((i) + rot) & 7); _Pragma("unroll") for (int t = 0; t < 3; ++t) \
                glds(p.wqkv, wv + (u32)t * 1024u + kc_ * (64u * 3072u), L_W + (u32)t * 32768u + (kc_ * 64u + (u32)hw * 16u) * 64u); } while (0)
#define XD(i) do { const u32 kc_ = (u32)(((i) + rot) & 7); _Pragma("unroll") for (int q = 0; q < 4; ++q) \
                glds(p.x0, xv[q] + kc_ * 128u, L_XR + (u32)((i) % 3) * 16384u + (u32)hw * 4096u + (u32)q * 1024u); } while (0)
#define QWAIT(n) asm volatile("s_waitcnt vmcnt(" #n ")\n\ts_barrier" ::: "memory")
#define CS(i) if (hw < 3) { _Pragma("unroll") for (int s_ = 0; s_ < 4; ++s_) { \
                const u32 a_ = trA + (u32)hw * 32768u + (u32)((((i) + rot) & 7) * 4 + s_) * 1024u; cs = MFMA32(frag_tr(a_, a_ + TR2), ones, cs); } }
            WD(0); XD(0); WD(1); XD(1); WD(2); XD(2); WD(3);
            f32x16 cs = {};
            const bf16x8 ones = __builtin_bit_cast(bf16x8, (u32x4){0x3f803f80u, 0x3f803f80u, 0x3f803f80u, 0x3f803f80u});
            asm volatile("s_waitcnt vmcnt(17)\n\ts_barrier" : "+v"(r0), "+v"(r1), "+v"(ep) :: "memory");
            c.tag = epoch_tag(ep); c0.tag = c.tag;
            {
                float s0 = 0, q0 = 0, s1 = 0, q1 = 0;
#pragma unroll
                for (int j = 0; j < 4; ++j) {
                    float a = bflo(r0[j]), b_ = bfhi(r0[j]); s0 += a + b_; q0 += a * a + b_ * b_;
                    float d = bflo(r1[j]), e = bfhi(r1[j]); s1 += d + e; q1 += d * d + e * e;
                }
                s0 = wave_sum(s0); q0 = wave_sum(q0); s1 = wave_sum(s1); q1 = wave_sum(q1);
                const float s = lane ? s1 : s0, q = lane ? q1 : q0;
                const float mu = s * (1.0f / 512.0f), var = fmaxf(q * (1.0f / 512.0f) - mu * mu, 0.f);
                const float A = __builtin_amdgcn_rsqf(var + 1e-5f);
                if (lane < 2) stw(c.ws, TS + (tok + (u32)lane) * 16u, mk_unit(__float_as_uint(A), __float_as_uint(A * mu), c.tag));
            }
            CS(0);
            QWAIT(11); XD(3); WD(4); CS(1);
            QWAIT(11); XD(4); WD(5); CS(2);
            QWAIT(10); XD(5); WD(6); CS(3);
            QWAIT(10); XD(6); WD(7); CS(4);
            QWAIT(10); XD(7); CS(5);
            QWAIT(4); CS(6); CS(7);
            if (hw < 3 && r == 0) {
#pragma unroll
                for (int g = 0; g < 4; ++g) lds_st<f32x4>(P_CS + (u32)(hw * 32 + 8 * g + 4 * h) * 4u, (f32x4){cs[4 * g], cs[4 * g + 1], cs[4 * g + 2], cs[4 * g + 3]});
            }
            asm volatile("s_waitcnt vmcnt(0) lgkmcnt(0)\n\ts_barrier" ::: "memory");
#undef WD
#undef XD
#undef QWAIT
#undef CS
        }
        DRAIN(); BAR();
        if (L == 0) keep_wq(c, pq);
        else {
            wait_sel(0);
            const int sel = __builtin_amdgcn_readfirstlane((int)lds_ld<u32>(P_SEL)) & 2047;
            if ((sel >> 7) == rb) { lds_wait_ge(P_WQ, 8u); qkv_patch(c, hsync); }
        }
    } else {
        const int T = rb * 4 + w;
        f32x16 aq = {}, ak = {}, av = {};
        float A, B;
        if (L == 0) {
            const u32 ep = __builtin_amdgcn_raw_buffer_load_b32(c.ws, OFF_EPOCH, 0, 16);
            u32 xa[4];
#pragma unroll
            for (int s_ = 0; s_ < 4; ++s_) xa[s_] = L_XR + (u32)w * 4096u + (u32)(r * 128 + (((2 * s_ + h) ^ ((r >> 1) & 7)) * 16));
            bf16x8 xr[2][4];
            u32x4 tsv;
            WFrag f0, f1;
            BAR();
#pragma unroll
            for (int s_ = 0; s_ < 4; ++s_) xr[0][s_] = lds_ld<bf16x8>(xa[s_]);
            { const u32 a01 = trA + (u32)(rot & 7) * 4096u; wfrag_load<TR2>(f0, a01, a01 + 65536u); }
#pragma unroll
            for (int i = 0; i < 8; ++i) {
                const u32 a01 = trA + (u32)((i + rot) & 7) * 4096u, a2 = a01 + 65536u;
                const u32 n01 = trA + (u32)((i + 1 + rot) & 7) * 4096u, n2 = n01 + 65536u;
                qstep<TR2>(aq, ak, av, xr[i & 1][0], f0, f1, a01 + 1024, a2 + 1024, true);
                qstep<TR2>(aq, ak, av, xr[i & 1][1], f1, f0, a01 + 2048, a2 + 2048, true);
                qstep<TR2>(aq, ak, av, xr[i & 1][2], f0, f1, a01 + 3072, a2 + 3072, true);
                {
                    const u32x4 pois = {POISON, POISON, POISON, POISON};
                    const u32 oo = (u32)(((4 * c.xg + (c.gi >> 3)) * 64 + (c.gi & 7) * 8 + 2 * w) * 64 + lane) * 16u;
                    const u32 oq = (u32)((hp * 2 * 64 + T) * 64 + lane) * 16u;
                    if (i == 0) { stw(c.ws, OFF_O0 + oo, pois); stw(c.ws, OFF_O0 + oo + 1024u, pois); }
                    if (i == 1) { stw(c.ws, OFF_Q0 + oq, pois); stw(c.ws, OFF_Q0 + oq + 65536u, pois); }
                    if (i == 2 && rb < 6) { stw(c.ws, OFF_K0 + oq, pois); stw(c.ws, OFF_K0 + oq + 65536u, pois); }
                    if (i == 3 && rb < 6) { stw(c.ws, OFF_V0 + oq, pois); stw(c.ws, OFF_V0 + oq + 65536u, pois); }
                    if (i == 4) { stw(c.ws, OFF_O1 + oo, pois); stw(c.ws, OFF_O1 + oo + 1024u, pois); }
                    if (i == 5 && w == 0) {
                        const u32 o = (u32)(((4 * c.xg + (c.gi >> 3)) * 8 + (c.gi & 7)) * 1024 + lane * 16);
                        stw(c.ws, OFF_GS0 + o, (u32x4){0u, 0u, 0u, 0u}); stw(c.ws, OFF_GS1 + o, (u32x4){0u, 0u, 0u, 0u});
                    }
                    if (i == 7) { DRAIN(); lds_signal_add(c, P_PD); }
                }
                if (i < 7) {
                    asm volatile("s_barrier" ::: "memory");
#pragma unroll
                    for (int s_ = 0; s_ < 4; ++s_) xr[(i + 1) & 1][s_] = lds_ld<bf16x8>(xa[s_] + (u32)((i + 1) % 3) * 16384u);
                }
                if (i == 6) tsv = ldw(c.ws, TS + (u32)(T * 32 + r) * 16u);
                qstep<TR2>(aq, ak, av, xr[i & 1][3], f1, f0, n01, n2, i < 7);
            }
            c.tag = epoch_tag(ep); c0.tag = c.tag;
            if (w == 0) { lds_wait_ge(P_PD, 4u); if (lane == 0) set_flag(c, OFF_FP + (u32)(hp * 16 + rb) * 128u); }
            if (w == 3) {
                const u32x4 v = *(const u32x4*)(p.wr + lane * 8); float s = 0;
#pragma unroll
                for (int j = 0; j < 4; ++j) s += bflo(v[j]) + bfhi(v[j]);
                s = wave_sum(s);
                if (lane == 0) lds_st<float>(P_WSUM, s);
            }
            u32 off[1] = {TS + (u32)(T * 32 + r) * 16u}; u32x4 v[1] = {tsv};
            if (!__all(unit_ok(tsv, c.tag))) wait_units<1>(c, off, v);
            A = __uint_as_float(v[0][0]); B = __uint_as_float(v[0][2]);
        } else {
            wait_flags(c, OFF_FO + (u32)(rb * 16 + (lane & 15)) * 128u);
            u32x4 xf[32];
            const u32 xo = OFF_X1 + (u32)T * 32768u + (u32)lane * 16u;
#pragma unroll
            for (int s = 0; s < 32; ++s) xf[s] = ldw(c.ws, xo + (u32)s * 1024u);
            const u32x4 tsv = ldw(c.ws, TS + (u32)(T * 32 + r) * 16u);
            lds_wait_ge(P_WQ, 8u);
            {
                u32 a01 = trA, a2 = trA + 65536u;
                asm volatile("" : "+v"(a01), "+v"(a2));
                WFrag f0, f1;
                wfrag_load<TR2>(f0, a01, a2);
#pragma unroll
                for (int st = 0; st < 32; st += 2) {
                    qstep<TR2>(aq, ak, av, __builtin_bit_cast(bf16x8, xf[st]), f0, f1, a01 + (u32)(st + 1) * 1024u, a2 + (u32)(st + 1) * 1024u, true);
                    qstep<TR2>(aq, ak, av, __builtin_bit_cast(bf16x8, xf[st + 1]), f1, f0, a01 + (u32)(st + 2) * 1024u, a2 + (u32)(st + 2) * 1024u, st + 2 < 32);
                }
            }
            u32 off[1] = {TS + (u32)(T * 32 + r) * 16u}; u32x4 v[1] = {tsv};
            if (!__all(unit_ok(tsv, c.tag))) wait_units<1>(c, off, v);
            A = __uint_as_float(v[0][0]); B = __uint_as_float(v[0][2]);
        }
        int ts = -1;
        if (L == 1) {
            wait_sel(0);
            const int sel = __builtin_amdgcn_readfirstlane((int)lds_ld<u32>(P_SEL)) & 2047;
            if ((sel >> 5) == T) ts = sel & 31;
        }
        const u32x4 pois = {POISON, POISON, POISON, POISON};
        const u32 tokl = L_TOK + (u32)w * 256u;
        if (h == 0) lds_st<f32x2>(tokl + (u32)r * 8u, (f32x2){A, B});
        constexpr float QS = 0.25f * 1.4426950408889634f;
#pragma unroll
        for (int t = 0; t < 2; ++t) {
            const f32x16& acc = t ? ak : aq;
            const float sA = t ? A : A * QS, sB = t ? B : B * QS;
            float o[16];
#pragma unroll
            for (int g = 0; g < 4; ++g) {
                const f32x4 cs = lds_ld<f32x4>(P_CS + (u32)(t * 32 + 8 * g + 4 * h) * 4u);
#pragma unroll
                for (int e = 0; e < 4; ++e) o[4 * g + e] = sA * acc[4 * g + e] - sB * cs[e];
            }
#pragma unroll
            for (int a = 0; a < 2; ++a) {
                u32x4 v = not_poison((u32x4){pk(o[8 * a], o[8 * a + 1]), pk(o[8 * a + 2], o[8 * a + 3]), pk(o[8 * a + 4], o[8 * a + 5]), pk(o[8 * a + 6], o[8 * a + 7])});
                if (L == 1 && r == ts) v = pois;
                stw(c.ws, (t ? OK_ : OQ) + (u32)(((hp * 2 + a) * 64 + T) * 64 + lane) * 16u, v);
            }
        }
        u32x4 vlate = pois;
        const u32 vbase = OV + (u32)(((hp * 2 + (r >> 4)) * 64 + T) * 1024 + h * 256 + (r & 15) * 16);
        {
            asm volatile("s_waitcnt lgkmcnt(0)" ::: "memory");
            const float cs = lds_ld<float>(P_CS + (u32)(64 + r) * 4u);
            float o[16];
#pragma unroll
            for (int g = 0; g < 4; ++g) {
                const f32x4 ab0 = lds_ld<f32x4>(tokl + (u32)(8 * g + 4 * h) * 8u), ab1 = lds_ld<f32x4>(tokl + (u32)(8 * g + 4 * h + 2) * 8u);
                o[4 * g] = ab0[0] * av[4 * g] - ab0[1] * cs; o[4 * g + 1] = ab0[2] * av[4 * g + 1] - ab0[3] * cs;
                o[4 * g + 2] = ab1[0] * av[4 * g + 2] - ab1[1] * cs; o[4 * g + 3] = ab1[2] * av[4 * g + 3] - ab1[3] * cs;
            }
#pragma unroll
            for (int s = 0; s < 2; ++s) {
                u32x4 v = not_poison((u32x4){pk(o[8 * s], o[8 * s + 1]), pk(o[8 * s + 2], o[8 * s + 3]), pk(o[8 * s + 4], o[8 * s + 5]), pk(o[8 * s + 6], o[8 * s + 7])});
                if (L == 1 && ts >= 0 && s == (ts >> 4) && h == ((ts >> 2) & 1)) { vlate = v; v = pois; }
                stw(c.ws, vbase + (u32)s * 512u, v);
            }
        }
        DRAIN(); BAR();
        if (w == 0 && lane == 0) set_flag(c, (L ? OFF_FQ1 : OFF_FQ0) + (u32)(hp * 16 + rb) * 128u);
        if (L == 1 && ts >= 0) {
            lds_wait_ge(P_PATCH, 1u);
            if (r == ts) {
#pragma unroll
                for (int t = 0; t < 2; ++t)
#pragma unroll
                    for (int a = 0; a < 2; ++a) {
                        const f32x4 p0 = lds_ld<f32x4>(FS0 + 4608 + (u32)(t * 32 + 16 * a + 4 * h) * 4u), p1 = lds_ld<f32x4>(FS0 + 4608 + (u32)(t * 32 + 16 * a + 8 + 4 * h) * 4u);
                        stw(c.ws, (t ? OK_ : OQ) + (u32)(((hp * 2 + a) * 64 + T) * 64 + lane) * 16u, not_poison((u32x4){pk(p0[0], p0[1]), pk(p0[2], p0[3]), pk(p1[0], p1[1]), pk(p1[2], p1[3])}));
                    }
            }
            if (h == ((ts >> 2) & 1)) {
                const u32 pv = pk(lds_ld<float>(FS0 + 4608 + (u32)(64 + r) * 4u), 0.f) & 0xFFFFu;
                const int jj = (ts & 3) + 4 * ((ts >> 3) & 1);
#pragma unroll
                for (int k = 0; k < 4; ++k) if (k == (jj >> 1)) vlate[k] = (jj & 1) ? ((vlate[k] & 0xFFFFu) | (pv << 16)) : ((vlate[k] & 0xFFFF0000u) | pv);
                stw(c.ws, vbase + (u32)(ts >> 4) * 512u, not_poison(vlate));
            }
        }
        if (L == 0) keep_wq(c, pq);
    }
}

struct AState { f32x16 Sa, Sb, Oa, Ob; bf16x8 kC, kN, v0, v1; u32x4 pb1; };
#define EXP2(x) __builtin_amdgcn_exp2f(x)
#define BC8(x) __builtin_bit_cast(bf16x8, x)
#define PVM(O, v, p) O = MFMA32(v, BC8(p), O)
template <bool USE_C, class HOOK>
DI void attn_step(AState& s, bf16x8 qa, bf16x8 qb, const f32x16& ca, const f32x16& cb, u32 kp, u32 vp0, u32 vp1, u32 knext, u32 voff, HOOK hook) {
    const f32x16 zero = {};
    u32x4 pa0, pa1, pb0;
    s.kN = lds_ld<bf16x8>(kp + knext); SBAR();
    s.v0 = lds_ld<bf16x8>(vp0 + voff); SBAR();
    PVM(s.Ob, s.v1, s.pb1); SBAR();
    s.v1 = lds_ld<bf16x8>(vp1 + voff); SBAR();
    float a0 = EXP2(s.Sa[0]); SBAR(); float a1 = EXP2(s.Sa[1]); SBAR(); float a2 = EXP2(s.Sa[2]); SBAR(); float a3 = EXP2(s.Sa[3]); SBAR();
    s.Sb = MFMA32(s.kC, qb, USE_C ? cb : zero); SBAR();
    float a4 = EXP2(s.Sa[4]); SBAR(); float a5 = EXP2(s.Sa[5]); SBAR(); float a6 = EXP2(s.Sa[6]); SBAR(); float a7 = EXP2(s.Sa[7]); SBAR();
    pa0[0] = pk(a0, a1); SBAR(); pa0[1] = pk(a2, a3); SBAR(); float a8 = EXP2(s.Sa[8]); SBAR(); pa0[2] = pk(a4, a5); SBAR(); pa0[3] = pk(a6, a7); SBAR();
    float a9 = EXP2(s.Sa[9]); SBAR();
    asm volatile("" : "+v"(s.v0), "+v"(s.v1), "+v"(s.kN)); SBAR();
    float a10 = EXP2(s.Sa[10]); SBAR();
    PVM(s.Oa, s.v0, pa0); SBAR();
    float a11 = EXP2(s.Sa[11]); SBAR(); float a12 = EXP2(s.Sa[12]); SBAR(); float a13 = EXP2(s.Sa[13]); SBAR(); float a14 = EXP2(s.Sa[14]); SBAR(); float a15 = EXP2(s.Sa[15]); SBAR();
    pa1[0] = pk(a8, a9); SBAR(); pa1[1] = pk(a10, a11); SBAR(); pa1[2] = pk(a12, a13); SBAR(); pa1[3] = pk(a14, a15); SBAR();
    float b0 = EXP2(s.Sb[0]); SBAR(); float b1 = EXP2(s.Sb[1]); SBAR();
    PVM(s.Oa, s.v1, pa1); SBAR();
    hook(); SBAR();
    float b2 = EXP2(s.Sb[2]); SBAR(); float b3 = EXP2(s.Sb[3]); SBAR(); float b4 = EXP2(s.Sb[4]); SBAR(); float b5 = EXP2(s.Sb[5]); SBAR();
    s.Sa = MFMA32(s.kN, qa, USE_C ? ca : zero); SBAR();
    float b6 = EXP2(s.Sb[6]); SBAR(); float b7 = EXP2(s.Sb[7]); SBAR();
    pb0[0] = pk(b0, b1); SBAR(); pb0[1] = pk(b2, b3); SBAR(); float b8 = EXP2(s.Sb[8]); SBAR(); pb0[2] = pk(b4, b5); SBAR(); pb0[3] = pk(b6, b7); SBAR();
    float b9 = EXP2(s.Sb[9]); SBAR(); float b10 = EXP2(s.Sb[10]); SBAR();
    PVM(s.Ob, s.v0, pb0); SBAR();
    float b11 = EXP2(s.Sb[11]); SBAR(); float b12 = EXP2(s.Sb[12]); SBAR(); float b13 = EXP2(s.Sb[13]); SBAR(); float b14 = EXP2(s.Sb[14]); SBAR(); float b15 = EXP2(s.Sb[15]); SBAR();
    s.pb1[0] = pk(b8, b9); SBAR(); s.pb1[1] = pk(b10, b11); SBAR(); s.pb1[2] = pk(b12, b13); SBAR(); s.pb1[3] = pk(b14, b15); SBAR();
    s.kC = s.kN;
}
struct NoHook { DI void operator()() const {} };
template <bool USE_C>
DI void attn_chunk(AState& s, bf16x8 qa, bf16x8 qb, const f32x16& ca, const f32x16& cb, u32 kp, u32 vp0, u32 vp1) {
    attn_step<USE_C>(s, qa, qb, ca, cb, kp, vp0, vp1, 1024, 0, NoHook());
    attn_step<USE_C>(s, qa, qb, ca, cb, kp, vp0, vp1, 2048, VSTR, NoHook());
    attn_step<USE_C>(s, qa, qb, ca, cb, kp, vp0, vp1, 3072, 2 * VSTR, NoHook());
    attn_step<USE_C>(s, qa, qb, ca, cb, kp, vp0, vp1, 8192, 3 * VSTR, NoHook());
}
DI void attn_begin(AState& s, bf16x8 qa, const f32x16& ca, u32 kp) {
    const f32x16 zero = {};
    s.Oa = zero; s.Ob = zero; s.pb1 = (u32x4){0u, 0u, 0u, 0u}; s.v1 = BC8(s.pb1);
    s.kC = lds_ld<bf16x8>(kp);
    s.Sa = MFMA32(s.kC, qa, ca);
}
DI void attn_combine(const Ctx& c, AState& s, float (&O)[9]) {
    s.Ob = MFMA32(s.v1, BC8(s.pb1), s.Ob);
    const f32x16& snd = s.Ob; const f32x16& keep = s.Oa;
    const u32 xb = L_AX + (u32)c.w * 2304u;
    lds_st<f32x4>(xb + (u32)c.lane * 16u, (f32x4){snd[0], snd[1], snd[2], snd[3]});
    lds_st<f32x4>(xb + 1024u + (u32)c.lane * 16u, (f32x4){snd[4], snd[5], snd[6], snd[7]});
    lds_st<float>(xb + 2048u + (u32)c.lane * 4u, snd[8]);
    BAR();
    const u32 xp = L_AX + (u32)(c.w ^ 4) * 2304u;
    const f32x4 u0 = lds_ld<f32x4>(xp + (u32)c.lane * 16u), u1 = lds_ld<f32x4>(xp + 1024u + (u32)c.lane * 16u);
    const float l2 = lds_ld<float>(xp + 2048u + (u32)c.lane * 4u);
#pragma unroll
    for (int q = 0; q < 4; ++q) { O[q] = keep[q] + u0[q]; O[4 + q] = keep[4 + q] + u1[q]; }
    O[8] = keep[8] + l2;
}

DI void phaseA(const Ctx& c0, const Params& p, const int L) {
    Ctx c = c0; FRESH(c);
    u32x4 pf[7] = {}, po[8];
    const int lane = c.lane, w = c.w, tid = w * 64 + lane;
    const int head = 4 * c.xg + (c.gi >> 3), qb = c.gi & 7;
    const int j = w & 3, kh = w >> 2;
    const u32 OQ = L ? OFF_Q1 : OFF_Q0, OK_ = L ? OFF_K1 : OFF_K0, OV = L ? OFF_V1 : OFF_V0, OO = L ? OFF_O1 : OFF_O0;
    if (L == 1 && w == 0) {
        wait_flags(c, OFF_FQ1 + (u32)((head >> 1) * 16 + (lane & 15)) * 128u);
        {
            const int sel = __builtin_amdgcn_readfirstlane((int)lds_ld<u32>(P_SEL)) & 2047, T = sel >> 5, ts = sel & 31;
            const u32 tk = (u32)((head * 64 + T) * 64 + (lane & 1) * 32 + ts) * 16u;
            u32 off = OK_ + tk;
            if (lane >= 2 && lane < 18) off = OV + (u32)((head * 64 + T) * 1024 + (ts >> 4) * 512 + ((ts >> 2) & 1) * 256 + (lane - 2) * 16);
            if (lane >= 18 && lane < 20 && (T >> 3) == qb) off = OQ + tk;
            u32x4 v = ldw(c.ws, off);
            wait_piece(c, off, v);
        }
        lds_signal_add(c, P_FQ);
    }
    if (L == 0 && w == 1) {
        const int qo_ = (4 * c.xg + (c.gi >> 3)) >> 2, hd = lane & 31, xg = hd >> 2, gi = (hd & 3) * 8 + qo_;
        u32 idx = (u32)((8 * (xg >> 2) + (gi >> 2)) * 16 + 4 * (xg & 3) + (gi & 3));
        if (lane >= 32) idx = (u32)((head >> 1) * 16 + (lane < 38 ? lane - 32 : lane < 40 ? 2 * qb + lane - 38 : 0));
        wait_flags(c, OFF_FP + idx * 128u);
    }
    BAR();
    if (tid == 0) lds_st<u32>(L_AX + 8 * 2304u, 0u);
    const u32 koff = OK_ + (u32)((head * 64 + w) * 64 + lane) * 16u, voff = OV + (u32)((head * 64 + w) * 64 + lane) * 16u;
    u32x4 rk[8], rv[8];
    const u32 qo = OQ + (u32)((head * 64 + qb * 8 + 2 * j) * 64 + lane) * 16u;
    u32x4 qar, qbr;
    {
        u32 n = 0;
        for (;;) {
            rk[0] = ldw(c.ws, koff); rv[0] = ldw(c.ws, voff);
            qar = ldw(c.ws, qo + (u32)kh * 1024u); qbr = ldw(c.ws, qo + (u32)(1 - kh) * 1024u);
            rk[1] = ldw(c.ws, koff + 8192u); rv[1] = ldw(c.ws, voff + 8192u); rk[2] = ldw(c.ws, koff + 16384u); rv[2] = ldw(c.ws, voff + 16384u);
            MEMFENCE();
            const bool bad = poisoned(rk[0]) || poisoned(rv[0]) || poisoned(qar) || poisoned(qbr) || poisoned(rk[1]) || poisoned(rv[1]) || poisoned(rk[2]) || poisoned(rv[2]);
            if (!__any(bad)) break;
            if (spin_fail(c, n)) break;
        }
    }
    { const int t = tid >> 3, s = tid & 7; const u32 val = (s & 3) == 0 ? 0x3f803f80u : 0u;
      lds_st<u32x4>(L_V + (u32)t * VSTR + 1024u + (u32)s * 16u, (u32x4){val, val, val, val}); }
    const u32 kw = L_K + (u32)w * 1024u + (u32)lane * 16u, vw = L_V + (u32)w * VSTR + (u32)lane * 16u;
#pragma unroll
    for (int ch = 0; ch < 3; ++ch) { lds_st<u32x4>(kw + (u32)ch * 8192u, rk[ch]); lds_st<u32x4>(vw + (u32)ch * (8u * VSTR), rv[ch]); }
    BAR();
    const int r = lane & 31, h = lane >> 5;
    const u32 kp0 = L_K + (u32)kh * 4096u + (u32)lane * 16u;
    const u32 onesoff = 1024u + ((r >= 20 && r < 28) ? 64u : 0u) + ((r == 16 || r == 20) ? 0u : 16u);
    const u32 vp00 = L_V + (u32)kh * (4u * VSTR) + (r < 16 ? (u32)(h * 256 + r * 16) : onesoff);
    const u32 vp10 = L_V + (u32)kh * (4u * VSTR) + (r < 16 ? (u32)(512 + h * 256 + r * 16) : onesoff);
    const bf16x8 qa = BC8(qar), qbf = BC8(qbr);
    const f32x16 cz = {};
    AState s;
#define PRIO(n) __builtin_amdgcn_s_setprio(n)
    PRIO(3);
    attn_begin(s, qa, cz, kp0);
    {
#define PUT(ch) [&]() { lds_st<u32x4>(kw + (u32)(ch) * 8192u, rk[ch]); lds_st<u32x4>(vw + (u32)(ch) * (8u * VSTR), rv[ch]); }
#define GET(ch) [&]() { rk[ch] = ldw(c.ws, koff + (u32)(ch) * 8192u); rv[ch] = ldw(c.ws, voff + (u32)(ch) * 8192u); }
#define FF(j_) [&]() { if (ff.on && ((j_) < 4 || !ff.gu)) pf[j_] = gld(ff.m, ff.vo + (u32)(j_) * ff.step); }
#define WO(j_) [&]() { po[j_] = gld(p.wo, wv + (u32)(j_) * 16384u); }
#define ST(ch, t, HOOK) attn_step<false>(s, qa, qbf, cz, cz, kp, vp0, vp1, ((t) == 3 ? 8192 : ((t) + 1) * 1024) + ((ch) & 1) * 8192, (((ch) & 1) * 8 + (t)) * VSTR, HOOK)
#define NEXT2() do { kp += 16384; vp0 += 16 * VSTR; vp1 += 16 * VSTR; asm volatile("" : "+v"(kp), "+v"(vp0), "+v"(vp1)); } while (0)
        u32 kp = kp0, vp0 = vp00, vp1 = vp10;
#define FLAGS() [&]() { if (L == 0 && w == 4) { wait_flags(c, OFF_FQ0 + (u32)((head >> 1) * 16 + (lane & 15)) * 128u); lds_signal_add(c, P_FQ); } }
#define GET2(a_, b_) [&]() { GET(a_)(); GET(b_)(); }
#define GO2(a_, b_) [&]() { lds_wait_ge(P_FQ, (u32)L + 1u); GET(a_)(); GET(b_)(); }
#define WO2(j_) [&]() { WO(j_)(); WO((j_) + 1)(); }
        ST(0, 0, FLAGS()); ST(0, 1, NoHook()); ST(0, 2, NoHook()); ST(0, 3, GO2(3, 4));
        ST(1, 0, GET2(5, 6)); ST(1, 1, GET(7)); ST(1, 2, PUT(3)); ST(1, 3, PUT(4));
        NEXT2(); PRIO(2);
        ST(2, 0, PUT(5)); ST(2, 1, PUT(6)); ST(2, 2, PUT(7));
        BAR();
        ST(2, 3, NoHook());
        const u32 wv = wo_lane_off(c);
        ST(3, 0, WO2(0)); ST(3, 1, WO2(2)); ST(3, 2, WO2(4)); ST(3, 3, WO2(6));
        NEXT2(); PRIO(1);
        const FfSrc ff = ff_src(c, p);
        ST(4, 0, FF(0)); ST(4, 1, FF(1)); ST(4, 2, FF(2)); ST(4, 3, FF(3));
        ST(5, 0, FF(4)); ST(5, 1, FF(5)); ST(5, 2, FF(6)); ST(5, 3, NoHook());
#undef FLAGS
#undef GET2
#undef GO2
#undef WO2
        NEXT2(); PRIO(0);
        for (int ch = 6; ch < 8; ++ch) {
            attn_chunk<false>(s, qa, qbf, cz, cz, kp, vp0, vp1);
            kp += 8192; vp0 += 8 * VSTR; vp1 += 8 * VSTR;
            asm volatile("" : "+v"(kp), "+v"(vp0), "+v"(vp1));
        }
#undef PUT
#undef GET
#undef FF
#undef WO
#undef ST
#undef NEXT2
    }
    FRESH(c);
    float O[9];
    attn_combine(c, s, O);
    const u32 oo = OO + (u32)((head * 64 + qb * 8 + 2 * j + kh) * 64 + c.lane) * 16u;
    bool redo;
    {
        bool bad = !(O[8] >= 0x1p-60f && O[8] <= 0x1p60f);
#pragma unroll
        for (int q = 0; q < 8; ++q) bad = bad || !(__builtin_fabsf(O[q]) <= 0x1p126f);
        redo = __any(bad);
        if (redo) { if (c.lane == 0) lds_st<u32>(L_AX + 8 * 2304u, 1u); }
        else {
            const float rl = __builtin_amdgcn_rcpf(O[8]);
            u32x4 o;
#pragma unroll
            for (int q = 0; q < 4; ++q) o[q] = pk(O[2 * q] * rl, O[2 * q + 1] * rl);
            stw(c.ws, oo, not_poison(o));
        }
    }
    pre_store_wo(c, po);
    BAR();
    if (__builtin_amdgcn_readfirstlane((int)lds_ld<u32>(L_AX + 8 * 2304u)) != 0) {
        {
            const u32 ko = OK_ + (u32)((head * 64 + w) * 64 + c.lane) * 16u, vo = OV + (u32)((head * 64 + w) * 64 + c.lane) * 16u;
            for (int ch = 0; ch < 4; ++ch) {
                const u32x4 a = ldw(c.ws, ko + (u32)ch * 8192u), b_ = ldw(c.ws, vo + (u32)ch * 8192u);
                lds_st<u32x4>(L_K + (u32)(ch * 8 + w) * 1024u + (u32)c.lane * 16u, a);
                lds_st<u32x4>(L_V + (u32)(ch * 8 + w) * VSTR + (u32)c.lane * 16u, b_);
            }
            { const int t = (w * 64 + c.lane) >> 3, s_ = c.lane & 7; const u32 val = (s_ & 3) == 0 ? 0x3f803f80u : 0u;
              lds_st<u32x4>(L_V + (u32)t * VSTR + 1024u + (u32)s_ * 16u, (u32x4){val, val, val, val}); }
        }
        BAR();
        float ma = -INFINITY, mb = -INFINITY;
        for (int ch = 0; ch < 8; ++ch)
            for (int t = 0; t < 4; ++t) {
                const bf16x8 kf = lds_ld<bf16x8>(kp0 + (u32)(ch * 8192 + t * 1024));
                const f32x16 Sa = MFMA32(kf, qa, cz), Sb = MFMA32(kf, qbf, cz);
#pragma unroll
                for (int q = 0; q < 16; ++q) { ma = fmaxf(ma, Sa[q]); mb = fmaxf(mb, Sb[q]); }
            }
        ma = fmaxf(ma, __shfl_xor(ma, 32)); mb = fmaxf(mb, __shfl_xor(mb, 32));
        const u32 xb = L_AX + (u32)w * 2304u, xp = L_AX + (u32)(w ^ 4) * 2304u;
        lds_st<f32x2>(xb + (u32)c.lane * 8u, (f32x2){ma, mb});
        BAR();
        const f32x2 mo = lds_ld<f32x2>(xp + (u32)c.lane * 8u);
        ma = fmaxf(ma, mo[1]); mb = fmaxf(mb, mo[0]);
        BAR();
        f32x16 ca, cb;
#pragma unroll
        for (int q = 0; q < 16; ++q) { ca[q] = -ma; cb[q] = -mb; }
        attn_begin(s, qa, ca, kp0);
        u32 kp = kp0, vp0 = vp00, vp1 = vp10;
        for (int ch = 0; ch < 8; ++ch) {
            attn_chunk<true>(s, qa, qbf, ca, cb, kp, vp0, vp1);
            kp += 8192; vp0 += 8 * VSTR; vp1 += 8 * VSTR;
        }
        attn_combine(c, s, O);
        if (redo) {
            const float rl = 1.0f / O[8];
            u32x4 o;
#pragma unroll
            for (int q = 0; q < 4; ++q) o[q] = pk(O[2 * q] * rl, O[2 * q + 1] * rl);
            stw(c.ws, oo, not_poison(o));
        }
        {
            const FfSrc ff = ff_src(c, p); const u32 wv = wo_lane_off(c);
#pragma unroll
            for (int q = 0; q < 7; ++q) if (ff.on && (q < 4 || !ff.gu)) pf[q] = gld(ff.m, ff.vo + (u32)q * ff.step);
#pragma unroll
            for (int q = 0; q < 8; ++q) po[q] = gld(p.wo, wv + (u32)q * 16384u);
        }
        BAR();
        pre_store_wo(c, po);
        BAR();
    }
    pre_store_ff(c, pf);
}

DI void phaseO(const Ctx& c0, const Params& p, u32x4 (&x2)[2], u32& hsync, const u32x4 (&pq)[12], const int L) {
    Ctx c = c0; FRESH(c);
    const int lane = c.lane, w = c.w;
    const int tb = 4 * c.xg + (c.gi >> 3), fb = c.gi & 7;
    const int r = lane & 31, h = lane >> 5;
    const u32 OO = L ? OFF_O1 : OFF_O0, GS = L ? OFF_GS1 : OFF_GS0, GC = L ? OFF_GC1 : OFF_GC0, GM = L ? OFF_GM1 : OFF_GM0, GX = L ? OFF_GX1 : OFF_GX0;
    constexpr u32 L_TS = L_SB, L_CAND = P_ST + 8;
    if (w >= 4) {
        const int hw = w - 4;
        BAR();
        BAR();
        __builtin_amdgcn_s_setprio(3);
        if (hw == 0) {
            const u32 a = L_SB + (u32)h * 384u + (u32)r * 12u;
            const float t1 = lds_ld<float>(a) + lds_ld<float>(a + 768), t2 = lds_ld<float>(a + 4) + lds_ld<float>(a + 772), t3 = lds_ld<float>(a + 8) + lds_ld<float>(a + 776);
            stw(c.ws, GS + (u32)((tb * 8 + fb) * 1024 + lane * 16), (u32x4){__float_as_uint(t1), __float_as_uint(t2), __float_as_uint(t3), c.tag});
        }
        if (L == 0) {
            if (hw != 0) { pre_store_wq(c, pq); lds_signal_add(c, P_WQ); }
            BAR();
        }
        if (hw == 0) {
            u32x4 v[8]; u32 n = 0;
            for (;;) {
                bool ok = true;
#pragma unroll
                for (int k = 0; k < 8; ++k) v[k] = ldw(c.ws, GS + (u32)((tb * 8 + k) * 1024 + lane * 16));
                MEMFENCE();
#pragma unroll
                for (int k = 0; k < 8; ++k) ok = ok && v[k][3] == c.tag;
                if (__all(ok)) break;
                if (spin_fail(c, n)) break;
            }
            float t1 = 0, t2 = 0, t3 = 0;
#pragma unroll
            for (int k = 0; k < 8; ++k) { t1 += __uint_as_float(v[k][0]); t2 += __uint_as_float(v[k][1]); t3 += __uint_as_float(v[k][2]); }
            const float mu = t1 * (1.0f / 512.0f), var = fmaxf(t2 * (1.0f / 512.0f) - mu * mu, 0.f);
            const float rs = __builtin_amdgcn_rsqf(var + 1e-5f);
            const float rt = rbf(rs * (t3 - mu * lds_ld<float>(P_WSUM)));
            const u32 key = half_umax(score_key(__float_as_uint(rt) >> 16, (u32)r));
            const int bt = (int)key_idx(key);
            if (fb == 0) {
                const u32 k0 = (u32)__builtin_amdgcn_readlane((int)key, 0), k1 = (u32)__builtin_amdgcn_readlane((int)key, 32);
                const u32 kT = (lane & 1) ? k1 : k0;
                const u64 g = (cand_tag(c.tag) << 21) | (u64)(key_bf(kT) << 5) | (u64)key_idx(kT);
                if (lane < 16) __builtin_amdgcn_raw_buffer_store_b64((u32x2){(u32)g, (u32)(g >> 32)}, c.ws, GC + (u32)(lane >> 1) * 512u + (u32)(tb * 2 + (lane & 1)) * 8u, 0, 16);
                if (r == bt) stw(c.ws, GM + (u32)(tb * 2 + h) * 16u, mk_unit(__float_as_uint(mu), __float_as_uint(rs), c.tag));
                if (L == 0) {
                    const float g1 = 1.0f + rs, A = g1 * __builtin_amdgcn_rsqf(var * g1 * g1 + 1e-5f);
                    stw(c.ws, OFF_TS1 + (u32)(tb * 64 + lane) * 16u, mk_unit(__float_as_uint(A), __float_as_uint(A * mu), c.tag));
                }
            }
            lds_st<f32x2>(L_TS + (u32)lane * 8u, (f32x2){mu, rs});
            if (r == 0) lds_st<u32>(L_CAND + (u32)h * 4u, (u32)bt);
            lds_signal_add(c, P_ST + L * 4);
            {
                const int ct = (lane >> 4) & 1, u = lane & 15;
                const int cb = __builtin_amdgcn_readlane(bt, 0), cb1 = __builtin_amdgcn_readlane(bt, 32);
                const int row = ct ? cb1 : cb;
                const u32x2 d = lds_ld<u32x2>(L_X1T + (u32)(ct + 2 * (u >> 3)) * 2048u + (u32)((u >> 2) & 1) * 1024u + (u32)((u & 1) * 32 + row) * 16u + (u32)((u >> 1) & 1) * 8u);
                if (lane < 32) stw(c.ws, GX + (u32)(tb * 2 + ct) * 2048u + (u32)(fb * 16 + u) * 16u, mk_unit(d[0], d[1], c.tag));
            }
            if (L == 0) { pre_store_wq(c, pq); lds_signal_add(c, P_WQ); }
        }
        ffn_chain(c, p, hsync, L);
        return;
    }
    const int tt = w & 1, kh = w >> 1;
    const int tile = tb * 2 + tt, tok = tile * 32 + r;
    const int f0 = fb * 64 + kh * 32;
    u32x2 res[4], wr[4];
#pragma unroll
    for (int g = 0; g < 4; ++g) {
        if (L == 0) res[g] = *(const u32x2*)(p.x0 + (size_t)tok * 512 + f0 + 8 * g + 4 * h);
        wr[g] = *(const u32x2*)(p.wr + f0 + 8 * g + 4 * h);
    }
    if (L == 1) {
        const int sel0 = __builtin_amdgcn_readfirstlane((int)lds_ld<u32>(P_SEL)) & 2047;
        if ((sel0 >> 5) == tile) {
            const u32 ub = OFF_GY + (u32)(f0 / 4 + h) * 16u;
            u32 off[4] = {ub, ub + 32u, ub + 64u, ub + 96u}; u32x4 v[4];
            wait_units<4>(c, off, v);
            if (r == (sel0 & 31)) { x2[0] = (u32x4){v[0][0], v[0][2], v[1][0], v[1][2]}; x2[1] = (u32x4){v[2][0], v[2][2], v[3][0], v[3][2]}; }
        }
#pragma unroll
        for (int g = 0; g < 4; ++g) res[g] = (u32x2){x2[g >> 1][(g & 1) * 2], x2[g >> 1][(g & 1) * 2 + 1]};
    }
    u32x4 bf[16];
    const u32 oo = OO + (u32)(((kh * 16) * 64 + tile) * 64 + lane) * 16u;
    {
        u32 n = 0;
        for (;;) {
#pragma unroll
            for (int s = 0; s < 16; ++s) bf[s] = ldw(c.ws, oo + (u32)s * 65536u);
            MEMFENCE();
            u32 m = 0;
#pragma unroll
            for (int s = 0; s < 16; ++s) m = max(m, max(max(bf[s][0], bf[s][1]), max(bf[s][2], bf[s][3])));
            if (!__any(m == POISON)) break;
            if (spin_fail(c, n)) break;
        }
    }
    const int i16 = lane & 15, blk = (lane >> 4) & 1;
    const u32 trA = L_WO + (u32)((kh * 256 + 4 * h + (i16 >> 2)) * 64 + blk * 32 + (i16 & 3) * 8);
    f32x16 a0 = {}, a1 = {};
    {
        u32 ta = trA + (u32)kh * WO_FT, tb_ = trA + (u32)(1 - kh) * WO_FT;
        asm volatile("" : "+v"(ta), "+v"(tb_));
        bf16x8 f0_[2], f1_[2];
        f0_[0] = frag_tr(ta, ta + 512); f0_[1] = frag_tr(tb_, tb_ + 512);
#pragma unroll
        for (int s = 0; s < 16; s += 2) {
            f1_[0] = frag_tr(ta + (u32)(s + 1) * 1024u, ta + (u32)(s + 1) * 1024u + 512); SBAR();
            a0 = MFMA32(f0_[0], BC8(bf[s]), a0); SBAR();
            f1_[1] = frag_tr(tb_ + (u32)(s + 1) * 1024u, tb_ + (u32)(s + 1) * 1024u + 512); SBAR();
            a1 = MFMA32(f0_[1], BC8(bf[s]), a1); SBAR();
            if (s + 2 < 16) f0_[0] = frag_tr(ta + (u32)(s + 2) * 1024u, ta + (u32)(s + 2) * 1024u + 512);
            SBAR(); a0 = MFMA32(f1_[0], BC8(bf[s + 1]), a0); SBAR();
            if (s + 2 < 16) f0_[1] = frag_tr(tb_ + (u32)(s + 2) * 1024u, tb_ + (u32)(s + 2) * 1024u + 512);
            SBAR(); a1 = MFMA32(f1_[1], BC8(bf[s + 1]), a1); SBAR();
        }
    }
    {
        const f32x16& snd = a1;
#pragma unroll
        for (int g = 0; g < 4; ++g) lds_st<f32x4>(L_XB + (u32)w * 4096u + (u32)g * 1024u + (u32)lane * 16u, (f32x4){snd[4 * g], snd[4 * g + 1], snd[4 * g + 2], snd[4 * g + 3]});
    }
    BAR();
    u32x4 xp[2];
    {
        const f32x16& keep = a0;
#pragma unroll
        for (int g = 0; g < 4; ++g) {
            const f32x4 o = lds_ld<f32x4>(L_XB + (u32)(w ^ 2) * 4096u + (u32)g * 1024u + (u32)lane * 16u);
            const u32 t0 = pk(keep[4 * g] + o[0], keep[4 * g + 1] + o[1]), t1 = pk(keep[4 * g + 2] + o[2], keep[4 * g + 3] + o[3]);
            xp[g >> 1][(g & 1) * 2] = pk(bflo(res[g][0]) + bflo(t0), bfhi(res[g][0]) + bfhi(t0));
            xp[g >> 1][(g & 1) * 2 + 1] = pk(bflo(res[g][1]) + bflo(t1), bfhi(res[g][1]) + bfhi(t1));
        }
    }
    if (L == 0) {
#pragma unroll
        for (int s = 0; s < 2; ++s) stw(c.ws, OFF_X1 + (u32)((tile * 32 + f0 / 16 + s) * 64 + lane) * 16u, xp[s]);
    }
#pragma unroll
    for (int s = 0; s < 2; ++s) lds_st<u32x4>(L_X1T + (u32)w * 2048u + (u32)s * 1024u + (u32)lane * 16u, xp[s]);
    float x1[16];
    {
        float s1 = 0, s2 = 0, s3 = 0;
#pragma unroll
        for (int i = 0; i < 16; ++i) {
            const u32 wd = xp[i >> 3][(i & 7) >> 1];
            x1[i] = (i & 1) ? bfhi(wd) : bflo(wd);
            const u32 ww = wr[i >> 2][(i & 3) >> 1];
            const float wv = (i & 1) ? bfhi(ww) : bflo(ww);
            s1 += x1[i]; s2 += x1[i] * x1[i]; s3 += x1[i] * wv;
        }
        s1 = swap32_add(s1); s2 = swap32_add(s2); s3 = swap32_add(s3);
        if (h == 0) { const u32 a = L_SB + (u32)w * 384u + (u32)r * 12u; lds_st<float>(a, s1); lds_st<float>(a + 4, s2); lds_st<float>(a + 8, s3); }
    }
    BAR();
    if (L == 0) {
        DRAIN(); BAR();
        if (w == 0 && lane == 0) set_flag(c, OFF_FO + (u32)(tb * 8 + fb) * 128u);
        pre_store_wq(c, pq); lds_signal_add(c, P_WQ);
    }
    lds_wait_ge(P_ST + L * 4, 1u);
    const f32x2 st = lds_ld<f32x2>(L_TS + (u32)(tt * 32 + r) * 8u);
    const int bt = (int)lds_ld<u32>(L_CAND + (u32)tt * 4u) & 31;
#pragma unroll
    for (int s = 0; s < 2; ++s)
#pragma unroll
        for (int j = 0; j < 4; ++j) {
            const float a = x1[8 * s + 2 * j], b_ = x1[8 * s + 2 * j + 1];
            const u32 hh = pk((a - st[0]) * st[1], (b_ - st[0]) * st[1]);
            x2[s][j] = pk(a + bflo(hh), b_ + bfhi(hh));
        }
    if (L == 1) {
        const rsrc_t outr = __builtin_amdgcn_make_buffer_rsrc(p.out, 0, 2097152, 0x00020000);
        const u32 tl = L_XB + (u32)w * 4096u;
#pragma unroll
        for (int g = 0; g < 4; ++g) lds_st<u32x2>(tl + (u32)(r * 80 + (8 * g + 4 * h) * 2), (u32x2){x2[g >> 1][(g & 1) * 2], x2[g >> 1][(g & 1) * 2 + 1]});
        asm volatile("s_waitcnt lgkmcnt(0)" ::: "memory");
        u32x4 rowv[2];
#pragma unroll
        for (int q = 0; q < 2; ++q) {
            const int row = q * 16 + (lane >> 2);
            rowv[q] = lds_ld<u32x4>(tl + (u32)(row * 80 + (lane & 3) * 16));
            if (row != bt) __builtin_amdgcn_raw_buffer_store_b128(rowv[q], outr, (u32)(((tile * 32 + row) * 512 + f0 + (lane & 3) * 8) * 2), 0, 16);
        }
        wait_sel(1);
        const int sel1 = __builtin_amdgcn_readfirstlane((int)lds_ld<u32>(P_SEL + 32)) & 2047;
        if (tile * 32 + bt != sel1) {
#pragma unroll
            for (int q = 0; q < 2; ++q) {
                const int row = q * 16 + (lane >> 2);
                if (row == bt) __builtin_amdgcn_raw_buffer_store_b128(rowv[q], outr, (u32)(((tile * 32 + row) * 512 + f0 + (lane & 3) * 8) * 2), 0, 16);
            }
        }
    }
}

#if defined(__HIP_DEVICE_COMPILE__)
__attribute__((target("no-packed-fp32-ops")))
#endif
__global__ __launch_bounds__(512, 2) void fused_layers_kernel(Params p) {
    extern __shared__ __attribute__((aligned(16))) char lds[];
    Ctx c;
    const int tid = threadIdx.x;
    c.lane = tid & 63; c.w = __builtin_amdgcn_readfirstlane(tid >> 6);
    c.xg = blockIdx.x & 7; c.gi = blockIdx.x >> 3;
    c.ws = __builtin_amdgcn_make_buffer_rsrc(p.ws, 0, WS_BYTES, 0x00020000);
    c.tag = 0u;
    if (c.w < 4) {
        u64 pc; u32 len;
        asm volatile("s_getpc_b64 %0\nfl_code_here:\n\ts_mov_b32 %1, fl_code_end-fl_code_here" : "=s"(pc), "=s"(len));
        if (c.w == 1) {
            const u32 o0 = min((u32)c.gi * 3072u + (u32)c.lane * 16u, len - 16u) & ~15u, o1 = min(o0 + 1024u, len - 16u) & ~15u, o2 = min(o0 + 2048u, len - 16u) & ~15u;
            const u64 base = pc & ~15ull;
            glds((const void*)base, o0, 147456u); glds((const void*)base, o1, 148480u); glds((const void*)base, o2, 149504u);
        }
        if (c.w == 2) {
            const int l = c.lane, k = (l - 22) % 14;
            const char* a = p.ws + (size_t)(l < 10 ? l : 0) * 0x200000;
            a = l >= 10 && l < 14 ? (const char*)p.wo + (l - 10) * 131072 : l == 14 ? (const char*)p.wr : a;
            a = l >= 22 ? (const char*)(l < 36 ? p.wg : l < 50 ? p.wu : p.wd) + k * 131072 : a;
            u32 sv;
            asm volatile("s_mov_b32 %0, m0\n\ts_mov_b32 m0, %2\n\ts_nop 0\n\tglobal_load_lds_dword %1, off\n\ts_mov_b32 m0, %0" : "=&s"(sv) : "v"(a), "s"(150528u) : "memory");
        }
        if (c.w == 3 && c.lane < 26 && c.lane != 3) lds_st<u32>(P_HCNT + (u32)c.lane * 4u, 0u);
    }
    asm volatile("" :: "v"(lds));
    u32 hsync = 0;
    u32x4 x2[2];
    asm volatile("" : "=v"(x2[0]), "=v"(x2[1]));
    u32x4 pq[12];
#pragma nounroll
    for (int Li = 0; Li < 2; ++Li) {
        int Lv = Li; asm volatile("" : "+v"(Lv));
        const int L = __builtin_amdgcn_readfirstlane(Lv);
        if (L == 0) phaseQ<0>(c, p, hsync, pq);
        else {
            phaseQ<1>(c, p, hsync, pq);
#pragma unroll
            for (int j = 0; j < 12; ++j) asm volatile("" : "=v"(pq[j]));
        }
        phaseA(c, p, L);
        phaseO(c, p, x2, hsync, pq, L);
    }
    if (blockIdx.x == 14 * 8 && c.w == 4 && c.lane == 0) __builtin_amdgcn_raw_buffer_store_b32(c.tag, c.ws, OFF_EPOCH, 0, 16);
    asm volatile("fl_code_end:" ::: "memory");
}

extern "C" void kernel_launch(void* const* d_in, const int* in_sizes, int n_in, void* d_out, int out_size, void* d_ws, size_t ws_size, hipStream_t stream) {
    Params p;
    p.x0 = (const uint16_t*)d_in[0]; p.wqkv = (const uint16_t*)d_in[1]; p.wo = (const uint16_t*)d_in[2];
    p.wr = (const uint16_t*)d_in[4]; p.wg = (const uint16_t*)d_in[5]; p.wu = (const uint16_t*)d_in[6]; p.wd = (const uint16_t*)d_in[7];
    p.out = (uint16_t*)d_out; p.ws = (char*)d_ws;
    (void)hipFuncSetAttribute((const void*)fused_layers_kernel, hipFuncAttributeMaxDynamicSharedMemorySize, LDS_BYTES);
    fused_layers_kernel<<<256, 512, LDS_BYTES, stream>>>(p);
}
```
